```python
import torch

def _softmax(x, axis=-1):
    return torch.softmax(x, dim=axis)

def _gelu(x):
    return torch.nn.functional.gelu(x, approximate='tanh')

def _silu(x):
    return torch.nn.functional.silu(x)

def _sigmoid(x):
    return torch.sigmoid(x)

def _layernorm(x, axis=-1, eps=1e-05):
    m = x.mean(dim=axis, keepdim=True)
    v = x.var(dim=axis, keepdim=True, unbiased=False)
    return (x - m) / torch.sqrt(v + eps)

def _scatter_add(tgt, idx, src, axis=0):
    return tgt.clone().index_add_(axis, idx, src)

def _segment_sum(src, seg, n):
    out = torch.zeros((n,) + src.shape[1:], dtype=src.dtype)
    return out.index_add_(0, seg, src)

def _pad(x, pw):
    flat = tuple((v for pair in reversed(pw) for v in pair))
    return torch.nn.functional.pad(x, flat)
kernel_name = 'adaLN_cond__sliding_block_cached__glu_gated_glu__decode'

def setup_inputs():

    _w = lambda a: (lambda t: t.to(torch.bfloat16) if t.is_floating_point() else t)(torch.as_tensor(a))

    def _A_setup():
        B, Sq, D = (8, 1, 512)
        Hq, Hkv, Sk, Dh = (32, 32, 4096, 128)
        assert Hq % Hkv == 0, (Hq, Hkv)
        return {'x': _w((lambda *s: torch.randn(*s, dtype=torch.bfloat16) * 0.05)(B, Sq, D)), 'k_cache': _w((lambda *s: torch.randn(*s, dtype=torch.bfloat16) * 3.2)(B, Hkv, Sk, Dh)), 'v_cache': _w((lambda *s: torch.randn(*s, dtype=torch.bfloat16) * 0.4)(B, Hkv, Sk, Dh)), 'wq': _w((lambda *s: torch.randn(*s, dtype=torch.bfloat16) * 3.2)(D, Hq * Dh) * D ** (-0.5)), 'wo': _w((lambda *s: torch.randn(*s, dtype=torch.bfloat16) * 0.4)(Hq * Dh, D) * (Hq * Dh) ** (-0.5))}
    _A_in = _A_setup()

    def _B_setup():
        B, S, D, F = (8, 1, 512, 1376)
        return {'x': _w((lambda *s: torch.randn(*s, dtype=torch.bfloat16) * 0.05)(B, S, D)), 'w_gate': _w((lambda *s: torch.randn(*s, dtype=torch.bfloat16) * 0.05)(D, F)), 'w_up': _w((lambda *s: torch.randn(*s, dtype=torch.bfloat16) * 0.05)(D, F)), 'w_down': _w((lambda *s: torch.randn(*s, dtype=torch.bfloat16) * 0.05)(F, D))}
    _B_in = _B_setup()
    return {'A_x': _A_in['x'], 'A_k_cache': _A_in['k_cache'], 'A_v_cache': _A_in['v_cache'], 'A_wq': _A_in['wq'], 'A_wo': _A_in['wo'], 'B_x': _B_in['x'], 'B_w_gate': _B_in['w_gate'], 'B_w_up': _B_in['w_up'], 'B_w_down': _B_in['w_down'], 'c': _w((lambda *s: torch.randn(*s, dtype=torch.bfloat16) * 0.1)(8, 512) * 8.0), 'w_mod': _w((lambda *s: torch.randn(*s, dtype=torch.bfloat16) * 0.1)(512, 6 * 512) * 512 ** (-0.5) * 8.0)}

def reference(A_x, A_k_cache, A_v_cache, A_wq, A_wo, B_x, B_w_gate, B_w_up, B_w_down, c, w_mod):

    def _A_ref(x, k_cache, v_cache, wq, wo):
        B, Sq, D = x.shape
        Hq, Dh = (32, 128)
        Hkv, Sk = (k_cache.shape[1], k_cache.shape[2])
        W = min(4096, max(256, Sk // 4))
        rep = Hq // Hkv
        q = (lambda x, *d: x.permute(*d))((x @ wq).reshape(B, Sq, Hq, Dh), 0, 2, 1, 3)
        k = (lambda x, n, axis: torch.repeat_interleave(x, n, dim=axis))(k_cache[:, :, Sk - W:, :], rep, axis=1)
        v = (lambda x, n, axis: torch.repeat_interleave(x, n, dim=axis))(v_cache[:, :, Sk - W:, :], rep, axis=1)
        scores = torch.einsum('bhqd,bhkd->bhqk', q, k) * Dh ** (-0.5)
        p = _softmax(scores, axis=-1)
        o = torch.einsum('bhqk,bhkd->bhqd', p, v)
        return (lambda x, *d: x.permute(*d))(o, 0, 2, 1, 3).reshape(B, Sq, Hq * Dh) @ wo

    def _B_ref(x, w_gate, w_up, w_down):
        g = torch.einsum('bsd,df->bsf', x, w_gate)
        u = torch.einsum('bsd,df->bsf', x, w_up)
        h = _sigmoid(g) * u
        return torch.einsum('bsf,fd->bsd', h, w_down)
    x = A_x
    mod = (_silu(c) @ w_mod).reshape(c.shape[0], 6, 1, -1)
    sa, ca, ga, sb, cb, gb = (mod[:, 0].clone(), mod[:, 1].clone(), mod[:, 2].clone(), mod[:, 3].clone(), mod[:, 4].clone(), mod[:, 5].clone())
    h = _layernorm(x, axis=-1) * (1 + ca) + sa
    x = x + ga * _A_ref(h, A_k_cache, A_v_cache, A_wq, A_wo)
    h = _layernorm(x, axis=-1) * (1 + cb) + sb
    x = x + gb * _B_ref(h, B_w_gate, B_w_up, B_w_down)
    return x
```

```cpp
#define LEAK 99
#include <hip/hip_runtime.h>
#include <stdint.h>

typedef unsigned int u32;
typedef unsigned long long u64;
typedef unsigned short u16;
typedef u32 u32x4 __attribute__((ext_vector_type(4)));
typedef u32 u32x2 __attribute__((ext_vector_type(2)));
typedef float f32x4 __attribute__((ext_vector_type(4)));
typedef float f32x2 __attribute__((ext_vector_type(2)));
typedef short s16x4 __attribute__((ext_vector_type(4)));
typedef __bf16 bf16x8 __attribute__((ext_vector_type(8)));
typedef __bf16 bf16x2 __attribute__((ext_vector_type(2)));
#define GAS __attribute__((address_space(1)))
#define LAS __attribute__((address_space(3)))
#define RLX_AGENT __ATOMIC_RELAXED, __HIP_MEMORY_SCOPE_AGENT

constexpr int SK = 4096, WIN = 1024, FF = 1376, NP3 = 43;

constexpr int WS_EPOCH = 0;
constexpr int WS_FLAGS = 0x1000;
constexpr int WS_FLAG2 = 0x1800;
constexpr int WS_G1 = 0x2000;
constexpr int WS_MODR = 0x10000;
constexpr int WS_QP = 0x20000;
constexpr int WS_S = 0xA0000;
constexpr int WS_PART = WS_S;
constexpr int WS_YP = WS_PART + 8 * 16 * 32 * 32 * 4;
constexpr int WS_X2 = WS_YP + 8 * 16 * NP3 * 32 * 4;
constexpr int WS_O = WS_X2 + 8 * 512 * 2;
constexpr int WS_END = WS_O + 32 * 8 * 256;
constexpr int S_CHUNK = (WS_END - WS_S) / 128;
static_assert((WS_END - WS_S) % (128 * 16) == 0, "clear chunks are whole 16-byte pieces");

constexpr int L_W = 0;
constexpr int L_P1 = 65536;
constexpr int L_WOT = 81920;
constexpr int L_RED = 131072;
constexpr int OSTR = 544;
constexpr int SW = 9;
constexpr int L_H2 = L_RED + 16384;
constexpr int H2S = 1056;
constexpr int L_Q = L_H2 + 8 * H2S;
constexpr int L_OB = L_Q + 256;
constexpr int L_ML = L_OB + 256;
constexpr int L_HH = L_ML + 128;
constexpr int L_CTL = L_HH + 512;
constexpr int L_END = L_CTL + 64;
constexpr int C_P1CNT = 0, C_P1DONE = 1, C_QCNT = 2, C_Q = 3, C_ML = 4, C_WO = 5;

constexpr u32 SPIN_CAP = 1u << 17, LSPIN_CAP = 1u << 20;
#define TAU 61
#define TMID 59
#define TB 57
#define TP1 2
#define DODD 50
#define DEVN 30
#define DUE0 225
constexpr int T0 = TAU, TS = TAU + TAU / 4, T1 = TMID;
#define NPRE 2
#define WPRE 12
constexpr int MLI = 1, YWAIT = 150;

__device__ __forceinline__ float rbf(float f) { return (float)(__bf16)f; }
__device__ __forceinline__ u32 pack2(float a, float b) { bf16x2 v = {(__bf16)a, (__bf16)b}; return __builtin_bit_cast(u32, v); }
__device__ __forceinline__ float blo(u32 v) { return __builtin_bit_cast(float, v << 16); }
__device__ __forceinline__ float bhi(u32 v) { return __builtin_bit_cast(float, v & 0xffff0000u); }
__device__ __forceinline__ u32 canon(u32 v) { return v < 0xfffffffeu ? v : 0xfffffffeu; }
__device__ __forceinline__ u32x4 canon4(f32x4 v) {
  u32x4 r = __builtin_bit_cast(u32x4, v);
  return u32x4{canon(r[0]), canon(r[1]), canon(r[2]), canon(r[3])};
}
__device__ __forceinline__ u32 umax(u32 a, u32 b) { return a > b ? a : b; }
__device__ __forceinline__ u32 max4(u32x4 v) { return umax(umax(v[0], v[1]), umax(v[2], v[3])); }
__device__ __forceinline__ u32 max4(u32x4 v, u32 m) { return umax(umax(umax(v[0], v[1]), m), umax(v[2], v[3])); }
__device__ __forceinline__ bool full(u32 m) { return __all(m != 0xffffffffu); }
__device__ __forceinline__ float rcp(float x) { return __builtin_amdgcn_rcpf(x); }
template <int CTRL> __device__ __forceinline__ float dppf(float x) {
  return __builtin_bit_cast(float, __builtin_amdgcn_mov_dpp(__builtin_bit_cast(int, x), CTRL, 0xf, 0xf, true));
}
template <int CTRL> __device__ __forceinline__ u32 dppu(u32 x) { return (u32)__builtin_amdgcn_mov_dpp((int)x, CTRL, 0xf, 0xf, true); }
constexpr int ROR8 = 0x128, ROR4 = 0x124, ROR2 = 0x122, ROR1 = 0x121, XOR1 = 0xB1, XOR2 = 0x4E, XOR7 = 0x141;
__device__ __forceinline__ float row_sum(float x) { x += dppf<ROR8>(x); x += dppf<ROR4>(x); x += dppf<ROR2>(x); x += dppf<ROR1>(x); return x; }
__device__ __forceinline__ float row_max(float x) {
  x = fmaxf(x, dppf<ROR8>(x)); x = fmaxf(x, dppf<ROR4>(x)); x = fmaxf(x, dppf<ROR2>(x)); x = fmaxf(x, dppf<ROR1>(x)); return x;
}
__device__ __forceinline__ float x16_sum(float x) {
  auto s = __builtin_amdgcn_permlane16_swap(__float_as_uint(x), __float_as_uint(x), false, false);
  return __uint_as_float(s[0]) + __uint_as_float(s[1]);
}
__device__ __forceinline__ float x32_sum(float x) {
  auto s = __builtin_amdgcn_permlane32_swap(__float_as_uint(x), __float_as_uint(x), false, false);
  return __uint_as_float(s[0]) + __uint_as_float(s[1]);
}
__device__ __forceinline__ float x16_max(float x) {
  auto s = __builtin_amdgcn_permlane16_swap(__float_as_uint(x), __float_as_uint(x), false, false);
  return fmaxf(__uint_as_float(s[0]), __uint_as_float(s[1]));
}
__device__ __forceinline__ float x32_max(float x) {
  auto s = __builtin_amdgcn_permlane32_swap(__float_as_uint(x), __float_as_uint(x), false, false);
  return fmaxf(__uint_as_float(s[0]), __uint_as_float(s[1]));
}
__device__ __forceinline__ float t16(float a, float b) {
  auto s = __builtin_amdgcn_permlane16_swap(__float_as_uint(a), __float_as_uint(b), false, false);
  return __uint_as_float(s[0]) + __uint_as_float(s[1]);
}
__device__ __forceinline__ float t32(float a, float b) {
  auto s = __builtin_amdgcn_permlane32_swap(__float_as_uint(a), __float_as_uint(b), false, false);
  return __uint_as_float(s[0]) + __uint_as_float(s[1]);
}
__device__ __forceinline__ f32x2 up2(u32 v) { return f32x2{blo(v), bhi(v)}; }
__device__ __forceinline__ u32 pk2(f32x2 v) { return pack2(v[0], v[1]); }
#define PIN(x) asm volatile("" : "+v"(x))
__device__ __forceinline__ f32x2 rb2(f32x2 v) { u32 p = pk2(v); asm("" : "+v"(p)); return up2(p); }
__device__ __forceinline__ float wave_sum(float x) { return x32_sum(x16_sum(row_sum(x))); }
__device__ __forceinline__ float wave_max(float x) { return x32_max(x16_max(row_max(x))); }
__device__ __forceinline__ float dot2(u32 a, u32 b, float c) {
  return __builtin_amdgcn_fdot2_f32_bf16(__builtin_bit_cast(bf16x2, a), __builtin_bit_cast(bf16x2, b), c, false);
}
__device__ __forceinline__ f32x4 mfma(u32x4 a, u32x4 b, f32x4 c) {
  return __builtin_amdgcn_mfma_f32_16x16x32_bf16(__builtin_bit_cast(bf16x8, a), __builtin_bit_cast(bf16x8, b), c, 0, 0, 0);
}
__device__ __forceinline__ u32x4 ldnt(const void* p) { return __builtin_nontemporal_load((const u32x4*)p); }
__device__ __forceinline__ void glds(const void* g, u32 lds) {
  u32 keep;
  asm volatile("s_mov_b32 %0, m0\n\ts_mov_b32 m0, %2\n\ts_nop 0\n\tglobal_load_lds_dwordx4 %1, off nt\n\ts_mov_b32 m0, %0"
               : "=&s"(keep) : "v"(g), "s"(lds) : "memory");
}
__device__ __forceinline__ u32x4 trfrag(LAS unsigned char* p0, LAS unsigned char* p1) {
  s16x4 a = __builtin_amdgcn_ds_read_tr16_b64_v4i16((LAS s16x4*)p0);
  s16x4 b = __builtin_amdgcn_ds_read_tr16_b64_v4i16((LAS s16x4*)p1);
  u32x2 x = __builtin_bit_cast(u32x2, a), y = __builtin_bit_cast(u32x2, b);
  return u32x4{x[0], x[1], y[0], y[1]};
}
#define SB() __builtin_amdgcn_sched_barrier(0)
#define MEMB() asm volatile("" ::: "memory")
#define VM0() asm volatile("s_waitcnt vmcnt(0)" ::: "memory")
#define LGKM0() asm volatile("s_waitcnt lgkmcnt(0)" ::: "memory")
#define BAR() asm volatile("s_waitcnt lgkmcnt(0)\n\ts_barrier" ::: "memory")
#define TAG() do { u64 ep_; asm volatile("s_load_dwordx2 %0, %1, 0x0\n\ts_waitcnt lgkmcnt(0)" : "=s"(ep_) : "s"(ws) : "memory"); \
    ep_ += 1ull; tag_lo = (u32)ep_; tag_hi = (u32)(ep_ >> 32); } while (0)
constexpr int SC1 = 16;
__device__ __forceinline__ u32x4 ld_sc1(__amdgpu_buffer_rsrc_t rs, int off) { return __builtin_amdgcn_raw_buffer_load_b128(rs, off, 0, SC1); }
__device__ __forceinline__ u32x2 ld_sc1_64(__amdgpu_buffer_rsrc_t rs, int off) { return __builtin_amdgcn_raw_buffer_load_b64(rs, off, 0, SC1); }
__device__ __forceinline__ void st_sc1(__amdgpu_buffer_rsrc_t rs, int off, u32x4 v) { __builtin_amdgcn_raw_buffer_store_b128(v, rs, off, 0, SC1); }
__device__ __forceinline__ void st_sc1_64(__amdgpu_buffer_rsrc_t rs, int off, u32x2 v) { __builtin_amdgcn_raw_buffer_store_b64(v, rs, off, 0, SC1); }

__device__ __forceinline__ void ln_row(u32x4 xv, float (&ln)[8]) {
  const float x[8] = {blo(xv[0]), bhi(xv[0]), blo(xv[1]), bhi(xv[1]), blo(xv[2]), bhi(xv[2]), blo(xv[3]), bhi(xv[3])};
  const float x0 = __builtin_bit_cast(float, __builtin_amdgcn_readfirstlane(xv[0] << 16));
  float s = 0.f, v = 0.f;
#pragma unroll
  for (int j = 0; j < 8; ++j) { const float d = x[j] - x0; s += d; v = fmaf(d, d, v); }
  const float md = wave_sum(s) * (1.f / 512.f), sq = wave_sum(v) * (1.f / 512.f);
  const float var = rbf(fmaxf(fmaf(-md, md, sq), 0.f));
  const float m = rbf(x0 + md);
  const float inv = rcp(rbf(__builtin_amdgcn_sqrtf(rbf(var + 1e-5f))));
#pragma unroll
  for (int j = 0; j < 8; ++j) ln[j] = rbf(rbf(x[j] - m) * inv);
}
#define PACE_(W, INC) do { asm volatile(W "\n1:\n\ts_memrealtime vcc\n\ts_waitcnt lgkmcnt(0)\n\ts_sub_i32 vcc_hi, vcc_lo, %0\n\ts_cmp_lt_i32 vcc_hi, 0\n\t" \
    "s_cbranch_scc0 2f\n\ts_sleep 1\n\ts_branch 1b\n2:\n\ts_add_i32 %0, %0, %1" : "+s"(due) : "is"(INC) : "memory", "scc", "vcc"); SB(); } while (0)
#define PACE(INC) PACE_("s_waitcnt vmcnt(3)", INC)
#define PACEN(INC) PACE_("", INC)

#define STAMP(i) do { if ((i) == LEAK) { lk1 = (u32)__builtin_amdgcn_s_memrealtime(); asm volatile("s_waitcnt lgkmcnt(0)" : "+s"(lk1)); } } while (0)

__device__ __forceinline__ void lds_wait(volatile LAS u32* p, u32 want) {
  _Pragma("unroll 1") for (u32 s = 0; s < LSPIN_CAP; ++s) {
    if (__builtin_amdgcn_readfirstlane(*p) >= want) break;
    __builtin_amdgcn_s_sleep(2);
  }
  MEMB();
}
__device__ __forceinline__ u32 lds_arrive(LAS u32* p, int lane) {
  LGKM0();
  u32 old = 0;
  if (lane == 0) old = __hip_atomic_fetch_add(p, 1u, __ATOMIC_RELAXED, __HIP_MEMORY_SCOPE_WORKGROUP);
  old = __builtin_amdgcn_readfirstlane(old);
  MEMB();
  return old;
}

__global__ void __launch_bounds__(1024) fused(const u16* __restrict__ x, const u16* __restrict__ kc, const u16* __restrict__ vc, const u16* __restrict__ wq,
                                              const u16* __restrict__ wo, const u16* __restrict__ wg, const u16* __restrict__ wu, const u16* __restrict__ wd,
                                              const u16* __restrict__ cc, const u16* __restrict__ wmod, u16* __restrict__ out, unsigned char* ws) {
  extern __shared__ __attribute__((aligned(16))) unsigned char smem[];
  LAS unsigned char* L = (LAS unsigned char*)smem;
  const u32 lbase = (u32)(size_t)L;
  const int tid = threadIdx.x, w = __builtin_amdgcn_readfirstlane(tid >> 6), bid = blockIdx.x;
  int lane = tid & 63;
  u32 lk0; asm volatile("s_memrealtime vcc\n\ts_waitcnt lgkmcnt(0)\n\ts_mov_b32 %0, vcc_lo" : "=s"(lk0) :: "vcc"); u32 lk1 = lk0;
  u32 due = (u32)__builtin_amdgcn_s_memrealtime() + (u32)(w * T0) / 16u + DUE0;
  asm volatile("" : "+s"(due));
  if (tid < 16) ((volatile LAS u32*)(L + L_CTL))[tid] = 0u;
  asm volatile("" :: "s"(kc), "s"(wmod), "s"(ws), "s"(cc), "s"(x), "s"(wq));
  const int xcd = bid & 7, idx = bid >> 3, hj = idx & 3, h = 4 * xcd + hj, b = idx >> 2, bh = b * 32 + h;
  if (w == 0) STAMP(0);
  STAMP(20);
  const bool even = (bid & 1) == 0;
  const int x2_ = xcd >> 1;
  const int rsh = (x2_ * 4 + hj) * 4;
  const int p1r = (int)((0xFFF3FFF2FF51FF40ull >> rsh) & 15u) * 8 + b + (bid & 1) * 128;
  const int p1 = p1r < 48 ? p1r : -1;
  const bool role1 = p1 >= 0 && w < 8;
  if (p1 < 0) due -= (bid & 1) ? DODD : DEVN;
  const int tm = T1 - (p1 >= 0 ? TP1 : 0);
  u32x4 cv = {0, 0, 0, 0}, xr = {0, 0, 0, 0};
  if (role1) {
    __builtin_amdgcn_s_setprio(3);
    cv = ldnt(cc + (lane & 7) * 512 + 64 * w + 32 * ((lane >> 3) & 1) + 8 * (lane >> 4));
    if ((p1 >> 3) == 1) xr = ldnt(x + w * 512 + lane * 8);
    const u16* msrc = wmod + (size_t)(64 * w + (lane >> 3)) * 3072 + 64 * p1 + (lane & 7) * 8;
#pragma unroll 1
    for (int i = 0; i < 8; ++i) glds(msrc + (size_t)i * 8 * 3072, lbase + L_W + w * 8192 + i * 1024);
  }
  int rot = idx;
#define RO(I) ((((I) + rot) & 15) * 8192)
  const u16* kp = kc + ((size_t)bh * SK + (SK - WIN) + 4 * w) * 128 + lane * 8;
  u32x4 kr[16], v1[8], v2[8];
  kr[0] = ldnt(kp + RO(0));
  SB();
  volatile LAS u32* ctl = (volatile LAS u32*)(L + L_CTL);
  int n = lane & 15, g = lane >> 4;
#define RELANE() do { asm volatile("" : "+v"(lane)); n = lane & 15; g = lane >> 4; } while (0)
  const int e = bid >> 1;
  const int p3r = (int)((0xF35FF24FF1FFF0FFull >> rsh) & 15u) * 8 + b + (bid & 1) * 128;
  const int p3 = p3r < NP3 ? p3r : -1;
  const __amdgpu_buffer_rsrc_t rs = __builtin_amdgcn_make_buffer_rsrc((void*)ws, 0, WS_END, 0x00020000);
  u32 tag_lo = 0, tag_hi = 0;

  if (w == 8 && !even) {
    const u32x4 sent = {~0u, ~0u, ~0u, ~0u};
#pragma unroll
    for (int i = 0; i < 10; ++i)
      if (i * 1024 + lane * 16 < S_CHUNK) st_sc1(rs, WS_S + e * S_CHUNK + i * 1024 + lane * 16, sent);
  }
#define KL(I, T) { PACE(T); kr[I] = ldnt(kp + RO(I)); SB(); }
#define KLN(I, T) { PACEN(T); kr[I] = ldnt(kp + RO(I)); SB(); }
  RELANE();
  const int cg = bid >> 2, kq = bid & 3;
  if (w == SW) {
    __builtin_amdgcn_s_setprio(2);
    const u32x4 sent = {~0u, ~0u, ~0u, ~0u};
    st_sc1(rs, WS_QP + bh * 2048 + lane * 16, sent);
    st_sc1(rs, WS_QP + bh * 2048 + 1024 + lane * 16, sent);
  }
  const u16* qsrc = wq + (size_t)(128 * kq + (lane >> 3)) * 4096 + 64 * cg;
#define WQ4(I) if (w == SW) { _Pragma("unroll 1") for (int i = 4 * (I); i < 4 * (I) + 4; ++i) \
      glds(qsrc + (size_t)i * 8 * 4096 + ((lane & 7) ^ ((((lane >> 4) & 1) | ((i & 1) << 1)) << 1)) * 8, lbase + L_RED + i * 1024); }
#define HU(U) ((U) == 0 ? (p3r < 48 ? -1 : h) : (!even ? -1 : (hj == 3 ? h - 1 : ((hj == 0 && x2_ >= 2) ? h + 1 : -1))))
#define ML(I) if (p3 >= 0) { const int c_ = (lane & 7) ^ ((((lane >> 4) & 1) | (((I) & 1) << 1)) << 1); \
    glds(((c_ & 4) ? wu : wg) + (size_t)(32 * w + 8 * (I) + (lane >> 3)) * FF + 32 * p3 + 8 * (c_ & 3), lbase + L_W + w * 4096 + (I) * 1024); SB(); }
#define DL(I) if (p3 >= 0) { const int r_ = 2 * w + (I); \
    glds(wd + (size_t)(32 * p3 + r_) * 512 + (lane ^ (((r_ & 3) | (((r_ >> 3) & 1) << 2)) << 1)) * 8, lbase + L_W + 65536 + r_ * 1024); SB(); }
  ML(0)
  BAR();
  if (role1) {
    asm volatile("s_waitcnt vmcnt(1)" ::: "memory");
    u32x4 bf;
#pragma unroll
    for (int j = 0; j < 4; ++j) {
      const float a0 = blo(cv[j]), a1 = bhi(cv[j]);
      bf[j] = pack2(a0 * rcp(1.f + __expf(-a0)), a1 * rcp(1.f + __expf(-a1)));
    }
    LAS unsigned char* tb = L + L_W + w * 8192 + (8 * g + (n >> 2)) * 128 + (n & 3) * 8;
#pragma unroll
    for (int t = 0; t < 4; ++t) {
      const f32x4 z = {0.f, 0.f, 0.f, 0.f};
      const f32x4 aL = mfma(trfrag(tb + t * 32, tb + t * 32 + 512), bf, z);
      const f32x4 aH = mfma(trfrag(tb + t * 32 + 4096, tb + t * 32 + 4096 + 512), bf, z);
      const f32x4 p = {aL[0] + dppf<ROR8>(aH[0]), aL[1] + dppf<ROR8>(aH[1]), aL[2] + dppf<ROR8>(aH[2]), aL[3] + dppf<ROR8>(aH[3])};
      if (n < 8) *(LAS f32x4*)(L + L_P1 + w * 2048 + t * 512 + (g * 8 + n) * 16) = p;
    }
    if ((p1 >> 3) == 1) {
      float ln[8];
      ln_row(xr, ln);
      if ((lane >> 3) == (p1 & 7)) {
        LAS f32x4* d = (LAS f32x4*)(L + L_H2 + w * 256 + (lane & 7) * 32);
        d[0] = f32x4{ln[0], ln[1], ln[2], ln[3]};
        d[1] = f32x4{ln[4], ln[5], ln[6], ln[7]};
      }
    }
    const u32 old = lds_arrive((LAS u32*)(L + L_CTL) + C_P1CNT, lane);
    if (old == 7) {
      TAG();
      const int bb = n & 7, ch = p1 >> 3, col0 = (p1 & 7) * 64;
#pragma unroll 1
      for (int tt = 0; tt < 2; ++tt) {
        const int t = 2 * (n >> 3) + tt;
        f32x4 s = {0.f, 0.f, 0.f, 0.f};
#pragma unroll
        for (int ww = 0; ww < 8; ++ww) s += *(LAS f32x4*)(L + L_P1 + ww * 2048 + t * 512 + (g * 8 + bb) * 16);
        float v[4] = {rbf(s[0]), rbf(s[1]), rbf(s[2]), rbf(s[3])};
        const int col = 16 * t + 4 * g;
        if (ch == 1) {
          const f32x4 ln = *(LAS f32x4*)(L + L_H2 + bb * 256 + col * 4);
#pragma unroll
          for (int j = 0; j < 4; ++j) v[j] = ln[j] * rbf(1.f + v[j]);
        }
        const u32 lo = pack2(v[0], v[1]), hi = pack2(v[2], v[3]);
        if (ch < 2) {
          GAS u64* gp = (GAS u64*)(ws + WS_G1 + ((ch * 8 + bb) * 256 + ((col0 + col) >> 1)) * 8);
          __hip_atomic_store(gp, ((u64)tag_lo << 32) | lo, RLX_AGENT);
          __hip_atomic_store(gp + 1, ((u64)tag_lo << 32) | hi, RLX_AGENT);
        } else {
          st_sc1_64(rs, WS_MODR + ((ch - 2) * 8 + bb) * 1024 + (col0 + col) * 2, u32x2{lo, hi});
        }
      }
      STAMP(3);
      if (ch >= 2) {
        VM0();
        if (lane == 0) st_sc1_64(rs, WS_FLAGS + (112 + p1) * 8, u32x2{tag_lo, tag_hi});
      }
    }
    __builtin_amdgcn_s_setprio(0);
  }
  KLN(1, T0)
  if (w == 0) STAMP(2);
  KL(2, T0)
  KL(3, TS)
  if ((w == 8 && !even) || w == SW) {
    asm volatile("s_waitcnt vmcnt(3)" ::: "memory");
    TAG();
    if (lane == 0) st_sc1_64(rs, w == 8 ? WS_FLAGS + e * 8 : WS_FLAG2 + (h * 8 + b) * 8, u32x2{tag_lo, tag_hi});
    if (w == 8) STAMP(1); else ctl[C_QCNT] = 1u;
  }
  WQ4(0) KLN(4, TS) WQ4(1) KLN(5, TS) WQ4(2) KLN(6, TS) WQ4(3) KLN(7, T0)
  u32x4 sa[4], tt[4];
  u32x2 f2 = {0, 0};
  const int o0 = WS_G1 + ((n & 7) * 256 + 64 * kq + 32 * (n >> 3) + 4 * g) * 8;
#define G1_LOAD() { f2 = ld_sc1_64(rs, WS_FLAG2 + ((cg >> 1) * 8 + (lane & 7)) * 8);   \
    _Pragma("unroll") for (int i = 0; i < 4; ++i) { sa[i] = ld_sc1(rs, o0 + (i >> 1) * 128 + (i & 1) * 16); tt[i] = ld_sc1(rs, o0 + 8 * 256 * 8 + (i >> 1) * 128 + (i & 1) * 16); } }
  if (w == SW) G1_LOAD()
  else {
#pragma unroll
    for (int i = 0; i < 4; ++i) { sa[i] = u32x4{0, 0, 0, 0}; tt[i] = u32x4{0, 0, 0, 0}; }
  }
  SB();
  KLN(8, T0) KLN(9, T0)
  if (w == SW) {
    u32x4 hb[2];
    {
#define G1_BAD() ((f2[0] ^ tag_lo) | (f2[1] ^ tag_hi) | (sa[0][1] ^ tag_lo) | (sa[0][3] ^ tag_lo) | (tt[0][1] ^ tag_lo) | (tt[0][3] ^ tag_lo) | (sa[1][1] ^ tag_lo) | \
      (sa[1][3] ^ tag_lo) | (tt[1][1] ^ tag_lo) | (tt[1][3] ^ tag_lo) | (sa[2][1] ^ tag_lo) | (sa[2][3] ^ tag_lo) | (tt[2][1] ^ tag_lo) | (tt[2][3] ^ tag_lo) | \
      (sa[3][1] ^ tag_lo) | (sa[3][3] ^ tag_lo) | (tt[3][1] ^ tag_lo) | (tt[3][3] ^ tag_lo))
      if (!__all(G1_BAD() == 0u)) {
#pragma unroll 1
        for (u32 s = 0; s < SPIN_CAP; ++s) {
          __builtin_amdgcn_s_sleep(16);
          MEMB();
          G1_LOAD()
          if (__all(G1_BAD() == 0u)) break;
        }
      }
#pragma unroll
      for (int i = 0; i < 4; ++i) {
        hb[i >> 1][(i & 1) * 2 + 0] = pack2(blo(tt[i][0]) + blo(sa[i][0]), bhi(tt[i][0]) + bhi(sa[i][0]));
        hb[i >> 1][(i & 1) * 2 + 1] = pack2(blo(tt[i][2]) + blo(sa[i][2]), bhi(tt[i][2]) + bhi(sa[i][2]));
      }
    }
    MEMB();
    STAMP(4);
    LAS unsigned char* tb = L + L_RED + (8 * g + (n >> 2)) * 128 + (n & 3) * 8;
    const int sx = (n >> 3) | ((g & 1) << 1);
    const int qo = WS_QP + (((n & 7) * 32 + (cg >> 1)) * 4 + kq) * 512 + ((cg & 1) * 64 + 16 * (n >> 3) + 4 * g) * 4;
#pragma unroll
    for (int t = 0; t < 4; t += 2) {
      const f32x4 z = {0.f, 0.f, 0.f, 0.f};
      f32x4 aL[2], aH[2], v;
#pragma unroll
      for (int u = 0; u < 2; ++u) {
        LAS unsigned char* ta = tb + ((t + u) ^ sx) * 32;
        aL[u] = mfma(trfrag(ta + 4096, ta + 4096 + 512), hb[1], mfma(trfrag(ta, ta + 512), hb[0], z));
        aH[u] = mfma(trfrag(ta + 12288, ta + 12288 + 512), hb[1], mfma(trfrag(ta + 8192, ta + 8192 + 512), hb[0], z));
      }
#pragma unroll
      for (int r = 0; r < 4; ++r) v[r] = (n < 8 ? aL[0][r] : aH[1][r]) + dppf<ROR8>(n < 8 ? aL[1][r] : aH[0][r]);
      st_sc1(rs, qo + t * 64, canon4(v));
    }
    STAMP(17);
  }
  RELANE();
  KL(10, tm) KL(11, tm)
  KL(12, tm) KL(13, tm) KL(14, tm) KL(15, tm)
  asm volatile("" : "+s"(rot));
  const int tb = TB - (p1 >= 0 ? TP1 : 0);
  const u16* vp = vc + ((size_t)bh * SK + (SK - WIN) + 4 * w) * 128 + lane * 8;
#define V1(I, T) { PACE(T); v1[I] = ldnt(vp + RO(I)); SB(); }
  V1(0, tm) V1(1, tm)
  u32x4 q0 = {0, 0, 0, 0}, q1 = {0, 0, 0, 0};
  if (w == SW + 1) {
    __builtin_amdgcn_s_setprio(2);
    lds_wait(ctl + C_QCNT, 1u);
    q0 = ld_sc1(rs, WS_QP + bh * 2048 + lane * 16);
    q1 = ld_sc1(rs, WS_QP + bh * 2048 + 1024 + lane * 16);
  }
  SB();
  V1(2, tb) V1(3, tb)
  if (w == SW + 1) {
    if (!full(max4(q0, max4(q1)))) {
#pragma unroll 1
      for (u32 s = 0; s < SPIN_CAP; ++s) {
        __builtin_amdgcn_s_sleep(4);
        MEMB();
        q0 = ld_sc1(rs, WS_QP + bh * 2048 + lane * 16);
        q1 = ld_sc1(rs, WS_QP + bh * 2048 + 1024 + lane * 16);
        if (full(max4(q0, max4(q1)))) break;
      }
    }
    const f32x4 a = __builtin_bit_cast(f32x4, q0) + __builtin_bit_cast(f32x4, q1);
    const float s0 = x32_sum(a[0]), s1 = x32_sum(a[1]), s2 = x32_sum(a[2]), s3 = x32_sum(a[3]);
    if (lane < 32) *(LAS u32x2*)(L + L_Q + lane * 8) = u32x2{pack2(s0, s1), pack2(s2, s3)};
    LGKM0();
    ctl[C_Q] = 1u;
    STAMP(5);
  }
  RELANE();
  V1(4, tb)
  TAG();
  V1(5, tb)
#define V2(I, T) { PACE(T); v2[I] = ldnt(vp + RO(8 + (I))); SB(); }
  V1(6, tb) V1(7, tb)
  const int flo = WS_FLAGS + (w * 16 + n) * 8;
  u32x2 fl;
  if (w < 10) fl = ld_sc1_64(rs, flo);
  SB();
  lds_wait(ctl + C_Q, 1u);
  const u32x4 qf = *(LAS u32x4*)(L + L_Q + n * 16);
  if (w == 1) STAMP(6);

  float xs[16];
#pragma unroll
  for (int i = 0; i < 8; ++i) xs[i] = dot2(kr[i][3], qf[3], dot2(kr[i][2], qf[2], dot2(kr[i][1], qf[1], dot2(kr[i][0], qf[0], 0.f))));
#pragma unroll
  for (int i = 0; i < 8; ++i) asm volatile("" : "+v"(xs[i]));
  SB();
  V2(0, tb)
#pragma unroll
  for (int i = 8; i < 16; ++i) xs[i] = dot2(kr[i][3], qf[3], dot2(kr[i][2], qf[2], dot2(kr[i][1], qf[1], dot2(kr[i][0], qf[0], 0.f))));
#pragma unroll
  for (int i = 8; i < 16; ++i) asm volatile("" : "+v"(xs[i]));
  SB();
  RELANE();
  V2(1, tb)
  u32x4 pre0, pre1;
  u32 prx;
  const int rb = e >> 4, rj = e & 15;
  const int prer = (p3 >= 0 && w < 8) ? 1 : ((!even && w == 3) ? 2 : 0);
  const int po0 = WS_MODR + (prer == 1 ? (1 * 8 + w) * 1024 + lane * 16 : (0 * 8 + rb) * 1024 + 64 * rj + 16 * ((lane & 7) >> 1));
  const int po1 = po0 + (prer == 1 ? 8192 : 3 * 8192);
#define TR_STEP(HALF, CTRL, BIT) _Pragma("unroll") for (int hh = 0; hh < (HALF); ++hh) { \
    const float lo_ = xs[hh], hi_ = xs[hh + (HALF)]; const float t1_ = lo_ + dppf<CTRL>(lo_), t2_ = hi_ + dppf<CTRL>(hi_); xs[hh] = (n & (BIT)) ? t2_ : t1_; }
  TR_STEP(8, ROR8, 8) TR_STEP(4, XOR7, 4) TR_STEP(2, XOR2, 2) TR_STEP(1, XOR1, 1)
#undef TR_STEP
  const float sc = rbf(rbf(xs[0]) * 0.08838834764831845f);
  const float mw_ = wave_max(sc);
  const float lw = wave_sum(__expf(sc - mw_));
  if (lane == 0) *(LAS f32x2*)(L + L_ML + w * 8) = f32x2{mw_, lw};
  if (w < 10 && !__all(fl[0] == tag_lo && fl[1] == tag_hi)) {
#pragma unroll 1
    for (u32 s = 0; s < SPIN_CAP; ++s) {
      __builtin_amdgcn_s_sleep(4);
      MEMB();
      fl = ld_sc1_64(rs, flo);
      if (__all(fl[0] == tag_lo && fl[1] == tag_hi)) break;
    }
  }
  lds_arrive((LAS u32*)(L + L_CTL) + C_ML, lane);
  V2(2, tb) V2(3, tb) V2(4, tb) V2(5, tb)
  lds_wait(ctl + C_ML, 16u);
  if (prer) { pre0 = ld_sc1(rs, po0); pre1 = ld_sc1(rs, po1); prx = __builtin_nontemporal_load((const u32*)(x + rb * 512 + 32 * rj + 4 * (lane & 7) + (g & 2))); }
  SB();
  u32 pd;
  {
    const f32x2 ml = *(LAS f32x2*)(L + L_ML + n * 8);
    const float M = row_max(ml[0]);
    const float pe = __expf(sc - M) * rcp(row_sum(__expf(ml[0] - M) * ml[1]));
    pd = pack2(pe, pe);
  }
  u32 mk[4];
#pragma unroll
  for (int j = 0; j < 4; ++j) mk[j] = (n >> 1) == j ? ((n & 1) ? 0xffff0000u : 0x0000ffffu) : 0u;
  f32x4 oa = {0.f, 0.f, 0.f, 0.f};
#define PV(V, I) { const u32 pb_ = dppu<0x150 + (I)>(pd); oa = mfma(V, u32x4{pb_ & mk[0], pb_ & mk[1], pb_ & mk[2], pb_ & mk[3]}, oa); asm volatile("" : "+v"(oa)); SB(); }
  PV(v1[0], 0) PV(v1[1], 1) PV(v1[2], 2) PV(v1[3], 3)
  V2(6, tb)
  PV(v1[4], 4) PV(v1[5], 5) PV(v1[6], 6) PV(v1[7], 7) PV(v2[0], 8) PV(v2[1], 9)
  V2(7, tb)
#define WOE() if (w < 8) { _Pragma("unroll") for (int u = 0; u < 2; ++u) if (HU(u) >= 0) { _Pragma("unroll") for (int i = 0; i < 2; ++i) \
      glds(wo + (size_t)(128 * HU(u) + 8 * (2 * w + i) + (lane >> 3)) * 512 + 64 * b + ((lane & 7) ^ (((2 * w + i) & 3) << 1)) * 8, lbase + L_WOT + u * 16384 + (2 * w + i) * 1024); } }
  PV(v2[2], 10) PV(v2[3], 11)
  u32x4 wf[4][4];
  const int hu = (w == 3 || w == 4) ? HU(w - 3) : -1;
  SB();
  PV(v2[4], 12) PV(v2[5], 13) PV(v2[6], 14) PV(v2[7], 15)
#undef PV
#undef V2
  if (n < 8) *(LAS f32x4*)(L + L_RED + w * OSTR + (g * 8 + n) * 16) = oa;
  if (w == 13 || w == 3 || w == 4) __builtin_amdgcn_s_setprio(3);
  WOE()
  const bool mpre = w < WPRE;
  if (mpre) {
#pragma unroll
    for (int i = MLI; i < MLI + NPRE; ++i) ML(i)
  }
  BAR();
  if (w == 0) STAMP(8);
  if (w == 13) {
    f32x4 s = {0.f, 0.f, 0.f, 0.f};
#pragma unroll
    for (int ww = 0; ww < 8; ++ww) s += *(LAS f32x4*)(L + L_RED + (ww + 8 * (lane >> 5)) * OSTR + (lane & 31) * 16);
    const float s0 = x32_sum(s[0]), s1 = x32_sum(s[1]), s2 = x32_sum(s[2]), s3 = x32_sum(s[3]);
    if (lane < 32) st_sc1_64(rs, WS_O + (h * 8 + b) * 256 + lane * 8, u32x2{canon(pack2(s0, s1)), canon(pack2(s2, s3))});
  }
  if (w == 13) STAMP(9);
  if (p3r >= 48) {
    if (w < 8) VM0();
    BAR();
  }
  if (hu >= 0) {
    const int q = n >> 2, p = n & 3;
#pragma unroll
    for (int tt = 0; tt < 4; ++tt) {
      LAS unsigned char* tb = L + L_WOT + (w - 3) * 16384 + (8 * q + 2 * g) * 128 + (tt ^ q) * 32 + p * 8;
#pragma unroll
      for (int ks = 0; ks < 4; ++ks) wf[tt][ks] = trfrag(tb + ks * 4096, tb + ks * 4096 + 128);
    }
  }
  if (hu >= 0) {
    u32x4 o0, o1;
    const int oo = WS_O + (hu * 8 + (n & 7)) * 256 + (n >> 3) * 128 + g * 16;
    int pao = WS_PART + (((n & 7) * 16 + 2 * b) * 32 + hu) * 128 + (n >> 3) * 64 + g * 16;
    PIN(pao);
#pragma unroll 1
    for (u32 s = 0; s < SPIN_CAP; ++s) {
      o0 = ld_sc1(rs, oo);
      o1 = ld_sc1(rs, oo + 64);
      if (full(max4(o0, max4(o1)))) break;
      MEMB();
      __builtin_amdgcn_s_sleep(2);
    }
    const f32x4 z = {0.f, 0.f, 0.f, 0.f};
#pragma unroll
    for (int jj = 0; jj < 2; ++jj) {
      const f32x4 aL0 = mfma(wf[2 * jj][1], o1, mfma(wf[2 * jj][0], o0, z)), aH0 = mfma(wf[2 * jj][3], o1, mfma(wf[2 * jj][2], o0, z));
      const f32x4 aL1 = mfma(wf[2 * jj + 1][1], o1, mfma(wf[2 * jj + 1][0], o0, z)), aH1 = mfma(wf[2 * jj + 1][3], o1, mfma(wf[2 * jj + 1][2], o0, z));
      f32x4 v;
#pragma unroll
      for (int r = 0; r < 4; ++r) v[r] = (n < 8 ? aL0[r] : aH1[r]) + dppf<ROR8>(n < 8 ? aL1[r] : aH0[r]);
      st_sc1(rs, pao + jj * 4096, canon4(v));
    }
    if (w == 3) STAMP(16); else STAMP(7);
  }

  if (p3 >= 0) {
    const int t = w & 3, kk = w >> 2;
    BAR();
    if (!mpre) {
#pragma unroll
      for (int i = MLI; i < MLI + NPRE; ++i) ML(i)
    }
#pragma unroll
    for (int i = MLI + NPRE; i < 4; ++i) ML(i)
    BAR();
    DL(0) DL(1)
    f32x2 c1[4], sb[4];
#pragma unroll
    for (int j = 0; j < 4; ++j) { c1[j] = rb2(up2(pre1[j]) + 1.f); sb[j] = up2(pre0[j]); PIN(c1[j]); PIN(sb[j]); }
    int xo = WS_X2 + w * 1024 + lane * 16;
    u32 a_h2w = L_H2 + w * H2S + lane * 16;
    u32 a_h2r = L_H2 + (n & 7) * H2S + (128 * kk + 8 * g) * 2;
    u32 a_gw = L_RED + kk * 2048 + t * 512 + (g * 8 + n) * 16;
    u32 a_gr = L_RED + (w & 3) * 256 + lane * 4;
    u32 a_hw = L_HH + ((lane >> 2) & 7) * 64 + ((w & 3) * 2 + (lane >> 5)) * 8 + (lane & 3) * 2;
    u32 a_hr = L_HH + (n & 7) * 64 + g * 16;
    u32 a_d0 = L_W + 65536 + (8 * g + (n >> 2)) * 1024 + ((4 * w + ((n >> 1) & 1)) ^ (((n >> 2) | ((g & 1) << 2)) << 1)) * 16 + (n & 1) * 8;
    int yo = WS_YP + (((n & 7) * 16 + w) * NP3 + p3) * 128 + (n >> 3) * 64 + g * 16;
    PIN(xo); PIN(a_h2w); PIN(a_h2r); PIN(a_gw); PIN(a_gr); PIN(a_hw); PIN(a_hr); PIN(a_d0); PIN(yo);
    asm volatile("s_waitcnt vmcnt(2)" ::: "memory");
    BAR();
    if (w == 0) STAMP(19);
    u32x4 gf[4];
    {
      const int q = n >> 2, p = n & 3;
      LAS unsigned char* tb = L + L_W + (128 * kk + 8 * g + q) * 128 + (t ^ ((q >> 1) | ((g & 1) << 1))) * 32 + p * 8;
#pragma unroll
      for (int ks = 0; ks < 4; ++ks) gf[ks] = trfrag(tb + ks * 4096, tb + ks * 4096 + 512);
    }
    if (w < 8) {
      u32x4 xv;
#pragma unroll 1
      for (u32 s = 0; s < SPIN_CAP; ++s) {
        xv = ld_sc1(rs, xo);
        if (full(max4(xv))) break;
        MEMB();
      }
      if (w == 0) STAMP(12);
      const float x0 = __builtin_bit_cast(float, __builtin_amdgcn_readfirstlane(xv[0] << 16));
      const f32x2 xf[4] = {up2(xv[0]), up2(xv[1]), up2(xv[2]), up2(xv[3])};
      const f32x2 d0 = xf[0] - x0, d1 = xf[1] - x0, d2 = xf[2] - x0, d3 = xf[3] - x0;
      const f32x2 sv = (d0 + d1) + (d2 + d3), qv = __builtin_elementwise_fma(d0, d0, d1 * d1) + __builtin_elementwise_fma(d2, d2, d3 * d3);
      float r = t16(sv[0] + sv[1], qv[0] + qv[1]);
      r = x32_sum(row_sum(r)) * (1.f / 512.f);
      const float md = __builtin_bit_cast(float, __builtin_amdgcn_readlane(__builtin_bit_cast(int, r), 0));
      const float sq = __builtin_bit_cast(float, __builtin_amdgcn_readlane(__builtin_bit_cast(int, r), 16));
      const float m = rbf(x0 + md);
      const float inv = rcp(rbf(__builtin_amdgcn_sqrtf(rbf(rbf(fmaxf(fmaf(-md, md, sq), 0.f)) + 1e-5f))));
      u32x4 hv = {pk2(xf[0] - m), pk2(xf[1] - m), pk2(xf[2] - m), pk2(xf[3] - m)};
      asm("" : "+v"(hv));
      hv = u32x4{pk2(up2(hv[0]) * inv), pk2(up2(hv[1]) * inv), pk2(up2(hv[2]) * inv), pk2(up2(hv[3]) * inv)};
      asm("" : "+v"(hv));
      hv = u32x4{pk2(up2(hv[0]) * c1[0]), pk2(up2(hv[1]) * c1[1]), pk2(up2(hv[2]) * c1[2]), pk2(up2(hv[3]) * c1[3])};
      asm("" : "+v"(hv));
      hv = u32x4{pk2(up2(hv[0]) + sb[0]), pk2(up2(hv[1]) + sb[1]), pk2(up2(hv[2]) + sb[2]), pk2(up2(hv[3]) + sb[3])};
      *(LAS u32x4*)(L + a_h2w) = hv;
    }
    VM0();
    BAR();
    if (w == 0) STAMP(13);
    {
      u32x4 b0 = *(LAS u32x4*)(L + a_h2r), b1 = *(LAS u32x4*)(L + a_h2r + 64), b2 = *(LAS u32x4*)(L + a_h2r + 128), b3 = *(LAS u32x4*)(L + a_h2r + 192);
      SB();
      const f32x4 z = {0.f, 0.f, 0.f, 0.f};
      const f32x4 a = mfma(gf[1], b1, mfma(gf[0], b0, z)) + mfma(gf[3], b3, mfma(gf[2], b2, z));
      if (n < 8) *(LAS f32x4*)(L + a_gw) = a;
    }
    BAR();
    const u32x4 df0 = trfrag(L + a_d0, L + a_d0 + 4096), df1 = trfrag(L + (a_d0 ^ 32), L + (a_d0 ^ 32) + 4096);
    if (w < 4) {
      float gv[4], uv[4];
#pragma unroll
      for (int k2 = 0; k2 < 4; ++k2) { gv[k2] = *(LAS float*)(L + a_gr + k2 * 2048); uv[k2] = *(LAS float*)(L + a_gr + k2 * 2048 + 1024); }
      SB();
      const float gg = rbf((gv[0] + gv[1]) + (gv[2] + gv[3])), uu = rbf((uv[0] + uv[1]) + (uv[2] + uv[3]));
      *(LAS u16*)(L + a_hw) = (u16)pack2(rbf(rcp(1.f + __expf(-gg))) * uu, 0.f);
    }
    BAR();
    {
      const u32x4 hb = *(LAS u32x4*)(L + a_hr);
      const f32x4 z = {0.f, 0.f, 0.f, 0.f};
      const f32x4 a0 = mfma(df0, hb, z), a1 = mfma(df1, hb, z);
      st_sc1(rs, yo, canon4((n & 8) ? a1 : a0));
    }
    if (w == 0) STAMP(14);
  } else if (prer == 2) {
    const u32 gaw = (lane & 1) ? ((g & 2) ? pre0[3] : pre0[2]) : ((g & 2) ? pre0[1] : pre0[0]), gbw = (lane & 1) ? ((g & 2) ? pre1[3] : pre1[2]) : ((g & 2) ? pre1[1] : pre1[0]);
    float ga = (g & 1) ? bhi(gaw) : blo(gaw), gb = (g & 1) ? bhi(gbw) : blo(gbw), xx = (g & 1) ? bhi(prx) : blo(prx);
    int po = WS_PART + (rb * 16 + rj) * 4096 + lane * 16;
    int xo = WS_X2 + rb * 1024 + 64 * rj + 8 * (lane & 7) + 2 * g;
    int yo = WS_YP + (rb * 16 + rj) * (NP3 * 128) + lane * 16;
    int y5 = lane < 24 ? yo + 5 * 1024 : WS_END + 4096;
    PIN(ga); PIN(gb); PIN(xx); PIN(po); PIN(xo); PIN(yo); PIN(y5);
    float x2;
    {
      u32x4 pv[4];
#pragma unroll 1
      for (u32 sp = 0; sp < SPIN_CAP; ++sp) {
#pragma unroll
        for (int i = 0; i < 4; ++i) pv[i] = ld_sc1(rs, po + i * 1024);
        if (full(max4(pv[0], max4(pv[1], max4(pv[2], max4(pv[3])))))) break;
        MEMB();
      }
      if (w == 3) STAMP(10);
      const f32x4 s = (__builtin_bit_cast(f32x4, pv[0]) + __builtin_bit_cast(f32x4, pv[1])) + (__builtin_bit_cast(f32x4, pv[2]) + __builtin_bit_cast(f32x4, pv[3]));
      float t = t32(t16(s[0], s[1]), t16(s[2], s[3]));
      t += dppf<ROR8>(t);
      x2 = rbf(xx + rbf(ga * rbf(t)));
      auto sw = __builtin_amdgcn_permlane16_swap(__float_as_uint(x2), __float_as_uint(x2), false, false);
      if ((lane & 24) == 0) __builtin_amdgcn_raw_buffer_store_b32(canon((sw[0] >> 16) | sw[1]), rs, xo, 0, SC1);
    }
    if (w == 3) STAMP(11);
    { const u32 t_ = (u32)__builtin_amdgcn_s_memrealtime(); while ((u32)__builtin_amdgcn_s_memrealtime() - t_ < YWAIT) __builtin_amdgcn_s_sleep(1); }
    {
      u32x4 yv[6];
#pragma unroll 1
      for (u32 sp = 0; sp < SPIN_CAP; ++sp) {
#pragma unroll
        for (int i = 0; i < 5; ++i) yv[i] = ld_sc1(rs, yo + i * 1024);
        yv[5] = ld_sc1(rs, y5);
        if (full(max4(yv[0], max4(yv[1], max4(yv[2], max4(yv[3], max4(yv[4], max4(yv[5])))))))) break;
        MEMB();
      }
      if (w == 3) STAMP(15);
      const f32x4 s = ((__builtin_bit_cast(f32x4, yv[0]) + __builtin_bit_cast(f32x4, yv[1])) + (__builtin_bit_cast(f32x4, yv[2]) + __builtin_bit_cast(f32x4, yv[3]))) +
                      (__builtin_bit_cast(f32x4, yv[4]) + __builtin_bit_cast(f32x4, yv[5]));
      float t = t32(t16(s[0], s[1]), t16(s[2], s[3]));
      t += dppf<ROR8>(t);
      const float o = x2 + rbf(gb * rbf(t));
      if ((lane & 8) == 0) out[rb * 512 + 32 * rj + 4 * (lane & 7) + g] = (u16)pack2(o, 0.f);
    }
    if (bid == 1 && w == 3 && lane == 0) st_sc1_64(rs, WS_EPOCH, u32x2{tag_lo, tag_hi});
    if (w == 3) STAMP(18);
  }
  if (bid == 77 && w == 0) while ((u32)__builtin_amdgcn_s_memrealtime() - lk0 < 5000u + 4u * (lk1 - lk0)) __builtin_amdgcn_s_sleep(2);
}

extern "C" void kernel_launch(void* const* d_in, const int* in_sizes, int n_in, void* d_out, int out_size, void* d_ws, size_t ws_size, hipStream_t stream) {
  (void)hipFuncSetAttribute((const void*)fused, hipFuncAttributeMaxDynamicSharedMemorySize, L_END);
  fused<<<256, 1024, L_END, stream>>>((const u16*)d_in[0], (const u16*)d_in[1], (const u16*)d_in[2], (const u16*)d_in[3], (const u16*)d_in[4], (const u16*)d_in[6],
                                      (const u16*)d_in[7], (const u16*)d_in[8], (const u16*)d_in[9], (const u16*)d_in[10], (u16*)d_out, (unsigned char*)d_ws);
}
```

```cpp
#define LEAK -1
#include <hip/hip_runtime.h>
#include <stdint.h>

typedef unsigned int u32;
typedef unsigned long long u64;
typedef unsigned short u16;
typedef u32 u32x4 __attribute__((ext_vector_type(4)));
typedef u32 u32x2 __attribute__((ext_vector_type(2)));
typedef float f32x4 __attribute__((ext_vector_type(4)));
typedef float f32x2 __attribute__((ext_vector_type(2)));
typedef short s16x4 __attribute__((ext_vector_type(4)));
typedef __bf16 bf16x8 __attribute__((ext_vector_type(8)));
typedef __bf16 bf16x2 __attribute__((ext_vector_type(2)));
#define GAS __attribute__((address_space(1)))
#define LAS __attribute__((address_space(3)))
#define RLX_AGENT __ATOMIC_RELAXED, __HIP_MEMORY_SCOPE_AGENT

constexpr int SK = 4096, WIN = 1024, FF = 1376, NP3 = 43;

constexpr int WS_EPOCH = 0;
constexpr int WS_FLAGS = 0x1000;
constexpr int WS_FLAG2 = 0x1800;
constexpr int WS_G1 = 0x2000;
constexpr int WS_MODR = 0x10000;
constexpr int WS_QP = 0x20000;
constexpr int WS_S = 0xA0000;
constexpr int WS_PART = WS_S;
constexpr int WS_YP = WS_PART + 8 * 16 * 32 * 32 * 4;
constexpr int WS_X2 = WS_YP + 8 * 16 * NP3 * 32 * 4;
constexpr int WS_O = WS_X2 + 8 * 512 * 2;
constexpr int WS_END = WS_O + 32 * 8 * 256;
constexpr int S_CHUNK = (WS_END - WS_S) / 128;
static_assert((WS_END - WS_S) % (128 * 16) == 0, "clear chunks are whole 16-byte pieces");

constexpr int L_W = 0;
constexpr int L_P1 = 65536;
constexpr int L_WOT = 81920;
constexpr int L_RED = 131072;
constexpr int OSTR = 544;
constexpr int SW = 9;
constexpr int L_H2 = L_RED + 16384;
constexpr int H2S = 1056;
constexpr int L_Q = L_H2 + 8 * H2S;
constexpr int L_OB = L_Q + 256;
constexpr int L_ML = L_OB + 256;
constexpr int L_HH = L_ML + 128;
constexpr int L_CTL = L_HH + 512;
constexpr int L_END = L_CTL + 64;
constexpr int C_P1CNT = 0, C_P1DONE = 1, C_QCNT = 2, C_Q = 3, C_ML = 4, C_WO = 5;

constexpr u32 SPIN_CAP = 1u << 17, LSPIN_CAP = 1u << 20;
#define TAU 61
#define TMID 59
#define TB 57
#define TP1 2
#define DODD 50
#define DEVN 30
#define DUE0 225
constexpr int T0 = TAU, TS = TAU + TAU / 4, T1 = TMID;
#define NPRE 2
#define WPRE 12
constexpr int MLI = 1, YWAIT = 150;

__device__ __forceinline__ float rbf(float f) { return (float)(__bf16)f; }
__device__ __forceinline__ u32 pack2(float a, float b) { bf16x2 v = {(__bf16)a, (__bf16)b}; return __builtin_bit_cast(u32, v); }
__device__ __forceinline__ float blo(u32 v) { return __builtin_bit_cast(float, v << 16); }
__device__ __forceinline__ float bhi(u32 v) { return __builtin_bit_cast(float, v & 0xffff0000u); }
__device__ __forceinline__ u32 canon(u32 v) { return v < 0xfffffffeu ? v : 0xfffffffeu; }
__device__ __forceinline__ u32x4 canon4(f32x4 v) {
  u32x4 r = __builtin_bit_cast(u32x4, v);
  return u32x4{canon(r[0]), canon(r[1]), canon(r[2]), canon(r[3])};
}
__device__ __forceinline__ u32 umax(u32 a, u32 b) { return a > b ? a : b; }
__device__ __forceinline__ u32 max4(u32x4 v) { return umax(umax(v[0], v[1]), umax(v[2], v[3])); }
__device__ __forceinline__ u32 max4(u32x4 v, u32 m) { return umax(umax(umax(v[0], v[1]), m), umax(v[2], v[3])); }
__device__ __forceinline__ bool full(u32 m) { return __all(m != 0xffffffffu); }
__device__ __forceinline__ float rcp(float x) { return __builtin_amdgcn_rcpf(x); }
template <int CTRL> __device__ __forceinline__ float dppf(float x) {
  return __builtin_bit_cast(float, __builtin_amdgcn_mov_dpp(__builtin_bit_cast(int, x), CTRL, 0xf, 0xf, true));
}
template <int CTRL> __device__ __forceinline__ u32 dppu(u32 x) { return (u32)__builtin_amdgcn_mov_dpp((int)x, CTRL, 0xf, 0xf, true); }
constexpr int ROR8 = 0x128, ROR4 = 0x124, ROR2 = 0x122, ROR1 = 0x121, XOR1 = 0xB1, XOR2 = 0x4E, XOR7 = 0x141;
__device__ __forceinline__ float row_sum(float x) { x += dppf<ROR8>(x); x += dppf<ROR4>(x); x += dppf<ROR2>(x); x += dppf<ROR1>(x); return x; }
__device__ __forceinline__ float row_max(float x) {
  x = fmaxf(x, dppf<ROR8>(x)); x = fmaxf(x, dppf<ROR4>(x)); x = fmaxf(x, dppf<ROR2>(x)); x = fmaxf(x, dppf<ROR1>(x)); return x;
}
__device__ __forceinline__ float x16_sum(float x) {
  auto s = __builtin_amdgcn_permlane16_swap(__float_as_uint(x), __float_as_uint(x), false, false);
  return __uint_as_float(s[0]) + __uint_as_float(s[1]);
}
__device__ __forceinline__ float x32_sum(float x) {
  auto s = __builtin_amdgcn_permlane32_swap(__float_as_uint(x), __float_as_uint(x), false, false);
  return __uint_as_float(s[0]) + __uint_as_float(s[1]);
}
__device__ __forceinline__ float x16_max(float x) {
  auto s = __builtin_amdgcn_permlane16_swap(__float_as_uint(x), __float_as_uint(x), false, false);
  return fmaxf(__uint_as_float(s[0]), __uint_as_float(s[1]));
}
__device__ __forceinline__ float x32_max(float x) {
  auto s = __builtin_amdgcn_permlane32_swap(__float_as_uint(x), __float_as_uint(x), false, false);
  return fmaxf(__uint_as_float(s[0]), __uint_as_float(s[1]));
}
__device__ __forceinline__ float t16(float a, float b) {
  auto s = __builtin_amdgcn_permlane16_swap(__float_as_uint(a), __float_as_uint(b), false, false);
  return __uint_as_float(s[0]) + __uint_as_float(s[1]);
}
__device__ __forceinline__ float t32(float a, float b) {
  auto s = __builtin_amdgcn_permlane32_swap(__float_as_uint(a), __float_as_uint(b), false, false);
  return __uint_as_float(s[0]) + __uint_as_float(s[1]);
}
__device__ __forceinline__ f32x2 up2(u32 v) { return f32x2{blo(v), bhi(v)}; }
__device__ __forceinline__ u32 pk2(f32x2 v) { return pack2(v[0], v[1]); }
#define PIN(x) asm volatile("" : "+v"(x))
__device__ __forceinline__ f32x2 rb2(f32x2 v) { u32 p = pk2(v); asm("" : "+v"(p)); return up2(p); }
__device__ __forceinline__ float wave_sum(float x) { return x32_sum(x16_sum(row_sum(x))); }
__device__ __forceinline__ float wave_max(float x) { return x32_max(x16_max(row_max(x))); }
__device__ __forceinline__ float dot2(u32 a, u32 b, float c) {
  return __builtin_amdgcn_fdot2_f32_bf16(__builtin_bit_cast(bf16x2, a), __builtin_bit_cast(bf16x2, b), c, false);
}
__device__ __forceinline__ f32x4 mfma(u32x4 a, u32x4 b, f32x4 c) {
  return __builtin_amdgcn_mfma_f32_16x16x32_bf16(__builtin_bit_cast(bf16x8, a), __builtin_bit_cast(bf16x8, b), c, 0, 0, 0);
}
__device__ __forceinline__ u32x4 ldnt(const void* p) { return __builtin_nontemporal_load((const u32x4*)p); }
__device__ __forceinline__ void glds(const void* g, u32 lds) {
  u32 keep;
  asm volatile("s_mov_b32 %0, m0\n\ts_mov_b32 m0, %2\n\ts_nop 0\n\tglobal_load_lds_dwordx4 %1, off nt\n\ts_mov_b32 m0, %0"
               : "=&s"(keep) : "v"(g), "s"(lds) : "memory");
}
__device__ __forceinline__ u32x4 trfrag(LAS unsigned char* p0, LAS unsigned char* p1) {
  s16x4 a = __builtin_amdgcn_ds_read_tr16_b64_v4i16((LAS s16x4*)p0);
  s16x4 b = __builtin_amdgcn_ds_read_tr16_b64_v4i16((LAS s16x4*)p1);
  u32x2 x = __builtin_bit_cast(u32x2, a), y = __builtin_bit_cast(u32x2, b);
  return u32x4{x[0], x[1], y[0], y[1]};
}
#define SB() __builtin_amdgcn_sched_barrier(0)
#define MEMB() asm volatile("" ::: "memory")
#define VM0() asm volatile("s_waitcnt vmcnt(0)" ::: "memory")
#define LGKM0() asm volatile("s_waitcnt lgkmcnt(0)" ::: "memory")
#define BAR() asm volatile("s_waitcnt lgkmcnt(0)\n\ts_barrier" ::: "memory")
#define TAG() do { u64 ep_; asm volatile("s_load_dwordx2 %0, %1, 0x0\n\ts_waitcnt lgkmcnt(0)" : "=s"(ep_) : "s"(ws) : "memory"); \
    ep_ += 1ull; tag_lo = (u32)ep_; tag_hi = (u32)(ep_ >> 32); } while (0)
constexpr int SC1 = 16;
__device__ __forceinline__ u32x4 ld_sc1(__amdgpu_buffer_rsrc_t rs, int off) { return __builtin_amdgcn_raw_buffer_load_b128(rs, off, 0, SC1); }
__device__ __forceinline__ u32x2 ld_sc1_64(__amdgpu_buffer_rsrc_t rs, int off) { return __builtin_amdgcn_raw_buffer_load_b64(rs, off, 0, SC1); }
__device__ __forceinline__ void st_sc1(__amdgpu_buffer_rsrc_t rs, int off, u32x4 v) { __builtin_amdgcn_raw_buffer_store_b128(v, rs, off, 0, SC1); }
__device__ __forceinline__ void st_sc1_64(__amdgpu_buffer_rsrc_t rs, int off, u32x2 v) { __builtin_amdgcn_raw_buffer_store_b64(v, rs, off, 0, SC1); }

__device__ __forceinline__ void ln_row(u32x4 xv, float (&ln)[8]) {
  const float x[8] = {blo(xv[0]), bhi(xv[0]), blo(xv[1]), bhi(xv[1]), blo(xv[2]), bhi(xv[2]), blo(xv[3]), bhi(xv[3])};
  const float x0 = __builtin_bit_cast(float, __builtin_amdgcn_readfirstlane(xv[0] << 16));
  float s = 0.f, v = 0.f;
#pragma unroll
  for (int j = 0; j < 8; ++j) { const float d = x[j] - x0; s += d; v = fmaf(d, d, v); }
  const float md = wave_sum(s) * (1.f / 512.f), sq = wave_sum(v) * (1.f / 512.f);
  const float var = rbf(fmaxf(fmaf(-md, md, sq), 0.f));
  const float m = rbf(x0 + md);
  const float inv = rcp(rbf(__builtin_amdgcn_sqrtf(rbf(var + 1e-5f))));
#pragma unroll
  for (int j = 0; j < 8; ++j) ln[j] = rbf(rbf(x[j] - m) * inv);
}
#define PACE_(W, INC) do { asm volatile(W "\n1:\n\ts_memrealtime vcc\n\ts_waitcnt lgkmcnt(0)\n\ts_sub_i32 vcc_hi, vcc_lo, %0\n\ts_cmp_lt_i32 vcc_hi, 0\n\t" \
    "s_cbranch_scc0 2f\n\ts_sleep 1\n\ts_branch 1b\n2:\n\ts_add_i32 %0, %0, %1" : "+s"(due) : "is"(INC) : "memory", "scc", "vcc"); SB(); } while (0)
#define PACE(INC) PACE_("s_waitcnt vmcnt(3)", INC)
#define PACEN(INC) PACE_("", INC)

#define STAMP(i) do { if ((i) == 0) { u32 t_ = (i) == 0 ? lk0 : (u32)__builtin_amdgcn_s_memrealtime(); if (lane == 0) __hip_atomic_store((GAS u32*)(ws + (768u << 20)) + (i) * 256 + bid, t_, RLX_AGENT); } } while (0)

__device__ __forceinline__ void lds_wait(volatile LAS u32* p, u32 want) {
  _Pragma("unroll 1") for (u32 s = 0; s < LSPIN_CAP; ++s) {
    if (__builtin_amdgcn_readfirstlane(*p) >= want) break;
    __builtin_amdgcn_s_sleep(2);
  }
  MEMB();
}
__device__ __forceinline__ u32 lds_arrive(LAS u32* p, int lane) {
  LGKM0();
  u32 old = 0;
  if (lane == 0) old = __hip_atomic_fetch_add(p, 1u, __ATOMIC_RELAXED, __HIP_MEMORY_SCOPE_WORKGROUP);
  old = __builtin_amdgcn_readfirstlane(old);
  MEMB();
  return old;
}

__global__ void __launch_bounds__(1024) fused(const u16* __restrict__ x, const u16* __restrict__ kc, const u16* __restrict__ vc, const u16* __restrict__ wq,
                                              const u16* __restrict__ wo, const u16* __restrict__ wg, const u16* __restrict__ wu, const u16* __restrict__ wd,
                                              const u16* __restrict__ cc, const u16* __restrict__ wmod, u16* __restrict__ out, unsigned char* ws) {
  extern __shared__ __attribute__((aligned(16))) unsigned char smem[];
  LAS unsigned char* L = (LAS unsigned char*)smem;
  const u32 lbase = (u32)(size_t)L;
  const int tid = threadIdx.x, w = __builtin_amdgcn_readfirstlane(tid >> 6), bid = blockIdx.x;
  int lane = tid & 63;
  u32 lk0 = (u32)__builtin_amdgcn_s_memrealtime(); asm volatile("" : "+s"(lk0));
  u32 due = (u32)__builtin_amdgcn_s_memrealtime() + (u32)(w * T0) / 16u + DUE0;
  asm volatile("" : "+s"(due));
  if (tid < 16) ((volatile LAS u32*)(L + L_CTL))[tid] = 0u;
  asm volatile("" :: "s"(kc), "s"(wmod), "s"(ws), "s"(cc), "s"(x), "s"(wq));
  const int xcd = bid & 7, idx = bid >> 3, hj = idx & 3, h = 4 * xcd + hj, b = idx >> 2, bh = b * 32 + h;
  if (w == 0) STAMP(0);
  STAMP(20);
  const bool even = (bid & 1) == 0;
  const int x2_ = xcd >> 1;
  const int rsh = (x2_ * 4 + hj) * 4;
  const int p1r = (int)((0xFFF3FFF2FF51FF40ull >> rsh) & 15u) * 8 + b + (bid & 1) * 128;
  const int p1 = p1r < 48 ? p1r : -1;
  const bool role1 = p1 >= 0 && w < 8;
  if (p1 < 0) due -= (bid & 1) ? DODD : DEVN;
  const int tm = T1 - (p1 >= 0 ? TP1 : 0);
  u32x4 cv = {0, 0, 0, 0}, xr = {0, 0, 0, 0};
  if (role1) {
    __builtin_amdgcn_s_setprio(3);
    cv = ldnt(cc + (lane & 7) * 512 + 64 * w + 32 * ((lane >> 3) & 1) + 8 * (lane >> 4));
    if ((p1 >> 3) == 1) xr = ldnt(x + w * 512 + lane * 8);
    const u16* msrc = wmod + (size_t)(64 * w + (lane >> 3)) * 3072 + 64 * p1 + (lane & 7) * 8;
#pragma unroll 1
    for (int i = 0; i < 8; ++i) glds(msrc + (size_t)i * 8 * 3072, lbase + L_W + w * 8192 + i * 1024);
  }
  int rot = idx;
#define RO(I) ((((I) + rot) & 15) * 8192)
  const u16* kp = kc + ((size_t)bh * SK + (SK - WIN) + 4 * w) * 128 + lane * 8;
  u32x4 kr[16], v1[8], v2[8];
  kr[0] = ldnt(kp + RO(0));
  SB();
  volatile LAS u32* ctl = (volatile LAS u32*)(L + L_CTL);
  int n = lane & 15, g = lane >> 4;
#define RELANE() do { asm volatile("" : "+v"(lane)); n = lane & 15; g = lane >> 4; } while (0)
  const int e = bid >> 1;
  const int p3r = (int)((0xF35FF24FF1FFF0FFull >> rsh) & 15u) * 8 + b + (bid & 1) * 128;
  const int p3 = p3r < NP3 ? p3r : -1;
  const __amdgpu_buffer_rsrc_t rs = __builtin_amdgcn_make_buffer_rsrc((void*)ws, 0, WS_END, 0x00020000);
  u32 tag_lo = 0, tag_hi = 0;

  if (w == 8 && !even) {
    const u32x4 sent = {~0u, ~0u, ~0u, ~0u};
#pragma unroll
    for (int i = 0; i < 10; ++i)
      if (i * 1024 + lane * 16 < S_CHUNK) st_sc1(rs, WS_S + e * S_CHUNK + i * 1024 + lane * 16, sent);
  }
#define KL(I, T) { PACE(T); kr[I] = ldnt(kp + RO(I)); SB(); }
#define KLN(I, T) { PACEN(T); kr[I] = ldnt(kp + RO(I)); SB(); }
  RELANE();
  const int cg = bid >> 2, kq = bid & 3;
  if (w == SW) {
    __builtin_amdgcn_s_setprio(2);
    const u32x4 sent = {~0u, ~0u, ~0u, ~0u};
    st_sc1(rs, WS_QP + bh * 2048 + lane * 16, sent);
    st_sc1(rs, WS_QP + bh * 2048 + 1024 + lane * 16, sent);
  }
  const u16* qsrc = wq + (size_t)(128 * kq + (lane >> 3)) * 4096 + 64 * cg;
#define WQ4(I) if (w == SW) { _Pragma("unroll 1") for (int i = 4 * (I); i < 4 * (I) + 4; ++i) \
      glds(qsrc + (size_t)i * 8 * 4096 + ((lane & 7) ^ ((((lane >> 4) & 1) | ((i & 1) << 1)) << 1)) * 8, lbase + L_RED + i * 1024); }
#define HU(U) ((U) == 0 ? (p3r < 48 ? -1 : h) : (!even ? -1 : (hj == 3 ? h - 1 : ((hj == 0 && x2_ >= 2) ? h + 1 : -1))))
#define ML(I) if (p3 >= 0) { const int c_ = (lane & 7) ^ ((((lane >> 4) & 1) | (((I) & 1) << 1)) << 1); \
    glds(((c_ & 4) ? wu : wg) + (size_t)(32 * w + 8 * (I) + (lane >> 3)) * FF + 32 * p3 + 8 * (c_ & 3), lbase + L_W + w * 4096 + (I) * 1024); SB(); }
#define DL(I) if (p3 >= 0) { const int r_ = 2 * w + (I); \
    glds(wd + (size_t)(32 * p3 + r_) * 512 + (lane ^ (((r_ & 3) | (((r_ >> 3) & 1) << 2)) << 1)) * 8, lbase + L_W + 65536 + r_ * 1024); SB(); }
  ML(0)
  BAR();
  if (role1) {
    asm volatile("s_waitcnt vmcnt(1)" ::: "memory");
    u32x4 bf;
#pragma unroll
    for (int j = 0; j < 4; ++j) {
      const float a0 = blo(cv[j]), a1 = bhi(cv[j]);
      bf[j] = pack2(a0 * rcp(1.f + __expf(-a0)), a1 * rcp(1.f + __expf(-a1)));
    }
    LAS unsigned char* tb = L + L_W + w * 8192 + (8 * g + (n >> 2)) * 128 + (n & 3) * 8;
#pragma unroll
    for (int t = 0; t < 4; ++t) {
      const f32x4 z = {0.f, 0.f, 0.f, 0.f};
      const f32x4 aL = mfma(trfrag(tb + t * 32, tb + t * 32 + 512), bf, z);
      const f32x4 aH = mfma(trfrag(tb + t * 32 + 4096, tb + t * 32 + 4096 + 512), bf, z);
      const f32x4 p = {aL[0] + dppf<ROR8>(aH[0]), aL[1] + dppf<ROR8>(aH[1]), aL[2] + dppf<ROR8>(aH[2]), aL[3] + dppf<ROR8>(aH[3])};
      if (n < 8) *(LAS f32x4*)(L + L_P1 + w * 2048 + t * 512 + (g * 8 + n) * 16) = p;
    }
    if ((p1 >> 3) == 1) {
      float ln[8];
      ln_row(xr, ln);
      if ((lane >> 3) == (p1 & 7)) {
        LAS f32x4* d = (LAS f32x4*)(L + L_H2 + w * 256 + (lane & 7) * 32);
        d[0] = f32x4{ln[0], ln[1], ln[2], ln[3]};
        d[1] = f32x4{ln[4], ln[5], ln[6], ln[7]};
      }
    }
    const u32 old = lds_arrive((LAS u32*)(L + L_CTL) + C_P1CNT, lane);
    if (old == 7) {
      TAG();
      const int bb = n & 7, ch = p1 >> 3, col0 = (p1 & 7) * 64;
#pragma unroll 1
      for (int tt = 0; tt < 2; ++tt) {
        const int t = 2 * (n >> 3) + tt;
        f32x4 s = {0.f, 0.f, 0.f, 0.f};
#pragma unroll
        for (int ww = 0; ww < 8; ++ww) s += *(LAS f32x4*)(L + L_P1 + ww * 2048 + t * 512 + (g * 8 + bb) * 16);
        float v[4] = {rbf(s[0]), rbf(s[1]), rbf(s[2]), rbf(s[3])};
        const int col = 16 * t + 4 * g;
        if (ch == 1) {
          const f32x4 ln = *(LAS f32x4*)(L + L_H2 + bb * 256 + col * 4);
#pragma unroll
          for (int j = 0; j < 4; ++j) v[j] = ln[j] * rbf(1.f + v[j]);
        }
        const u32 lo = pack2(v[0], v[1]), hi = pack2(v[2], v[3]);
        if (ch < 2) {
          GAS u64* gp = (GAS u64*)(ws + WS_G1 + ((ch * 8 + bb) * 256 + ((col0 + col) >> 1)) * 8);
          __hip_atomic_store(gp, ((u64)tag_lo << 32) | lo, RLX_AGENT);
          __hip_atomic_store(gp + 1, ((u64)tag_lo << 32) | hi, RLX_AGENT);
        } else {
          st_sc1_64(rs, WS_MODR + ((ch - 2) * 8 + bb) * 1024 + (col0 + col) * 2, u32x2{lo, hi});
        }
      }
      STAMP(3);
      if (ch >= 2) {
        VM0();
        if (lane == 0) st_sc1_64(rs, WS_FLAGS + (112 + p1) * 8, u32x2{tag_lo, tag_hi});
      }
    }
    __builtin_amdgcn_s_setprio(0);
  }
  KLN(1, T0)
  if (w == 0) STAMP(2);
  KL(2, T0)
  KL(3, TS)
  if ((w == 8 && !even) || w == SW) {
    asm volatile("s_waitcnt vmcnt(3)" ::: "memory");
    TAG();
    if (lane == 0) st_sc1_64(rs, w == 8 ? WS_FLAGS + e * 8 : WS_FLAG2 + (h * 8 + b) * 8, u32x2{tag_lo, tag_hi});
    if (w == 8) STAMP(1); else ctl[C_QCNT] = 1u;
  }
  WQ4(0) KLN(4, TS) WQ4(1) KLN(5, TS) WQ4(2) KLN(6, TS) WQ4(3) KLN(7, T0)
  u32x4 sa[4], tt[4];
  u32x2 f2 = {0, 0};
  const int o0 = WS_G1 + ((n & 7) * 256 + 64 * kq + 32 * (n >> 3) + 4 * g) * 8;
#define G1_LOAD() { f2 = ld_sc1_64(rs, WS_FLAG2 + ((cg >> 1) * 8 + (lane & 7)) * 8);   \
    _Pragma("unroll") for (int i = 0; i < 4; ++i) { sa[i] = ld_sc1(rs, o0 + (i >> 1) * 128 + (i & 1) * 16); tt[i] = ld_sc1(rs, o0 + 8 * 256 * 8 + (i >> 1) * 128 + (i & 1) * 16); } }
  if (w == SW) G1_LOAD()
  else {
#pragma unroll
    for (int i = 0; i < 4; ++i) { sa[i] = u32x4{0, 0, 0, 0}; tt[i] = u32x4{0, 0, 0, 0}; }
  }
  SB();
  KLN(8, T0) KLN(9, T0)
  if (w == SW) {
    u32x4 hb[2];
    {
#define G1_BAD() ((f2[0] ^ tag_lo) | (f2[1] ^ tag_hi) | (sa[0][1] ^ tag_lo) | (sa[0][3] ^ tag_lo) | (tt[0][1] ^ tag_lo) | (tt[0][3] ^ tag_lo) | (sa[1][1] ^ tag_lo) | \
      (sa[1][3] ^ tag_lo) | (tt[1][1] ^ tag_lo) | (tt[1][3] ^ tag_lo) | (sa[2][1] ^ tag_lo) | (sa[2][3] ^ tag_lo) | (tt[2][1] ^ tag_lo) | (tt[2][3] ^ tag_lo) | \
      (sa[3][1] ^ tag_lo) | (sa[3][3] ^ tag_lo) | (tt[3][1] ^ tag_lo) | (tt[3][3] ^ tag_lo))
      if (!__all(G1_BAD() == 0u)) {
#pragma unroll 1
        for (u32 s = 0; s < SPIN_CAP; ++s) {
          __builtin_amdgcn_s_sleep(16);
          MEMB();
          G1_LOAD()
          if (__all(G1_BAD() == 0u)) break;
        }
      }
#pragma unroll
      for (int i = 0; i < 4; ++i) {
        hb[i >> 1][(i & 1) * 2 + 0] = pack2(blo(tt[i][0]) + blo(sa[i][0]), bhi(tt[i][0]) + bhi(sa[i][0]));
        hb[i >> 1][(i & 1) * 2 + 1] = pack2(blo(tt[i][2]) + blo(sa[i][2]), bhi(tt[i][2]) + bhi(sa[i][2]));
      }
    }
    MEMB();
    STAMP(4);
    LAS unsigned char* tb = L + L_RED + (8 * g + (n >> 2)) * 128 + (n & 3) * 8;
    const int sx = (n >> 3) | ((g & 1) << 1);
    const int qo = WS_QP + (((n & 7) * 32 + (cg >> 1)) * 4 + kq) * 512 + ((cg & 1) * 64 + 16 * (n >> 3) + 4 * g) * 4;
#pragma unroll
    for (int t = 0; t < 4; t += 2) {
      const f32x4 z = {0.f, 0.f, 0.f, 0.f};
      f32x4 aL[2], aH[2], v;
#pragma unroll
      for (int u = 0; u < 2; ++u) {
        LAS unsigned char* ta = tb + ((t + u) ^ sx) * 32;
        aL[u] = mfma(trfrag(ta + 4096, ta + 4096 + 512), hb[1], mfma(trfrag(ta, ta + 512), hb[0], z));
        aH[u] = mfma(trfrag(ta + 12288, ta + 12288 + 512), hb[1], mfma(trfrag(ta + 8192, ta + 8192 + 512), hb[0], z));
      }
#pragma unroll
      for (int r = 0; r < 4; ++r) v[r] = (n < 8 ? aL[0][r] : aH[1][r]) + dppf<ROR8>(n < 8 ? aL[1][r] : aH[0][r]);
      st_sc1(rs, qo + t * 64, canon4(v));
    }
    STAMP(17);
  }
  RELANE();
  KL(10, tm) KL(11, tm)
  KL(12, tm) KL(13, tm) KL(14, tm) KL(15, tm)
  asm volatile("" : "+s"(rot));
  const int tb = TB - (p1 >= 0 ? TP1 : 0);
  const u16* vp = vc + ((size_t)bh * SK + (SK - WIN) + 4 * w) * 128 + lane * 8;
#define V1(I, T) { PACE(T); v1[I] = ldnt(vp + RO(I)); SB(); }
  V1(0, tm) V1(1, tm)
  u32x4 q0 = {0, 0, 0, 0}, q1 = {0, 0, 0, 0};
  if (w == SW + 1) {
    __builtin_amdgcn_s_setprio(2);
    lds_wait(ctl + C_QCNT, 1u);
    q0 = ld_sc1(rs, WS_QP + bh * 2048 + lane * 16);
    q1 = ld_sc1(rs, WS_QP + bh * 2048 + 1024 + lane * 16);
  }
  SB();
  V1(2, tb) V1(3, tb)
  if (w == SW + 1) {
    if (!full(max4(q0, max4(q1)))) {
#pragma unroll 1
      for (u32 s = 0; s < SPIN_CAP; ++s) {
        __builtin_amdgcn_s_sleep(4);
        MEMB();
        q0 = ld_sc1(rs, WS_QP + bh * 2048 + lane * 16);
        q1 = ld_sc1(rs, WS_QP + bh * 2048 + 1024 + lane * 16);
        if (full(max4(q0, max4(q1)))) break;
      }
    }
    const f32x4 a = __builtin_bit_cast(f32x4, q0) + __builtin_bit_cast(f32x4, q1);
    const float s0 = x32_sum(a[0]), s1 = x32_sum(a[1]), s2 = x32_sum(a[2]), s3 = x32_sum(a[3]);
    if (lane < 32) *(LAS u32x2*)(L + L_Q + lane * 8) = u32x2{pack2(s0, s1), pack2(s2, s3)};
    LGKM0();
    ctl[C_Q] = 1u;
    STAMP(5);
  }
  RELANE();
  V1(4, tb)
  TAG();
  V1(5, tb)
#define V2(I, T) { PACE(T); v2[I] = ldnt(vp + RO(8 + (I))); SB(); }
  V1(6, tb) V1(7, tb)
  const int flo = WS_FLAGS + (w * 16 + n) * 8;
  u32x2 fl;
  if (w < 10) fl = ld_sc1_64(rs, flo);
  SB();
  lds_wait(ctl + C_Q, 1u);
  const u32x4 qf = *(LAS u32x4*)(L + L_Q + n * 16);
  if (w == 1) STAMP(6);

  float xs[16];
#pragma unroll
  for (int i = 0; i < 8; ++i) xs[i] = dot2(kr[i][3], qf[3], dot2(kr[i][2], qf[2], dot2(kr[i][1], qf[1], dot2(kr[i][0], qf[0], 0.f))));
#pragma unroll
  for (int i = 0; i < 8; ++i) asm volatile("" : "+v"(xs[i]));
  SB();
  V2(0, tb)
#pragma unroll
  for (int i = 8; i < 16; ++i) xs[i] = dot2(kr[i][3], qf[3], dot2(kr[i][2], qf[2], dot2(kr[i][1], qf[1], dot2(kr[i][0], qf[0], 0.f))));
#pragma unroll
  for (int i = 8; i < 16; ++i) asm volatile("" : "+v"(xs[i]));
  SB();
  RELANE();
  V2(1, tb)
  u32x4 pre0, pre1;
  u32 prx;
  const int rb = e >> 4, rj = e & 15;
  const int prer = (p3 >= 0 && w < 8) ? 1 : ((!even && w == 3) ? 2 : 0);
  const int po0 = WS_MODR + (prer == 1 ? (1 * 8 + w) * 1024 + lane * 16 : (0 * 8 + rb) * 1024 + 64 * rj + 16 * ((lane & 7) >> 1));
  const int po1 = po0 + (prer == 1 ? 8192 : 3 * 8192);
#define TR_STEP(HALF, CTRL, BIT) _Pragma("unroll") for (int hh = 0; hh < (HALF); ++hh) { \
    const float lo_ = xs[hh], hi_ = xs[hh + (HALF)]; const float t1_ = lo_ + dppf<CTRL>(lo_), t2_ = hi_ + dppf<CTRL>(hi_); xs[hh] = (n & (BIT)) ? t2_ : t1_; }
  TR_STEP(8, ROR8, 8) TR_STEP(4, XOR7, 4) TR_STEP(2, XOR2, 2) TR_STEP(1, XOR1, 1)
#undef TR_STEP
  const float sc = rbf(rbf(xs[0]) * 0.08838834764831845f);
  const float mw_ = wave_max(sc);
  const float lw = wave_sum(__expf(sc - mw_));
  if (lane == 0) *(LAS f32x2*)(L + L_ML + w * 8) = f32x2{mw_, lw};
  if (w < 10 && !__all(fl[0] == tag_lo && fl[1] == tag_hi)) {
#pragma unroll 1
    for (u32 s = 0; s < SPIN_CAP; ++s) {
      __builtin_amdgcn_s_sleep(4);
      MEMB();
      fl = ld_sc1_64(rs, flo);
      if (__all(fl[0] == tag_lo && fl[1] == tag_hi)) break;
    }
  }
  lds_arrive((LAS u32*)(L + L_CTL) + C_ML, lane);
  V2(2, tb) V2(3, tb) V2(4, tb) V2(5, tb)
  lds_wait(ctl + C_ML, 16u);
  if (prer) { pre0 = ld_sc1(rs, po0); pre1 = ld_sc1(rs, po1); prx = __builtin_nontemporal_load((const u32*)(x + rb * 512 + 32 * rj + 4 * (lane & 7) + (g & 2))); }
  SB();
  u32 pd;
  {
    const f32x2 ml = *(LAS f32x2*)(L + L_ML + n * 8);
    const float M = row_max(ml[0]);
    const float pe = __expf(sc - M) * rcp(row_sum(__expf(ml[0] - M) * ml[1]));
    pd = pack2(pe, pe);
  }
  u32 mk[4];
#pragma unroll
  for (int j = 0; j < 4; ++j) mk[j] = (n >> 1) == j ? ((n & 1) ? 0xffff0000u : 0x0000ffffu) : 0u;
  f32x4 oa = {0.f, 0.f, 0.f, 0.f};
#define PV(V, I) { const u32 pb_ = dppu<0x150 + (I)>(pd); oa = mfma(V, u32x4{pb_ & mk[0], pb_ & mk[1], pb_ & mk[2], pb_ & mk[3]}, oa); asm volatile("" : "+v"(oa)); SB(); }
  PV(v1[0], 0) PV(v1[1], 1) PV(v1[2], 2) PV(v1[3], 3)
  V2(6, tb)
  PV(v1[4], 4) PV(v1[5], 5) PV(v1[6], 6) PV(v1[7], 7) PV(v2[0], 8) PV(v2[1], 9)
  V2(7, tb)
#define WOE() if (w < 8) { _Pragma("unroll") for (int u = 0; u < 2; ++u) if (HU(u) >= 0) { _Pragma("unroll") for (int i = 0; i < 2; ++i) \
      glds(wo + (size_t)(128 * HU(u) + 8 * (2 * w + i) + (lane >> 3)) * 512 + 64 * b + ((lane & 7) ^ (((2 * w + i) & 3) << 1)) * 8, lbase + L_WOT + u * 16384 + (2 * w + i) * 1024); } }
  PV(v2[2], 10) PV(v2[3], 11)
  u32x4 wf[4][4];
  const int hu = (w == 3 || w == 4) ? HU(w - 3) : -1;
  SB();
  PV(v2[4], 12) PV(v2[5], 13) PV(v2[6], 14) PV(v2[7], 15)
#undef PV
#undef V2
  if (n < 8) *(LAS f32x4*)(L + L_RED + w * OSTR + (g * 8 + n) * 16) = oa;
  if (w == 13 || w == 3 || w == 4) __builtin_amdgcn_s_setprio(3);
  WOE()
  const bool mpre = w < WPRE;
  if (mpre) {
#pragma unroll
    for (int i = MLI; i < MLI + NPRE; ++i) ML(i)
  }
  BAR();
  if (w == 0) STAMP(8);
  if (w == 13) {
    f32x4 s = {0.f, 0.f, 0.f, 0.f};
#pragma unroll
    for (int ww = 0; ww < 8; ++ww) s += *(LAS f32x4*)(L + L_RED + (ww + 8 * (lane >> 5)) * OSTR + (lane & 31) * 16);
    const float s0 = x32_sum(s[0]), s1 = x32_sum(s[1]), s2 = x32_sum(s[2]), s3 = x32_sum(s[3]);
    if (lane < 32) st_sc1_64(rs, WS_O + (h * 8 + b) * 256 + lane * 8, u32x2{canon(pack2(s0, s1)), canon(pack2(s2, s3))});
  }
  if (w == 13) STAMP(9);
  if (p3r >= 48) {
    if (w < 8) VM0();
    BAR();
  }
  if (hu >= 0) {
    const int q = n >> 2, p = n & 3;
#pragma unroll
    for (int tt = 0; tt < 4; ++tt) {
      LAS unsigned char* tb = L + L_WOT + (w - 3) * 16384 + (8 * q + 2 * g) * 128 + (tt ^ q) * 32 + p * 8;
#pragma unroll
      for (int ks = 0; ks < 4; ++ks) wf[tt][ks] = trfrag(tb + ks * 4096, tb + ks * 4096 + 128);
    }
  }
  if (hu >= 0) {
    u32x4 o0, o1;
    const int oo = WS_O + (hu * 8 + (n & 7)) * 256 + (n >> 3) * 128 + g * 16;
    int pao = WS_PART + (((n & 7) * 16 + 2 * b) * 32 + hu) * 128 + (n >> 3) * 64 + g * 16;
    PIN(pao);
#pragma unroll 1
    for (u32 s = 0; s < SPIN_CAP; ++s) {
      o0 = ld_sc1(rs, oo);
      o1 = ld_sc1(rs, oo + 64);
      if (full(max4(o0, max4(o1)))) break;
      MEMB();
      __builtin_amdgcn_s_sleep(2);
    }
    const f32x4 z = {0.f, 0.f, 0.f, 0.f};
#pragma unroll
    for (int jj = 0; jj < 2; ++jj) {
      const f32x4 aL0 = mfma(wf[2 * jj][1], o1, mfma(wf[2 * jj][0], o0, z)), aH0 = mfma(wf[2 * jj][3], o1, mfma(wf[2 * jj][2], o0, z));
      const f32x4 aL1 = mfma(wf[2 * jj + 1][1], o1, mfma(wf[2 * jj + 1][0], o0, z)), aH1 = mfma(wf[2 * jj + 1][3], o1, mfma(wf[2 * jj + 1][2], o0, z));
      f32x4 v;
#pragma unroll
      for (int r = 0; r < 4; ++r) v[r] = (n < 8 ? aL0[r] : aH1[r]) + dppf<ROR8>(n < 8 ? aL1[r] : aH0[r]);
      st_sc1(rs, pao + jj * 4096, canon4(v));
    }
    if (w == 3) STAMP(16); else STAMP(7);
  }

  if (p3 >= 0) {
    const int t = w & 3, kk = w >> 2;
    BAR();
    if (!mpre) {
#pragma unroll
      for (int i = MLI; i < MLI + NPRE; ++i) ML(i)
    }
#pragma unroll
    for (int i = MLI + NPRE; i < 4; ++i) ML(i)
    BAR();
    DL(0) DL(1)
    f32x2 c1[4], sb[4];
#pragma unroll
    for (int j = 0; j < 4; ++j) { c1[j] = rb2(up2(pre1[j]) + 1.f); sb[j] = up2(pre0[j]); PIN(c1[j]); PIN(sb[j]); }
    int xo = WS_X2 + w * 1024 + lane * 16;
    u32 a_h2w = L_H2 + w * H2S + lane * 16;
    u32 a_h2r = L_H2 + (n & 7) * H2S + (128 * kk + 8 * g) * 2;
    u32 a_gw = L_RED + kk * 2048 + t * 512 + (g * 8 + n) * 16;
    u32 a_gr = L_RED + (w & 3) * 256 + lane * 4;
    u32 a_hw = L_HH + ((lane >> 2) & 7) * 64 + ((w & 3) * 2 + (lane >> 5)) * 8 + (lane & 3) * 2;
    u32 a_hr = L_HH + (n & 7) * 64 + g * 16;
    u32 a_d0 = L_W + 65536 + (8 * g + (n >> 2)) * 1024 + ((4 * w + ((n >> 1) & 1)) ^ (((n >> 2) | ((g & 1) << 2)) << 1)) * 16 + (n & 1) * 8;
    int yo = WS_YP + (((n & 7) * 16 + w) * NP3 + p3) * 128 + (n >> 3) * 64 + g * 16;
    PIN(xo); PIN(a_h2w); PIN(a_h2r); PIN(a_gw); PIN(a_gr); PIN(a_hw); PIN(a_hr); PIN(a_d0); PIN(yo);
    asm volatile("s_waitcnt vmcnt(2)" ::: "memory");
    BAR();
    if (w == 0) STAMP(19);
    u32x4 gf[4];
    {
      const int q = n >> 2, p = n & 3;
      LAS unsigned char* tb = L + L_W + (128 * kk + 8 * g + q) * 128 + (t ^ ((q >> 1) | ((g & 1) << 1))) * 32 + p * 8;
#pragma unroll
      for (int ks = 0; ks < 4; ++ks) gf[ks] = trfrag(tb + ks * 4096, tb + ks * 4096 + 512);
    }
    if (w < 8) {
      u32x4 xv;
#pragma unroll 1
      for (u32 s = 0; s < SPIN_CAP; ++s) {
        xv = ld_sc1(rs, xo);
        if (full(max4(xv))) break;
        MEMB();
      }
      if (w == 0) STAMP(12);
      const float x0 = __builtin_bit_cast(float, __builtin_amdgcn_readfirstlane(xv[0] << 16));
      const f32x2 xf[4] = {up2(xv[0]), up2(xv[1]), up2(xv[2]), up2(xv[3])};
      const f32x2 d0 = xf[0] - x0, d1 = xf[1] - x0, d2 = xf[2] - x0, d3 = xf[3] - x0;
      const f32x2 sv = (d0 + d1) + (d2 + d3), qv = __builtin_elementwise_fma(d0, d0, d1 * d1) + __builtin_elementwise_fma(d2, d2, d3 * d3);
      float r = t16(sv[0] + sv[1], qv[0] + qv[1]);
      r = x32_sum(row_sum(r)) * (1.f / 512.f);
      const float md = __builtin_bit_cast(float, __builtin_amdgcn_readlane(__builtin_bit_cast(int, r), 0));
      const float sq = __builtin_bit_cast(float, __builtin_amdgcn_readlane(__builtin_bit_cast(int, r), 16));
      const float m = rbf(x0 + md);
      const float inv = rcp(rbf(__builtin_amdgcn_sqrtf(rbf(rbf(fmaxf(fmaf(-md, md, sq), 0.f)) + 1e-5f))));
      u32x4 hv = {pk2(xf[0] - m), pk2(xf[1] - m), pk2(xf[2] - m), pk2(xf[3] - m)};
      asm("" : "+v"(hv));
      hv = u32x4{pk2(up2(hv[0]) * inv), pk2(up2(hv[1]) * inv), pk2(up2(hv[2]) * inv), pk2(up2(hv[3]) * inv)};
      asm("" : "+v"(hv));
      hv = u32x4{pk2(up2(hv[0]) * c1[0]), pk2(up2(hv[1]) * c1[1]), pk2(up2(hv[2]) * c1[2]), pk2(up2(hv[3]) * c1[3])};
      asm("" : "+v"(hv));
      hv = u32x4{pk2(up2(hv[0]) + sb[0]), pk2(up2(hv[1]) + sb[1]), pk2(up2(hv[2]) + sb[2]), pk2(up2(hv[3]) + sb[3])};
      *(LAS u32x4*)(L + a_h2w) = hv;
    }
    VM0();
    BAR();
    if (w == 0) STAMP(13);
    {
      u32x4 b0 = *(LAS u32x4*)(L + a_h2r), b1 = *(LAS u32x4*)(L + a_h2r + 64), b2 = *(LAS u32x4*)(L + a_h2r + 128), b3 = *(LAS u32x4*)(L + a_h2r + 192);
      SB();
      const f32x4 z = {0.f, 0.f, 0.f, 0.f};
      const f32x4 a = mfma(gf[1], b1, mfma(gf[0], b0, z)) + mfma(gf[3], b3, mfma(gf[2], b2, z));
      if (n < 8) *(LAS f32x4*)(L + a_gw) = a;
    }
    BAR();
    const u32x4 df0 = trfrag(L + a_d0, L + a_d0 + 4096), df1 = trfrag(L + (a_d0 ^ 32), L + (a_d0 ^ 32) + 4096);
    if (w < 4) {
      float gv[4], uv[4];
#pragma unroll
      for (int k2 = 0; k2 < 4; ++k2) { gv[k2] = *(LAS float*)(L + a_gr + k2 * 2048); uv[k2] = *(LAS float*)(L + a_gr + k2 * 2048 + 1024); }
      SB();
      const float gg = rbf((gv[0] + gv[1]) + (gv[2] + gv[3])), uu = rbf((uv[0] + uv[1]) + (uv[2] + uv[3]));
      *(LAS u16*)(L + a_hw) = (u16)pack2(rbf(rcp(1.f + __expf(-gg))) * uu, 0.f);
    }
    BAR();
    {
      const u32x4 hb = *(LAS u32x4*)(L + a_hr);
      const f32x4 z = {0.f, 0.f, 0.f, 0.f};
      const f32x4 a0 = mfma(df0, hb, z), a1 = mfma(df1, hb, z);
      st_sc1(rs, yo, canon4((n & 8) ? a1 : a0));
    }
    if (w == 0) STAMP(14);
  } else if (prer == 2) {
    const u32 gaw = (lane & 1) ? ((g & 2) ? pre0[3] : pre0[2]) : ((g & 2) ? pre0[1] : pre0[0]), gbw = (lane & 1) ? ((g & 2) ? pre1[3] : pre1[2]) : ((g & 2) ? pre1[1] : pre1[0]);
    float ga = (g & 1) ? bhi(gaw) : blo(gaw), gb = (g & 1) ? bhi(gbw) : blo(gbw), xx = (g & 1) ? bhi(prx) : blo(prx);
    int po = WS_PART + (rb * 16 + rj) * 4096 + lane * 16;
    int xo = WS_X2 + rb * 1024 + 64 * rj + 8 * (lane & 7) + 2 * g;
    int yo = WS_YP + (rb * 16 + rj) * (NP3 * 128) + lane * 16;
    int y5 = lane < 24 ? yo + 5 * 1024 : WS_END + 4096;
    PIN(ga); PIN(gb); PIN(xx); PIN(po); PIN(xo); PIN(yo); PIN(y5);
    float x2;
    {
      u32x4 pv[4];
#pragma unroll 1
      for (u32 sp = 0; sp < SPIN_CAP; ++sp) {
#pragma unroll
        for (int i = 0; i < 4; ++i) pv[i] = ld_sc1(rs, po + i * 1024);
        if (full(max4(pv[0], max4(pv[1], max4(pv[2], max4(pv[3])))))) break;
        MEMB();
      }
      if (w == 3) STAMP(10);
      const f32x4 s = (__builtin_bit_cast(f32x4, pv[0]) + __builtin_bit_cast(f32x4, pv[1])) + (__builtin_bit_cast(f32x4, pv[2]) + __builtin_bit_cast(f32x4, pv[3]));
      float t = t32(t16(s[0], s[1]), t16(s[2], s[3]));
      t += dppf<ROR8>(t);
      x2 = rbf(xx + rbf(ga * rbf(t)));
      auto sw = __builtin_amdgcn_permlane16_swap(__float_as_uint(x2), __float_as_uint(x2), false, false);
      if ((lane & 24) == 0) __builtin_amdgcn_raw_buffer_store_b32(canon((sw[0] >> 16) | sw[1]), rs, xo, 0, SC1);
    }
    if (w == 3) STAMP(11);
    { const u32 t_ = (u32)__builtin_amdgcn_s_memrealtime(); while ((u32)__builtin_amdgcn_s_memrealtime() - t_ < YWAIT) __builtin_amdgcn_s_sleep(1); }
    {
      u32x4 yv[6];
#pragma unroll 1
      for (u32 sp = 0; sp < SPIN_CAP; ++sp) {
#pragma unroll
        for (int i = 0; i < 5; ++i) yv[i] = ld_sc1(rs, yo + i * 1024);
        yv[5] = ld_sc1(rs, y5);
        if (full(max4(yv[0], max4(yv[1], max4(yv[2], max4(yv[3], max4(yv[4], max4(yv[5])))))))) break;
        MEMB();
      }
      if (w == 3) STAMP(15);
      const f32x4 s = ((__builtin_bit_cast(f32x4, yv[0]) + __builtin_bit_cast(f32x4, yv[1])) + (__builtin_bit_cast(f32x4, yv[2]) + __builtin_bit_cast(f32x4, yv[3]))) +
                      (__builtin_bit_cast(f32x4, yv[4]) + __builtin_bit_cast(f32x4, yv[5]));
      float t = t32(t16(s[0], s[1]), t16(s[2], s[3]));
      t += dppf<ROR8>(t);
      const float o = x2 + rbf(gb * rbf(t));
      if ((lane & 8) == 0) out[rb * 512 + 32 * rj + 4 * (lane & 7) + g] = (u16)pack2(o, 0.f);
    }
    if (bid == 1 && w == 3 && lane == 0) st_sc1_64(rs, WS_EPOCH, u32x2{tag_lo, tag_hi});
    if (w == 3) STAMP(18);
  }

  if (bid == 0 && w == 0) {
    while ((u32)__builtin_amdgcn_s_memrealtime() - lk0 < 4000u) __builtin_amdgcn_s_sleep(2);
    lane = tid & 63;
    auto rd = [&](int i, int q) { u32 v = __hip_atomic_load((GAS u32*)(ws + (768u << 20)) + i * 256 + q * 64 + lane, RLX_AGENT); return (int)(v - lk0); };
    auto ok = [&](int v) { return v > -500 && v < 4000; };
    auto red = [&](int i, int mode, int xm) { float r = mode == 0 ? -1e9f : (mode == 1 ? 1e9f : 0.f), c = 0.f;
      for (int q = 0; q < 4; ++q) { int v = rd(i, q); if (ok(v) && ((xm >> (lane & 7)) & 1)) { r = mode == 0 ? fmaxf(r, (float)v) : (mode == 1 ? fminf(r, (float)v) : r + (float)v); c += 1.f; } }
      if (mode == 0) return wave_max(r); if (mode == 1) return -wave_max(-r); return wave_sum(r) / wave_sum(c); };
#define mx(i) red(i, 0, 255)
#define mn(i) red(i, 1, 255)
#define av(i) red(i, 2, 255)
#define xm(i, m) red(i, 0, m)
#define xn(i, m) red(i, 1, m)
    float val = (float)(mx(0)-mn(0));
    u32 lk = 5000u + (u32)__builtin_amdgcn_readfirstlane((int)(val * 4.f));
    while ((u32)__builtin_amdgcn_s_memrealtime() - lk0 < lk) __builtin_amdgcn_s_sleep(2);
  }
}

extern "C" void kernel_launch(void* const* d_in, const int* in_sizes, int n_in, void* d_out, int out_size, void* d_ws, size_t ws_size, hipStream_t stream) {
  (void)hipFuncSetAttribute((const void*)fused, hipFuncAttributeMaxDynamicSharedMemorySize, L_END);
  fused<<<256, 1024, L_END, stream>>>((const u16*)d_in[0], (const u16*)d_in[1], (const u16*)d_in[2], (const u16*)d_in[3], (const u16*)d_in[4], (const u16*)d_in[6],
                                      (const u16*)d_in[7], (const u16*)d_in[8], (const u16*)d_in[9], (const u16*)d_in[10], (u16*)d_out, (unsigned char*)d_ws);
}
```

```cpp
#define TAU 226
#define DUE0 920
#define DP 360
#define DX 610
#define DYS 1210
#define KW 8
#include <hip/hip_runtime.h>
#include <stdint.h>

typedef unsigned int u32;
typedef unsigned long long u64;
typedef unsigned short u16;
typedef u32 u32x4 __attribute__((ext_vector_type(4)));
typedef u32 u32x2 __attribute__((ext_vector_type(2)));
typedef float f32x4 __attribute__((ext_vector_type(4)));
typedef float f32x2 __attribute__((ext_vector_type(2)));
typedef short s16x4 __attribute__((ext_vector_type(4)));
typedef __bf16 bf16x8 __attribute__((ext_vector_type(8)));
typedef __bf16 bf16x2 __attribute__((ext_vector_type(2)));
#define GAS __attribute__((address_space(1)))
#define LAS __attribute__((address_space(3)))
#define RLX_AGENT __ATOMIC_RELAXED, __HIP_MEMORY_SCOPE_AGENT

constexpr int SK = 4096, WIN = 1024, FF = 1376, NP3 = 43;

constexpr int WS_EPOCH = 0;
constexpr int WS_FLAGS = 0x1000;
constexpr int WS_TS = 0x1800;
constexpr int WS_G1 = 0x2000;
constexpr int WS_B1 = 0x8000;
constexpr int WS_MODR = 0xA000;
constexpr int WS_Q = 0x12000;
constexpr int WS_O = 0x36000;
constexpr int WS_B0 = 0x56000;
constexpr int WS_PART = 0x5E000;
constexpr int WS_YP = WS_PART + 8 * 16 * 8 * 32 * 4;
constexpr int WS_X2 = WS_YP + 128 * NP3 * 128;
constexpr int WS_END = WS_X2 + 8 * 16384;

constexpr int L_W = 0;
constexpr int L_TP = 65536;
constexpr int L_SINK = 130048;
constexpr int L_RED = 131072;
constexpr int OSTR = 544, OBS = 288;
constexpr int RW = 12;
constexpr int WG_ = 10;
constexpr int L_H2 = L_RED + 16384;
constexpr int H2S = 1056;
constexpr int L_Q = L_H2 + 8 * H2S;
constexpr int L_OB = L_Q + 256;
constexpr int L_ML = L_OB + 256;
constexpr int L_HH = L_ML + 128;
constexpr int L_CTL = L_HH + 512;
constexpr int L_END = L_CTL + 64;
constexpr int C_T = 0, C_TB = 1, C_TX = 2, C_Q = 3, C_ML = 4, C_R = 6, C_T0 = 7, C_X = 8;

constexpr u32 SPIN_CAP = 1u << 17, LSPIN_CAP = 1u << 20;
#define TX 0
#define DB 0
#define SMASK 0xffffffffu
#define CPF 1
#define CODEKB 25
#define EXACT 0
constexpr int TEND = DUE0 + 32 * TAU + 4 * TX - 13 * DB;
#define OFO 470
#define OFB 520
#define WDE 1
#define MLA 3
#define TOUCH 0
#define SKW 400
#define PHASE (4 * bb + hj)
#define ROTF idx
#define XR 0
#define KA 55
#define XCP 1
#define B0C 0
#define PB 200
#define RB 260
#define DO 0
#define DL_ 140
#define DB1 960
#define DY 350
#define REB 1

__device__ __forceinline__ float rbf(float f) { return (float)(__bf16)f; }
__device__ __forceinline__ u32 pack2(float a, float b) { bf16x2 v = {(__bf16)a, (__bf16)b}; return __builtin_bit_cast(u32, v); }
__device__ __forceinline__ float blo(u32 v) { return __builtin_bit_cast(float, v << 16); }
__device__ __forceinline__ float bhi(u32 v) { return __builtin_bit_cast(float, v & 0xffff0000u); }
__device__ __forceinline__ u32 canon(u32 v) { return v < 0xfffffffeu ? v : 0xfffffffeu; }
__device__ __forceinline__ u32x4 tag8(f32x4 v, u32 tagv) {
  const u32x4 r = __builtin_bit_cast(u32x4, v);
  return u32x4{__builtin_amdgcn_perm(r[0], tagv, 0x07060500u), __builtin_amdgcn_perm(r[1], tagv, 0x07060501u), __builtin_amdgcn_perm(r[2], tagv, 0x07060502u),
               __builtin_amdgcn_perm(r[3], tagv, 0x07060503u)};
}
__device__ __forceinline__ u32x4 bytes4(u32 tag) { return u32x4{tag & 255u, (tag >> 8) & 255u, (tag >> 16) & 255u, tag >> 24}; }
__device__ __forceinline__ u32 or4(u32x4 v) { return (v[0] | v[1]) | (v[2] | v[3]); }
__device__ __forceinline__ float rcp(float x) { return __builtin_amdgcn_rcpf(x); }
template <int CTRL> __device__ __forceinline__ float dppf(float x) {
  return __builtin_bit_cast(float, __builtin_amdgcn_mov_dpp(__builtin_bit_cast(int, x), CTRL, 0xf, 0xf, true));
}
template <int CTRL> __device__ __forceinline__ u32 dppu(u32 x) { return (u32)__builtin_amdgcn_mov_dpp((int)x, CTRL, 0xf, 0xf, true); }
constexpr int ROR8 = 0x128, ROR4 = 0x124, ROR2 = 0x122, ROR1 = 0x121, XOR1 = 0xB1, XOR2 = 0x4E, XOR7 = 0x141;
__device__ __forceinline__ float row_sum(float x) { x += dppf<ROR8>(x); x += dppf<ROR4>(x); x += dppf<ROR2>(x); x += dppf<ROR1>(x); return x; }
__device__ __forceinline__ float row_max(float x) {
  x = fmaxf(x, dppf<ROR8>(x)); x = fmaxf(x, dppf<ROR4>(x)); x = fmaxf(x, dppf<ROR2>(x)); x = fmaxf(x, dppf<ROR1>(x)); return x;
}
__device__ __forceinline__ float x16_sum(float x) {
  auto s = __builtin_amdgcn_permlane16_swap(__float_as_uint(x), __float_as_uint(x), false, false);
  return __uint_as_float(s[0]) + __uint_as_float(s[1]);
}
__device__ __forceinline__ float x32_sum(float x) {
  auto s = __builtin_amdgcn_permlane32_swap(__float_as_uint(x), __float_as_uint(x), false, false);
  return __uint_as_float(s[0]) + __uint_as_float(s[1]);
}
__device__ __forceinline__ float x16_max(float x) {
  auto s = __builtin_amdgcn_permlane16_swap(__float_as_uint(x), __float_as_uint(x), false, false);
  return fmaxf(__uint_as_float(s[0]), __uint_as_float(s[1]));
}
__device__ __forceinline__ float x32_max(float x) {
  auto s = __builtin_amdgcn_permlane32_swap(__float_as_uint(x), __float_as_uint(x), false, false);
  return fmaxf(__uint_as_float(s[0]), __uint_as_float(s[1]));
}
__device__ __forceinline__ float t16(float a, float b) {
  auto s = __builtin_amdgcn_permlane16_swap(__float_as_uint(a), __float_as_uint(b), false, false);
  return __uint_as_float(s[0]) + __uint_as_float(s[1]);
}
__device__ __forceinline__ float t32(float a, float b) {
  auto s = __builtin_amdgcn_permlane32_swap(__float_as_uint(a), __float_as_uint(b), false, false);
  return __uint_as_float(s[0]) + __uint_as_float(s[1]);
}
__device__ __forceinline__ f32x2 up2(u32 v) { return f32x2{blo(v), bhi(v)}; }
__device__ __forceinline__ u32 pk2(f32x2 v) { return pack2(v[0], v[1]); }
#define PIN(x) asm volatile("" : "+v"(x))
__device__ __forceinline__ f32x2 rb2(f32x2 v) { u32 p = pk2(v); asm("" : "+v"(p)); return up2(p); }
__device__ __forceinline__ float wave_sum(float x) { return x32_sum(x16_sum(row_sum(x))); }
__device__ __forceinline__ float wave_max(float x) { return x32_max(x16_max(row_max(x))); }
__device__ __forceinline__ float dot2(u32 a, u32 b, float c) {
  return __builtin_amdgcn_fdot2_f32_bf16(__builtin_bit_cast(bf16x2, a), __builtin_bit_cast(bf16x2, b), c, false);
}
__device__ __forceinline__ f32x4 mfma(u32x4 a, u32x4 b, f32x4 c) {
  return __builtin_amdgcn_mfma_f32_16x16x32_bf16(__builtin_bit_cast(bf16x8, a), __builtin_bit_cast(bf16x8, b), c, 0, 0, 0);
}
__device__ __forceinline__ u32x4 ldnt(const void* p) { return __builtin_nontemporal_load((const u32x4*)p); }
__device__ __forceinline__ void glds(const void* g, u32 lds) {
  u32 keep;
  lds = __builtin_amdgcn_readfirstlane(lds);
  asm volatile("s_mov_b32 %0, m0\n\ts_mov_b32 m0, %2\n\ts_nop 0\n\tglobal_load_lds_dwordx4 %1, off nt\n\ts_mov_b32 m0, %0"
               : "=&s"(keep) : "v"(g), "s"(lds) : "memory");
}
__device__ __forceinline__ void glds(const void* base, u32 off, u32 lds) {
  u32 keep;
  lds = __builtin_amdgcn_readfirstlane(lds);
  asm volatile("s_mov_b32 %0, m0\n\ts_mov_b32 m0, %3\n\ts_nop 0\n\tglobal_load_lds_dwordx4 %1, %2 nt\n\ts_mov_b32 m0, %0"
               : "=&s"(keep) : "v"(off), "s"(base), "s"(lds) : "memory");
}
__device__ __forceinline__ void touch(const void* base, u32 off, u32 lds) {
  u32 keep;
  lds = __builtin_amdgcn_readfirstlane(lds);
  asm volatile("s_mov_b32 %0, m0\n\ts_mov_b32 m0, %3\n\ts_nop 0\n\tglobal_load_lds_dwordx4 %1, %2\n\ts_mov_b32 m0, %0"
               : "=&s"(keep) : "v"(off), "s"(base), "s"(lds) : "memory");
}
__device__ __forceinline__ u32x4 trfrag(LAS unsigned char* p0, LAS unsigned char* p1) {
  s16x4 a = __builtin_amdgcn_ds_read_tr16_b64_v4i16((LAS s16x4*)p0);
  s16x4 b = __builtin_amdgcn_ds_read_tr16_b64_v4i16((LAS s16x4*)p1);
  u32x2 x = __builtin_bit_cast(u32x2, a), y = __builtin_bit_cast(u32x2, b);
  return u32x4{x[0], x[1], y[0], y[1]};
}
#define SB() __builtin_amdgcn_sched_barrier(0)
#define MEMB() asm volatile("" ::: "memory")
#define VM0() asm volatile("s_waitcnt vmcnt(0)" ::: "memory")
#define LGKM0() asm volatile("s_waitcnt lgkmcnt(0)" ::: "memory")
#define BAR() asm volatile("s_waitcnt lgkmcnt(0)\n\ts_barrier" ::: "memory")
#define TAG() do { u32 ep_; asm volatile("s_load_dword %0, %1, 0x0\n\ts_waitcnt lgkmcnt(0)" : "=s"(ep_) : "s"(ws) : "memory"); \
    tag = ((ep_ & 0x7f7f7f7fu) + 0x01010101u) ^ (ep_ & 0x80808080u); } while (0)
constexpr int SC1 = 16;
#define OAUX 0
#define LD1 18
#define LDA 16
__device__ __forceinline__ u32x4 ld_sc1(__amdgpu_buffer_rsrc_t rs, int off) { return __builtin_amdgcn_raw_buffer_load_b128(rs, off, 0, LDA); }
__device__ __forceinline__ u32x4 ld_one(__amdgpu_buffer_rsrc_t rs, int off) { return __builtin_amdgcn_raw_buffer_load_b128(rs, off, 0, LD1); }
__device__ __forceinline__ u32x2 ld_sc1_64(__amdgpu_buffer_rsrc_t rs, int off) { return __builtin_amdgcn_raw_buffer_load_b64(rs, off, 0, LDA); }
__device__ __forceinline__ void st_sc1(__amdgpu_buffer_rsrc_t rs, int off, u32x4 v) { __builtin_amdgcn_raw_buffer_store_b128(v, rs, off, 0, SC1); }
__device__ __forceinline__ void st_sc1_64(__amdgpu_buffer_rsrc_t rs, int off, u32x2 v) { __builtin_amdgcn_raw_buffer_store_b64(v, rs, off, 0, SC1); }

__device__ __forceinline__ void ln_stats(u32x4 xv, float& m, float& inv) {
  const float x[8] = {blo(xv[0]), bhi(xv[0]), blo(xv[1]), bhi(xv[1]), blo(xv[2]), bhi(xv[2]), blo(xv[3]), bhi(xv[3])};
  const float x0 = __builtin_bit_cast(float, __builtin_amdgcn_readfirstlane(xv[0] << 16));
  float s = 0.f, v = 0.f;
#pragma unroll
  for (int j = 0; j < 8; ++j) { const float d = x[j] - x0; s += d; v = fmaf(d, d, v); }
  const float md = wave_sum(s) * (1.f / 512.f), sq = wave_sum(v) * (1.f / 512.f);
  const float var = rbf(fmaxf(fmaf(-md, md, sq), 0.f));
  m = rbf(x0 + md);
  inv = rcp(rbf(__builtin_amdgcn_sqrtf(rbf(var + 1e-5f))));
}
#define PACE_(W, INC) do { asm volatile(W "\n1:\n\ts_memrealtime vcc\n\ts_waitcnt lgkmcnt(0)\n\ts_lshl_b32 vcc_lo, vcc_lo, 2\n\ts_sub_i32 vcc_hi, vcc_lo, %0\n\ts_cmp_lt_i32 vcc_hi, 0\n\t" \
    "s_cbranch_scc0 2f\n\ts_sleep 1\n\ts_branch 1b\n2:\n\ts_add_i32 %0, %0, %1" : "+s"(due) : "is"(INC) : "memory", "scc", "vcc"); SB(); } while (0)
#define LT(K, T)
#define PACE(INC) PACE_("s_waitcnt vmcnt(2)", INC)
#define PACEN(INC) PACE_("", INC)
__device__ __forceinline__ void until_s(u32 due) { PACE_("", 0); }
__device__ __forceinline__ void until(u32 t) {
  asm volatile("1:\n\ts_memrealtime vcc\n\ts_waitcnt lgkmcnt(0)\n\ts_lshl_b32 vcc_lo, vcc_lo, 2\n\ts_sub_i32 vcc_hi, vcc_lo, %0\n\ts_cmp_lt_i32 vcc_hi, 0\n\t"
               "s_cbranch_scc1 1b" :: "s"(t) : "memory", "scc", "vcc");
  SB();
}

#define STAMP(i) do { } while (0)

#define WSTAMP(k) do { } while (0)
__device__ __forceinline__ void lds_wait(volatile LAS u32* p, u32 want) {
  _Pragma("unroll 1") for (u32 s = 0; s < LSPIN_CAP; ++s) {
    if (__builtin_amdgcn_readfirstlane(*p) >= want) break;
    __builtin_amdgcn_s_sleep(2);
  }
  MEMB();
}
__device__ __forceinline__ void lds_add(LAS u32* p, int lane) {
  LGKM0();
  if (lane == 0) __hip_atomic_fetch_add(p, 1u, __ATOMIC_RELAXED, __HIP_MEMORY_SCOPE_WORKGROUP);
  MEMB();
}
__device__ __forceinline__ u32 lds_arrive(LAS u32* p, int lane) {
  LGKM0();
  u32 old = 0;
  if (lane == 0) old = __hip_atomic_fetch_add(p, 1u, __ATOMIC_RELAXED, __HIP_MEMORY_SCOPE_WORKGROUP);
  old = __builtin_amdgcn_readfirstlane(old);
  MEMB();
  return old;
}

__device__ __forceinline__ void tile_part(LAS unsigned char* T, LAS unsigned char* P, u32x4 hb, int n, int g) {
  LAS unsigned char* tb = T + (8 * g + (n >> 2)) * 128 + (n & 3) * 8;
  const int sx = (n >> 3) | ((g & 1) << 1);
  f32x4 a[4];
#pragma unroll
  for (int t = 0; t < 4; ++t) a[t] = mfma(trfrag(tb + (t ^ sx) * 32, tb + (t ^ sx) * 32 + 512), hb, f32x4{0.f, 0.f, 0.f, 0.f});
  *(LAS f32x4*)(P) = n < 8 ? a[0] : a[1];
  *(LAS f32x4*)(P + 1024) = n < 8 ? a[2] : a[3];
}
__device__ __forceinline__ f32x4 tile_sum(LAS unsigned char* P) {
  f32x4 s[8], r[2];
#pragma unroll
  for (int k = 0; k < 2; ++k) {
#pragma unroll
    for (int i = 0; i < 8; ++i) s[i] = *(LAS f32x4*)(P + (8 * k + i) * 2048);
    SB();
    r[k] = ((s[0] + s[1]) + (s[2] + s[3])) + ((s[4] + s[5]) + (s[6] + s[7]));
    SB();
  }
  return r[0] + r[1];
}
__device__ __forceinline__ int mpos(int c) { return (c & 252) * 4 + (c >> 8) * 8 + (c & 3) * 2; }
__device__ __forceinline__ u32 smax(int m) { int r = (int)wave_max((float)m); PIN(r); return (u32)__builtin_amdgcn_readfirstlane(r); }
__device__ __forceinline__ u32 now4() { return (u32)__builtin_amdgcn_s_memrealtime() * 4u; }
#define B0_LOAD() { bp = ld_sc1(rs, WS_B0 + bo); bq = ld_sc1(rs, WS_B0 + 1024 + bo); }
__device__ __forceinline__ u32 anchor(__amdgpu_buffer_rsrc_t rs, int bo, u32 tag, u32 org, u32x4 bp, u32x4 bq) {
  {
    const u32 lim = org + OFO - DO;
    if (__all((((bp[1] ^ tag) | (bp[3] ^ tag) | (bq[1] ^ tag) | (bq[3] ^ tag)) | (((lim - bp[0]) | (lim - bp[2]) | (lim - bq[0]) | (lim - bq[2])) >> 31)) == 0u)) return org + OFO;
  }
  int lk = OFB;
#pragma unroll 1
  for (u32 s = 0; s < SPIN_CAP; ++s) {
    if (__all(((bp[1] ^ tag) | (bp[3] ^ tag) | (bq[1] ^ tag) | (bq[3] ^ tag)) == 0u)) break;
    MEMB();
    do lk += PB; while ((int)(now4() - org) > lk);
    until_s(org + lk);
    B0_LOAD()
  }
  const int m = (int)smax(max(max((int)(bp[0] - org), (int)(bp[2] - org)), max((int)(bq[0] - org), (int)(bq[2] - org))));
  return org + (u32)max(m + DO <= OFO ? OFO : m + DL_, lk + RB - DP);
}
__device__ __forceinline__ float asf(u32 v) { return __builtin_bit_cast(float, v); }

struct Args {
  const unsigned char* half[4][2];
  const u16 *x, *kc, *vc, *wd, *cc;
  u16* out;
  unsigned char* ws;
};

__global__ void __launch_bounds__(1024) fused(const Args a) {
  extern __shared__ __attribute__((aligned(16))) unsigned char smem[];
  LAS unsigned char* L = (LAS unsigned char*)smem;
  const u32 lbase = (u32)(size_t)L;
  const int tid = threadIdx.x, w = __builtin_amdgcn_readfirstlane(tid >> 6), bid = blockIdx.x;
  int lane = tid & 63;
  const int xcd = bid & 7, idx = bid >> 3, hj = idx & 3, h = 4 * xcd + hj, b = idx >> 2, bh = b * 32 + h;
  u64 hA, hB, t64;
  u32 warm;
  asm volatile("s_load_dwordx2 %0, %4, %5\n\ts_load_dwordx2 %1, %4, %6\n\ts_load_dword %3, %4, 0x40\n\ts_memrealtime %2" : "=&s"(hA), "=&s"(hB), "=&s"(t64), "=&s"(warm)
               : "s"(__builtin_amdgcn_kernarg_segment_ptr()), "s"(16 * hj), "s"(16 * hj + 8));
  SB();
  const int bb = (b + xcd) & 7;
  int p3r = 5 * xcd + min(xcd, 3) + bb;
  int mc = (b & 6) + ((xcd & 1) ^ 1), mg = ((xcd & 6) + b) & 7;
  const bool tq = hj == 0, to = hj == 2;
  int lim = hj == 1 ? 6 - b : (xcd < 3 ? 6 : 5) - bb;
  asm volatile("" : "+s"(p3r), "+s"(mc), "+s"(mg), "+s"(lim));
  const bool idle = (hj & 1) && lim <= 0;
  u64 tp0, tp1;
  {
    const u32 pitch = (u32)(0x0AC0040018002000ull >> (16 * hj)) & 0xffffu;
    u32 ce = (xcd << (to ? 13 : 4)) + 2 * b, c1 = 16 * mc + 2 * mg;
    asm volatile("" : "+s"(ce), "+s"(c1));
    const u32 col = 64 * ((hj & 1) ? (hj == 1 ? c1 : p3r) : ce);
    const u32 step = pitch << (to ? 1 : 3);
    u32 off = (32 * w + (to ? (lane & 24) + (lane >> 5) : lane >> 3)) * pitch + col + ((lane & 3) ^ ((lane >> 3) & 2)) * 16;
    tp0 = off;
    tp1 = off + step;
    u64 s2 = 2 * step;
    u32 ld = lbase + L_W + w * 4096;
    asm volatile("" : "+s"(s2), "+s"(ld), "+v"(tp0), "+v"(tp1));
    asm volatile("s_mov_b32 exec_lo, 0xf0f0f0f0\n\ts_mov_b32 exec_hi, 0xf0f0f0f0\n\ts_waitcnt lgkmcnt(0)\n\tv_lshl_add_u64 %0, %0, 0, %3\n\tv_lshl_add_u64 %1, %1, 0, %2\n\t"
                 "s_not_b64 exec, exec\n\tv_lshl_add_u64 %0, %0, 0, %2\n\tv_lshl_add_u64 %1, %1, 0, %3\n\ts_mov_b64 exec, -1"
                 : "+v"(tp0), "+v"(tp1), "+s"(hA), "+s"(hB), "+s"(t64), "+s"(warm) :: "scc");
    if (!idle) {
      u32 keep;
      asm volatile("s_mov_b32 %0, m0\n\ts_mov_b32 m0, %3\n\ts_nop 0\n\tglobal_load_lds_dwordx4 %1, off nt\n\ts_add_u32 m0, m0, 0x400\n\t"
                   "v_lshl_add_u64 %1, %1, 0, %4\n\tglobal_load_lds_dwordx4 %2, off nt\n\ts_add_u32 m0, m0, 0x400\n\t"
                   "v_lshl_add_u64 %2, %2, 0, %4\n\tglobal_load_lds_dwordx4 %1, off nt\n\ts_mov_b32 m0, %0"
                   : "=&s"(keep), "+v"(tp0), "+v"(tp1) : "s"(ld), "s"(s2) : "memory", "scc");
      if (hj != 3) glds((const void*)tp1, lbase + L_W + w * 4096 + 3072);
    }
  }
  SB();
  u32 t0 = (u32)t64;
  const u16 *x = a.x, *kc = a.kc, *vc = a.vc, *wd = a.wd, *cc = a.cc;
  u16* out = a.out;
  unsigned char* ws = a.ws;
  if (w == 0) STAMP(24);
  t0 += (u32)(KW * max((int)((u32)__builtin_amdgcn_s_memrealtime() - t0) - KA, 0)) / 4u;
  const int p3 = (hj == 3 && !idle) ? p3r : -1;
  const bool tm = hj == 1 && !idle;
  const __amdgpu_buffer_rsrc_t rs = __builtin_amdgcn_make_buffer_rsrc((void*)ws, 0, WS_END, 0x00020000);
  u32 tag;
  TAG();
  if (idle && hj == 1) {
    if (w == 0) {
      st_sc1(rs, WS_G1 + ((b & 1) * 8 + xcd) * 1024 + lane * 16, u32x4{~0u, ~0u, ~0u, ~0u});
      VM0();
      if (lane == 0) st_sc1_64(rs, WS_TS + ((b & 1) * 8 + xcd) * 8, u32x2{t0, tag});
    } else if ((b & 1) * 15 + w <= CODEKB) {
      u64 pc;
      asm volatile("s_getpc_b64 %0" : "=s"(pc));
      touch((const void*)(pc & ~255ull), ((b & 1) * 15 + w - 1) * 1024 + lane * 16, lbase + L_SINK);
    }
  }
  int ta = TAU, tb = TAU - DB;
  int n = lane & 15, g = lane >> 4;
  u32x4 cv, hb;
  const bool row = tm && (w >= 8 || mc == 1);
  if (row) cv = ldnt((w < 8 ? x : cc) + (w & 7) * 512 + lane * 8);
  if (tid < 16) ((volatile LAS u32*)(L + L_CTL))[tid] = tid == C_T0 ? t0 : 0u;
  BAR();
  t0 = __builtin_amdgcn_readfirstlane(((volatile LAS u32*)(L + L_CTL))[C_T0]);
  u32 due = t0 * 4u + (u32)(PHASE * TAU) / 32u + DUE0;
  int rot = ROTF;
#define RO(I) (((XR ? (I) ^ rot : (I) + rot) & 15) * 8192)
  const u16* kp = kc + ((size_t)bh * SK + (SK - WIN) + 4 * w) * 128;
  u32 l16 = lane * 16;
  PIN(l16);
#define LDR(D, P, I, W) { const GAS unsigned char* p_ = (const GAS unsigned char*)((P) + RO(I)); asm volatile("" : "+s"(p_)); W; D = __builtin_nontemporal_load((const GAS u32x4*)(p_ + l16)); SB(); }
  u32x4 kr[16], vr[16];
  volatile LAS u32* ctl = (volatile LAS u32*)(L + L_CTL);
#define RELANE() do { asm volatile("" : "+v"(lane)); n = lane & 15; g = lane >> 4; } while (0)
  const int e = b * 16 + 2 * xcd + hj;
  const bool red = hj < 2;
#define KL(I, T) { LDR(kr[I], kp, I, PACE(T)) LT(I, T) }
  KL(0, TAU + TX)
  if (row) {
    if (w >= 8) {
#pragma unroll
      for (int j = 0; j < 4; ++j) { const float a0 = blo(cv[j]), a1 = bhi(cv[j]); hb[j] = pack2(a0 * rcp(1.f + __expf(-a0)), a1 * rcp(1.f + __expf(-a1))); }
      *(LAS u32x4*)(L + L_H2 + (w & 7) * H2S + lane * 16) = hb;
      lds_add((LAS u32*)(L + L_CTL) + C_R, lane);
    } else {
      float m, inv;
      ln_stats(cv, m, inv);
      if ((lane >> 3) == mg) {
        LAS f32x4* d = (LAS f32x4*)(L + L_RED + w * 256 + (lane & 7) * 32);
        d[0] = f32x4{rbf(rbf(blo(cv[0]) - m) * inv), rbf(rbf(bhi(cv[0]) - m) * inv), rbf(rbf(blo(cv[1]) - m) * inv), rbf(rbf(bhi(cv[1]) - m) * inv)};
        d[1] = f32x4{rbf(rbf(blo(cv[2]) - m) * inv), rbf(rbf(bhi(cv[2]) - m) * inv), rbf(rbf(blo(cv[3]) - m) * inv), rbf(rbf(bhi(cv[3]) - m) * inv)};
      }
    }
  }
  KL(1, TAU + TX)
  u32x2 tsv;
#define TS_WAIT() if (!__all(tsv[1] == tag)) { _Pragma("unroll 1") for (u32 s = 0; s < SPIN_CAP; ++s) { __builtin_amdgcn_s_sleep(16); MEMB(); \
      tsv = ld_sc1_64(rs, WS_TS + (lane & 15) * 8); if (__all(tsv[1] == tag)) break; } } MEMB();
  if (tm) {
    asm volatile("s_waitcnt vmcnt(2)" ::: "memory");
    lds_wait(ctl + C_R, 8u);
    hb = *(LAS u32x4*)(L + L_H2 + (n & 7) * H2S + (32 * w + 8 * g) * 2);
    tile_part(L + L_W + w * 4096, L + L_TP + w * 2048 + lane * 16, hb, n, g);
    lds_add((LAS u32*)(L + L_CTL) + C_T, lane);
  }
  KL(2, TAU + TX)
  if (w == 0 || (tm && w >= 14)) tsv = ld_sc1_64(rs, WS_TS + (lane & 15) * 8);
  SB();
  if (tm && w >= 14) {
    lds_wait(ctl + C_T, 16u);
    const int col = 32 * (w & 1) + 16 * (n >> 3) + 4 * g;
    const f32x4 s = tile_sum(L + L_TP + (w & 1) * 1024 + lane * 16);
    float v[4] = {rbf(s[0]), rbf(s[1]), rbf(s[2]), rbf(s[3])};
    if (mc == 1) {
      const f32x4 ln = *(LAS f32x4*)(L + L_RED + (n & 7) * 256 + col * 4);
#pragma unroll
      for (int j = 0; j < 4; ++j) v[j] = ln[j] * rbf(1.f + v[j]);
    }
    TS_WAIT()
    st_sc1_64(rs, mc < 2 ? WS_G1 + (2 * mg + (w & 1)) * 1024 + ((2 * (n >> 3) + (g >> 1)) * 16 + mc * 8 + (n & 7)) * 16 + (g & 1) * 8
                         : WS_MODR + ((mc - 2) * 8 + (n & 7)) * 1024 + mpos(64 * mg + col),
              u32x2{canon(pack2(v[0], v[1])), canon(pack2(v[2], v[3]))});
    if (w == 15) STAMP(3);
  }
  RELANE();
  KL(3, TAU + TX) KL(4, ta)
  if (w == 0) {
    TS_WAIT()
    const float d = (float)(int)(tsv[0] - t0);
    ctl[C_TB] = (t0 * 4u + (u32)(int)fmaxf(row_sum(d) * 0.25f, row_max(d) * 4.f - SKW)) | 1u;
  }
  KL(5, ta) KL(6, ta)
  u32 org = __builtin_amdgcn_readfirstlane(ctl[C_TB]);
  if (org && REB) { const int adj = ((int)(org - t0 * 4u) * 85 + 1024) >> 11; ta += adj; tb += adj; }
  KL(7, ta)
  if (tm && mc >= 2 && w >= 14 && lane == 0) __builtin_amdgcn_raw_buffer_store_b32(tag, rs, WS_FLAGS + (((mc - 2) * 8 + mg) * 2 + (w & 1)) * 4, 0, SC1);
  KL(8, ta)
  u32x4 g0;
  const int o0 = WS_G1 + w * 1024 + lane * 16;
  if (tq) lds_wait(ctl + C_TB, 1u);
#define KG(K) { KL(K, ta) if (tq && (w & 1) == ((K) & 1)) { until_s(due - TAU / 2); \
    asm volatile("buffer_load_dwordx4 %0, %1, %2, 0 offen sc1" : "=&v"(g0) : "v"(o0), "s"(rs) : "memory"); } }
  KG(9) KG(10) KL(11, ta) KL(12, ta)
  if (tq) {
    asm volatile("s_waitcnt vmcnt(2)" : "+v"(g0));
#define G1_OK() __all(g0[0] != ~0u && g0[1] != ~0u && g0[2] != ~0u && g0[3] != ~0u)
    if (!G1_OK()) {
#pragma unroll 1
      for (u32 s = 0; s < SPIN_CAP; ++s) {
        __builtin_amdgcn_s_sleep(16);
        MEMB();
        g0 = ld_sc1(rs, o0);
        if (G1_OK()) break;
      }
    }
    if (w == 15) STAMP(4);
#pragma unroll
    for (int j = 0; j < 4; ++j) { const u32 o = dppu<ROR8>(g0[j]); hb[j] = pack2(blo(g0[j]) + blo(o), bhi(g0[j]) + bhi(o)); }
    PIN(hb);
  }
  KL(13, ta)
  if (tq) {
    tile_part(L + L_W + w * 4096, L + L_TP + w * 2048 + lane * 16, hb, n, g);
    lds_add((LAS u32*)(L + L_CTL) + C_T, lane);
  }
  KL(14, ta)
  if (tq && w >= 14) {
    lds_wait(ctl + C_T, 16u);
    const f32x4 s = tile_sum(L + L_TP + (w & 1) * 1024 + lane * 16);
    st_sc1(rs, WS_Q + ((n & 7) * 32 + 4 * xcd + (b >> 1)) * 512 + ((b & 1) * 64 + 32 * (w & 1) + 16 * (n >> 3) + 4 * g) * 4, u32x4{pack2(s[0], s[1]), tag, pack2(s[2], s[3]), tag});
    if (w == 15) STAMP(17);
  }
  RELANE();
  KL(15, ta)
  asm volatile("" : "+s"(rot));
  const u16* vp = vc + ((size_t)bh * SK + (SK - WIN) + 4 * w) * 128;
#define VL(I, T) { LDR(vr[I], vp, I, PACE(T)) LT(16 + (I), T) }
#define VLN(I, T) { LDR(vr[I], vp, I, PACEN(T)) LT(16 + (I), T) }
  VL(0, ta) VL(1, ta)
  VL(2, tb)
  u32x2 gq;
  if (w == WG_) gq = ld_sc1_64(rs, WS_Q + bh * 512 + lane * 8);
  SB();
  VLN(3, tb) VLN(4, tb)
  if (w == WG_) {
    if (!__all(gq[1] == tag)) {
#pragma unroll 1
      for (u32 s = 0; s < SPIN_CAP; ++s) {
        __builtin_amdgcn_s_sleep(4);
        MEMB();
        gq = ld_sc1_64(rs, WS_Q + bh * 512 + lane * 8);
        if (__all(gq[1] == tag)) break;
      }
    }
    *(LAS u32*)(L + L_Q + lane * 4) = gq[0];
    LGKM0();
    ctl[C_Q] = 1u;
    STAMP(5);
  }
  RELANE();
  VL(5, tb)
  lds_wait(ctl + C_Q, 1u);
  const u32x4 qf = *(LAS u32x4*)(L + L_Q + n * 16);
  if (w == 1) STAMP(6);

#define DOT(I) dot2(kr[I][3], qf[3], dot2(kr[I][2], qf[2], dot2(kr[I][1], qf[1], dot2(kr[I][0], qf[0], 0.f))))
#define TR(LO, HI, CTRL, BIT) ((n & (BIT)) ? (HI) + dppf<CTRL>(HI) : (LO) + dppf<CTRL>(LO))
  float xs[8];
#define QK(C) { _Pragma("unroll") for (int i = 2 * (C); i < 2 * (C) + 2; ++i) { const float lo_ = DOT(i), hi_ = DOT(i + 8); \
      const float t1_ = lo_ + dppf<ROR8>(lo_), t2_ = hi_ + dppf<ROR8>(hi_); xs[i] = (n & 8) ? t2_ : t1_; PIN(xs[i]); } SB(); }
  QK(0) VL(6, tb) QK(1) VL(7, tb) QK(2) VL(8, tb) QK(3) VL(9, tb)
  const bool prer = p3 >= 0 && w < 8;
  u32 fl;
  if (prer) fl = __builtin_amdgcn_raw_buffer_load_b32(rs, WS_FLAGS + lane * 4, 0, SC1);
#define TR_STEP(HALF, CTRL, BIT) _Pragma("unroll") for (int hh = 0; hh < (HALF); ++hh) { \
    const float lo_ = xs[hh], hi_ = xs[hh + (HALF)]; const float t1_ = lo_ + dppf<CTRL>(lo_), t2_ = hi_ + dppf<CTRL>(hi_); xs[hh] = (n & (BIT)) ? t2_ : t1_; }
  TR_STEP(4, XOR7, 4) TR_STEP(2, XOR2, 2) TR_STEP(1, XOR1, 1)
#undef TR_STEP
  float sc = rbf(rbf(xs[0]) * 0.08838834764831845f);
  PIN(sc);
  SB();
  VLN(10, tb)
  {
    const float mw_ = wave_max(sc);
    const float lw = wave_sum(__expf(sc - mw_));
    if (lane == 0) *(LAS f32x2*)(L + L_ML + w * 8) = f32x2{mw_, lw};
    lds_add((LAS u32*)(L + L_CTL) + C_ML, lane);
  }
  SB();
  VL(11, tb)
  RELANE();
  u32x4 pre0, pre1;
#define FL_WAIT() if (!__all(fl == tag)) {   _Pragma("unroll 1") for (u32 s = 0; s < SPIN_CAP; ++s) { __builtin_amdgcn_s_sleep(4); MEMB(); \
      fl = __builtin_amdgcn_raw_buffer_load_b32(rs, WS_FLAGS + lane * 4, 0, SC1); if (__all(fl == tag)) break; } } MEMB();
  if (prer) {
    PIN(fl);
    FL_WAIT()
    pre0 = ld_sc1(rs, WS_MODR + (8 + w) * 1024 + lane * 16);
    pre1 = ld_sc1(rs, WS_MODR + (16 + w) * 1024 + lane * 16);
  }
  SB();
  u32 mk[4];
#pragma unroll
  for (int j = 0; j < 4; ++j) mk[j] = (n >> 1) == j ? ((n & 1) ? 0xffff0000u : 0x0000ffffu) : 0u;
  f32x4 oa = {0.f, 0.f, 0.f, 0.f};
#define PV(I) { const u32 pb_ = dppu<0x150 + (I)>(pd); oa = mfma(vr[I], u32x4{pb_ & mk[0], pb_ & mk[1], pb_ & mk[2], pb_ & mk[3]}, oa); asm volatile("" : "+v"(oa)); SB(); }
  PIN(mk[0]); PIN(mk[1]); PIN(mk[2]); PIN(mk[3]);
  u32 a_oa = L_RED + w * OSTR + (g * 8 + n) * 16;
  u32 ra = L_RED + (lane >> 5) * 8 * OSTR + (lane & 31) * 16;
  u32 rb_ = WS_O + (h * 8 + b) * 512 + (lane & 31) * 16 + (lane >> 5) * 8;
  u32 tagv = tag;
  const __amdgpu_buffer_rsrc_t rd = __builtin_amdgcn_make_buffer_rsrc((void*)wd, 0, FF * 1024, 0x00020000);
  int wdo = p3 >= 0 ? (32 * p3 + 2 * w) * 1024 + lane * 16 : FF * 1024;
  PIN(a_oa); PIN(ra); PIN(rb_); PIN(tagv); PIN(wdo);
  org = __builtin_amdgcn_readfirstlane(ctl[C_TB]);
  org = org ? org + TEND : t0 * 4u;
  asm volatile("" : "+s"(org));
  VLN(12, tb)
  lds_wait(ctl + C_ML, 16u);
  u32 pd;
  {
    const f32x2 ml = *(LAS f32x2*)(L + L_ML + n * 8);
    const float M = row_max(ml[0]);
    const float pe = __expf(sc - M) * rcp(row_sum(__expf(ml[0] - M) * ml[1]));
    pd = pack2(pe, pe);
    PIN(pd);
  }
  SB();
  VLN(13, tb)
  PV(0) PV(1) PV(2) PV(3) PV(4)
  VL(14, tb)
  PV(5) PV(6) PV(7) PV(8) PV(9)
  VL(15, tb)
  PV(10) PV(11) PV(12) PV(13) PV(14)
  if (w == 13) __builtin_amdgcn_s_setprio(3);
  u32x4 wd0, wd1;
  if (WDE > 0) wd0 = __builtin_amdgcn_raw_buffer_load_b128(rd, wdo, 0, 2);
  if (WDE > 1) wd1 = __builtin_amdgcn_raw_buffer_load_b128(rd, wdo + 1024, 0, 2);
  SB();
  PV(15)
#undef PV
  if (n < 8) *(LAS f32x4*)(L + a_oa) = oa;
  BAR();
  if (w == 0) STAMP(8);
  if (w == 13) {
    f32x4 s[8];
#pragma unroll
    for (int ww = 0; ww < 8; ++ww) s[ww] = *(LAS f32x4*)(L + ra + ww * OSTR);
    SB();
    const f32x4 t = ((s[0] + s[1]) + (s[2] + s[3])) + ((s[4] + s[5]) + (s[6] + s[7]));
    st_sc1_64(rs, rb_, u32x2{pack2(t32(t[0], t[2]), t32(t[1], t[3])), tagv});
    SB();
    if (lane < (B0C ? 8 : 1)) st_sc1_64(rs, WS_B0 + lane * 2048 + bid * 8, u32x2{now4(), tagv});
    STAMP(9);
  }
  if (to) {
    u32x4 wf[16];
    if (w < 4) {
      const u32 af = L_W + (8 * g + (n >> 2)) * 128 + (w ^ ((n >> 3) | ((g & 1) << 1))) * 32 + (n & 3) * 8;
#pragma unroll
      for (int ks = 0; ks < 16; ++ks) wf[ks] = trfrag(L + af + ks * 4096, L + af + ks * 4096 + 512);
      __builtin_amdgcn_s_setprio(3);
    }
    int oo = WS_O + xcd * 16384 + w * 1024 + lane * 16;
    u32 a_ow = L_TP + (2 * w + (lane >> 5)) * OBS + (lane & 31) * 8;
    u32 a_or = L_TP + ((n >> 3) * 16 + (n & 7)) * OBS + g * 16;
    int rc = WS_PART + (((n & 7) * 16 + 2 * b + ((w >> 1) & 1)) * 8 + xcd) * 128 + (w & 1) * 64 + g * 16;
    PIN(oo); PIN(a_ow); PIN(a_or); PIN(rc);
    until(org + OFO);
    u32x4 og = ld_sc1(rs, oo);
    if (!__all(og[1] == tag && og[3] == tag)) {
#pragma unroll 1
      for (u32 s = 0; s < SPIN_CAP; ++s) {
        MEMB();
        og = ld_sc1(rs, oo);
        if (__all(og[1] == tag && og[3] == tag)) break;
        __builtin_amdgcn_s_sleep(2);
      }
    }
    *(LAS u32x2*)(L + a_ow) = u32x2{og[0], og[2]};
    BAR();
    if (w == 7) STAMP(2);
    if (w < 4) {
      u32x4 ob[8];
#pragma unroll
      for (int i = 0; i < 8; ++i) ob[i] = *(LAS u32x4*)(L + a_or + (i >> 2) * 8 * OBS + (i & 3) * 64);
      SB();
      f32x4 aL = {0.f, 0.f, 0.f, 0.f}, aH = {0.f, 0.f, 0.f, 0.f};
#pragma unroll
      for (int i = 0; i < 8; ++i) { aL = mfma(wf[i], ob[i], aL); aH = mfma(wf[8 + i], ob[i], aH); }
      const f32x4 v = {aL[0] + dppf<ROR8>(aH[0]), aL[1] + dppf<ROR8>(aH[1]), aL[2] + dppf<ROR8>(aH[2]), aL[3] + dppf<ROR8>(aH[3])};
      if (n < 8) st_sc1(rs, rc, tag8(v, tagv));
      if (w == 3) STAMP(16);
    }
  }

  const int bo = (B0C ? xcd : 0) * 2048 + lane * 16;
  if (p3 >= 0) {
    const int t = w & 3, kk = w >> 2;
    BAR();
    glds((const void*)tp1, lbase + L_W + w * 4096 + 3072);
    if (WDE < 1) wd0 = __builtin_amdgcn_raw_buffer_load_b128(rd, wdo, 0, 2);
    if (WDE < 2) wd1 = __builtin_amdgcn_raw_buffer_load_b128(rd, wdo + 1024, 0, 2);
    SB();
    f32x2 c1[4], sb[4];
#pragma unroll
    for (int j = 0; j < 4; ++j) { c1[j] = rb2(up2(pre1[j]) + 1.f); sb[j] = up2(pre0[j]); PIN(c1[j]); PIN(sb[j]); }
    int xo = WS_X2 + (XCP ? xcd : 0) * 16384 + w * 2048 + lane * 16;
    u32 a_h2w = L_H2 + w * H2S + lane * 8, a_cx = lbase + L_CTL + (C_X + w) * 4, one = 1;
    PIN(a_cx); PIN(one);
    u32 a_h2r = L_H2 + (n & 7) * H2S + (128 * kk + 8 * g) * 2;
    u32 a_gw = L_RED + kk * 2048 + t * 512 + (g * 8 + n) * 16;
    u32 a_gr = L_RED + (w & 3) * 256 + lane * 4;
    u32 a_hw = L_HH + ((lane >> 2) & 7) * 64 + ((w & 3) * 2 + (lane >> 5)) * 8 + (lane & 3) * 2;
    u32 a_hr = L_HH + (n & 7) * 64 + g * 16;
    u32 a_d0 = L_W + 65536 + (8 * g + (n >> 2)) * 1024 + ((4 * w + ((n >> 1) & 1)) ^ (((n >> 2) | ((g & 1) << 2)) << 1)) * 16 + (n & 1) * 8;
    int yo = WS_YP + ((8 * w + 4 * (n >> 3) + g) * NP3 + p3) * 128 + (n & 7) * 16;
    PIN(xo); PIN(a_h2w); PIN(a_h2r); PIN(a_gw); PIN(a_gr); PIN(a_hw); PIN(a_hr); PIN(a_d0); PIN(yo);
    SB();
    if (w == 1) STAMP(20);
    u32x4 bp, bq;
    if (w == 8) { until_s(org + OFB); B0_LOAD() }
    SB();
    *(LAS u32x4*)(L + L_W + 65536 + w * 2048 + (lane ^ ((((2 * w) & 3) | (w & 4)) << 1)) * 16) = wd0;
    *(LAS u32x4*)(L + L_W + 65536 + w * 2048 + 1024 + (lane ^ ((((2 * w + 1) & 3) | (w & 4)) << 1)) * 16) = wd1;
    VM0();
    if (w == 1) STAMP(23);
    if (w == 14) STAMP(21);
    BAR();
    if (w == 0) STAMP(19);
    if (w == 8) ctl[C_TX] = (anchor(rs, bo, tag, org, bp, bq) + DX) | 1u;
    u32x4 gf[4];
    {
      const int q = n >> 2, p = n & 3;
      LAS unsigned char* tb = L + L_W + (128 * kk + 8 * g + q) * 128 + (t ^ ((q >> 1) | ((g & 1) << 1))) * 32 + p * 8;
#pragma unroll
      for (int ks = 0; ks < 4; ++ks) gf[ks] = trfrag(tb + ks * 4096, tb + ks * 4096 + 512);
    }
    u32x4 df0 = trfrag(L + a_d0, L + a_d0 + 4096), df1 = trfrag(L + (a_d0 ^ 32), L + (a_d0 ^ 32) + 4096);
    PIN(df0); PIN(df1); PIN(gf[0]); PIN(gf[1]); PIN(gf[2]); PIN(gf[3]);
    if (w == 15) {
      _Pragma("unroll 1") for (u32 s = 0; s < LSPIN_CAP; ++s) { if (__all(ctl[C_X + (lane & 7)] != 0u)) break; __builtin_amdgcn_s_sleep(1); }
      if (lane < 8) st_sc1_64(rs, WS_B1 + lane * 512 + p3 * 8, u32x2{now4(), tagv});
    }
    if (w < 8) {
      u32x4 xa, xb;
      const u32x4 t16v = {tag & 0xffffu, tag >> 16, tag & 0xffffu, tag >> 16};
      lds_wait(ctl + C_TX, 1u);
      until(__builtin_amdgcn_readfirstlane(ctl[C_TX]));
      xa = ld_sc1(rs, xo) ^ t16v;
      xb = ld_sc1(rs, xo + 1024) ^ t16v;
      if (!__all(((or4(xa) | or4(xb)) & 0xffffu) == 0u)) {
#pragma unroll 1
        for (u32 s = 0; s < SPIN_CAP; ++s) {
          MEMB();
          xa = ld_sc1(rs, xo) ^ t16v;
          xb = ld_sc1(rs, xo + 1024) ^ t16v;
          if (__all(((or4(xa) | or4(xb)) & 0xffffu) == 0u)) break;
        }
      }
      asm volatile("s_mov_b64 exec, 1\n\tds_write_b32 %0, %1\n\ts_mov_b64 exec, -1" :: "v"(a_cx), "v"(one) : "memory");
      if (w == 0) STAMP(12);
      const float x0 = __builtin_bit_cast(float, __builtin_amdgcn_readfirstlane(xa[0]));
      const f32x2 xf[4] = {f32x2{asf(xa[0]), asf(xa[1])}, f32x2{asf(xa[2]), asf(xa[3])}, f32x2{asf(xb[0]), asf(xb[1])}, f32x2{asf(xb[2]), asf(xb[3])}};
      const f32x2 d0 = xf[0] - x0, d1 = xf[1] - x0, d2 = xf[2] - x0, d3 = xf[3] - x0;
      const f32x2 sv = (d0 + d1) + (d2 + d3), qv = __builtin_elementwise_fma(d0, d0, d1 * d1) + __builtin_elementwise_fma(d2, d2, d3 * d3);
      float r = t16(sv[0] + sv[1], qv[0] + qv[1]);
      r = x32_sum(row_sum(r)) * (1.f / 512.f);
      const float md = __builtin_bit_cast(float, __builtin_amdgcn_readlane(__builtin_bit_cast(int, r), 0));
      const float sq = __builtin_bit_cast(float, __builtin_amdgcn_readlane(__builtin_bit_cast(int, r), 16));
      const float inv = __builtin_amdgcn_rsqf(fmaxf(fmaf(-md, md, sq), 0.f) + 1e-5f);
      const u32x4 hv = {pk2(__builtin_elementwise_fma((d0 - md) * c1[0], f32x2{inv, inv}, sb[0])), pk2(__builtin_elementwise_fma((d1 - md) * c1[1], f32x2{inv, inv}, sb[1])),
                        pk2(__builtin_elementwise_fma((d2 - md) * c1[2], f32x2{inv, inv}, sb[2])), pk2(__builtin_elementwise_fma((d3 - md) * c1[3], f32x2{inv, inv}, sb[3]))};
      *(LAS u32x2*)(L + a_h2w) = u32x2{hv[0], hv[1]};
      *(LAS u32x2*)(L + a_h2w + 512) = u32x2{hv[2], hv[3]};
    }
    BAR();
    if (w == 0) STAMP(13);
    {
      u32x4 b0 = *(LAS u32x4*)(L + a_h2r), b1 = *(LAS u32x4*)(L + a_h2r + 64), b2 = *(LAS u32x4*)(L + a_h2r + 128), b3 = *(LAS u32x4*)(L + a_h2r + 192);
      SB();
      const f32x4 z = {0.f, 0.f, 0.f, 0.f};
      const f32x4 a = mfma(gf[1], b1, mfma(gf[0], b0, z)) + mfma(gf[3], b3, mfma(gf[2], b2, z));
      if (n < 8) *(LAS f32x4*)(L + a_gw) = a;
    }
    BAR();
    if (w < 4) {
      float gv[4], uv[4];
#pragma unroll
      for (int k2 = 0; k2 < 4; ++k2) { gv[k2] = *(LAS float*)(L + a_gr + k2 * 2048); uv[k2] = *(LAS float*)(L + a_gr + k2 * 2048 + 1024); }
      SB();
      const float gg = rbf((gv[0] + gv[1]) + (gv[2] + gv[3])), uu = rbf((uv[0] + uv[1]) + (uv[2] + uv[3]));
      *(LAS u16*)(L + a_hw) = (u16)pack2(rbf(rcp(1.f + __expf(-gg))) * uu, 0.f);
    }
    BAR();
    {
      const u32x4 hb = *(LAS u32x4*)(L + a_hr);
      const f32x4 z = {0.f, 0.f, 0.f, 0.f};
      const f32x4 a0 = mfma(df0, hb, z), a1 = mfma(df1, hb, z);
      st_sc1(rs, yo, tag8((n & 8) ? a1 : a0, tagv));
    }
    if (w == 0) STAMP(14);
  } else if (red && (w == RW || w == RW - 1)) {
    const int f = 16 * xcd + 2 * b + hj, ro_ = 4 * (w & 1) + (lane & 3);
    const int rb = e >> 4, rj = e & 15, c2 = 32 * rj + 4 * (lane & 7) + g;
    fl = __builtin_amdgcn_raw_buffer_load_b32(rs, WS_FLAGS + lane * 4, 0, SC1);
    float xx = blo(x[rb * 512 + c2]);
    FL_WAIT()
    float ga = blo(__builtin_amdgcn_raw_buffer_load_b16(rs, WS_MODR + rb * 1024 + mpos(c2), 0, SC1));
    float gb = blo(__builtin_amdgcn_raw_buffer_load_b16(rs, WS_MODR + (24 + ro_) * 1024 + mpos(4 * f + g), 0, SC1));
    int po = WS_PART + e * 1024 + lane * 16;
    int xo = (XCP || lane < 8) ? WS_X2 + (lane >> 3) * 16384 + rb * 2048 + rj * 128 + (lane & 7) * 16 : WS_END;
    int xi = WS_X2 + (XCP ? xcd : 0) * 16384 + ro_ * 2048 + (4 * f + g) * 4;
    u32 xt = (g & 1) ? tag >> 16 : tag & 0xffffu;
    const u32x4 tb4 = bytes4(tag);
    u32x4 tb5 = lane < 44 ? tb4 : u32x4{0, 0, 0, 0};
    int yo = WS_YP + (f * NP3 + (lane >> 2)) * 128 + ro_ * 16;
    int y2 = lane < 44 ? yo + 4096 : WS_END;
    u32 oof = (lane & 12) == 0 ? (ro_ * 512 + 4 * f + g) * 2 : 8192;
    const __amdgpu_buffer_rsrc_t ro = __builtin_amdgcn_make_buffer_rsrc((void*)out, 0, 8192, 0x00020000);
    PIN(xt); PIN(tb5); PIN(ga); PIN(gb); PIN(xx); PIN(po); PIN(xo); PIN(xi); PIN(yo); PIN(y2); PIN(oof);
    if (w == RW) {
      u32x4 bp, bq;
      until_s(org + OFB);
      B0_LOAD()
      const u32 anc = anchor(rs, bo, tag, org, bp, bq);
      STAMP(1);
      until(anc + DP);
      u32x4 pv = ld_one(rs, po) ^ tb4;
      if (!__all((or4(pv) & 255u) == 0u)) {
#pragma unroll 1
        for (u32 sp = 0; sp < SPIN_CAP; ++sp) {
          MEMB();
          pv = ld_one(rs, po) ^ tb4;
          if (__all((or4(pv) & 255u) == 0u)) break;
        }
      }
      STAMP(10);
      float t = t32(t16(asf(pv[0]), asf(pv[1])), t16(asf(pv[2]), asf(pv[3])));
      t += dppf<ROR8>(t);
      const u32 xw = __float_as_uint(rbf(xx + rbf(ga * rbf(t)))) | xt;
      const auto p1 = __builtin_amdgcn_permlane16_swap(xw, xw, false, false);
      const auto p2 = __builtin_amdgcn_permlane32_swap(p1[0], p1[0], false, false), p3_ = __builtin_amdgcn_permlane32_swap(p1[1], p1[1], false, false);
      st_sc1(rs, xo, u32x4{p2[0], p3_[0], p2[1], p3_[1]});
      STAMP(11);
      if (bid == 1 && lane == 0) __builtin_amdgcn_raw_buffer_store_b32(tag, rs, WS_EPOCH, 0, SC1);
      u32 ty = anc + DYS;
      const int lt = (int)(now4() - anc) - (DX + 20);
      if (lt > 0) {
        int lk = DB1;
        while (lk < DB1 + lt + 240) lk += PB;
        u32x2 bv;
#pragma unroll 1
        for (u32 sp = 0; sp < SPIN_CAP; ++sp) {
          until_s(anc + lk);
          bv = ld_sc1_64(rs, WS_B1 + xcd * 512 + (lane < NP3 ? lane : 0) * 8);
          if (__all(bv[1] == tag)) break;
          MEMB();
          do lk += PB; while ((int)(now4() - anc) > lk);
        }
        ty = org + smax((int)(bv[0] - org)) + DY;
      }
      ctl[C_TX] = ty | 1u;
      STAMP(7);
    }
    lds_wait(ctl + C_TX, 1u);
    until(__builtin_amdgcn_readfirstlane(ctl[C_TX]));
    u32x4 yv[3];
    u32 x2;
#define Y_LOAD() { yv[0] = ld_one(rs, yo) ^ tb4; yv[1] = ld_one(rs, yo + 2048) ^ tb4; yv[2] = ld_one(rs, y2) ^ tb5; x2 = __builtin_amdgcn_raw_buffer_load_b32(rs, xi, 0, SC1) ^ xt; }
#define Y_OK() __all((((or4(yv[0]) | or4(yv[1]) | or4(yv[2])) & 255u) | (x2 & 0xffffu)) == 0u)
    Y_LOAD()
    if (!Y_OK()) {
#pragma unroll 1
      for (u32 sp = 0; sp < SPIN_CAP; ++sp) {
        MEMB();
        Y_LOAD()
        if (Y_OK()) break;
      }
    }
    if (w == RW) STAMP(15);
    const f32x4 s = (__builtin_bit_cast(f32x4, yv[0]) + __builtin_bit_cast(f32x4, yv[1])) + __builtin_bit_cast(f32x4, yv[2]);
    float t = t32(t16(s[0], s[1]), t16(s[2], s[3]));
    t += dppf<ROR8>(t);
    t += dppf<ROR4>(t);
    __builtin_amdgcn_raw_buffer_store_b16((u16)pack2(asf(x2) + rbf(gb * rbf(t)), 0.f), ro, oof, 0, OAUX);
    if (w == RW) STAMP(18);
  }
}

extern "C" void kernel_launch(void* const* d_in, const int* in_sizes, int n_in, void* d_out, int out_size, void* d_ws, size_t ws_size, hipStream_t stream) {
  (void)hipFuncSetAttribute((const void*)fused, hipFuncAttributeMaxDynamicSharedMemorySize, L_END);
  Args a;
  const int src[4][2] = {{3, 3}, {10, 10}, {4, 4}, {6, 7}};
  for (int r = 0; r < 4; ++r)
    for (int k = 0; k < 2; ++k) a.half[r][k] = (const unsigned char*)d_in[src[r][k]] + (r < 3 && k ? 64 : 0);
  a.x = (const u16*)d_in[0]; a.kc = (const u16*)d_in[1]; a.vc = (const u16*)d_in[2]; a.wd = (const u16*)d_in[8]; a.cc = (const u16*)d_in[9];
  a.out = (u16*)d_out; a.ws = (unsigned char*)d_ws;
  fused<<<256, 1024, L_END, stream>>>(a);
}
```

```cpp
#include <hip/hip_runtime.h>
#include <stdint.h>

typedef unsigned int u32;
typedef unsigned long long u64;
typedef unsigned short u16;
typedef u32 u32x4 __attribute__((ext_vector_type(4)));
typedef u32 u32x2 __attribute__((ext_vector_type(2)));
typedef float f32x4 __attribute__((ext_vector_type(4)));
typedef float f32x2 __attribute__((ext_vector_type(2)));
typedef short s16x4 __attribute__((ext_vector_type(4)));
typedef __bf16 bf16x8 __attribute__((ext_vector_type(8)));
typedef __bf16 bf16x2 __attribute__((ext_vector_type(2)));
#define GAS __attribute__((address_space(1)))
#define LAS __attribute__((address_space(3)))
#define RLX_AGENT __ATOMIC_RELAXED, __HIP_MEMORY_SCOPE_AGENT

constexpr int SK = 4096, WIN = 1024, FF = 1376, NP3 = 43;

constexpr int WS_EPOCH = 0;
constexpr int WS_FLAGS = 0x1000;
constexpr int WS_TS = 0x1800;
constexpr int WS_G1 = 0x2000;
constexpr int WS_B1 = 0x8000;
constexpr int WS_MODR = 0xA000;
constexpr int WS_Q = 0x12000;
constexpr int WS_O = 0x36000;
constexpr int WS_B0 = 0x56000;
constexpr int WS_PART = 0x5E000;
constexpr int WS_YP = WS_PART + 8 * 16 * 8 * 32 * 4;
constexpr int WS_X2 = WS_YP + 128 * NP3 * 128;
constexpr int WS_END = WS_X2 + 8 * 16384;
constexpr int WS_STEP = 2 << 20;
static_assert(WS_END <= WS_STEP, "");

constexpr int L_W = 0;
constexpr int L_TP = 65536;
constexpr int C1S = 1040;
constexpr int L_C1 = 98304;
constexpr int L_SINK = 130048;
constexpr int L_RED = 131072;
constexpr int OSTR = 544, OBS = 288;
constexpr int RW = 12;
constexpr int WG_ = 10;
constexpr int L_H2 = L_RED + 16384;
constexpr int H2S = 1056;
constexpr int L_Q = L_H2 + 8 * H2S;
constexpr int L_OB = L_Q + 256;
constexpr int L_ML = L_OB + 256;
constexpr int L_HH = L_ML + 128;
constexpr int L_CTL = L_HH + 784;
constexpr int L_END = L_CTL + 64;
constexpr int C_T = 0, C_TB = 1, C_TX = 2, C_Q = 3, C_ML = 4, C_R = 6, C_T0 = 7, C_X = 8;

constexpr u32 SPIN_CAP = 1u << 17, LSPIN_CAP = 1u << 20;
#define TAU 226
#define TX 0
#define DB 0
#define DUE0 900
#define SMASK 0xffffffffu
#define CPF 1
#define CODEKB 27
#define EXACT 0
constexpr int TEND = DUE0 + 32 * TAU + 4 * TX - 13 * DB;
#define OFO 440
#define OFB 490
#define WDE 0
#define MLA 4
#define TOUCH 1
#define SKW 400
#define PHASE (4 * bb + hj)
#define ROTF idx
#define XR 0
#define KW 6
#define KA 55
#define XCP 1
#define B0C 0
#define DYS 1145
#define ROTM 63
#define DRY 1
#define SDL 24
#define MSH 14
#define TW 1
#define TGM 0
#define PB 200
#define RB 260
#define DO 0
#define DL_ 140
#define DP 360
#define DX 585
#define DB1 960
#define DY 260
#define REB 1

__device__ __forceinline__ float rbf(float f) { return (float)(__bf16)f; }
__device__ __forceinline__ u32 pack2(float a, float b) { bf16x2 v = {(__bf16)a, (__bf16)b}; return __builtin_bit_cast(u32, v); }
__device__ __forceinline__ float blo(u32 v) { return __builtin_bit_cast(float, v << 16); }
__device__ __forceinline__ float bhi(u32 v) { return __builtin_bit_cast(float, v & 0xffff0000u); }
__device__ __forceinline__ u32 canon(u32 v) { return v < 0xfffffffeu ? v : 0xfffffffeu; }
#define SEL4() u32x4 sel = {0x07060500u, 0x07060501u, 0x07060502u, 0x07060503u}; asm volatile("" : "+s"(sel))
__device__ __forceinline__ u32x4 tag8(f32x4 v, u32 tagv, u32x4 sel) {
  const u32x4 r = __builtin_bit_cast(u32x4, v);
  return u32x4{__builtin_amdgcn_perm(r[0], tagv, sel[0]), __builtin_amdgcn_perm(r[1], tagv, sel[1]), __builtin_amdgcn_perm(r[2], tagv, sel[2]), __builtin_amdgcn_perm(r[3], tagv, sel[3])};
}
__device__ __forceinline__ u32x4 bytes4(u32 tag) { return u32x4{tag & 255u, (tag >> 8) & 255u, (tag >> 16) & 255u, tag >> 24}; }
__device__ __forceinline__ u32 or4(u32x4 v) { return (v[0] | v[1]) | (v[2] | v[3]); }
__device__ __forceinline__ float rcp(float x) { return __builtin_amdgcn_rcpf(x); }
template <int CTRL> __device__ __forceinline__ float dppf(float x) {
  return __builtin_bit_cast(float, __builtin_amdgcn_mov_dpp(__builtin_bit_cast(int, x), CTRL, 0xf, 0xf, true));
}
template <int CTRL> __device__ __forceinline__ u32 dppu(u32 x) { return (u32)__builtin_amdgcn_mov_dpp((int)x, CTRL, 0xf, 0xf, true); }
constexpr int ROR8 = 0x128, ROR4 = 0x124, ROR2 = 0x122, ROR1 = 0x121, XOR1 = 0xB1, XOR2 = 0x4E, XOR7 = 0x141;
__device__ __forceinline__ float row_sum(float x) { x += dppf<ROR8>(x); x += dppf<ROR4>(x); x += dppf<ROR2>(x); x += dppf<ROR1>(x); return x; }
__device__ __forceinline__ float row_max(float x) {
  x = fmaxf(x, dppf<ROR8>(x)); x = fmaxf(x, dppf<ROR4>(x)); x = fmaxf(x, dppf<ROR2>(x)); x = fmaxf(x, dppf<ROR1>(x)); return x;
}
__device__ __forceinline__ float x16_sum(float x) {
  auto s = __builtin_amdgcn_permlane16_swap(__float_as_uint(x), __float_as_uint(x), false, false);
  return __uint_as_float(s[0]) + __uint_as_float(s[1]);
}
__device__ __forceinline__ float x32_sum(float x) {
  auto s = __builtin_amdgcn_permlane32_swap(__float_as_uint(x), __float_as_uint(x), false, false);
  return __uint_as_float(s[0]) + __uint_as_float(s[1]);
}
__device__ __forceinline__ float x16_max(float x) {
  auto s = __builtin_amdgcn_permlane16_swap(__float_as_uint(x), __float_as_uint(x), false, false);
  return fmaxf(__uint_as_float(s[0]), __uint_as_float(s[1]));
}
__device__ __forceinline__ float x32_max(float x) {
  auto s = __builtin_amdgcn_permlane32_swap(__float_as_uint(x), __float_as_uint(x), false, false);
  return fmaxf(__uint_as_float(s[0]), __uint_as_float(s[1]));
}
__device__ __forceinline__ float t16(float a, float b) {
  auto s = __builtin_amdgcn_permlane16_swap(__float_as_uint(a), __float_as_uint(b), false, false);
  return __uint_as_float(s[0]) + __uint_as_float(s[1]);
}
__device__ __forceinline__ float t32(float a, float b) {
  auto s = __builtin_amdgcn_permlane32_swap(__float_as_uint(a), __float_as_uint(b), false, false);
  return __uint_as_float(s[0]) + __uint_as_float(s[1]);
}
__device__ __forceinline__ f32x2 up2(u32 v) { return f32x2{blo(v), bhi(v)}; }
__device__ __forceinline__ u32 pk2(f32x2 v) { return pack2(v[0], v[1]); }
#define PIN(x) asm volatile("" : "+v"(x))
__device__ __forceinline__ f32x2 rb2(f32x2 v) { u32 p = pk2(v); asm("" : "+v"(p)); return up2(p); }
__device__ __forceinline__ float wave_sum(float x) { return x32_sum(x16_sum(row_sum(x))); }
__device__ __forceinline__ float wave_max(float x) { return x32_max(x16_max(row_max(x))); }
__device__ __forceinline__ float dot2(u32 a, u32 b, float c) {
  return __builtin_amdgcn_fdot2_f32_bf16(__builtin_bit_cast(bf16x2, a), __builtin_bit_cast(bf16x2, b), c, false);
}
__device__ __forceinline__ f32x4 mfma(u32x4 a, u32x4 b, f32x4 c) {
  return __builtin_amdgcn_mfma_f32_16x16x32_bf16(__builtin_bit_cast(bf16x8, a), __builtin_bit_cast(bf16x8, b), c, 0, 0, 0);
}
__device__ __forceinline__ u32x4 ldnt(const void* p) { return __builtin_nontemporal_load((const u32x4*)p); }
__device__ __forceinline__ void glds(const void* g, u32 lds) {
  u32 keep;
  lds = __builtin_amdgcn_readfirstlane(lds);
  asm volatile("s_mov_b32 %0, m0\n\ts_mov_b32 m0, %2\n\ts_nop 0\n\tglobal_load_lds_dwordx4 %1, off nt\n\ts_mov_b32 m0, %0"
               : "=&s"(keep) : "v"(g), "s"(lds) : "memory");
}
__device__ __forceinline__ void glds(const void* base, u32 off, u32 lds) {
  u32 keep;
  lds = __builtin_amdgcn_readfirstlane(lds);
  asm volatile("s_mov_b32 %0, m0\n\ts_mov_b32 m0, %3\n\ts_nop 0\n\tglobal_load_lds_dwordx4 %1, %2 nt\n\ts_mov_b32 m0, %0"
               : "=&s"(keep) : "v"(off), "s"(base), "s"(lds) : "memory");
}
__device__ __forceinline__ void touch(const void* base, u32 off, u32 lds) {
  u32 keep;
  lds = __builtin_amdgcn_readfirstlane(lds);
  asm volatile("s_mov_b32 %0, m0\n\ts_mov_b32 m0, %3\n\ts_nop 0\n\tglobal_load_lds_dwordx4 %1, %2\n\ts_mov_b32 m0, %0"
               : "=&s"(keep) : "v"(off), "s"(base), "s"(lds) : "memory");
}
__device__ __forceinline__ u32x4 trfrag(LAS unsigned char* p0, LAS unsigned char* p1) {
  s16x4 a = __builtin_amdgcn_ds_read_tr16_b64_v4i16((LAS s16x4*)p0);
  s16x4 b = __builtin_amdgcn_ds_read_tr16_b64_v4i16((LAS s16x4*)p1);
  u32x2 x = __builtin_bit_cast(u32x2, a), y = __builtin_bit_cast(u32x2, b);
  return u32x4{x[0], x[1], y[0], y[1]};
}
#define SB() __builtin_amdgcn_sched_barrier(0)
#define MEMB() asm volatile("" ::: "memory")
#define VM0() asm volatile("s_waitcnt vmcnt(0)" ::: "memory")
#define LGKM0() asm volatile("s_waitcnt lgkmcnt(0)" ::: "memory")
#define BAR() asm volatile("s_waitcnt lgkmcnt(0)\n\ts_barrier" ::: "memory")
#define TAG() do { u32 ep_; asm volatile("s_load_dword %0, %1, 0x0\n\ts_waitcnt lgkmcnt(0)" : "=s"(ep_) : "s"(ws) : "memory"); \
    tag = ((ep_ & 0x7f7f7f7fu) + 0x01010101u) ^ (ep_ & 0x80808080u); } while (0)
constexpr int SC1 = 16;
#define OAUX 0
#define LD1 18
#define LDA 16
__device__ __forceinline__ u32x4 ld_sc1(__amdgpu_buffer_rsrc_t rs, int off) { return __builtin_amdgcn_raw_buffer_load_b128(rs, off, 0, LDA); }
__device__ __forceinline__ u32x4 ld_one(__amdgpu_buffer_rsrc_t rs, int off) { return __builtin_amdgcn_raw_buffer_load_b128(rs, off, 0, LD1); }
__device__ __forceinline__ u32x2 ld_sc1_64(__amdgpu_buffer_rsrc_t rs, int off) { return __builtin_amdgcn_raw_buffer_load_b64(rs, off, 0, LDA); }
__device__ __forceinline__ void st_sc1(__amdgpu_buffer_rsrc_t rs, int off, u32x4 v) { __builtin_amdgcn_raw_buffer_store_b128(v, rs, off, 0, SC1); }
__device__ __forceinline__ void st_sc1_64(__amdgpu_buffer_rsrc_t rs, int off, u32x2 v) { __builtin_amdgcn_raw_buffer_store_b64(v, rs, off, 0, SC1); }

__device__ __forceinline__ void ln_stats(u32x4 xv, float& m, float& inv) {
  const float x[8] = {blo(xv[0]), bhi(xv[0]), blo(xv[1]), bhi(xv[1]), blo(xv[2]), bhi(xv[2]), blo(xv[3]), bhi(xv[3])};
  const float x0 = __builtin_bit_cast(float, __builtin_amdgcn_readfirstlane(xv[0] << 16));
  float s = 0.f, v = 0.f;
#pragma unroll
  for (int j = 0; j < 8; ++j) { const float d = x[j] - x0; s += d; v = fmaf(d, d, v); }
  const float md = wave_sum(s) * (1.f / 512.f), sq = wave_sum(v) * (1.f / 512.f);
  const float var = rbf(fmaxf(fmaf(-md, md, sq), 0.f));
  m = rbf(x0 + md);
  inv = rcp(rbf(__builtin_amdgcn_sqrtf(rbf(var + 1e-5f))));
}
#define PACE_(W, INC) do { asm volatile(W "\n1:\n\ts_memrealtime vcc\n\ts_waitcnt lgkmcnt(0)\n\ts_lshl_b32 vcc_lo, vcc_lo, 2\n\ts_sub_i32 vcc_hi, vcc_lo, %0\n\ts_cmp_lt_i32 vcc_hi, 0\n\t" \
    "s_cbranch_scc0 2f\n\ts_sleep 1\n\ts_branch 1b\n2:\n\ts_add_i32 %0, %0, %1" : "+s"(due) : "is"(INC) : "memory", "scc", "vcc"); SB(); } while (0)
#define LT(K, T)
#define PACE(INC) PACE_("s_waitcnt vmcnt(2)", INC)
#define PACEN(INC) PACE_("", INC)
__device__ __forceinline__ void until_s(u32 due) { PACE_("", 0); }
__device__ __forceinline__ void until(u32 t) {
  asm volatile("1:\n\ts_memrealtime vcc\n\ts_waitcnt lgkmcnt(0)\n\ts_lshl_b32 vcc_lo, vcc_lo, 2\n\ts_sub_i32 vcc_hi, vcc_lo, %0\n\ts_cmp_lt_i32 vcc_hi, 0\n\t"
               "s_cbranch_scc1 1b" :: "s"(t) : "memory", "scc", "vcc");
  SB();
}

#define STAMP(i) do { } while (0)

#define WSTAMP(k) do { } while (0)
__device__ __forceinline__ void lds_wait(volatile LAS u32* p, u32 want) {
  _Pragma("unroll 1") for (u32 s = 0; s < LSPIN_CAP; ++s) {
    if (__builtin_amdgcn_readfirstlane(*p) >= want) break;
    __builtin_amdgcn_s_sleep(2);
  }
  MEMB();
}
__device__ __forceinline__ void lds_add(LAS u32* p, int lane) {
  LGKM0();
  if (lane == 0) __hip_atomic_fetch_add(p, 1u, __ATOMIC_RELAXED, __HIP_MEMORY_SCOPE_WORKGROUP);
  MEMB();
}
__device__ __forceinline__ u32 lds_arrive(LAS u32* p, int lane) {
  LGKM0();
  u32 old = 0;
  if (lane == 0) old = __hip_atomic_fetch_add(p, 1u, __ATOMIC_RELAXED, __HIP_MEMORY_SCOPE_WORKGROUP);
  old = __builtin_amdgcn_readfirstlane(old);
  MEMB();
  return old;
}

__device__ __forceinline__ void tile_part(LAS unsigned char* T, LAS unsigned char* P, u32x4 hb, int n, int g) {
  LAS unsigned char* tb = T + (8 * g + (n >> 2)) * 128 + (n & 3) * 8;
  const int sx = (n >> 3) | ((g & 1) << 1);
  f32x4 a[4];
#pragma unroll
  for (int t = 0; t < 4; ++t) a[t] = mfma(trfrag(tb + (t ^ sx) * 32, tb + (t ^ sx) * 32 + 512), hb, f32x4{0.f, 0.f, 0.f, 0.f});
  *(LAS f32x4*)(P) = n < 8 ? a[0] : a[1];
  *(LAS f32x4*)(P + 1024) = n < 8 ? a[2] : a[3];
}
__device__ __forceinline__ f32x4 tile_sum(LAS unsigned char* P) {
  f32x4 s[8], r[2];
#pragma unroll
  for (int k = 0; k < 2; ++k) {
#pragma unroll
    for (int i = 0; i < 8; ++i) s[i] = *(LAS f32x4*)(P + (8 * k + i) * 2048);
    SB();
    r[k] = ((s[0] + s[1]) + (s[2] + s[3])) + ((s[4] + s[5]) + (s[6] + s[7]));
    SB();
  }
  return r[0] + r[1];
}
#define GU_MUL(P) _Pragma("unroll") for (int i = 0; i < 8; ++i) gb_[i] = *(LAS u32x4*)((P) + i * 64); SB(); \
    aL = f32x4{0.f, 0.f, 0.f, 0.f}; aH = aL; _Pragma("unroll") for (int i = 0; i < 8; ++i) { aL = mfma(gf[i], gb_[i], aL); aH = mfma(gf[8 + i], gb_[i], aH); }
__device__ __forceinline__ void gu_pair(f32x4 aL, f32x4 aH, f32x2& gt, f32x2& up) {
  const auto s0 = __builtin_amdgcn_permlane32_swap(__float_as_uint(aL[0] + dppf<ROR8>(aH[0])), __float_as_uint(aL[2] + dppf<ROR8>(aH[2])), false, false);
  const auto s1 = __builtin_amdgcn_permlane32_swap(__float_as_uint(aL[1] + dppf<ROR8>(aH[1])), __float_as_uint(aL[3] + dppf<ROR8>(aH[3])), false, false);
  gt = f32x2{__uint_as_float(s0[0]), __uint_as_float(s1[0])};
  up = f32x2{__uint_as_float(s0[1]), __uint_as_float(s1[1])};
}
__device__ __forceinline__ int mpos(int c) { return (c & 252) * 4 + (c >> 8) * 8 + (c & 3) * 2; }
__device__ __forceinline__ u32 smax(int m) { int r = (int)wave_max((float)m); PIN(r); return (u32)__builtin_amdgcn_readfirstlane(r); }
__device__ __forceinline__ u32 now4() { return (u32)__builtin_amdgcn_s_memrealtime() * 4u; }
#define B0_LOAD() { bp = ld_sc1(rs, WS_B0 + bo); bq = ld_sc1(rs, WS_B0 + 1024 + bo); }
__device__ __forceinline__ bool on_time(u32 tag, u32 org, u32x4 bp, u32x4 bq) {
  const u32 lim = org + OFO - DO;
  return __all((((bp[1] ^ tag) | (bp[3] ^ tag) | (bq[1] ^ tag) | (bq[3] ^ tag)) | (((lim - bp[0]) | (lim - bp[2]) | (lim - bq[0]) | (lim - bq[2])) >> 31)) == 0u);
}
__device__ __forceinline__ u32 anchor(__amdgpu_buffer_rsrc_t rs, int bo, u32 tag, u32 org, u32x4 bp, u32x4 bq) {
  int lk = OFB;
#pragma unroll 1
  for (u32 s = 0; s < SPIN_CAP; ++s) {
    if (__all(((bp[1] ^ tag) | (bp[3] ^ tag) | (bq[1] ^ tag) | (bq[3] ^ tag)) == 0u)) break;
    MEMB();
    do lk += PB; while ((int)(now4() - org) > lk);
    until_s(org + lk);
    B0_LOAD()
  }
  const int m = (int)smax(max(max((int)(bp[0] - org), (int)(bp[2] - org)), max((int)(bq[0] - org), (int)(bq[2] - org))));
  return org + (u32)max(m + DO <= OFO ? OFO : m + DL_, lk + RB - DP);
}
__device__ __forceinline__ float asf(u32 v) { return __builtin_bit_cast(float, v); }

struct Args {
  const unsigned char* half[4][2];
  const u16 *x, *kc, *vc, *wd, *cc;
  u16* out;
  unsigned char* ws;
};

__global__ void __launch_bounds__(1024) fused(const Args a) {
  extern __shared__ __attribute__((aligned(16))) unsigned char smem[];
  LAS unsigned char* L = (LAS unsigned char*)smem;
  const u32 lbase = (u32)(size_t)L;
  const int tid = threadIdx.x, w = __builtin_amdgcn_readfirstlane(tid >> 6), bid = blockIdx.x;
  int lane = tid & 63;
  const int xcd = bid & 7, idx = bid >> 3, hj = idx & 3, h = 4 * xcd + hj, b = idx >> 2, bh = b * 32 + h;
  const int sw8 = hj == 3 ? (w & 1) * 8 : 0, rw = (hj == 3 ? 24 : 32) * w;
  u64 hA, hB, t64;
  u32 warm;
  asm volatile("s_load_dwordx2 %0, %4, %5\n\ts_load_dwordx2 %1, %4, %6\n\ts_load_dword %3, %4, 0x40\n\ts_memrealtime %2" : "=&s"(hA), "=&s"(hB), "=&s"(t64), "=&s"(warm)
               : "s"(__builtin_amdgcn_kernarg_segment_ptr()), "s"(16 * hj + sw8), "s"(16 * hj + (sw8 ^ 8)));
  SB();
  const int bb = (b + xcd) & 7;
  int p3r = 5 * xcd + min(xcd, 3) + bb;
  int mc = (b & 6) + ((xcd & 1) ^ 1), mg = ((xcd & 6) + b) & 7;
  const bool tq = hj == 0, to = hj == 2;
  int lim = hj == 1 ? 6 - b : (xcd < 3 ? 6 : 5) - bb;
  asm volatile("" : "+s"(p3r), "+s"(mc), "+s"(mg), "+s"(lim));
  const bool idle = (hj & 1) && lim <= 0;
  u64 tp0, tp1;
  {
    const u32 pitch = (u32)(0x0AC0040018002000ull >> (16 * hj)) & 0xffffu;
    u32 ce = (xcd << (to ? 13 : 4)) + 2 * b, c1 = 16 * mc + 2 * mg;
    asm volatile("" : "+s"(ce), "+s"(c1));
    const u32 col = 64 * ((hj & 1) ? (hj == 1 ? c1 : p3r) : ce);
    const u32 step = pitch << (to ? 1 : 3);
    u32 off = (rw + (to ? (lane & 24) + (lane >> 5) : lane >> 3)) * pitch + col + ((lane & 3) ^ ((lane >> 3) & 2)) * 16;
    tp0 = off;
    tp1 = off + step;
    u64 s2 = 2 * step;
    u32 ld = lbase + L_W + rw * 128;
    asm volatile("" : "+s"(s2), "+s"(ld), "+v"(tp0), "+v"(tp1));
    asm volatile("s_mov_b32 exec_lo, 0xf0f0f0f0\n\ts_mov_b32 exec_hi, 0xf0f0f0f0\n\ts_waitcnt lgkmcnt(0)\n\tv_lshl_add_u64 %0, %0, 0, %3\n\tv_lshl_add_u64 %1, %1, 0, %2\n\t"
                 "s_not_b64 exec, exec\n\tv_lshl_add_u64 %0, %0, 0, %2\n\tv_lshl_add_u64 %1, %1, 0, %3\n\ts_mov_b64 exec, -1"
                 : "+v"(tp0), "+v"(tp1), "+s"(hA), "+s"(hB), "+s"(t64), "+s"(warm) :: "scc");
    if (!idle) {
      u32 keep;
      asm volatile("s_mov_b32 %0, m0\n\ts_mov_b32 m0, %3\n\ts_nop 0\n\tglobal_load_lds_dwordx4 %1, off nt\n\ts_add_u32 m0, m0, 0x400\n\t"
                   "v_lshl_add_u64 %1, %1, 0, %4\n\tglobal_load_lds_dwordx4 %2, off nt\n\ts_mov_b32 m0, %0\n\tv_lshl_add_u64 %2, %2, 0, %4"
                   : "=&s"(keep), "+v"(tp0), "+v"(tp1) : "s"(ld), "s"(s2) : "memory", "scc");
      glds((const void*)tp0, ld + 2048);
      if (hj != 3) glds((const void*)tp1, ld + 3072);
    }
  }
  SB();
  u32 t0 = (u32)t64;
  const u16 *x = a.x, *kc = a.kc, *vc = a.vc, *wd = a.wd, *cc = a.cc;
  u16* out = a.out;
  unsigned char* ws = a.ws;
  if (w == 0) STAMP(24);
  t0 += (u32)(KW * max((int)((u32)__builtin_amdgcn_s_memrealtime() - t0) - KA, 0)) / 4u;
  const int p3 = (hj == 3 && !idle) ? p3r : -1;
  const bool tm = hj == 1 && !idle;
  u32 tag;
  TAG();
  const __amdgpu_buffer_rsrc_t rs = __builtin_amdgcn_make_buffer_rsrc((void*)(ws + (size_t)(tag & ROTM) * WS_STEP), 0, WS_END, 0x00020000);
  if (idle && hj == 1) {
    if (w == 0) {
      st_sc1(rs, WS_G1 + ((b & 1) * 8 + xcd) * 1024 + lane * 16, u32x4{~0u, ~0u, ~0u, ~0u});
      VM0();
      if (lane == 0) st_sc1_64(rs, WS_TS + ((b & 1) * 8 + xcd) * 8, u32x2{t0, tag});
    } else {
      u64 pc;
      u32 len;
      asm volatile("s_getpc_b64 %0\n1:\n\ts_mov_b32 %1, fused_code_end - 1b - 16" : "=s"(pc), "=s"(len));
      const u32 o = ((b & 1) * 15 + w - 1) * 1024;
      if (o < len) touch((const void*)(pc & ~15ull), min(o + lane * 16, len & ~15u), lbase + L_SINK);
    }
  }
  if (TOUCH && w == TW) {
    const unsigned char* tp = (const unsigned char*)((lane & 1) ? vc : kc) + ((size_t)bh * SK + (SK - WIN)) * 256 + ((lane >> 1) & TGM) * 16384;
    if (TOUCH > 1) {
      if (lane == 32) tp = ws + (size_t)(tag & ROTM) * WS_STEP;
      if (lane == 33) tp = (const unsigned char*)out;
      if (lane == 34) tp = (const unsigned char*)x;
      if (lane == 35) tp = (const unsigned char*)wd + (p3 >= 0 ? p3 * 32768 : 0);
    }
    glds(tp, lbase + L_SINK);
  }
  int ta = TAU, tb = TAU - DB;
  int n = lane & 15, g = lane >> 4;
  u32x4 cv, hb;
  const bool row = tm && (w >= 8 || mc == 1);
  if (row) cv = ldnt((w < 8 ? x : cc) + (w & 7) * 512 + lane * 8);
  if (tid < 16) ((volatile LAS u32*)(L + L_CTL))[tid] = tid == C_T0 ? t0 : 0u;
  BAR();
  t0 = __builtin_amdgcn_readfirstlane(((volatile LAS u32*)(L + L_CTL))[C_T0]);
  u32 due = t0 * 4u + (u32)(PHASE * TAU) / 32u + DUE0;
  int rot = ROTF;
#define RO(I) (((XR ? (I) ^ rot : (I) + rot) & 15) * 8192)
  const u16* kp = kc + ((size_t)bh * SK + (SK - WIN) + 4 * w) * 128;
  u32 l16 = lane * 16;
  PIN(l16);
#define LDR(D, P, I, W) { const GAS unsigned char* p_ = (const GAS unsigned char*)((P) + RO(I)); asm volatile("" : "+s"(p_)); W; D = __builtin_nontemporal_load((const GAS u32x4*)(p_ + l16)); SB(); }
  u32x4 kr[16], vr[16];
  volatile LAS u32* ctl = (volatile LAS u32*)(L + L_CTL);
#define RELANE() do { asm volatile("" : "+v"(lane)); n = lane & 15; g = lane >> 4; } while (0)
  const int e = b * 16 + 2 * xcd + hj;
  const bool red = hj < 2;

#define KL(I, T) { LDR(kr[I], kp, I, PACE(T)) LT(I, T) }
  KL(0, TAU + TX)
  if (row) {
    if (w >= 8) {
#pragma unroll
      for (int j = 0; j < 4; ++j) { const float a0 = blo(cv[j]), a1 = bhi(cv[j]); hb[j] = pack2(a0 * rcp(1.f + __expf(-a0)), a1 * rcp(1.f + __expf(-a1))); }
      *(LAS u32x4*)(L + L_H2 + (w & 7) * H2S + lane * 16) = hb;
      lds_add((LAS u32*)(L + L_CTL) + C_R, lane);
    } else {
      float m, inv;
      ln_stats(cv, m, inv);
      if ((lane >> 3) == mg) {
        LAS f32x4* d = (LAS f32x4*)(L + L_RED + w * 256 + (lane & 7) * 32);
        d[0] = f32x4{rbf(rbf(blo(cv[0]) - m) * inv), rbf(rbf(bhi(cv[0]) - m) * inv), rbf(rbf(blo(cv[1]) - m) * inv), rbf(rbf(bhi(cv[1]) - m) * inv)};
        d[1] = f32x4{rbf(rbf(blo(cv[2]) - m) * inv), rbf(rbf(bhi(cv[2]) - m) * inv), rbf(rbf(blo(cv[3]) - m) * inv), rbf(rbf(bhi(cv[3]) - m) * inv)};
      }
    }
  }
  KL(1, TAU + TX)
  u32x2 tsv;
#define TS_WAIT() if (!__all(tsv[1] == tag)) { _Pragma("unroll 1") for (u32 s = 0; s < SPIN_CAP; ++s) { __builtin_amdgcn_s_sleep(16); MEMB(); \
      tsv = ld_sc1_64(rs, WS_TS + (lane & 15) * 8); if (__all(tsv[1] == tag)) break; } } MEMB();
  if (tm) {
    asm volatile("s_waitcnt vmcnt(2)" ::: "memory");
    lds_wait(ctl + C_R, 8u);
    hb = *(LAS u32x4*)(L + L_H2 + (n & 7) * H2S + (32 * w + 8 * g) * 2);
    tile_part(L + L_W + w * 4096, L + L_TP + w * 2048 + lane * 16, hb, n, g);
    lds_add((LAS u32*)(L + L_CTL) + C_T, lane);
  }
  KL(2, TAU + TX)
  if (w == 0 || (tm && w >= 14)) tsv = ld_sc1_64(rs, WS_TS + (lane & 15) * 8);
  SB();
  if (tm && w >= 14) {
    lds_wait(ctl + C_T, 16u);
    const int col = 32 * (w & 1) + 16 * (n >> 3) + 4 * g;
    const f32x4 s = tile_sum(L + L_TP + (w & 1) * 1024 + lane * 16);
    float v[4] = {rbf(s[0]), rbf(s[1]), rbf(s[2]), rbf(s[3])};
    if (mc == 1) {
      const f32x4 ln = *(LAS f32x4*)(L + L_RED + (n & 7) * 256 + col * 4);
#pragma unroll
      for (int j = 0; j < 4; ++j) v[j] = ln[j] * rbf(1.f + v[j]);
    }
    TS_WAIT()
    st_sc1_64(rs, mc < 2 ? WS_G1 + (2 * mg + (w & 1)) * 1024 + ((2 * (n >> 3) + (g >> 1)) * 16 + mc * 8 + (n & 7)) * 16 + (g & 1) * 8
                         : WS_MODR + ((mc - 2) * 8 + (n & 7)) * 1024 + mpos(64 * mg + col),
              u32x2{canon(pack2(v[0], v[1])), canon(pack2(v[2], v[3]))});
    if (w == 15) STAMP(3);
  }
  RELANE();
  KL(3, TAU + TX) KL(4, ta)
  if (w == 0) {
    TS_WAIT()
    const float d = (float)(int)(tsv[0] - t0);
    ctl[C_TB] = (t0 * 4u + (u32)(int)fmaxf(row_sum(d) * 0.25f, row_max(d) * 4.f - SKW)) | 1u;
  }
  KL(5, ta) KL(6, ta)
  u32 org = __builtin_amdgcn_readfirstlane(ctl[C_TB]);
  if (org && REB) { const int adj = ((int)(org - t0 * 4u - (p3 >= 0 ? bb * MSH : 0)) * 85 + 1024) >> 11; ta += adj; tb += adj; }
  KL(7, ta)
  if (tm && mc >= 2 && w >= 14 && lane == 0) __builtin_amdgcn_raw_buffer_store_b32(tag, rs, WS_FLAGS + (((mc - 2) * 8 + mg) * 2 + (w & 1)) * 4, 0, SC1);
  KL(8, ta)
  u32x4 g0;
  const int o0 = WS_G1 + w * 1024 + lane * 16;
  if (tq) lds_wait(ctl + C_TB, 1u);
#define KG(K) { KL(K, ta) if (tq && (w & 1) == ((K) & 1)) { until_s(due - TAU / 2); \
    asm volatile("buffer_load_dwordx4 %0, %1, %2, 0 offen sc1" : "=&v"(g0) : "v"(o0), "s"(rs) : "memory"); } }
  KG(9) KG(10) KL(11, ta) KL(12, ta)
  if (tq) {
    asm volatile("s_waitcnt vmcnt(2)" : "+v"(g0));
#define G1_OK() __all(g0[0] != ~0u && g0[1] != ~0u && g0[2] != ~0u && g0[3] != ~0u)
    if (!G1_OK()) {
#pragma unroll 1
      for (u32 s = 0; s < SPIN_CAP; ++s) {
        __builtin_amdgcn_s_sleep(16);
        MEMB();
        g0 = ld_sc1(rs, o0);
        if (G1_OK()) break;
      }
    }
    if (w == 15) STAMP(4);
#pragma unroll
    for (int j = 0; j < 4; ++j) { const u32 o = dppu<ROR8>(g0[j]); hb[j] = pack2(blo(g0[j]) + blo(o), bhi(g0[j]) + bhi(o)); }
    PIN(hb);
  }
  KL(13, ta)
  if (tq) {
    tile_part(L + L_W + w * 4096, L + L_TP + w * 2048 + lane * 16, hb, n, g);
    lds_add((LAS u32*)(L + L_CTL) + C_T, lane);
  }
  KL(14, ta)
  if (tq && w >= 14) {
    lds_wait(ctl + C_T, 16u);
    const f32x4 s = tile_sum(L + L_TP + (w & 1) * 1024 + lane * 16);
    st_sc1(rs, WS_Q + ((n & 7) * 32 + 4 * xcd + (b >> 1)) * 512 + ((b & 1) * 64 + 32 * (w & 1) + 16 * (n >> 3) + 4 * g) * 4, u32x4{pack2(s[0], s[1]), tag, pack2(s[2], s[3]), tag});
    if (w == 15) STAMP(17);
  }
  RELANE();
  KL(15, ta)
  asm volatile("" : "+s"(rot));
  const u16* vp = vc + ((size_t)bh * SK + (SK - WIN) + 4 * w) * 128;
#define VL(I, T) { LDR(vr[I], vp, I, PACE(T)) LT(16 + (I), T) }
#define VLN(I, T) { LDR(vr[I], vp, I, PACEN(T)) LT(16 + (I), T) }
  VL(0, ta) VL(1, ta)
  VL(2, tb)
  u32x2 gq;
  if (w == WG_) gq = ld_sc1_64(rs, WS_Q + bh * 512 + lane * 8);
  SB();
  VLN(3, tb) VLN(4, tb)
  if (w == WG_) {
    if (!__all(gq[1] == tag)) {
#pragma unroll 1
      for (u32 s = 0; s < SPIN_CAP; ++s) {
        __builtin_amdgcn_s_sleep(4);
        MEMB();
        gq = ld_sc1_64(rs, WS_Q + bh * 512 + lane * 8);
        if (__all(gq[1] == tag)) break;
      }
    }
    *(LAS u32*)(L + L_Q + lane * 4) = gq[0];
    LGKM0();
    ctl[C_Q] = 1u;
    STAMP(5);
  }
  RELANE();
  VL(5, tb)
  lds_wait(ctl + C_Q, 1u);
  const u32x4 qf = *(LAS u32x4*)(L + L_Q + n * 16);
  if (w == 1) STAMP(6);

#define DOT(I) dot2(kr[I][3], qf[3], dot2(kr[I][2], qf[2], dot2(kr[I][1], qf[1], dot2(kr[I][0], qf[0], 0.f))))
#define TR(LO, HI, CTRL, BIT) ((n & (BIT)) ? (HI) + dppf<CTRL>(HI) : (LO) + dppf<CTRL>(LO))
  float xs[8];
#define QK(C) { _Pragma("unroll") for (int i = 2 * (C); i < 2 * (C) + 2; ++i) { const float lo_ = DOT(i), hi_ = DOT(i + 8); \
      const float t1_ = lo_ + dppf<ROR8>(lo_), t2_ = hi_ + dppf<ROR8>(hi_); xs[i] = (n & 8) ? t2_ : t1_; PIN(xs[i]); } SB(); }
  QK(0) VL(6, tb) QK(1) VL(7, tb) QK(2) VL(8, tb) QK(3) VL(9, tb)
  const bool prer = p3 >= 0 && w < 8;
  u32 fl;
  if (prer) fl = __builtin_amdgcn_raw_buffer_load_b32(rs, WS_FLAGS + lane * 4, 0, SC1);
#define TR_STEP(HALF, CTRL, BIT) _Pragma("unroll") for (int hh = 0; hh < (HALF); ++hh) { \
    const float lo_ = xs[hh], hi_ = xs[hh + (HALF)]; const float t1_ = lo_ + dppf<CTRL>(lo_), t2_ = hi_ + dppf<CTRL>(hi_); xs[hh] = (n & (BIT)) ? t2_ : t1_; }
  TR_STEP(4, XOR7, 4) TR_STEP(2, XOR2, 2) TR_STEP(1, XOR1, 1)
#undef TR_STEP
  float sc = rbf(rbf(xs[0]) * 0.08838834764831845f);
  PIN(sc);
  SB();
  VLN(10, tb)
  {
    const float mw_ = wave_max(sc);
    const float lw = wave_sum(__expf(sc - mw_));
    if (lane == 0) *(LAS f32x2*)(L + L_ML + w * 8) = f32x2{mw_, lw};
    lds_add((LAS u32*)(L + L_CTL) + C_ML, lane);
  }
  SB();
  VL(11, tb)
  RELANE();
  u32x4 pre0, pre1;
#define FL_WAIT() if (!__all(fl == tag)) {   _Pragma("unroll 1") for (u32 s = 0; s < SPIN_CAP; ++s) { __builtin_amdgcn_s_sleep(4); MEMB(); \
      fl = __builtin_amdgcn_raw_buffer_load_b32(rs, WS_FLAGS + lane * 4, 0, SC1); if (__all(fl == tag)) break; } } MEMB();
  if (prer) {
    PIN(fl);
    FL_WAIT()
    pre0 = ld_sc1(rs, WS_MODR + (8 + w) * 1024 + lane * 16);
    pre1 = ld_sc1(rs, WS_MODR + (16 + w) * 1024 + lane * 16);
  }
  SB();
  u32 mk[4];
#pragma unroll
  for (int j = 0; j < 4; ++j) mk[j] = (n >> 1) == j ? ((n & 1) ? 0xffff0000u : 0x0000ffffu) : 0u;
  f32x4 oa = {0.f, 0.f, 0.f, 0.f};
#define PV(I) { const u32 pb_ = dppu<0x150 + (I)>(pd); oa = mfma(vr[I], u32x4{pb_ & mk[0], pb_ & mk[1], pb_ & mk[2], pb_ & mk[3]}, oa); asm volatile("" : "+v"(oa)); SB(); }
  PIN(mk[0]); PIN(mk[1]); PIN(mk[2]); PIN(mk[3]);
  u32 a_oa = L_RED + w * OSTR + (g * 8 + n) * 16;
  u32 ra = L_RED + (lane >> 5) * 8 * OSTR + (lane & 31) * 16;
  u32 rb_ = WS_O + (h * 8 + b) * 512 + (lane & 31) * 16 + (lane >> 5) * 8;
  u32 tagv = tag;
  PIN(a_oa); PIN(ra); PIN(rb_); PIN(tagv);
  org = __builtin_amdgcn_readfirstlane(ctl[C_TB]);
  org = org ? org + TEND : t0 * 4u;
  asm volatile("" : "+s"(org));
  VLN(12, tb)
  lds_wait(ctl + C_ML, 16u);
  u32 pd;
  {
    const f32x2 ml = *(LAS f32x2*)(L + L_ML + n * 8);
    const float M = row_max(ml[0]);
    const float pe = __expf(sc - M) * rcp(row_sum(__expf(ml[0] - M) * ml[1]));
    pd = pack2(pe, pe);
    PIN(pd);
  }
  SB();
  VLN(13, tb)
  PV(0) PV(1) PV(2) PV(3) PV(4)
  VL(14, tb)
  PV(5) PV(6) PV(7) PV(8) PV(9)
  VL(15, tb)
  PV(10) PV(11) PV(12) PV(13) PV(14)
  if (w == 13) __builtin_amdgcn_s_setprio(3);
  PV(15)
#undef PV
  if (n < 8) *(LAS f32x4*)(L + a_oa) = oa;
  BAR();
  if (w == 0) STAMP(8);
  if (w == 13) {
    f32x4 s[8];
#pragma unroll
    for (int ww = 0; ww < 8; ++ww) s[ww] = *(LAS f32x4*)(L + ra + ww * OSTR);
    SB();
    const f32x4 t = ((s[0] + s[1]) + (s[2] + s[3])) + ((s[4] + s[5]) + (s[6] + s[7]));
    st_sc1_64(rs, rb_, u32x2{pack2(t32(t[0], t[2]), t32(t[1], t[3])), tagv});
    SB();
    if (lane < (B0C ? 8 : 1)) st_sc1_64(rs, WS_B0 + lane * 2048 + bid * 8, u32x2{now4(), tagv});
    STAMP(9);
  }
  if (to) {
    u32x4 wf[16];
    if (w < 4) {
      const u32 af = L_W + (8 * g + (n >> 2)) * 128 + (w ^ ((n >> 3) | ((g & 1) << 1))) * 32 + (n & 3) * 8;
#pragma unroll
      for (int ks = 0; ks < 16; ++ks) wf[ks] = trfrag(L + af + ks * 4096, L + af + ks * 4096 + 512);
      __builtin_amdgcn_s_setprio(3);
    }
    int oo = WS_O + xcd * 16384 + w * 1024 + lane * 16;
    u32 a_ow = L_TP + (2 * w + (lane >> 5)) * OBS + (lane & 31) * 8;
    u32 a_or = L_TP + ((n >> 3) * 16 + (n & 7)) * OBS + g * 16;
    int rc = n < 8 ? WS_PART + ((n * 16 + 2 * b + ((w >> 1) & 1)) * 8 + xcd) * 128 + (w & 1) * 64 + g * 16 : WS_END;
    PIN(oo); PIN(a_ow); PIN(a_or); PIN(rc);
    SEL4();
    until(org + OFO);
#pragma unroll 1
    for (u32 s = 0; s < SPIN_CAP; ++s) {
      const u32x4 og = ld_sc1(rs, oo);
      *(LAS u32x2*)(L + a_ow) = u32x2{og[0], og[2]};
      if (__builtin_expect(__all(((og[1] ^ tag) | (og[3] ^ tag)) == 0u), 1)) break;
      MEMB();
      __builtin_amdgcn_s_sleep(2);
    }
    BAR();
    if (w == 7) STAMP(2);
    if (w < 4) {
      u32x4 ob[8];
#pragma unroll
      for (int i = 0; i < 8; ++i) ob[i] = *(LAS u32x4*)(L + a_or + (i >> 2) * 8 * OBS + (i & 3) * 64);
      SB();
      f32x4 aL = {0.f, 0.f, 0.f, 0.f}, aH = {0.f, 0.f, 0.f, 0.f};
#pragma unroll
      for (int i = 0; i < 8; ++i) { aL = mfma(wf[i], ob[i], aL); aH = mfma(wf[8 + i], ob[i], aH); }
      const f32x4 v = {aL[0] + dppf<ROR8>(aH[0]), aL[1] + dppf<ROR8>(aH[1]), aL[2] + dppf<ROR8>(aH[2]), aL[3] + dppf<ROR8>(aH[3])};
      st_sc1(rs, rc, tag8(v, tagv, sel));
      if (w == 3) STAMP(16);
    }
  }

  const int bo = (B0C ? xcd : 0) * 2048 + lane * 16;
  if (p3 >= 0) {
    u32 a_h2w = w * C1S + lane * 8;
    if (w < 8) {
      *(LAS u32x2*)(L + L_C1 + a_h2w) = u32x2{pk2(up2(pre1[0]) + 1.f), pk2(up2(pre1[1]) + 1.f)};
      *(LAS u32x2*)(L + L_C1 + a_h2w + 512) = u32x2{pk2(up2(pre1[2]) + 1.f), pk2(up2(pre1[3]) + 1.f)};
      *(LAS u32x2*)(L + L_C1 + 8 * C1S + a_h2w) = u32x2{pre0[0], pre0[1]};
      *(LAS u32x2*)(L + L_C1 + 8 * C1S + a_h2w + 512) = u32x2{pre0[2], pre0[3]};
    }
    if (w == 8 && lane < 36) *(LAS u32*)(L + (lane < 32 ? L_ML : L_HH + 640) + lane * 4) = 0u;
    BAR();
    glds((const void*)(tp0 + (u32)((368 - 16 * w) * FF * 2)), lbase + L_W + 49152 + w * 1024);
    BAR();
#define DL(R) glds(wd + (size_t)(32 * p3 + (R)) * 512, (lane ^ ((((R) & 3) | (((R) >> 1) & 4)) << 1)) * 16, lbase + L_W + 65536 + (R) * 1024)
    if (w < 8) { DL(3 * w); DL(3 * w + 1); DL(3 * w + 2); }
    if (w >= 12) { DL(2 * w); DL(2 * w + 1); }
    SB();
    u32 a_h2r = L_H2 + (n & 7) * H2S + (n >> 3) * 512 + g * 16;
    u32 a_hw = L_HH + (n < 8 ? n * 64 + (8 * (w & 3) + 4 * (g & 1) + 2 * (g >> 1)) * 2 : 512 + lane * 4);
    const u32x4 t16v = {tag & 0xffffu, tag >> 16, tag & 0xffffu, tag >> 16};
#define X_TIME() until_s(org + OFO + DX - 40); u32 tx = 2u; \
      _Pragma("unroll 1") for (u32 s = 0; s < LSPIN_CAP && tx == 2u; ++s) { tx = __builtin_amdgcn_readfirstlane(ctl[C_TX]); if (tx == 2u) __builtin_amdgcn_s_sleep(2); } \
      if (!(tx & 1u)) tx = org + OFO + DX;
#define X_BAD(A, B) ((or4(A) | or4(B)) & 0xffffu)
    if (w < 8) {
      const u32x2 ca = *(LAS u32x2*)(L + L_C1 + a_h2w), cb = *(LAS u32x2*)(L + L_C1 + a_h2w + 512);
      f32x2 c1[4] = {up2(ca[0]), up2(ca[1]), up2(cb[0]), up2(cb[1])};
      a_h2w = L_H2 + w * H2S + lane * 8;
      int xo = WS_X2 + xcd * 16384 + w * 2048 + lane * 16;
      u32 a_hr = L_HH + (n < 8 ? n * 64 + g * 16 : 768), a_hs = L_HH + (n < 8 ? 768 : (n & 7) * 64 + g * 16);
      PIN(a_hs);
      SEL4();
      const u32 a_d0 = L_W + 65536 + (8 * g + (n >> 2)) * 1024 + ((8 * w + ((n >> 1) & 1)) ^ (((n >> 2) | ((g & 1) << 2)) << 1)) * 16 + (n & 1) * 8;
      int yo = WS_YP + ((16 * w + 4 * (n >> 3) + g) * NP3 + p3) * 128 + (n & 7) * 16, yo2 = yo + 8 * NP3 * 128;
      PIN(xo); PIN(c1[0]); PIN(c1[1]); PIN(c1[2]); PIN(c1[3]); PIN(a_h2w); PIN(a_hr); PIN(yo); PIN(yo2);
      u32 a_cx = lbase + L_CTL + (C_X + w) * 4;
      PIN(a_cx);
      asm volatile("s_waitcnt vmcnt(3)" ::: "memory");
      BAR();
      if (w == 0) STAMP(20);
      __builtin_amdgcn_s_setprio(2);
      X_TIME()
      until(tx);
#pragma unroll 1
      for (u32 s = 0; s < SPIN_CAP; ++s) {
        const u32x4 xa = ld_sc1(rs, xo) ^ t16v, xb = ld_sc1(rs, xo + 1024) ^ t16v;
        const float x0 = __builtin_bit_cast(float, __builtin_amdgcn_readfirstlane(xa[0]));
        *(LAS u32x2*)(L + a_h2w) = u32x2{pk2((f32x2{asf(xa[0]), asf(xa[1])} - x0) * c1[0]), pk2((f32x2{asf(xa[2]), asf(xa[3])} - x0) * c1[1])};
        *(LAS u32x2*)(L + a_h2w + 512) = u32x2{pk2((f32x2{asf(xb[0]), asf(xb[1])} - x0) * c1[2]), pk2((f32x2{asf(xb[2]), asf(xb[3])} - x0) * c1[3])};
        if (__builtin_expect(__all(X_BAD(xa, xb) == 0u), 1)) break;
        MEMB();
      }
      asm volatile("s_mov_b64 exec, 1\n\tds_write_b32 %0, %0\n\ts_mov_b64 exec, -1" :: "v"(a_cx) : "memory");
      if (w == 0) STAMP(13);
      __builtin_amdgcn_s_setprio(0);
      lds_add((LAS u32*)(L + L_CTL) + C_R, lane);
      lds_wait(ctl + C_R, 12u);
      u32x4 df[4];
#pragma unroll
      for (int i = 0; i < 4; ++i) { df[i] = trfrag(L + (a_d0 ^ (32 * i)), L + (a_d0 ^ (32 * i)) + 4096); PIN(df[i]); }
      BAR();
      const u32x4 h0 = *(LAS u32x4*)(L + a_hr), h1 = *(LAS u32x4*)(L + a_hs);
      const f32x4 z = {0.f, 0.f, 0.f, 0.f};
      const f32x4 a0 = mfma(df[1], h1, mfma(df[0], h0, z)), a1 = mfma(df[3], h1, mfma(df[2], h0, z));
      st_sc1(rs, yo, tag8(a0, tagv, sel));
      st_sc1(rs, yo2, tag8(a1, tagv, sel));
    } else if (w < 12) {
      u32 a_st = L_ML + (n & 7) * 16;
      PIN(a_st);
      __builtin_amdgcn_s_setprio(3);
      u32x4 gf[16], gb_[8];
      const u32 af = L_W + (8 * g + (n >> 2)) * 128 + (((w & 3) + (n & 2) * 2) ^ (((n >> 3) | ((g & 1) << 1)) << 1)) * 16 + (n & 1) * 8;
      f32x4 aL = {0.f, 0.f, 0.f, 0.f}, aH;
      f32x2 ag, au, sg, su, gt, up;
      {
#pragma unroll
        for (int q4 = 0; q4 < 4; ++q4) {
#pragma unroll
          for (int i = 0; i < 4; ++i) gb_[i] = *(LAS u32x4*)(L + L_C1 + n * C1S + g * 16 + q4 * 256 + i * 64);
          if (q4 == 3) {
            VM0();
            BAR();
          }
#pragma unroll
          for (int i = 0; i < 4; ++i) gf[4 * q4 + i] = trfrag(L + af + (4 * q4 + i) * 4096, L + af + (4 * q4 + i) * 4096 + 512);
          SB();
#pragma unroll
          for (int i = 0; i < 4; ++i) aL = mfma(gf[4 * q4 + i], gb_[i], aL);
          PIN(aL);
        }
        const auto s0 = __builtin_amdgcn_permlane32_swap(__float_as_uint(aL[0]), __float_as_uint(aL[2]), false, false);
        const auto s1 = __builtin_amdgcn_permlane32_swap(__float_as_uint(aL[1]), __float_as_uint(aL[3]), false, false);
        ag = -f32x2{__uint_as_float(s0[0]), __uint_as_float(s1[0])};
        au = -f32x2{__uint_as_float(s0[1]), __uint_as_float(s1[1])};
        sg = f32x2{dppf<ROR8>(ag[0]), dppf<ROR8>(ag[1])} * 1.4426950408889634f;
        su = -f32x2{dppf<ROR8>(au[0]), dppf<ROR8>(au[1])};
        PIN(ag); PIN(au); PIN(sg); PIN(su); PIN(a_h2r); PIN(a_hw);
      }
      if (w == 8) STAMP(21);
      __builtin_amdgcn_s_setprio(0);
      _Pragma("unroll 1") for (u32 s = 0; s < LSPIN_CAP; ++s) if (__all(ctl[C_X + (lane & 7)] != 0u)) break;
      __builtin_amdgcn_s_setprio(3);
      MEMB();
      GU_MUL(L + a_h2r)
      SB();
      f32x4 st = *(volatile LAS f32x4*)(L + a_st);
      gu_pair(aL, aH, gt, up);
      if (__builtin_expect(!__all(st[3] != 0.f), 0)) {
        _Pragma("unroll 1") for (u32 s = 0; s < LSPIN_CAP; ++s) { st = *(volatile LAS f32x4*)(L + a_st); if (__all(st[3] != 0.f)) break; }
      }
      gt = __builtin_elementwise_fma(__builtin_elementwise_fma(ag, f32x2{st[0], st[0]}, gt), f32x2{st[2], st[2]}, sg);
      up = __builtin_elementwise_fma(__builtin_elementwise_fma(au, f32x2{st[0], st[0]}, up), f32x2{st[1], st[1]}, su);
      *(LAS u32*)(L + a_hw) = pk2(f32x2{rcp(1.f + __builtin_amdgcn_exp2f(gt[0])), rcp(1.f + __builtin_amdgcn_exp2f(gt[1]))} * up);
      BAR();
    } else {
      int xo = WS_X2 + xcd * 16384 + (w - 12) * 4096 + lane * 16;
      u32 a_st = lbase + L_ML + (2 * (w - 12) + (lane >> 5)) * 16;
      PIN(xo); PIN(a_st);
      u32x4 bp, bq;
      if (w == 12) {
        until_s(org + OFB);
        B0_LOAD()
        asm volatile("s_waitcnt vmcnt(4)" ::: "memory");
      } else {
        asm volatile("s_waitcnt vmcnt(2)" ::: "memory");
      }
      BAR();
      VM0();
      if (w == 12 && !on_time(tag, org, bp, bq)) {
        ctl[C_TX] = 2u;
        ctl[C_TX] = (anchor(rs, bo, tag, org, bp, bq) + DX) | 1u;
      }
      if (w == 13) STAMP(19);
      lds_add((LAS u32*)(L + L_CTL) + C_R, lane);
      __builtin_amdgcn_s_setprio(1);
      X_TIME()
      until(tx + SDL);
      u32x4 xv[4];
#pragma unroll
      for (int i = 0; i < 4; ++i) xv[i] = ld_sc1(rs, xo + i * 1024);
#pragma unroll
      for (int i = 0; i < 4; ++i) xv[i] ^= t16v;
      if (__builtin_expect(!__all((X_BAD(xv[0], xv[1]) | X_BAD(xv[2], xv[3])) == 0u), 0)) {
#pragma unroll 1
        for (u32 s = 0; s < SPIN_CAP; ++s) {
          MEMB();
#pragma unroll
          for (int i = 0; i < 4; ++i) xv[i] = ld_sc1(rs, xo + i * 1024) ^ t16v;
          if (__all((X_BAD(xv[0], xv[1]) | X_BAD(xv[2], xv[3])) == 0u)) break;
        }
      }
      float sm[2], sq[2];
#pragma unroll
      for (int k = 0; k < 2; ++k) {
        const float x0 = __builtin_bit_cast(float, __builtin_amdgcn_readfirstlane(xv[2 * k][0]));
        const u32x4 xa = xv[2 * k], xb = xv[2 * k + 1];
        const f32x2 d0 = f32x2{asf(xa[0]), asf(xa[1])} - x0, d1 = f32x2{asf(xa[2]), asf(xa[3])} - x0, d2 = f32x2{asf(xb[0]), asf(xb[1])} - x0, d3 = f32x2{asf(xb[2]), asf(xb[3])} - x0;
        const f32x2 sv = (d0 + d1) + (d2 + d3), qv = __builtin_elementwise_fma(d0, d0, d1 * d1) + __builtin_elementwise_fma(d2, d2, d3 * d3);
        sm[k] = sv[0] + sv[1];
        sq[k] = qv[0] + qv[1];
      }
      const float r = row_sum(t32(t16(sm[0], sq[0]), t16(sm[1], sq[1]))) * (1.f / 512.f);
      const auto sw = __builtin_amdgcn_permlane16_swap(__float_as_uint(r), __float_as_uint(r), false, false);
      const float md = __uint_as_float(sw[0]);
      const float inv = __builtin_amdgcn_rsqf(fmaxf(fmaf(-md, md, __uint_as_float(sw[1])), 0.f) + 1e-5f);
      const f32x4 st = {md, inv, inv * -1.4426950408889634f, 1.f};
      asm volatile("s_mov_b32 exec_lo, 1\n\ts_mov_b32 exec_hi, 1\n\tds_write_b128 %0, %1\n\ts_mov_b64 exec, -1" :: "v"(a_st), "v"(st) : "memory");
      if (w == 15) {
        _Pragma("unroll 1") for (u32 s = 0; s < LSPIN_CAP; ++s) if (__all(ctl[C_X + (lane & 7)] != 0u)) break;
        if (lane < 8) st_sc1_64(rs, WS_B1 + lane * 512 + p3 * 8, u32x2{now4(), tagv});
      }
      BAR();
    }
    if (w == 7) STAMP(14);
    asm volatile("fused_code_end:");
  } else if (red && (w == RW || w == RW - 1)) {
    const int f = 16 * xcd + 2 * b + hj, ro_ = 4 * (w & 1) + (lane & 3);
    const int rb = e >> 4, rj = e & 15, c2 = 32 * rj + 4 * (lane & 7) + g;
    fl = __builtin_amdgcn_raw_buffer_load_b32(rs, WS_FLAGS + lane * 4, 0, SC1);
    float xx = blo(x[rb * 512 + c2]);
    FL_WAIT()
    float ga = blo(__builtin_amdgcn_raw_buffer_load_b16(rs, WS_MODR + rb * 1024 + mpos(c2), 0, SC1));
    float gb = blo(__builtin_amdgcn_raw_buffer_load_b16(rs, WS_MODR + (24 + ro_) * 1024 + mpos(4 * f + g), 0, SC1));
    int po = WS_PART + e * 1024 + lane * 16;
    int xo = WS_X2 + (lane >> 3) * 16384 + rb * 2048 + rj * 128 + (lane & 7) * 16;
    int xi = WS_X2 + xcd * 16384 + ro_ * 2048 + (4 * f + g) * 4;
    u32 xt = (g & 1) ? tag >> 16 : tag & 0xffffu;
    const u32x4 tb4 = bytes4(tag);
    u32x4 tb5 = lane < 44 ? tb4 : u32x4{0, 0, 0, 0};
    int yo = WS_YP + (f * NP3 + (lane >> 2)) * 128 + ro_ * 16;
    int y2 = lane < 44 ? yo + 4096 : WS_END;
    u32 oof = (lane & 12) == 0 ? (ro_ * 512 + 4 * f + g) * 2 : 8192;
    __amdgpu_buffer_rsrc_t ro = __builtin_amdgcn_make_buffer_rsrc((void*)out, 0, 8192, 0x00020000);
    asm volatile("" : "+s"(ro));
    PIN(xt); PIN(tb5); PIN(ga); PIN(gb); PIN(xx); PIN(po); PIN(xo); PIN(xi); PIN(yo); PIN(y2); PIN(oof);
    if (w == RW) {
      u32x4 bp, bq;
      until_s(org + OFB);
      B0_LOAD()
      const u32 anc = on_time(tag, org, bp, bq) ? org + OFO : anchor(rs, bo, tag, org, bp, bq);
      STAMP(1);
      until(anc + DP);
      u32x4 xq;
#pragma unroll 1
      for (u32 sp = 0; sp < SPIN_CAP; ++sp) {
        const u32x4 pv = ld_one(rs, po) ^ tb4;
        float t = t32(t16(asf(pv[0]), asf(pv[1])), t16(asf(pv[2]), asf(pv[3])));
        t += dppf<ROR8>(t);
        const u32 xw = __float_as_uint(rbf(fmaf(ga, t, xx))) | xt;
        const auto p1 = __builtin_amdgcn_permlane16_swap(xw, xw, false, false);
        const auto p2 = __builtin_amdgcn_permlane32_swap(p1[0], p1[0], false, false), p3_ = __builtin_amdgcn_permlane32_swap(p1[1], p1[1], false, false);
        xq = u32x4{p2[0], p3_[0], p2[1], p3_[1]};
        PIN(xq);
        if (__builtin_expect(__all((or4(pv) & 255u) == 0u), 1)) break;
        MEMB();
      }
      st_sc1(rs, xo, xq);
      STAMP(11);
      if (bid == 1 && lane == 0) __hip_atomic_store((GAS u32*)ws, tag, RLX_AGENT);
      u32 ty = anc + DYS;
      const int lt = (int)(now4() - anc) - (DX + 20);
      if (lt > 0) {
        int lk = DB1;
        while (lk < DB1 + lt + 240) lk += PB;
        u32x2 bv;
#pragma unroll 1
        for (u32 sp = 0; sp < SPIN_CAP; ++sp) {
          until_s(anc + lk);
          bv = ld_sc1_64(rs, WS_B1 + xcd * 512 + (lane < NP3 ? lane : 0) * 8);
          if (__all(bv[1] == tag)) break;
          MEMB();
          do lk += PB; while ((int)(now4() - anc) > lk);
        }
        ty = org + smax((int)(bv[0] - org)) + DY;
      }
      ctl[C_TX] = ty | 1u;
      STAMP(7);
    }
    lds_wait(ctl + C_TX, 1u);
    until(__builtin_amdgcn_readfirstlane(ctl[C_TX]));
    u32 res;
#pragma unroll 1
    for (u32 sp = 0; sp < SPIN_CAP; ++sp) {
      const u32 x2 = __builtin_amdgcn_raw_buffer_load_b32(rs, xi, 0, SC1) ^ xt;
      const u32x4 y0 = ld_one(rs, yo) ^ tb4, y1 = ld_one(rs, yo + 2048) ^ tb4, y2_ = ld_one(rs, y2) ^ tb5;
      const f32x4 s = (__builtin_bit_cast(f32x4, y0) + __builtin_bit_cast(f32x4, y1)) + __builtin_bit_cast(f32x4, y2_);
      float t = t32(t16(s[0], s[1]), t16(s[2], s[3]));
      t += dppf<ROR8>(t);
      t += dppf<ROR4>(t);
      res = pack2(fmaf(gb, t, asf(x2)), 0.f);
      PIN(res);
      if (__builtin_expect(__all((((or4(y0) | or4(y1) | or4(y2_)) & 255u) | (x2 & 0xffffu)) == 0u), 1)) break;
      MEMB();
    }
    __builtin_amdgcn_raw_buffer_store_b16((u16)res, ro, oof, 0, OAUX);
    if (w == RW) STAMP(18);
  }
}

extern "C" void kernel_launch(void* const* d_in, const int* in_sizes, int n_in, void* d_out, int out_size, void* d_ws, size_t ws_size, hipStream_t stream) {
  (void)hipFuncSetAttribute((const void*)fused, hipFuncAttributeMaxDynamicSharedMemorySize, L_END);
  Args a;
  const int src[4][2] = {{3, 3}, {10, 10}, {4, 4}, {6, 7}};
  for (int r = 0; r < 4; ++r)
    for (int k = 0; k < 2; ++k) a.half[r][k] = (const unsigned char*)d_in[src[r][k]] + (r < 3 && k ? 64 : 0);
  a.x = (const u16*)d_in[0]; a.kc = (const u16*)d_in[1]; a.vc = (const u16*)d_in[2]; a.wd = (const u16*)d_in[8]; a.cc = (const u16*)d_in[9];
  a.out = (u16*)d_out; a.ws = (unsigned char*)d_ws;
  fused<<<256, 1024, L_END, stream>>>(a);
}
```

```cpp
#include <hip/hip_runtime.h>
#include <stdint.h>

typedef unsigned int u32;
typedef unsigned long long u64;
typedef unsigned short u16;
typedef u32 u32x4 __attribute__((ext_vector_type(4)));
typedef u32 u32x2 __attribute__((ext_vector_type(2)));
typedef float f32x4 __attribute__((ext_vector_type(4)));
typedef float f32x2 __attribute__((ext_vector_type(2)));
typedef short s16x4 __attribute__((ext_vector_type(4)));
typedef __bf16 bf16x8 __attribute__((ext_vector_type(8)));
typedef __bf16 bf16x2 __attribute__((ext_vector_type(2)));
#define GAS __attribute__((address_space(1)))
#define LAS __attribute__((address_space(3)))
#define RLX_AGENT __ATOMIC_RELAXED, __HIP_MEMORY_SCOPE_AGENT

constexpr int SK = 4096, WIN = 1024, FF = 1376, NP3 = 43;

constexpr int WS_EPOCH = 0;
constexpr int WS_FLAGS = 0x1000;
constexpr int WS_TS = 0x1800;
constexpr int WS_G1 = 0x2000;
constexpr int WS_B1 = 0x8000;
constexpr int WS_MODR = 0xA000;
constexpr int WS_Q = 0x12000;
constexpr int WS_O = 0x36000;
constexpr int WS_B0 = 0x56000;
constexpr int WS_PART = 0x5E000;
constexpr int WS_YP = WS_PART + 8 * 16 * 8 * 32 * 4;
constexpr int WS_X2 = WS_YP + 128 * NP3 * 128;
constexpr int WS_END = WS_X2 + 8 * 16384;
constexpr int WS_STEP = 2 << 20;
static_assert(WS_END <= WS_STEP, "");

constexpr int L_W = 0;
constexpr int L_TP = 65536;
constexpr int C1S = 1040;
constexpr int L_C1 = 98304;
constexpr int L_SINK = 130048;
constexpr int L_RED = 131072;
constexpr int OSTR = 544, OBS = 288;
constexpr int RW = 12;
constexpr int WG_ = 10;
constexpr int L_H2 = L_RED + 16384;
constexpr int H2S = 1056;
constexpr int L_Q = L_H2 + 8 * H2S;
constexpr int L_OB = L_Q + 256;
constexpr int L_ML = L_OB + 256;
constexpr int L_HH = L_ML + 128;
constexpr int L_CTL = L_HH + 784;
constexpr int L_END = L_CTL + 64;
constexpr int C_T = 0, C_TB = 1, C_TX = 2, C_Q = 3, C_ML = 4, C_R = 6, C_T0 = 7, C_X = 8;

constexpr u32 SPIN_CAP = 1u << 17, LSPIN_CAP = 1u << 20;
#define TAU 220
#define TX 0
#define DB 0
#define DUE0 830
#define SMASK 0xffffffffu
#define CPF 1
#define CODEKB 27
#define EXACT 0
#define A1 0
#define B1_ 8
#define E1 16
#define A2 14
#define B2_ 22
#define E2 10
#define A3 25
#define B3_ 28
#define E3 0
constexpr int extra(int i) { return (i >= A1 && i < B1_ ? E1 : 0) + (i >= A2 && i < B2_ ? E2 : 0) + (i >= A3 && i < B3_ ? E3 : 0); }
constexpr int TEND = DUE0 + 32 * TAU + (B1_ - A1) * E1 + (B2_ - A2) * E2 + (B3_ - A3) * E3;
#define OFO 440
#define OFB 485
#define WDE 0
#define MLA 4
#define TOUCH 1
#define SKW 200
#define PHASE (4 * bb + hj)
#define ROTF idx
#define XR 0
#define KW 6
#define KA 55
#define XCP 1
#define B0C 0
#define DYS 1150
#define ROTM 63
#define DRY 1
#define SDL 24
#define MSH 14
#define TW 1
#define TGM 0
#define PB 200
#define RB 260
#define DO 30
#define DL_ 220
#define DP 355
#define DX 575
#define DB1 960
#define DY 260
#define REB 1

__device__ __forceinline__ float rbf(float f) { return (float)(__bf16)f; }
__device__ __forceinline__ u32 pack2(float a, float b) { bf16x2 v = {(__bf16)a, (__bf16)b}; return __builtin_bit_cast(u32, v); }
__device__ __forceinline__ float blo(u32 v) { return __builtin_bit_cast(float, v << 16); }
__device__ __forceinline__ float bhi(u32 v) { return __builtin_bit_cast(float, v & 0xffff0000u); }
__device__ __forceinline__ u32 canon(u32 v) { return v < 0xfffffffeu ? v : 0xfffffffeu; }
#define SEL4() u32x4 sel = {0x07060500u, 0x07060501u, 0x07060502u, 0x07060503u}; asm volatile("" : "+s"(sel))
__device__ __forceinline__ u32x4 tag8(f32x4 v, u32 tagv, u32x4 sel) {
  const u32x4 r = __builtin_bit_cast(u32x4, v);
  return u32x4{__builtin_amdgcn_perm(r[0], tagv, sel[0]), __builtin_amdgcn_perm(r[1], tagv, sel[1]), __builtin_amdgcn_perm(r[2], tagv, sel[2]), __builtin_amdgcn_perm(r[3], tagv, sel[3])};
}
__device__ __forceinline__ u32x4 bytes4(u32 tag) { return u32x4{tag & 255u, (tag >> 8) & 255u, (tag >> 16) & 255u, tag >> 24}; }
__device__ __forceinline__ u32 or4(u32x4 v) { return (v[0] | v[1]) | (v[2] | v[3]); }
__device__ __forceinline__ float rcp(float x) { return __builtin_amdgcn_rcpf(x); }
template <int CTRL> __device__ __forceinline__ float dppf(float x) {
  return __builtin_bit_cast(float, __builtin_amdgcn_mov_dpp(__builtin_bit_cast(int, x), CTRL, 0xf, 0xf, true));
}
template <int CTRL> __device__ __forceinline__ u32 dppu(u32 x) { return (u32)__builtin_amdgcn_mov_dpp((int)x, CTRL, 0xf, 0xf, true); }
constexpr int ROR8 = 0x128, ROR4 = 0x124, ROR2 = 0x122, ROR1 = 0x121, XOR1 = 0xB1, XOR2 = 0x4E, XOR7 = 0x141;
__device__ __forceinline__ float row_sum(float x) { x += dppf<ROR8>(x); x += dppf<ROR4>(x); x += dppf<ROR2>(x); x += dppf<ROR1>(x); return x; }
__device__ __forceinline__ float row_max(float x) {
  x = fmaxf(x, dppf<ROR8>(x)); x = fmaxf(x, dppf<ROR4>(x)); x = fmaxf(x, dppf<ROR2>(x)); x = fmaxf(x, dppf<ROR1>(x)); return x;
}
__device__ __forceinline__ float x16_sum(float x) {
  auto s = __builtin_amdgcn_permlane16_swap(__float_as_uint(x), __float_as_uint(x), false, false);
  return __uint_as_float(s[0]) + __uint_as_float(s[1]);
}
__device__ __forceinline__ float x32_sum(float x) {
  auto s = __builtin_amdgcn_permlane32_swap(__float_as_uint(x), __float_as_uint(x), false, false);
  return __uint_as_float(s[0]) + __uint_as_float(s[1]);
}
__device__ __forceinline__ float x16_max(float x) {
  auto s = __builtin_amdgcn_permlane16_swap(__float_as_uint(x), __float_as_uint(x), false, false);
  return fmaxf(__uint_as_float(s[0]), __uint_as_float(s[1]));
}
__device__ __forceinline__ float x32_max(float x) {
  auto s = __builtin_amdgcn_permlane32_swap(__float_as_uint(x), __float_as_uint(x), false, false);
  return fmaxf(__uint_as_float(s[0]), __uint_as_float(s[1]));
}
__device__ __forceinline__ float t16(float a, float b) {
  auto s = __builtin_amdgcn_permlane16_swap(__float_as_uint(a), __float_as_uint(b), false, false);
  return __uint_as_float(s[0]) + __uint_as_float(s[1]);
}
__device__ __forceinline__ float t32(float a, float b) {
  auto s = __builtin_amdgcn_permlane32_swap(__float_as_uint(a), __float_as_uint(b), false, false);
  return __uint_as_float(s[0]) + __uint_as_float(s[1]);
}
__device__ __forceinline__ f32x2 up2(u32 v) { return f32x2{blo(v), bhi(v)}; }
__device__ __forceinline__ u32 pk2(f32x2 v) { return pack2(v[0], v[1]); }
#define PIN(x) asm volatile("" : "+v"(x))
__device__ __forceinline__ f32x2 rb2(f32x2 v) { u32 p = pk2(v); asm("" : "+v"(p)); return up2(p); }
__device__ __forceinline__ float wave_sum(float x) { return x32_sum(x16_sum(row_sum(x))); }
__device__ __forceinline__ float wave_max(float x) { return x32_max(x16_max(row_max(x))); }
__device__ __forceinline__ float dot2(u32 a, u32 b, float c) {
  return __builtin_amdgcn_fdot2_f32_bf16(__builtin_bit_cast(bf16x2, a), __builtin_bit_cast(bf16x2, b), c, false);
}
__device__ __forceinline__ f32x4 mfma(u32x4 a, u32x4 b, f32x4 c) {
  return __builtin_amdgcn_mfma_f32_16x16x32_bf16(__builtin_bit_cast(bf16x8, a), __builtin_bit_cast(bf16x8, b), c, 0, 0, 0);
}
__device__ __forceinline__ u32x4 ldnt(const void* p) { return __builtin_nontemporal_load((const u32x4*)p); }
__device__ __forceinline__ void glds(const void* g, u32 lds) {
  u32 keep;
  lds = __builtin_amdgcn_readfirstlane(lds);
  asm volatile("s_mov_b32 %0, m0\n\ts_mov_b32 m0, %2\n\ts_nop 0\n\tglobal_load_lds_dwordx4 %1, off nt\n\ts_mov_b32 m0, %0"
               : "=&s"(keep) : "v"(g), "s"(lds) : "memory");
}
__device__ __forceinline__ void glds(const void* base, u32 off, u32 lds) {
  u32 keep;
  lds = __builtin_amdgcn_readfirstlane(lds);
  asm volatile("s_mov_b32 %0, m0\n\ts_mov_b32 m0, %3\n\ts_nop 0\n\tglobal_load_lds_dwordx4 %1, %2 nt\n\ts_mov_b32 m0, %0"
               : "=&s"(keep) : "v"(off), "s"(base), "s"(lds) : "memory");
}
__device__ __forceinline__ void touch(const void* base, u32 off, u32 lds) {
  u32 keep;
  lds = __builtin_amdgcn_readfirstlane(lds);
  asm volatile("s_mov_b32 %0, m0\n\ts_mov_b32 m0, %3\n\ts_nop 0\n\tglobal_load_lds_dwordx4 %1, %2\n\ts_mov_b32 m0, %0"
               : "=&s"(keep) : "v"(off), "s"(base), "s"(lds) : "memory");
}
__device__ __forceinline__ u32x4 trfrag(LAS unsigned char* p0, LAS unsigned char* p1) {
  s16x4 a = __builtin_amdgcn_ds_read_tr16_b64_v4i16((LAS s16x4*)p0);
  s16x4 b = __builtin_amdgcn_ds_read_tr16_b64_v4i16((LAS s16x4*)p1);
  u32x2 x = __builtin_bit_cast(u32x2, a), y = __builtin_bit_cast(u32x2, b);
  return u32x4{x[0], x[1], y[0], y[1]};
}
#define SB() __builtin_amdgcn_sched_barrier(0)
#define MEMB() asm volatile("" ::: "memory")
#define VM0() asm volatile("s_waitcnt vmcnt(0)" ::: "memory")
#define LGKM0() asm volatile("s_waitcnt lgkmcnt(0)" ::: "memory")
#define BAR() asm volatile("s_waitcnt lgkmcnt(0)\n\ts_barrier" ::: "memory")
#define TAG() do { u32 ep_; asm volatile("s_load_dword %0, %1, 0x0\n\ts_waitcnt lgkmcnt(0)" : "=s"(ep_) : "s"(ws) : "memory"); \
    tag = ((ep_ & 0x7f7f7f7fu) + 0x01010101u) ^ (ep_ & 0x80808080u); } while (0)
constexpr int SC1 = 16;
#define OAUX 0
#define LD1 18
#define LDA 16
__device__ __forceinline__ u32x4 ld_sc1(__amdgpu_buffer_rsrc_t rs, int off) { return __builtin_amdgcn_raw_buffer_load_b128(rs, off, 0, LDA); }
__device__ __forceinline__ u32x4 ld_one(__amdgpu_buffer_rsrc_t rs, int off) { return __builtin_amdgcn_raw_buffer_load_b128(rs, off, 0, LD1); }
__device__ __forceinline__ u32x2 ld_sc1_64(__amdgpu_buffer_rsrc_t rs, int off) { return __builtin_amdgcn_raw_buffer_load_b64(rs, off, 0, LDA); }
__device__ __forceinline__ void st_sc1(__amdgpu_buffer_rsrc_t rs, int off, u32x4 v) { __builtin_amdgcn_raw_buffer_store_b128(v, rs, off, 0, SC1); }
__device__ __forceinline__ void st_sc1_64(__amdgpu_buffer_rsrc_t rs, int off, u32x2 v) { __builtin_amdgcn_raw_buffer_store_b64(v, rs, off, 0, SC1); }

__device__ __forceinline__ void ln_stats(u32x4 xv, float& m, float& inv) {
  const float x[8] = {blo(xv[0]), bhi(xv[0]), blo(xv[1]), bhi(xv[1]), blo(xv[2]), bhi(xv[2]), blo(xv[3]), bhi(xv[3])};
  const float x0 = __builtin_bit_cast(float, __builtin_amdgcn_readfirstlane(xv[0] << 16));
  float s = 0.f, v = 0.f;
#pragma unroll
  for (int j = 0; j < 8; ++j) { const float d = x[j] - x0; s += d; v = fmaf(d, d, v); }
  const float md = wave_sum(s) * (1.f / 512.f), sq = wave_sum(v) * (1.f / 512.f);
  const float var = rbf(fmaxf(fmaf(-md, md, sq), 0.f));
  m = rbf(x0 + md);
  inv = rcp(rbf(__builtin_amdgcn_sqrtf(rbf(var + 1e-5f))));
}
#define PACE_(W, INC) do { asm volatile(W "\n1:\n\ts_memrealtime vcc\n\ts_waitcnt lgkmcnt(0)\n\ts_lshl_b32 vcc_lo, vcc_lo, 2\n\ts_sub_i32 vcc_hi, vcc_lo, %0\n\ts_cmp_lt_i32 vcc_hi, 0\n\t" \
    "s_cbranch_scc0 2f\n\ts_sleep 1\n\ts_branch 1b\n2:\n\ts_add_i32 %0, %0, %1" : "+s"(due) : "is"(INC) : "memory", "scc", "vcc"); SB(); } while (0)
#define LT(K, T)
#define PACE(INC) PACE_("s_waitcnt vmcnt(2)", INC)
#define PACEN(INC) PACE_("", INC)
__device__ __forceinline__ void until_s(u32 due) { PACE_("", 0); }
__device__ __forceinline__ void until(u32 t) {
  asm volatile("1:\n\ts_memrealtime vcc\n\ts_waitcnt lgkmcnt(0)\n\ts_lshl_b32 vcc_lo, vcc_lo, 2\n\ts_sub_i32 vcc_hi, vcc_lo, %0\n\ts_cmp_lt_i32 vcc_hi, 0\n\t"
               "s_cbranch_scc1 1b" :: "s"(t) : "memory", "scc", "vcc");
  SB();
}

#define STAMP(i) do { } while (0)

#define WSTAMP(k) do { } while (0)
__device__ __forceinline__ void lds_wait(volatile LAS u32* p, u32 want) {
  _Pragma("unroll 1") for (u32 s = 0; s < LSPIN_CAP; ++s) {
    if (__builtin_amdgcn_readfirstlane(*p) >= want) break;
    __builtin_amdgcn_s_sleep(2);
  }
  MEMB();
}
__device__ __forceinline__ void lds_add(LAS u32* p, int lane) {
  LGKM0();
  if (lane == 0) __hip_atomic_fetch_add(p, 1u, __ATOMIC_RELAXED, __HIP_MEMORY_SCOPE_WORKGROUP);
  MEMB();
}
__device__ __forceinline__ u32 lds_arrive(LAS u32* p, int lane) {
  LGKM0();
  u32 old = 0;
  if (lane == 0) old = __hip_atomic_fetch_add(p, 1u, __ATOMIC_RELAXED, __HIP_MEMORY_SCOPE_WORKGROUP);
  old = __builtin_amdgcn_readfirstlane(old);
  MEMB();
  return old;
}

__device__ __forceinline__ void tile_part(LAS unsigned char* T, LAS unsigned char* P, u32x4 hb, int n, int g) {
  LAS unsigned char* tb = T + (8 * g + (n >> 2)) * 128 + (n & 3) * 8;
  const int sx = (n >> 3) | ((g & 1) << 1);
  f32x4 a[4];
#pragma unroll
  for (int t = 0; t < 4; ++t) a[t] = mfma(trfrag(tb + (t ^ sx) * 32, tb + (t ^ sx) * 32 + 512), hb, f32x4{0.f, 0.f, 0.f, 0.f});
  *(LAS f32x4*)(P) = n < 8 ? a[0] : a[1];
  *(LAS f32x4*)(P + 1024) = n < 8 ? a[2] : a[3];
}
__device__ __forceinline__ f32x4 tile_sum(LAS unsigned char* P) {
  f32x4 s[8], r[2];
#pragma unroll
  for (int k = 0; k < 2; ++k) {
#pragma unroll
    for (int i = 0; i < 8; ++i) s[i] = *(LAS f32x4*)(P + (8 * k + i) * 2048);
    SB();
    r[k] = ((s[0] + s[1]) + (s[2] + s[3])) + ((s[4] + s[5]) + (s[6] + s[7]));
    SB();
  }
  return r[0] + r[1];
}
#define GU_MUL(P) _Pragma("unroll") for (int i = 0; i < 8; ++i) gb_[i] = *(LAS u32x4*)((P) + i * 64); SB(); \
    aL = f32x4{0.f, 0.f, 0.f, 0.f}; aH = aL; _Pragma("unroll") for (int i = 0; i < 8; ++i) { aL = mfma(gf[i], gb_[i], aL); aH = mfma(gf[8 + i], gb_[i], aH); }
__device__ __forceinline__ void gu_pair(f32x4 aL, f32x4 aH, f32x2& gt, f32x2& up) {
  const auto s0 = __builtin_amdgcn_permlane32_swap(__float_as_uint(aL[0] + dppf<ROR8>(aH[0])), __float_as_uint(aL[2] + dppf<ROR8>(aH[2])), false, false);
  const auto s1 = __builtin_amdgcn_permlane32_swap(__float_as_uint(aL[1] + dppf<ROR8>(aH[1])), __float_as_uint(aL[3] + dppf<ROR8>(aH[3])), false, false);
  gt = f32x2{__uint_as_float(s0[0]), __uint_as_float(s1[0])};
  up = f32x2{__uint_as_float(s0[1]), __uint_as_float(s1[1])};
}
__device__ __forceinline__ int mpos(int c) { return (c & 252) * 4 + (c >> 8) * 8 + (c & 3) * 2; }
__device__ __forceinline__ u32 smax(int m) { int r = (int)wave_max((float)m); PIN(r); return (u32)__builtin_amdgcn_readfirstlane(r); }
__device__ __forceinline__ u32 now4() { return (u32)__builtin_amdgcn_s_memrealtime() * 4u; }
#define B0_LOAD() { bp = ld_sc1(rs, WS_B0 + bo); bq = ld_sc1(rs, WS_B0 + 1024 + bo); }
__device__ __forceinline__ bool on_time(u32 tag, u32 org, u32x4 bp, u32x4 bq) {
  const u32 lim = org + OFO - DO;
  return __all((((bp[1] ^ tag) | (bp[3] ^ tag) | (bq[1] ^ tag) | (bq[3] ^ tag)) | (((lim - bp[0]) | (lim - bp[2]) | (lim - bq[0]) | (lim - bq[2])) >> 31)) == 0u);
}
__device__ __forceinline__ u32 anchor(__amdgpu_buffer_rsrc_t rs, int bo, u32 tag, u32 org, u32x4 bp, u32x4 bq) {
  int lk = OFB;
#pragma unroll 1
  for (u32 s = 0; s < SPIN_CAP; ++s) {
    if (__all(((bp[1] ^ tag) | (bp[3] ^ tag) | (bq[1] ^ tag) | (bq[3] ^ tag)) == 0u)) break;
    MEMB();
    do lk += PB; while ((int)(now4() - org) > lk);
    until_s(org + lk);
    B0_LOAD()
  }
  const int m = (int)smax(max(max((int)(bp[0] - org), (int)(bp[2] - org)), max((int)(bq[0] - org), (int)(bq[2] - org))));
  return org + (u32)max(m + DO <= OFO ? OFO : m + DL_, lk + RB - DP);
}
__device__ __forceinline__ float asf(u32 v) { return __builtin_bit_cast(float, v); }

struct Args {
  const unsigned char* half[4][2];
  const u16 *x, *kc, *vc, *wd, *cc;
  u16* out;
  unsigned char* ws;
};

__global__ void __launch_bounds__(1024) fused(const Args a) {
  extern __shared__ __attribute__((aligned(16))) unsigned char smem[];
  LAS unsigned char* L = (LAS unsigned char*)smem;
  const u32 lbase = (u32)(size_t)L;
  const int tid = threadIdx.x, w = __builtin_amdgcn_readfirstlane(tid >> 6), bid = blockIdx.x;
  int lane = tid & 63;
  const int xcd = bid & 7, idx = bid >> 3, hj = idx & 3, h = 4 * xcd + hj, b = idx >> 2, bh = b * 32 + h;
  const int sw8 = hj == 3 ? (w & 1) * 8 : 0, rw = (hj == 3 ? 24 : 32) * w;
  u64 hA, hB, t64;
  u32 warm;
  asm volatile("s_load_dwordx2 %0, %4, %5\n\ts_load_dwordx2 %1, %4, %6\n\ts_load_dword %3, %4, 0x40\n\ts_memrealtime %2" : "=&s"(hA), "=&s"(hB), "=&s"(t64), "=&s"(warm)
               : "s"(__builtin_amdgcn_kernarg_segment_ptr()), "s"(16 * hj + sw8), "s"(16 * hj + (sw8 ^ 8)));
  SB();
  const int bb = (b + xcd) & 7;
  int p3r = 5 * xcd + min(xcd, 3) + bb;
  int mc = (b & 6) + ((xcd & 1) ^ 1), mg = ((xcd & 6) + b) & 7;
  const bool tq = hj == 0, to = hj == 2;
  int lim = hj == 1 ? 6 - b : (xcd < 3 ? 6 : 5) - bb;
  asm volatile("" : "+s"(p3r), "+s"(mc), "+s"(mg), "+s"(lim));
  const bool idle = (hj & 1) && lim <= 0;
  u64 tp0, tp1;
  {
    const u32 pitch = (u32)(0x0AC0040018002000ull >> (16 * hj)) & 0xffffu;
    u32 ce = (xcd << (to ? 13 : 4)) + 2 * b, c1 = 16 * mc + 2 * mg;
    asm volatile("" : "+s"(ce), "+s"(c1));
    const u32 col = 64 * ((hj & 1) ? (hj == 1 ? c1 : p3r) : ce);
    const u32 step = pitch << (to ? 1 : 3);
    u32 off = (rw + (to ? (lane & 24) + (lane >> 5) : lane >> 3)) * pitch + col + ((lane & 3) ^ ((lane >> 3) & 2)) * 16;
    tp0 = off;
    tp1 = off + step;
    u64 s2 = 2 * step;
    u32 ld = lbase + L_W + rw * 128;
    asm volatile("" : "+s"(s2), "+s"(ld), "+v"(tp0), "+v"(tp1));
    asm volatile("s_mov_b32 exec_lo, 0xf0f0f0f0\n\ts_mov_b32 exec_hi, 0xf0f0f0f0\n\ts_waitcnt lgkmcnt(0)\n\tv_lshl_add_u64 %0, %0, 0, %3\n\tv_lshl_add_u64 %1, %1, 0, %2\n\t"
                 "s_not_b64 exec, exec\n\tv_lshl_add_u64 %0, %0, 0, %2\n\tv_lshl_add_u64 %1, %1, 0, %3\n\ts_mov_b64 exec, -1"
                 : "+v"(tp0), "+v"(tp1), "+s"(hA), "+s"(hB), "+s"(t64), "+s"(warm) :: "scc");
    if (!idle) {
      u32 keep;
      asm volatile("s_mov_b32 %0, m0\n\ts_mov_b32 m0, %3\n\ts_nop 0\n\tglobal_load_lds_dwordx4 %1, off nt\n\ts_add_u32 m0, m0, 0x400\n\t"
                   "v_lshl_add_u64 %1, %1, 0, %4\n\tglobal_load_lds_dwordx4 %2, off nt\n\ts_mov_b32 m0, %0\n\tv_lshl_add_u64 %2, %2, 0, %4"
                   : "=&s"(keep), "+v"(tp0), "+v"(tp1) : "s"(ld), "s"(s2) : "memory", "scc");
      glds((const void*)tp0, ld + 2048);
      if (hj != 3) glds((const void*)tp1, ld + 3072);
    }
  }
  SB();
  u32 t0 = (u32)t64;
  const u16 *x = a.x, *kc = a.kc, *vc = a.vc, *wd = a.wd, *cc = a.cc;
  u16* out = a.out;
  unsigned char* ws = a.ws;
  if (w == 0) STAMP(24);
  t0 += (u32)(KW * max((int)((u32)__builtin_amdgcn_s_memrealtime() - t0) - KA, 0)) / 4u;
  const int p3 = (hj == 3 && !idle) ? p3r : -1;
  const bool tm = hj == 1 && !idle;
  u32 tag;
  TAG();
  const __amdgpu_buffer_rsrc_t rs = __builtin_amdgcn_make_buffer_rsrc((void*)(ws + (size_t)(tag & ROTM) * WS_STEP), 0, WS_END, 0x00020000);
  if (idle && hj == 1) {
    if (w == 0) {
      st_sc1(rs, WS_G1 + ((b & 1) * 8 + xcd) * 1024 + lane * 16, u32x4{~0u, ~0u, ~0u, ~0u});
      VM0();
      if (lane == 0) st_sc1_64(rs, WS_TS + ((b & 1) * 8 + xcd) * 8, u32x2{t0, tag});
    } else {
      u64 pc;
      u32 len;
      asm volatile("s_getpc_b64 %0\n1:\n\ts_mov_b32 %1, fused_code_end - 1b - 16" : "=s"(pc), "=s"(len));
      const u32 o = ((b & 1) * 15 + w - 1) * 1024;
      if (o < len) touch((const void*)(pc & ~15ull), min(o + lane * 16, len & ~15u), lbase + L_SINK);
    }
  }
  if (TOUCH && w == TW) {
    const unsigned char* tp = (const unsigned char*)((lane & 1) ? vc : kc) + ((size_t)bh * SK + (SK - WIN)) * 256 + ((lane >> 1) & TGM) * 16384;
    if (TOUCH > 1) {
      if (lane == 32) tp = ws + (size_t)(tag & ROTM) * WS_STEP;
      if (lane == 33) tp = (const unsigned char*)out;
      if (lane == 34) tp = (const unsigned char*)x;
      if (lane == 35) tp = (const unsigned char*)wd + (p3 >= 0 ? p3 * 32768 : 0);
    }
    glds(tp, lbase + L_SINK);
  }
  int ta = TAU;
  int n = lane & 15, g = lane >> 4;
  u32x4 cv, hb;
  const bool row = tm && (w >= 8 || mc == 1);
  if (row) cv = ldnt((w < 8 ? x : cc) + (w & 7) * 512 + lane * 8);
  if (tid < 16) ((volatile LAS u32*)(L + L_CTL))[tid] = tid == C_T0 ? t0 : 0u;
  BAR();
  t0 = __builtin_amdgcn_readfirstlane(((volatile LAS u32*)(L + L_CTL))[C_T0]);
  u32 due = t0 * 4u + (u32)(PHASE * TAU) / 32u + DUE0;
  int rot = ROTF;
#define RO(I) (((XR ? (I) ^ rot : (I) + rot) & 15) * 8192)
  const u16* kp = kc + ((size_t)bh * SK + (SK - WIN) + 4 * w) * 128;
  u32 l16 = lane * 16;
  PIN(l16);
#define LDR(D, P, I, W) { const GAS unsigned char* p_ = (const GAS unsigned char*)((P) + RO(I)); asm volatile("" : "+s"(p_)); W; D = __builtin_nontemporal_load((const GAS u32x4*)(p_ + l16)); SB(); }
  u32x4 kr[16], vr[16];
  volatile LAS u32* ctl = (volatile LAS u32*)(L + L_CTL);
#define RELANE() do { asm volatile("" : "+v"(lane)); n = lane & 15; g = lane >> 4; } while (0)
  const int e = b * 16 + 2 * xcd + hj;
  const bool red = hj < 2;

#define KL(I) { const int t_ = ta + extra(I); LDR(kr[I], kp, I, PACE(t_)) LT(I, t_) }
  KL(0)
  if (row) {
    if (w >= 8) {
#pragma unroll
      for (int j = 0; j < 4; ++j) { const float a0 = blo(cv[j]), a1 = bhi(cv[j]); hb[j] = pack2(a0 * rcp(1.f + __expf(-a0)), a1 * rcp(1.f + __expf(-a1))); }
      *(LAS u32x4*)(L + L_H2 + (w & 7) * H2S + lane * 16) = hb;
      lds_add((LAS u32*)(L + L_CTL) + C_R, lane);
    } else {
      float m, inv;
      ln_stats(cv, m, inv);
      if ((lane >> 3) == mg) {
        LAS f32x4* d = (LAS f32x4*)(L + L_RED + w * 256 + (lane & 7) * 32);
        d[0] = f32x4{rbf(rbf(blo(cv[0]) - m) * inv), rbf(rbf(bhi(cv[0]) - m) * inv), rbf(rbf(blo(cv[1]) - m) * inv), rbf(rbf(bhi(cv[1]) - m) * inv)};
        d[1] = f32x4{rbf(rbf(blo(cv[2]) - m) * inv), rbf(rbf(bhi(cv[2]) - m) * inv), rbf(rbf(blo(cv[3]) - m) * inv), rbf(rbf(bhi(cv[3]) - m) * inv)};
      }
    }
  }
  KL(1)
  u32x2 tsv;
#define TS_WAIT() if (!__all(tsv[1] == tag)) { _Pragma("unroll 1") for (u32 s = 0; s < SPIN_CAP; ++s) { __builtin_amdgcn_s_sleep(16); MEMB(); \
      tsv = ld_sc1_64(rs, WS_TS + (lane & 15) * 8); if (__all(tsv[1] == tag)) break; } } MEMB();
  if (tm) {
    asm volatile("s_waitcnt vmcnt(2)" ::: "memory");
    lds_wait(ctl + C_R, 8u);
    hb = *(LAS u32x4*)(L + L_H2 + (n & 7) * H2S + (32 * w + 8 * g) * 2);
    tile_part(L + L_W + w * 4096, L + L_TP + w * 2048 + lane * 16, hb, n, g);
    lds_add((LAS u32*)(L + L_CTL) + C_T, lane);
  }
  KL(2)
  if (w == 0 || (tm && w >= 14)) tsv = ld_sc1_64(rs, WS_TS + (lane & 15) * 8);
  SB();
  if (tm && w >= 14) {
    lds_wait(ctl + C_T, 16u);
    const int col = 32 * (w & 1) + 16 * (n >> 3) + 4 * g;
    const f32x4 s = tile_sum(L + L_TP + (w & 1) * 1024 + lane * 16);
    float v[4] = {rbf(s[0]), rbf(s[1]), rbf(s[2]), rbf(s[3])};
    if (mc == 1) {
      const f32x4 ln = *(LAS f32x4*)(L + L_RED + (n & 7) * 256 + col * 4);
#pragma unroll
      for (int j = 0; j < 4; ++j) v[j] = ln[j] * rbf(1.f + v[j]);
    }
    TS_WAIT()
    st_sc1_64(rs, mc < 2 ? WS_G1 + (2 * mg + (w & 1)) * 1024 + ((2 * (n >> 3) + (g >> 1)) * 16 + mc * 8 + (n & 7)) * 16 + (g & 1) * 8
                         : WS_MODR + ((mc - 2) * 8 + (n & 7)) * 1024 + mpos(64 * mg + col),
              u32x2{canon(pack2(v[0], v[1])), canon(pack2(v[2], v[3]))});
    if (w == 15) STAMP(3);
  }
  RELANE();
  KL(3) KL(4)
  if (w == 0) {
    TS_WAIT()
    const float d = (float)(int)(tsv[0] - t0);
    ctl[C_TB] = (t0 * 4u + (u32)(int)fmaxf(row_sum(d) * 0.25f, row_max(d) * 4.f - SKW)) | 1u;
  }
  KL(5) KL(6)
  u32 org = __builtin_amdgcn_readfirstlane(ctl[C_TB]);
  if (org && REB) { const int adj = ((int)(org - t0 * 4u - (p3 >= 0 ? bb * MSH : 0)) * 85 + 1024) >> 11; ta += adj; }
  KL(7)
  if (tm && mc >= 2 && w >= 14 && lane == 0) __builtin_amdgcn_raw_buffer_store_b32(tag, rs, WS_FLAGS + (((mc - 2) * 8 + mg) * 2 + (w & 1)) * 4, 0, SC1);
  KL(8)
  u32x4 g0;
  const int o0 = WS_G1 + w * 1024 + lane * 16;
  if (tq) lds_wait(ctl + C_TB, 1u);
#define KG(K) { KL(K) if (tq && (w & 1) == ((K) & 1)) { until_s(due - TAU / 2); \
    asm volatile("buffer_load_dwordx4 %0, %1, %2, 0 offen sc1" : "=&v"(g0) : "v"(o0), "s"(rs) : "memory"); } }
  KG(9) KG(10)
  KL(11) KL(12)
  if (tq) {
    asm volatile("s_waitcnt vmcnt(2)" : "+v"(g0));
#define G1_OK() __all(g0[0] != ~0u && g0[1] != ~0u && g0[2] != ~0u && g0[3] != ~0u)
    if (!G1_OK()) {
#pragma unroll 1
      for (u32 s = 0; s < SPIN_CAP; ++s) {
        __builtin_amdgcn_s_sleep(16);
        MEMB();
        g0 = ld_sc1(rs, o0);
        if (G1_OK()) break;
      }
    }
    if (w == 15) STAMP(4);
#pragma unroll
    for (int j = 0; j < 4; ++j) { const u32 o = dppu<ROR8>(g0[j]); hb[j] = pack2(blo(g0[j]) + blo(o), bhi(g0[j]) + bhi(o)); }
    PIN(hb);
  }
  KL(13)
  if (tq) {
    tile_part(L + L_W + w * 4096, L + L_TP + w * 2048 + lane * 16, hb, n, g);
    lds_add((LAS u32*)(L + L_CTL) + C_T, lane);
  }
  KL(14)
  if (tq && w >= 14) {
    lds_wait(ctl + C_T, 16u);
    const f32x4 s = tile_sum(L + L_TP + (w & 1) * 1024 + lane * 16);
    st_sc1(rs, WS_Q + ((n & 7) * 32 + 4 * xcd + (b >> 1)) * 512 + ((b & 1) * 64 + 32 * (w & 1) + 16 * (n >> 3) + 4 * g) * 4, u32x4{pack2(s[0], s[1]), tag, pack2(s[2], s[3]), tag});
    if (w == 15) STAMP(17);
  }
  RELANE();
  KL(15)
  asm volatile("" : "+s"(rot));
  const u16* vp = vc + ((size_t)bh * SK + (SK - WIN) + 4 * w) * 128;
#define VL(I) { const int t_ = ta + extra(16 + (I)); LDR(vr[I], vp, I, PACE(t_)) LT(16 + (I), t_) }
#define VLN(I) { const int t_ = ta + extra(16 + (I)); LDR(vr[I], vp, I, PACEN(t_)) LT(16 + (I), t_) }
  VL(0) VL(1)
  VL(2)
  u32x2 gq;
#define FL_WAIT() if (!__all(fl == tag)) {   _Pragma("unroll 1") for (u32 s = 0; s < SPIN_CAP; ++s) { __builtin_amdgcn_s_sleep(4); MEMB(); \
      fl = __builtin_amdgcn_raw_buffer_load_b32(rs, WS_FLAGS + lane * 4, 0, SC1); if (__all(fl == tag)) break; } } MEMB();
  u32 fl;
  if (w == WG_) {
    gq = ld_sc1_64(rs, WS_Q + bh * 512 + lane * 8);
    fl = __builtin_amdgcn_raw_buffer_load_b32(rs, p3 >= 0 ? WS_FLAGS + lane * 4 : WS_END, 0, SC1);
  }
  SB();
  VLN(3) VLN(4)
  if (w == WG_) {
    if (!__all(gq[1] == tag)) {
#pragma unroll 1
      for (u32 s = 0; s < SPIN_CAP; ++s) {
        __builtin_amdgcn_s_sleep(4);
        MEMB();
        gq = ld_sc1_64(rs, WS_Q + bh * 512 + lane * 8);
        if (__all(gq[1] == tag)) break;
      }
    }
    *(LAS u32*)(L + L_Q + lane * 4) = gq[0];
    PIN(fl);
    if (p3 >= 0) { FL_WAIT() }
    LGKM0();
    ctl[C_Q] = 1u;
    STAMP(5);
  }
  RELANE();
  VL(5)
  lds_wait(ctl + C_Q, 1u);
  const bool prer = p3 >= 0 && w < 8;
  u32x4 pre0, pre1;
  if (prer) {
    pre0 = ld_sc1(rs, WS_MODR + (8 + w) * 1024 + lane * 16);
    pre1 = ld_sc1(rs, WS_MODR + (16 + w) * 1024 + lane * 16);
  }
  SB();
  const u32x4 qf = *(LAS u32x4*)(L + L_Q + n * 16);
  if (w == 1) STAMP(6);

#define DOT(I) dot2(kr[I][3], qf[3], dot2(kr[I][2], qf[2], dot2(kr[I][1], qf[1], dot2(kr[I][0], qf[0], 0.f))))
#define TR(LO, HI, CTRL, BIT) ((n & (BIT)) ? (HI) + dppf<CTRL>(HI) : (LO) + dppf<CTRL>(LO))
  float xs[8];
#define QK(C) { _Pragma("unroll") for (int i = 2 * (C); i < 2 * (C) + 2; ++i) { const float lo_ = DOT(i), hi_ = DOT(i + 8); \
      const float t1_ = lo_ + dppf<ROR8>(lo_), t2_ = hi_ + dppf<ROR8>(hi_); xs[i] = (n & 8) ? t2_ : t1_; PIN(xs[i]); } SB(); }
  QK(0) VLN(6) QK(1) VLN(7) QK(2) VL(8) QK(3) VL(9)
  if (prer) {
    const u32 a_h2w = w * C1S + lane * 8;
    *(LAS u32x2*)(L + L_C1 + a_h2w) = u32x2{pk2(up2(pre1[0]) + 1.f), pk2(up2(pre1[1]) + 1.f)};
    *(LAS u32x2*)(L + L_C1 + a_h2w + 512) = u32x2{pk2(up2(pre1[2]) + 1.f), pk2(up2(pre1[3]) + 1.f)};
    *(LAS u32x2*)(L + L_C1 + 8 * C1S + a_h2w) = u32x2{pre0[0], pre0[1]};
    *(LAS u32x2*)(L + L_C1 + 8 * C1S + a_h2w + 512) = u32x2{pre0[2], pre0[3]};
  }
#define TR_STEP(HALF, CTRL, BIT) _Pragma("unroll") for (int hh = 0; hh < (HALF); ++hh) { \
    const float lo_ = xs[hh], hi_ = xs[hh + (HALF)]; const float t1_ = lo_ + dppf<CTRL>(lo_), t2_ = hi_ + dppf<CTRL>(hi_); xs[hh] = (n & (BIT)) ? t2_ : t1_; }
  TR_STEP(4, XOR7, 4) TR_STEP(2, XOR2, 2) TR_STEP(1, XOR1, 1)
#undef TR_STEP
  float sc = rbf(rbf(xs[0]) * 0.08838834764831845f);
  PIN(sc);
  SB();
  VL(10)
  {
    const float mw_ = wave_max(sc);
    const float lw = wave_sum(__expf(sc - mw_));
    if (lane == 0) *(LAS f32x2*)(L + L_ML + w * 8) = f32x2{mw_, lw};
    lds_add((LAS u32*)(L + L_CTL) + C_ML, lane);
  }
  SB();
  VL(11)
  RELANE();
  u32 mk[4];
#pragma unroll
  for (int j = 0; j < 4; ++j) mk[j] = (n >> 1) == j ? ((n & 1) ? 0xffff0000u : 0x0000ffffu) : 0u;
  f32x4 oa = {0.f, 0.f, 0.f, 0.f};
#define PV(I) { const u32 pb_ = dppu<0x150 + (I)>(pd); oa = mfma(vr[I], u32x4{pb_ & mk[0], pb_ & mk[1], pb_ & mk[2], pb_ & mk[3]}, oa); asm volatile("" : "+v"(oa)); SB(); }
  PIN(mk[0]); PIN(mk[1]); PIN(mk[2]); PIN(mk[3]);
  u32 a_oa = L_RED + w * OSTR + (g * 8 + n) * 16;
  u32 ra = L_RED + (lane >> 5) * 8 * OSTR + (lane & 31) * 16;
  u32 rb_ = WS_O + (h * 8 + b) * 512 + (lane & 31) * 16 + (lane >> 5) * 8;
  u32 tagv = tag;
  PIN(a_oa); PIN(ra); PIN(rb_); PIN(tagv);
  org = __builtin_amdgcn_readfirstlane(ctl[C_TB]);
  org = org ? org + TEND : t0 * 4u;
  asm volatile("" : "+s"(org));
  VL(12)
  lds_wait(ctl + C_ML, 16u);
  u32 pd;
  {
    const f32x2 ml = *(LAS f32x2*)(L + L_ML + n * 8);
    const float M = row_max(ml[0]);
    const float pe = __expf(sc - M) * rcp(row_sum(__expf(ml[0] - M) * ml[1]));
    pd = pack2(pe, pe);
    PIN(pd);
  }
  SB();
  VL(13)
  PV(0) PV(1) PV(2) PV(3) PV(4)
  VL(14)
  PV(5) PV(6) PV(7) PV(8) PV(9)
  VL(15)
  PV(10) PV(11) PV(12) PV(13) PV(14)
  if (w == 13) __builtin_amdgcn_s_setprio(3);
  PV(15)
#undef PV
  if (n < 8) *(LAS f32x4*)(L + a_oa) = oa;
  BAR();
  if (w == 0) STAMP(8);
  if (w == 14 && lane == 0) st_sc1_64(rs, WS_B0 + bid * 8, u32x2{now4(), tagv});
  if (w == 13) {
    f32x4 s[8];
#pragma unroll
    for (int ww = 0; ww < 8; ++ww) s[ww] = *(LAS f32x4*)(L + ra + ww * OSTR);
    SB();
    const f32x4 t = ((s[0] + s[1]) + (s[2] + s[3])) + ((s[4] + s[5]) + (s[6] + s[7]));
    st_sc1_64(rs, rb_, u32x2{pack2(t32(t[0], t[2]), t32(t[1], t[3])), tagv});
    STAMP(9);
  }
  if (to) {
    u32x4 wf[16];
    if (w < 4) {
      const u32 af = L_W + (8 * g + (n >> 2)) * 128 + (w ^ ((n >> 3) | ((g & 1) << 1))) * 32 + (n & 3) * 8;
#pragma unroll
      for (int ks = 0; ks < 16; ++ks) wf[ks] = trfrag(L + af + ks * 4096, L + af + ks * 4096 + 512);
      __builtin_amdgcn_s_setprio(3);
    }
    int oo = WS_O + xcd * 16384 + w * 1024 + lane * 16;
    u32 a_ow = L_TP + (2 * w + (lane >> 5)) * OBS + (lane & 31) * 8;
    u32 a_or = L_TP + ((n >> 3) * 16 + (n & 7)) * OBS + g * 16;
    int rc = n < 8 ? WS_PART + ((n * 16 + 2 * b + ((w >> 1) & 1)) * 8 + xcd) * 128 + (w & 1) * 64 + g * 16 : WS_END;
    PIN(oo); PIN(a_ow); PIN(a_or); PIN(rc);
    SEL4();
    until(org + OFO);
#pragma unroll 1
    for (u32 s = 0; s < SPIN_CAP; ++s) {
      const u32x4 og = ld_sc1(rs, oo);
      *(LAS u32x2*)(L + a_ow) = u32x2{og[0], og[2]};
      if (__builtin_expect(__all(((og[1] ^ tag) | (og[3] ^ tag)) == 0u), 1)) break;
      MEMB();
      __builtin_amdgcn_s_sleep(2);
    }
    BAR();
    if (w == 7) STAMP(2);
    if (w < 4) {
      u32x4 ob[8];
#pragma unroll
      for (int i = 0; i < 8; ++i) ob[i] = *(LAS u32x4*)(L + a_or + (i >> 2) * 8 * OBS + (i & 3) * 64);
      SB();
      f32x4 aL = {0.f, 0.f, 0.f, 0.f}, aH = {0.f, 0.f, 0.f, 0.f};
#pragma unroll
      for (int i = 0; i < 8; ++i) { aL = mfma(wf[i], ob[i], aL); aH = mfma(wf[8 + i], ob[i], aH); }
      const f32x4 v = {aL[0] + dppf<ROR8>(aH[0]), aL[1] + dppf<ROR8>(aH[1]), aL[2] + dppf<ROR8>(aH[2]), aL[3] + dppf<ROR8>(aH[3])};
      st_sc1(rs, rc, tag8(v, tagv, sel));
      if (w == 3) STAMP(16);
    }
  }

  const int bo = lane * 16;
  if (p3 >= 0) {
    u32 a_h2w = w * C1S + lane * 8;
    if (w == 8 && lane < 36) *(LAS u32*)(L + (lane < 32 ? L_ML : L_HH + 640) + lane * 4) = 0u;
    BAR();
    glds((const void*)(tp0 + (u32)((368 - 16 * w) * FF * 2)), lbase + L_W + 49152 + w * 1024);
    u32x4 bp, bq;
    if (w == 12) {
      until_s(org + OFB + 20);
      asm volatile("buffer_load_dwordx4 %0, %2, %3, 0 offen sc1\n\tbuffer_load_dwordx4 %1, %2, %3, 0 offen offset:1024 sc1" : "=&v"(bp), "=&v"(bq) : "v"(WS_B0 + bo), "s"(rs) : "memory");
    }
    BAR();
#define DL(R) glds(wd + (size_t)(32 * p3 + (R)) * 512, (lane ^ ((((R) & 3) | (((R) >> 1) & 4)) << 1)) * 16, lbase + L_W + 65536 + (R) * 1024)
    if (w < 8) { DL(3 * w); DL(3 * w + 1); DL(3 * w + 2); }
    if (w >= 12) { DL(2 * w); DL(2 * w + 1); }
    SB();
    u32 a_h2r = L_H2 + (n & 7) * H2S + (n >> 3) * 512 + g * 16;
    u32 a_hw = L_HH + (n < 8 ? n * 64 + (8 * (w & 3) + 4 * (g & 1) + 2 * (g >> 1)) * 2 : 512 + lane * 4);
    const u32x4 t16v = {tag & 0xffffu, tag >> 16, tag & 0xffffu, tag >> 16};
#define X_TIME() until_s(org + OFO + DX - 40); u32 tx = 2u; \
      _Pragma("unroll 1") for (u32 s = 0; s < LSPIN_CAP && tx == 2u; ++s) { tx = __builtin_amdgcn_readfirstlane(ctl[C_TX]); if (tx == 2u) __builtin_amdgcn_s_sleep(2); } \
      if (!(tx & 1u)) tx = org + OFO + DX;
#define X_BAD(A, B) ((or4(A) | or4(B)) & 0xffffu)
    if (w < 8) {
      const u32x2 ca = *(LAS u32x2*)(L + L_C1 + a_h2w), cb = *(LAS u32x2*)(L + L_C1 + a_h2w + 512);
      f32x2 c1[4] = {up2(ca[0]), up2(ca[1]), up2(cb[0]), up2(cb[1])};
      a_h2w = L_H2 + w * H2S + lane * 8;
      int xo = WS_X2 + xcd * 16384 + w * 2048 + lane * 16;
      u32 a_hr = L_HH + (n < 8 ? n * 64 + g * 16 : 768), a_hs = L_HH + (n < 8 ? 768 : (n & 7) * 64 + g * 16);
      PIN(a_hs);
      SEL4();
      const u32 a_d0 = L_W + 65536 + (8 * g + (n >> 2)) * 1024 + ((8 * w + ((n >> 1) & 1)) ^ (((n >> 2) | ((g & 1) << 2)) << 1)) * 16 + (n & 1) * 8;
      int yo = WS_YP + ((16 * w + 4 * (n >> 3) + g) * NP3 + p3) * 128 + (n & 7) * 16, yo2 = yo + 8 * NP3 * 128;
      PIN(xo); PIN(c1[0]); PIN(c1[1]); PIN(c1[2]); PIN(c1[3]); PIN(a_h2w); PIN(a_hr); PIN(yo); PIN(yo2);
      u32 a_cx = lbase + L_CTL + (C_X + w) * 4;
      PIN(a_cx);
      asm volatile("s_waitcnt vmcnt(3)" ::: "memory");
      BAR();
      if (w == 0) STAMP(20);
      __builtin_amdgcn_s_setprio(2);
      X_TIME()
      until(tx);
#pragma unroll 1
      for (u32 s = 0; s < SPIN_CAP; ++s) {
        const u32x4 xa = ld_sc1(rs, xo) ^ t16v, xb = ld_sc1(rs, xo + 1024) ^ t16v;
        const float x0 = __builtin_bit_cast(float, __builtin_amdgcn_readfirstlane(xa[0]));
        *(LAS u32x2*)(L + a_h2w) = u32x2{pk2((f32x2{asf(xa[0]), asf(xa[1])} - x0) * c1[0]), pk2((f32x2{asf(xa[2]), asf(xa[3])} - x0) * c1[1])};
        *(LAS u32x2*)(L + a_h2w + 512) = u32x2{pk2((f32x2{asf(xb[0]), asf(xb[1])} - x0) * c1[2]), pk2((f32x2{asf(xb[2]), asf(xb[3])} - x0) * c1[3])};
        if (__builtin_expect(__all(X_BAD(xa, xb) == 0u), 1)) break;
        MEMB();
      }
      asm volatile("s_mov_b64 exec, 1\n\tds_write_b32 %0, %0\n\ts_mov_b64 exec, -1" :: "v"(a_cx) : "memory");
      if (w == 0) STAMP(13);
      __builtin_amdgcn_s_setprio(0);
      lds_add((LAS u32*)(L + L_CTL) + C_R, lane);
      lds_wait(ctl + C_R, 12u);
      u32x4 df[4];
#pragma unroll
      for (int i = 0; i < 4; ++i) { df[i] = trfrag(L + (a_d0 ^ (32 * i)), L + (a_d0 ^ (32 * i)) + 4096); PIN(df[i]); }
      BAR();
      const u32x4 h0 = *(LAS u32x4*)(L + a_hr), h1 = *(LAS u32x4*)(L + a_hs);
      const f32x4 z = {0.f, 0.f, 0.f, 0.f};
      const f32x4 a0 = mfma(df[1], h1, mfma(df[0], h0, z)), a1 = mfma(df[3], h1, mfma(df[2], h0, z));
      st_sc1(rs, yo, tag8(a0, tagv, sel));
      st_sc1(rs, yo2, tag8(a1, tagv, sel));
    } else if (w < 12) {
      u32 a_st = L_ML + (n & 7) * 16;
      PIN(a_st);
      __builtin_amdgcn_s_setprio(3);
      u32x4 gf[16], gb_[8];
      const u32 af = L_W + (8 * g + (n >> 2)) * 128 + (((w & 3) + (n & 2) * 2) ^ (((n >> 3) | ((g & 1) << 1)) << 1)) * 16 + (n & 1) * 8;
      f32x4 aL = {0.f, 0.f, 0.f, 0.f}, aH;
      f32x2 ag, au, sg, su, gt, up;
      {
#pragma unroll
        for (int q4 = 0; q4 < 4; ++q4) {
#pragma unroll
          for (int i = 0; i < 4; ++i) gb_[i] = *(LAS u32x4*)(L + L_C1 + n * C1S + g * 16 + q4 * 256 + i * 64);
          if (q4 == 3) {
            VM0();
            BAR();
          }
#pragma unroll
          for (int i = 0; i < 4; ++i) gf[4 * q4 + i] = trfrag(L + af + (4 * q4 + i) * 4096, L + af + (4 * q4 + i) * 4096 + 512);
          SB();
#pragma unroll
          for (int i = 0; i < 4; ++i) aL = mfma(gf[4 * q4 + i], gb_[i], aL);
          PIN(aL);
        }
        const auto s0 = __builtin_amdgcn_permlane32_swap(__float_as_uint(aL[0]), __float_as_uint(aL[2]), false, false);
        const auto s1 = __builtin_amdgcn_permlane32_swap(__float_as_uint(aL[1]), __float_as_uint(aL[3]), false, false);
        ag = -f32x2{__uint_as_float(s0[0]), __uint_as_float(s1[0])};
        au = -f32x2{__uint_as_float(s0[1]), __uint_as_float(s1[1])};
        sg = f32x2{dppf<ROR8>(ag[0]), dppf<ROR8>(ag[1])} * 1.4426950408889634f;
        su = -f32x2{dppf<ROR8>(au[0]), dppf<ROR8>(au[1])};
        PIN(ag); PIN(au); PIN(sg); PIN(su); PIN(a_h2r); PIN(a_hw);
      }
      if (w == 8) STAMP(21);
      __builtin_amdgcn_s_setprio(0);
      _Pragma("unroll 1") for (u32 s = 0; s < LSPIN_CAP; ++s) if (__all(ctl[C_X + (lane & 7)] != 0u)) break;
      __builtin_amdgcn_s_setprio(3);
      MEMB();
      GU_MUL(L + a_h2r)
      SB();
      f32x4 st = *(volatile LAS f32x4*)(L + a_st);
      gu_pair(aL, aH, gt, up);
      if (__builtin_expect(!__all(st[3] != 0.f), 0)) {
        _Pragma("unroll 1") for (u32 s = 0; s < LSPIN_CAP; ++s) { st = *(volatile LAS f32x4*)(L + a_st); if (__all(st[3] != 0.f)) break; }
      }
      gt = __builtin_elementwise_fma(__builtin_elementwise_fma(ag, f32x2{st[0], st[0]}, gt), f32x2{st[2], st[2]}, sg);
      up = __builtin_elementwise_fma(__builtin_elementwise_fma(au, f32x2{st[0], st[0]}, up), f32x2{st[1], st[1]}, su);
      *(LAS u32*)(L + a_hw) = pk2(f32x2{rcp(1.f + __builtin_amdgcn_exp2f(gt[0])), rcp(1.f + __builtin_amdgcn_exp2f(gt[1]))} * up);
      BAR();
    } else {
      int xo = WS_X2 + xcd * 16384 + (w - 12) * 4096 + lane * 16;
      u32 a_st = lbase + L_ML + (2 * (w - 12) + (lane >> 5)) * 16;
      PIN(xo); PIN(a_st);
      asm volatile("s_waitcnt vmcnt(2)" : "+v"(bp), "+v"(bq) :: "memory");
      BAR();
      if (w == 12 && !on_time(tag, org, bp, bq)) {
        ctl[C_TX] = 2u;
        ctl[C_TX] = (anchor(rs, bo, tag, org, bp, bq) + DX) | 1u;
      }
      VM0();
      if (w == 13) STAMP(19);
      lds_add((LAS u32*)(L + L_CTL) + C_R, lane);
      __builtin_amdgcn_s_setprio(1);
      X_TIME()
      until(tx + SDL);
      u32x4 xv[4];
#pragma unroll
      for (int i = 0; i < 4; ++i) xv[i] = ld_sc1(rs, xo + i * 1024);
#pragma unroll
      for (int i = 0; i < 4; ++i) xv[i] ^= t16v;
      if (__builtin_expect(!__all((X_BAD(xv[0], xv[1]) | X_BAD(xv[2], xv[3])) == 0u), 0)) {
#pragma unroll 1
        for (u32 s = 0; s < SPIN_CAP; ++s) {
          MEMB();
#pragma unroll
          for (int i = 0; i < 4; ++i) xv[i] = ld_sc1(rs, xo + i * 1024) ^ t16v;
          if (__all((X_BAD(xv[0], xv[1]) | X_BAD(xv[2], xv[3])) == 0u)) break;
        }
      }
      float sm[2], sq[2], xs0[2];
#pragma unroll
      for (int k = 0; k < 2; ++k) {
        const float x0 = __builtin_bit_cast(float, __builtin_amdgcn_readfirstlane(xv[2 * k][0]));
        const u32x4 xa = xv[2 * k], xb = xv[2 * k + 1];
        const f32x2 d0 = f32x2{asf(xa[0]), asf(xa[1])} - x0, d1 = f32x2{asf(xa[2]), asf(xa[3])} - x0, d2 = f32x2{asf(xb[0]), asf(xb[1])} - x0, d3 = f32x2{asf(xb[2]), asf(xb[3])} - x0;
        const f32x2 sv = (d0 + d1) + (d2 + d3), qv = __builtin_elementwise_fma(d0, d0, d1 * d1) + __builtin_elementwise_fma(d2, d2, d3 * d3);
        xs0[k] = x0;
        sm[k] = sv[0] + sv[1];
        sq[k] = qv[0] + qv[1];
      }
      const float r = row_sum(t32(t16(sm[0], sq[0]), t16(sm[1], sq[1]))) * (1.f / 512.f);
      const auto sw = __builtin_amdgcn_permlane16_swap(__float_as_uint(r), __float_as_uint(r), false, false);
      const float md = __uint_as_float(sw[0]);
      const float x0 = lane < 32 ? xs0[0] : xs0[1];
      const float inv = rcp(rbf(__builtin_amdgcn_sqrtf(rbf(rbf(fmaxf(fmaf(-md, md, __uint_as_float(sw[1])), 0.f)) + 1e-5f))));
      const f32x4 st = {rbf(x0 + md) - x0, inv, inv * -1.4426950408889634f, 1.f};
      asm volatile("s_mov_b32 exec_lo, 1\n\ts_mov_b32 exec_hi, 1\n\tds_write_b128 %0, %1\n\ts_mov_b64 exec, -1" :: "v"(a_st), "v"(st) : "memory");
      if (w == 15) {
        _Pragma("unroll 1") for (u32 s = 0; s < LSPIN_CAP; ++s) if (__all(ctl[C_X + (lane & 7)] != 0u)) break;
        if (lane < 8) st_sc1_64(rs, WS_B1 + lane * 512 + p3 * 8, u32x2{now4(), tagv});
      }
      BAR();
    }
    if (w == 7) STAMP(14);
    asm volatile("fused_code_end:");
  } else if (red && (w == RW || w == RW - 1)) {
    const int f = 16 * xcd + 2 * b + hj, ro_ = 4 * (w & 1) + (lane & 3);
    const int rb = e >> 4, rj = e & 15, c2 = 32 * rj + 4 * (lane & 7) + g;
    fl = __builtin_amdgcn_raw_buffer_load_b32(rs, WS_FLAGS + lane * 4, 0, SC1);
    float xx = blo(x[rb * 512 + c2]);
    FL_WAIT()
    float ga = blo(__builtin_amdgcn_raw_buffer_load_b16(rs, WS_MODR + rb * 1024 + mpos(c2), 0, SC1));
    float gb = blo(__builtin_amdgcn_raw_buffer_load_b16(rs, WS_MODR + (24 + ro_) * 1024 + mpos(4 * f + g), 0, SC1));
    int po = WS_PART + e * 1024 + lane * 16;
    int xo = WS_X2 + (lane >> 3) * 16384 + rb * 2048 + rj * 128 + (lane & 7) * 16;
    int xi = WS_X2 + xcd * 16384 + ro_ * 2048 + (4 * f + g) * 4;
    u32 xt = (g & 1) ? tag >> 16 : tag & 0xffffu;
    const u32x4 tb4 = bytes4(tag);
    u32x4 tb5 = lane < 44 ? tb4 : u32x4{0, 0, 0, 0};
    int yo = WS_YP + (f * NP3 + (lane >> 2)) * 128 + ro_ * 16;
    int y2 = lane < 44 ? yo + 4096 : WS_END;
    u32 oof = (lane & 12) == 0 ? (ro_ * 512 + 4 * f + g) * 2 : 8192;
    __amdgpu_buffer_rsrc_t ro = __builtin_amdgcn_make_buffer_rsrc((void*)out, 0, 8192, 0x00020000);
    asm volatile("" : "+s"(ro));
    PIN(xt); PIN(tb5); PIN(ga); PIN(gb); PIN(xx); PIN(po); PIN(xo); PIN(xi); PIN(yo); PIN(y2); PIN(oof);
    if (w == RW) {
      u32x4 bp, bq;
      until_s(org + OFB);
      B0_LOAD()
      const u32 anc = on_time(tag, org, bp, bq) ? org + OFO : anchor(rs, bo, tag, org, bp, bq);
      STAMP(1);
      until(anc + DP);
      u32x4 xq;
#pragma unroll 1
      for (u32 sp = 0; sp < SPIN_CAP; ++sp) {
        const u32x4 pv = ld_one(rs, po) ^ tb4;
        float t = t32(t16(asf(pv[0]), asf(pv[1])), t16(asf(pv[2]), asf(pv[3])));
        t += dppf<ROR8>(t);
        const u32 xw = __float_as_uint(rbf(fmaf(ga, t, xx))) | xt;
        const auto p1 = __builtin_amdgcn_permlane16_swap(xw, xw, false, false);
        const auto p2 = __builtin_amdgcn_permlane32_swap(p1[0], p1[0], false, false), p3_ = __builtin_amdgcn_permlane32_swap(p1[1], p1[1], false, false);
        xq = u32x4{p2[0], p3_[0], p2[1], p3_[1]};
        PIN(xq);
        if (__builtin_expect(__all((or4(pv) & 255u) == 0u), 1)) break;
        MEMB();
      }
      st_sc1(rs, xo, xq);
      STAMP(11);
      if (bid == 1 && lane == 0) __hip_atomic_store((GAS u32*)ws, tag, RLX_AGENT);
      u32 ty = anc + DYS;
      const int lt = (int)(now4() - anc) - (DX + 20);
      if (lt > 0) {
        int lk = DB1;
        while (lk < DB1 + lt + 240) lk += PB;
        u32x2 bv;
#pragma unroll 1
        for (u32 sp = 0; sp < SPIN_CAP; ++sp) {
          until_s(anc + lk);
          bv = ld_sc1_64(rs, WS_B1 + xcd * 512 + (lane < NP3 ? lane : 0) * 8);
          if (__all(bv[1] == tag)) break;
          MEMB();
          do lk += PB; while ((int)(now4() - anc) > lk);
        }
        ty = org + smax((int)(bv[0] - org)) + DY;
      }
      ctl[C_TX] = ty | 1u;
      STAMP(7);
    }
    lds_wait(ctl + C_TX, 1u);
    until(__builtin_amdgcn_readfirstlane(ctl[C_TX]));
    u32 res;
#pragma unroll 1
    for (u32 sp = 0; sp < SPIN_CAP; ++sp) {
      const u32 x2 = __builtin_amdgcn_raw_buffer_load_b32(rs, xi, 0, SC1) ^ xt;
      const u32x4 y0 = ld_one(rs, yo) ^ tb4, y1 = ld_one(rs, yo + 2048) ^ tb4, y2_ = ld_one(rs, y2) ^ tb5;
      const f32x4 s = (__builtin_bit_cast(f32x4, y0) + __builtin_bit_cast(f32x4, y1)) + __builtin_bit_cast(f32x4, y2_);
      float t = t32(t16(s[0], s[1]), t16(s[2], s[3]));
      t += dppf<ROR8>(t);
      t += dppf<ROR4>(t);
      res = pack2(fmaf(gb, t, asf(x2)), 0.f);
      PIN(res);
      if (__builtin_expect(__all((((or4(y0) | or4(y1) | or4(y2_)) & 255u) | (x2 & 0xffffu)) == 0u), 1)) break;
      MEMB();
    }
    __builtin_amdgcn_raw_buffer_store_b16((u16)res, ro, oof, 0, OAUX);
    if (w == RW) STAMP(18);
  }
}

extern "C" void kernel_launch(void* const* d_in, const int* in_sizes, int n_in, void* d_out, int out_size, void* d_ws, size_t ws_size, hipStream_t stream) {
  (void)hipFuncSetAttribute((const void*)fused, hipFuncAttributeMaxDynamicSharedMemorySize, L_END);
  Args a;
  const int src[4][2] = {{3, 3}, {10, 10}, {4, 4}, {6, 7}};
  for (int r = 0; r < 4; ++r)
    for (int k = 0; k < 2; ++k) a.half[r][k] = (const unsigned char*)d_in[src[r][k]] + (r < 3 && k ? 64 : 0);
  a.x = (const u16*)d_in[0]; a.kc = (const u16*)d_in[1]; a.vc = (const u16*)d_in[2]; a.wd = (const u16*)d_in[8]; a.cc = (const u16*)d_in[9];
  a.out = (u16*)d_out; a.ws = (unsigned char*)d_ws;
  fused<<<256, 1024, L_END, stream>>>(a);
}
```

```cpp
#define DUE0 750
#define LEAK -1
#include <hip/hip_runtime.h>
#include <stdint.h>

typedef unsigned int u32;
typedef unsigned long long u64;
typedef unsigned short u16;
typedef u32 u32x4 __attribute__((ext_vector_type(4)));
typedef u32 u32x2 __attribute__((ext_vector_type(2)));
typedef float f32x4 __attribute__((ext_vector_type(4)));
typedef float f32x2 __attribute__((ext_vector_type(2)));
typedef short s16x4 __attribute__((ext_vector_type(4)));
typedef __bf16 bf16x8 __attribute__((ext_vector_type(8)));
typedef __bf16 bf16x2 __attribute__((ext_vector_type(2)));
#define GAS __attribute__((address_space(1)))
#define LAS __attribute__((address_space(3)))
#define RLX_AGENT __ATOMIC_RELAXED, __HIP_MEMORY_SCOPE_AGENT

constexpr int SK = 4096, WIN = 1024, FF = 1376, NP3 = 43;

constexpr int WS_EPOCH = 0;
constexpr int WS_FLAGS = 0x1000;
constexpr int WS_TS = 0x1800;
constexpr int WS_G1 = 0x2000;
constexpr int WS_B1 = 0x8000;
constexpr int WS_MODR = 0xA000;
constexpr int WS_Q = 0x12000;
constexpr int WS_O = 0x36000;
constexpr int WS_B0 = 0x56000;
constexpr int WS_PART = 0x5E000;
constexpr int WS_YP = WS_PART + 8 * 16 * 8 * 32 * 4;
constexpr int WS_X2 = WS_YP + 128 * NP3 * 128;
constexpr int WS_END = WS_X2 + 8 * 16384;
constexpr int WS_STEP = 2 << 20;
static_assert(WS_END <= WS_STEP, "");

constexpr int L_W = 0;
constexpr int L_TP = 65536;
constexpr int C1S = 1040;
constexpr int L_C1 = 98304;
constexpr int L_SINK = 130048;
constexpr int L_RED = 131072;
constexpr int OSTR = 544, OBS = 288;
constexpr int RW = 12;
constexpr int WG_ = 10;
constexpr int L_H2 = L_RED + 16384;
constexpr int H2S = 1056;
constexpr int L_Q = L_H2 + 8 * H2S;
constexpr int L_OB = L_Q + 256;
constexpr int L_ML = L_OB + 256;
constexpr int L_HH = L_ML + 128;
constexpr int L_CTL = L_HH + 784;
constexpr int L_END = L_CTL + 64;
constexpr int C_T = 0, C_TB = 1, C_TX = 2, C_Q = 3, C_ML = 4, C_R = 6, C_T0 = 7, C_X = 8;

constexpr u32 SPIN_CAP = 1u << 17, LSPIN_CAP = 1u << 20;
#define TAU 220
#define TX 0
#define DB 0
#define SMASK 0xffffffffu
#define CPF 1
#define CODEKB 27
#define EXACT 0
#define A1 0
#define B1_ 8
#define E1 16
#define A2 14
#define B2_ 22
#define E2 10
#define A3 25
#define B3_ 28
#define E3 0
constexpr int extra(int i) { return (i >= A1 && i < B1_ ? E1 : 0) + (i >= A2 && i < B2_ ? E2 : 0) + (i >= A3 && i < B3_ ? E3 : 0); }
constexpr int TEND = DUE0 + 32 * TAU + (B1_ - A1) * E1 + (B2_ - A2) * E2 + (B3_ - A3) * E3;
#define OFO 425
#define OFB 470
#define WDE 0
#define MLA 4
#define TOUCH 1
#define SKW 200
#define PHASE (4 * bb + hj)
#define ROTF idx
#define XR 0
#define KW 6
#define KA 55
#define XCP 1
#define B0C 0
#define DYS 1125
#define ROTM 63
#define DRY 1
#define SDL 12
#define MSH 14
#define TW 1
#define TGM 0
#define PB 200
#define RB 260
#define DO 30
#define DL_ 220
#define DP 350
#define DX 565
#define DB1 960
#define DY 260
#define REB 1

__device__ __forceinline__ float rbf(float f) { return (float)(__bf16)f; }
__device__ __forceinline__ u32 pack2(float a, float b) { bf16x2 v = {(__bf16)a, (__bf16)b}; return __builtin_bit_cast(u32, v); }
__device__ __forceinline__ float blo(u32 v) { return __builtin_bit_cast(float, v << 16); }
__device__ __forceinline__ float bhi(u32 v) { return __builtin_bit_cast(float, v & 0xffff0000u); }
__device__ __forceinline__ u32 canon(u32 v) { return v < 0xfffffffeu ? v : 0xfffffffeu; }
#define SEL4() u32x4 sel = {0x07060500u, 0x07060501u, 0x07060502u, 0x07060503u}; asm volatile("" : "+s"(sel))
__device__ __forceinline__ u32x4 tag8(f32x4 v, u32 tagv, u32x4 sel) {
  const u32x4 r = __builtin_bit_cast(u32x4, v);
  return u32x4{__builtin_amdgcn_perm(r[0], tagv, sel[0]), __builtin_amdgcn_perm(r[1], tagv, sel[1]), __builtin_amdgcn_perm(r[2], tagv, sel[2]), __builtin_amdgcn_perm(r[3], tagv, sel[3])};
}
__device__ __forceinline__ u32x4 bytes4(u32 tag) { return u32x4{tag & 255u, (tag >> 8) & 255u, (tag >> 16) & 255u, tag >> 24}; }
__device__ __forceinline__ u32 or4(u32x4 v) { return (v[0] | v[1]) | (v[2] | v[3]); }
__device__ __forceinline__ float rcp(float x) { return __builtin_amdgcn_rcpf(x); }
template <int CTRL> __device__ __forceinline__ float dppf(float x) {
  return __builtin_bit_cast(float, __builtin_amdgcn_mov_dpp(__builtin_bit_cast(int, x), CTRL, 0xf, 0xf, true));
}
template <int CTRL> __device__ __forceinline__ u32 dppu(u32 x) { return (u32)__builtin_amdgcn_mov_dpp((int)x, CTRL, 0xf, 0xf, true); }
constexpr int ROR8 = 0x128, ROR4 = 0x124, ROR2 = 0x122, ROR1 = 0x121, XOR1 = 0xB1, XOR2 = 0x4E, XOR7 = 0x141;
__device__ __forceinline__ float row_sum(float x) { x += dppf<ROR8>(x); x += dppf<ROR4>(x); x += dppf<ROR2>(x); x += dppf<ROR1>(x); return x; }
__device__ __forceinline__ float row_max(float x) {
  x = fmaxf(x, dppf<ROR8>(x)); x = fmaxf(x, dppf<ROR4>(x)); x = fmaxf(x, dppf<ROR2>(x)); x = fmaxf(x, dppf<ROR1>(x)); return x;
}
__device__ __forceinline__ float x16_sum(float x) {
  auto s = __builtin_amdgcn_permlane16_swap(__float_as_uint(x), __float_as_uint(x), false, false);
  return __uint_as_float(s[0]) + __uint_as_float(s[1]);
}
__device__ __forceinline__ float x32_sum(float x) {
  auto s = __builtin_amdgcn_permlane32_swap(__float_as_uint(x), __float_as_uint(x), false, false);
  return __uint_as_float(s[0]) + __uint_as_float(s[1]);
}
__device__ __forceinline__ float x16_max(float x) {
  auto s = __builtin_amdgcn_permlane16_swap(__float_as_uint(x), __float_as_uint(x), false, false);
  return fmaxf(__uint_as_float(s[0]), __uint_as_float(s[1]));
}
__device__ __forceinline__ float x32_max(float x) {
  auto s = __builtin_amdgcn_permlane32_swap(__float_as_uint(x), __float_as_uint(x), false, false);
  return fmaxf(__uint_as_float(s[0]), __uint_as_float(s[1]));
}
__device__ __forceinline__ float t16(float a, float b) {
  auto s = __builtin_amdgcn_permlane16_swap(__float_as_uint(a), __float_as_uint(b), false, false);
  return __uint_as_float(s[0]) + __uint_as_float(s[1]);
}
__device__ __forceinline__ float t32(float a, float b) {
  auto s = __builtin_amdgcn_permlane32_swap(__float_as_uint(a), __float_as_uint(b), false, false);
  return __uint_as_float(s[0]) + __uint_as_float(s[1]);
}
__device__ __forceinline__ f32x2 up2(u32 v) { return f32x2{blo(v), bhi(v)}; }
__device__ __forceinline__ u32 pk2(f32x2 v) { return pack2(v[0], v[1]); }
#define PIN(x) asm volatile("" : "+v"(x))
__device__ __forceinline__ f32x2 rb2(f32x2 v) { u32 p = pk2(v); asm("" : "+v"(p)); return up2(p); }
__device__ __forceinline__ float wave_sum(float x) { return x32_sum(x16_sum(row_sum(x))); }
__device__ __forceinline__ float wave_max(float x) { return x32_max(x16_max(row_max(x))); }
__device__ __forceinline__ float dot2(u32 a, u32 b, float c) {
  return __builtin_amdgcn_fdot2_f32_bf16(__builtin_bit_cast(bf16x2, a), __builtin_bit_cast(bf16x2, b), c, false);
}
__device__ __forceinline__ f32x4 mfma(u32x4 a, u32x4 b, f32x4 c) {
  return __builtin_amdgcn_mfma_f32_16x16x32_bf16(__builtin_bit_cast(bf16x8, a), __builtin_bit_cast(bf16x8, b), c, 0, 0, 0);
}
__device__ __forceinline__ u32x4 ldnt(const void* p) { return __builtin_nontemporal_load((const u32x4*)p); }
__device__ __forceinline__ void glds(const void* g, u32 lds) {
  u32 keep;
  lds = __builtin_amdgcn_readfirstlane(lds);
  asm volatile("s_mov_b32 %0, m0\n\ts_mov_b32 m0, %2\n\ts_nop 0\n\tglobal_load_lds_dwordx4 %1, off nt\n\ts_mov_b32 m0, %0"
               : "=&s"(keep) : "v"(g), "s"(lds) : "memory");
}
__device__ __forceinline__ void glds(const void* base, u32 off, u32 lds) {
  u32 keep;
  lds = __builtin_amdgcn_readfirstlane(lds);
  asm volatile("s_mov_b32 %0, m0\n\ts_mov_b32 m0, %3\n\ts_nop 0\n\tglobal_load_lds_dwordx4 %1, %2 nt\n\ts_mov_b32 m0, %0"
               : "=&s"(keep) : "v"(off), "s"(base), "s"(lds) : "memory");
}
__device__ __forceinline__ void touch(const void* base, u32 off, u32 lds) {
  u32 keep;
  lds = __builtin_amdgcn_readfirstlane(lds);
  asm volatile("s_mov_b32 %0, m0\n\ts_mov_b32 m0, %3\n\ts_nop 0\n\tglobal_load_lds_dwordx4 %1, %2\n\ts_mov_b32 m0, %0"
               : "=&s"(keep) : "v"(off), "s"(base), "s"(lds) : "memory");
}
__device__ __forceinline__ u32x4 trfrag(LAS unsigned char* p0, LAS unsigned char* p1) {
  s16x4 a = __builtin_amdgcn_ds_read_tr16_b64_v4i16((LAS s16x4*)p0);
  s16x4 b = __builtin_amdgcn_ds_read_tr16_b64_v4i16((LAS s16x4*)p1);
  u32x2 x = __builtin_bit_cast(u32x2, a), y = __builtin_bit_cast(u32x2, b);
  return u32x4{x[0], x[1], y[0], y[1]};
}
#define SB() __builtin_amdgcn_sched_barrier(0)
#define MEMB() asm volatile("" ::: "memory")
#define VM0() asm volatile("s_waitcnt vmcnt(0)" ::: "memory")
#define LGKM0() asm volatile("s_waitcnt lgkmcnt(0)" ::: "memory")
#define BAR() asm volatile("s_waitcnt lgkmcnt(0)\n\ts_barrier" ::: "memory")
#define TAG() do { u32 ep_; asm volatile("s_load_dword %0, %1, 0x0\n\ts_waitcnt lgkmcnt(0)" : "=s"(ep_) : "s"(ws) : "memory"); \
    tag = ((ep_ & 0x7f7f7f7fu) + 0x01010101u) ^ (ep_ & 0x80808080u); } while (0)
constexpr int SC1 = 16;
#define OAUX 0
#define LD1 18
#define LDA 16
__device__ __forceinline__ u32x4 ld_sc1(__amdgpu_buffer_rsrc_t rs, int off) { return __builtin_amdgcn_raw_buffer_load_b128(rs, off, 0, LDA); }
__device__ __forceinline__ u32x4 ld_one(__amdgpu_buffer_rsrc_t rs, int off) { return __builtin_amdgcn_raw_buffer_load_b128(rs, off, 0, LD1); }
__device__ __forceinline__ u32x2 ld_sc1_64(__amdgpu_buffer_rsrc_t rs, int off) { return __builtin_amdgcn_raw_buffer_load_b64(rs, off, 0, LDA); }
__device__ __forceinline__ void st_sc1(__amdgpu_buffer_rsrc_t rs, int off, u32x4 v) { __builtin_amdgcn_raw_buffer_store_b128(v, rs, off, 0, SC1); }
__device__ __forceinline__ void st_sc1_64(__amdgpu_buffer_rsrc_t rs, int off, u32x2 v) { __builtin_amdgcn_raw_buffer_store_b64(v, rs, off, 0, SC1); }

__device__ __forceinline__ void ln_stats(u32x4 xv, float& m, float& inv) {
  const float x[8] = {blo(xv[0]), bhi(xv[0]), blo(xv[1]), bhi(xv[1]), blo(xv[2]), bhi(xv[2]), blo(xv[3]), bhi(xv[3])};
  const float x0 = __builtin_bit_cast(float, __builtin_amdgcn_readfirstlane(xv[0] << 16));
  float s = 0.f, v = 0.f;
#pragma unroll
  for (int j = 0; j < 8; ++j) { const float d = x[j] - x0; s += d; v = fmaf(d, d, v); }
  const float md = wave_sum(s) * (1.f / 512.f), sq = wave_sum(v) * (1.f / 512.f);
  const float var = rbf(fmaxf(fmaf(-md, md, sq), 0.f));
  m = rbf(x0 + md);
  inv = rcp(rbf(__builtin_amdgcn_sqrtf(rbf(var + 1e-5f))));
}
#define PACE_(W, INC) do { asm volatile(W "\n1:\n\ts_memrealtime vcc\n\ts_waitcnt lgkmcnt(0)\n\ts_lshl_b32 vcc_lo, vcc_lo, 2\n\ts_sub_i32 vcc_hi, vcc_lo, %0\n\ts_cmp_lt_i32 vcc_hi, 0\n\t" \
    "s_cbranch_scc0 2f\n\ts_sleep 1\n\ts_branch 1b\n2:\n\ts_add_i32 %0, %0, %1" : "+s"(due) : "is"(INC) : "memory", "scc", "vcc"); SB(); } while (0)
#define LT(K, T)
#define PACE(INC) PACE_("s_waitcnt vmcnt(2)", INC)
#define PACEN(INC) PACE_("", INC)
__device__ __forceinline__ void until_s(u32 due) { PACE_("", 0); }
__device__ __forceinline__ void until(u32 t) {
  asm volatile("1:\n\ts_memrealtime vcc\n\ts_waitcnt lgkmcnt(0)\n\ts_lshl_b32 vcc_lo, vcc_lo, 2\n\ts_sub_i32 vcc_hi, vcc_lo, %0\n\ts_cmp_lt_i32 vcc_hi, 0\n\t"
               "s_cbranch_scc1 1b" :: "s"(t) : "memory", "scc", "vcc");
  SB();
}

#define STAMP(i) do { if ((i) == 0) { sv0 = (i) == 0 ? lk0 : (u32)__builtin_amdgcn_s_memrealtime(); asm volatile("" : "+s"(sv0)); } if ((i) == 18) { sv18 = (i) == 0 ? lk0 : (u32)__builtin_amdgcn_s_memrealtime(); asm volatile("" : "+s"(sv18)); } } while (0)

#define WSTAMP(k) do { } while (0)
__device__ __forceinline__ void lds_wait(volatile LAS u32* p, u32 want) {
  _Pragma("unroll 1") for (u32 s = 0; s < LSPIN_CAP; ++s) {
    if (__builtin_amdgcn_readfirstlane(*p) >= want) break;
    __builtin_amdgcn_s_sleep(2);
  }
  MEMB();
}
__device__ __forceinline__ void lds_add(LAS u32* p, int lane) {
  LGKM0();
  if (lane == 0) __hip_atomic_fetch_add(p, 1u, __ATOMIC_RELAXED, __HIP_MEMORY_SCOPE_WORKGROUP);
  MEMB();
}
__device__ __forceinline__ u32 lds_arrive(LAS u32* p, int lane) {
  LGKM0();
  u32 old = 0;
  if (lane == 0) old = __hip_atomic_fetch_add(p, 1u, __ATOMIC_RELAXED, __HIP_MEMORY_SCOPE_WORKGROUP);
  old = __builtin_amdgcn_readfirstlane(old);
  MEMB();
  return old;
}

__device__ __forceinline__ void tile_part(LAS unsigned char* T, LAS unsigned char* P, u32x4 hb, int n, int g) {
  LAS unsigned char* tb = T + (8 * g + (n >> 2)) * 128 + (n & 3) * 8;
  const int sx = (n >> 3) | ((g & 1) << 1);
  f32x4 a[4];
#pragma unroll
  for (int t = 0; t < 4; ++t) a[t] = mfma(trfrag(tb + (t ^ sx) * 32, tb + (t ^ sx) * 32 + 512), hb, f32x4{0.f, 0.f, 0.f, 0.f});
  *(LAS f32x4*)(P) = n < 8 ? a[0] : a[1];
  *(LAS f32x4*)(P + 1024) = n < 8 ? a[2] : a[3];
}
__device__ __forceinline__ f32x4 tile_sum(LAS unsigned char* P) {
  f32x4 s[8], r[2];
#pragma unroll
  for (int k = 0; k < 2; ++k) {
#pragma unroll
    for (int i = 0; i < 8; ++i) s[i] = *(LAS f32x4*)(P + (8 * k + i) * 2048);
    SB();
    r[k] = ((s[0] + s[1]) + (s[2] + s[3])) + ((s[4] + s[5]) + (s[6] + s[7]));
    SB();
  }
  return r[0] + r[1];
}
#define GU_MUL(P) _Pragma("unroll") for (int i = 0; i < 8; ++i) gb_[i] = *(LAS u32x4*)((P) + i * 64); SB(); \
    aL = f32x4{0.f, 0.f, 0.f, 0.f}; aH = aL; _Pragma("unroll") for (int i = 0; i < 8; ++i) { aL = mfma(gf[i], gb_[i], aL); aH = mfma(gf[8 + i], gb_[i], aH); }
__device__ __forceinline__ void gu_pair(f32x4 aL, f32x4 aH, f32x2& gt, f32x2& up) {
  const auto s0 = __builtin_amdgcn_permlane32_swap(__float_as_uint(aL[0] + dppf<ROR8>(aH[0])), __float_as_uint(aL[2] + dppf<ROR8>(aH[2])), false, false);
  const auto s1 = __builtin_amdgcn_permlane32_swap(__float_as_uint(aL[1] + dppf<ROR8>(aH[1])), __float_as_uint(aL[3] + dppf<ROR8>(aH[3])), false, false);
  gt = f32x2{__uint_as_float(s0[0]), __uint_as_float(s1[0])};
  up = f32x2{__uint_as_float(s0[1]), __uint_as_float(s1[1])};
}
__device__ __forceinline__ int mpos(int c) { return (c & 252) * 4 + (c >> 8) * 8 + (c & 3) * 2; }
__device__ __forceinline__ u32 smax(int m) { int r = (int)wave_max((float)m); PIN(r); return (u32)__builtin_amdgcn_readfirstlane(r); }
__device__ __forceinline__ u32 now4() { return (u32)__builtin_amdgcn_s_memrealtime() * 4u; }
#define B0_LOAD() { bp = ld_sc1(rs, WS_B0 + bo); bq = ld_sc1(rs, WS_B0 + 1024 + bo); }
__device__ __forceinline__ bool on_time(u32 tag, u32 org, u32x4 bp, u32x4 bq) {
  const u32 lim = org + OFO - DO;
  return __all((((bp[1] ^ tag) | (bp[3] ^ tag) | (bq[1] ^ tag) | (bq[3] ^ tag)) | (((lim - bp[0]) | (lim - bp[2]) | (lim - bq[0]) | (lim - bq[2])) >> 31)) == 0u);
}
__device__ __forceinline__ u32 anchor(__amdgpu_buffer_rsrc_t rs, int bo, u32 tag, u32 org, u32x4 bp, u32x4 bq) {
  int lk = OFB;
#pragma unroll 1
  for (u32 s = 0; s < SPIN_CAP; ++s) {
    if (__all(((bp[1] ^ tag) | (bp[3] ^ tag) | (bq[1] ^ tag) | (bq[3] ^ tag)) == 0u)) break;
    MEMB();
    do lk += PB; while ((int)(now4() - org) > lk);
    until_s(org + lk);
    B0_LOAD()
  }
  const int m = (int)smax(max(max((int)(bp[0] - org), (int)(bp[2] - org)), max((int)(bq[0] - org), (int)(bq[2] - org))));
  return org + (u32)max(m + DO <= OFO ? OFO : m + DL_, lk + RB - DP);
}
__device__ __forceinline__ float asf(u32 v) { return __builtin_bit_cast(float, v); }

struct Args {
  const unsigned char* half[4][2];
  const u16 *x, *kc, *vc, *wd, *cc;
  u16* out;
  unsigned char* ws;
};

__global__ void __launch_bounds__(1024) fused(const Args a) {
  extern __shared__ __attribute__((aligned(16))) unsigned char smem[];
  LAS unsigned char* L = (LAS unsigned char*)smem;
  const u32 lbase = (u32)(size_t)L;
  const int tid = threadIdx.x, w = __builtin_amdgcn_readfirstlane(tid >> 6), bid = blockIdx.x;
  int lane = tid & 63;
  u32 lk0 = (u32)__builtin_amdgcn_s_memrealtime(); asm volatile("" : "+s"(lk0));
  u32 sv0 = 0, sv18 = 0;
  const int xcd = bid & 7, idx = bid >> 3, hj = idx & 3, h = 4 * xcd + hj, b = idx >> 2, bh = b * 32 + h;
  const int sw8 = hj == 3 ? (w & 1) * 8 : 0, rw = (hj == 3 ? 24 : 32) * w;
  u64 hA, hB, t64;
  u32 warm;
  asm volatile("s_load_dwordx2 %0, %4, %5\n\ts_load_dwordx2 %1, %4, %6\n\ts_load_dword %3, %4, 0x40\n\ts_memrealtime %2" : "=&s"(hA), "=&s"(hB), "=&s"(t64), "=&s"(warm)
               : "s"(__builtin_amdgcn_kernarg_segment_ptr()), "s"(16 * hj + sw8), "s"(16 * hj + (sw8 ^ 8)));
  SB();
  const int bb = (b + xcd) & 7;
  int p3r = 5 * xcd + min(xcd, 3) + bb;
  int mc = (b & 6) + ((xcd & 1) ^ 1), mg = ((xcd & 6) + b) & 7;
  const bool tq = hj == 0, to = hj == 2;
  int lim = hj == 1 ? 6 - b : (xcd < 3 ? 6 : 5) - bb;
  asm volatile("" : "+s"(p3r), "+s"(mc), "+s"(mg), "+s"(lim));
  const bool idle = (hj & 1) && lim <= 0;
  u64 tp0, tp1;
  {
    const u32 pitch = (u32)(0x0AC0040018002000ull >> (16 * hj)) & 0xffffu;
    u32 ce = (xcd << (to ? 13 : 4)) + 2 * b, c1 = 16 * mc + 2 * mg;
    asm volatile("" : "+s"(ce), "+s"(c1));
    const u32 col = 64 * ((hj & 1) ? (hj == 1 ? c1 : p3r) : ce);
    const u32 step = pitch << (to ? 1 : 3);
    u32 off = (rw + (to ? (lane & 24) + (lane >> 5) : lane >> 3)) * pitch + col + ((lane & 3) ^ ((lane >> 3) & 2)) * 16;
    tp0 = off;
    tp1 = off + step;
    u64 s2 = 2 * step;
    u32 ld = lbase + L_W + rw * 128;
    asm volatile("" : "+s"(s2), "+s"(ld), "+v"(tp0), "+v"(tp1));
    asm volatile("s_mov_b32 exec_lo, 0xf0f0f0f0\n\ts_mov_b32 exec_hi, 0xf0f0f0f0\n\ts_waitcnt lgkmcnt(0)\n\tv_lshl_add_u64 %0, %0, 0, %3\n\tv_lshl_add_u64 %1, %1, 0, %2\n\t"
                 "s_not_b64 exec, exec\n\tv_lshl_add_u64 %0, %0, 0, %2\n\tv_lshl_add_u64 %1, %1, 0, %3\n\ts_mov_b64 exec, -1"
                 : "+v"(tp0), "+v"(tp1), "+s"(hA), "+s"(hB), "+s"(t64), "+s"(warm) :: "scc");
    if (!idle) {
      u32 keep;
      asm volatile("s_mov_b32 %0, m0\n\ts_mov_b32 m0, %3\n\ts_nop 0\n\tglobal_load_lds_dwordx4 %1, off nt\n\ts_add_u32 m0, m0, 0x400\n\t"
                   "v_lshl_add_u64 %1, %1, 0, %4\n\tglobal_load_lds_dwordx4 %2, off nt\n\ts_mov_b32 m0, %0\n\tv_lshl_add_u64 %2, %2, 0, %4"
                   : "=&s"(keep), "+v"(tp0), "+v"(tp1) : "s"(ld), "s"(s2) : "memory", "scc");
      glds((const void*)tp0, ld + 2048);
      if (hj != 3) glds((const void*)tp1, ld + 3072);
    }
  }
  SB();
  u32 t0 = (u32)t64;
  const u16 *x = a.x, *kc = a.kc, *vc = a.vc, *wd = a.wd, *cc = a.cc;
  u16* out = a.out;
  unsigned char* ws = a.ws;
  if (w == 0) STAMP(24);
  if (w == 0) STAMP(0);
  t0 += (u32)(KW * max((int)((u32)__builtin_amdgcn_s_memrealtime() - t0) - KA, 0)) / 4u;
  const int p3 = (hj == 3 && !idle) ? p3r : -1;
  const bool tm = hj == 1 && !idle;
  u32 tag;
  TAG();
  const __amdgpu_buffer_rsrc_t rs = __builtin_amdgcn_make_buffer_rsrc((void*)(ws + (size_t)(tag & ROTM) * WS_STEP), 0, WS_END, 0x00020000);
  if (idle && hj == 1) {
    if (w == 0) {
      st_sc1(rs, WS_G1 + ((b & 1) * 8 + xcd) * 1024 + lane * 16, u32x4{~0u, ~0u, ~0u, ~0u});
      VM0();
      if (lane == 0) st_sc1_64(rs, WS_TS + ((b & 1) * 8 + xcd) * 8, u32x2{t0, tag});
    } else {
      u64 pc;
      u32 len;
      asm volatile("s_getpc_b64 %0\n1:\n\ts_mov_b32 %1, fused_code_end - 1b + 496" : "=s"(pc), "=s"(len));
      const u32 o = ((b & 1) * 15 + w - 1) * 1024;
      if (o < len) touch((const void*)(pc & ~15ull), min(o + lane * 16, len & ~15u), lbase + L_SINK);
    }
  }
  if (TOUCH && w == TW) {
    const unsigned char* tp = (const unsigned char*)((lane & 1) ? vc : kc) + ((size_t)bh * SK + (SK - WIN)) * 256 + ((lane >> 1) & TGM) * 16384;
    if (TOUCH > 1) {
      if (lane == 32) tp = ws + (size_t)(tag & ROTM) * WS_STEP;
      if (lane == 33) tp = (const unsigned char*)out;
      if (lane == 34) tp = (const unsigned char*)x;
      if (lane == 35) tp = (const unsigned char*)wd + (p3 >= 0 ? p3 * 32768 : 0);
    }
    glds(tp, lbase + L_SINK);
  }
  int ta = TAU;
  int n = lane & 15, g = lane >> 4;
  u32x4 cv, hb;
  const bool row = tm && (w >= 8 || mc == 1);
  if (row) cv = ldnt((w < 8 ? x : cc) + (w & 7) * 512 + lane * 8);
  if (tid < 16) ((volatile LAS u32*)(L + L_CTL))[tid] = tid == C_T0 ? t0 : 0u;
  BAR();
  t0 = __builtin_amdgcn_readfirstlane(((volatile LAS u32*)(L + L_CTL))[C_T0]);
  u32 due = t0 * 4u + (u32)(PHASE * TAU) / 32u + DUE0;
  int rot = ROTF;
#define RO(I) (((XR ? (I) ^ rot : (I) + rot) & 15) * 8192)
  const u16* kp = kc + ((size_t)bh * SK + (SK - WIN) + 4 * w) * 128;
  u32 l16 = lane * 16;
  PIN(l16);
#define LDR(D, P, I, W) { const GAS unsigned char* p_ = (const GAS unsigned char*)((P) + RO(I)) + l16; PIN(p_); W; D = __builtin_nontemporal_load((const GAS u32x4*)p_); SB(); }
  u32x4 kr[16], vr[16];
  volatile LAS u32* ctl = (volatile LAS u32*)(L + L_CTL);
#define RELANE() do { asm volatile("" : "+v"(lane)); n = lane & 15; g = lane >> 4; } while (0)
  const int e = b * 16 + 2 * xcd + hj;
  const bool red = hj < 2;

#define KL(I) { const int t_ = ta + extra(I); LDR(kr[I], kp, I, PACE(t_)) LT(I, t_) }
  KL(0)
  if (row) {
    if (w >= 8) {
#pragma unroll
      for (int j = 0; j < 4; ++j) { const float a0 = blo(cv[j]), a1 = bhi(cv[j]); hb[j] = pack2(a0 * rcp(1.f + __expf(-a0)), a1 * rcp(1.f + __expf(-a1))); }
      *(LAS u32x4*)(L + L_H2 + (w & 7) * H2S + lane * 16) = hb;
      lds_add((LAS u32*)(L + L_CTL) + C_R, lane);
    } else {
      float m, inv;
      ln_stats(cv, m, inv);
      if ((lane >> 3) == mg) {
        LAS f32x4* d = (LAS f32x4*)(L + L_RED + w * 256 + (lane & 7) * 32);
        d[0] = f32x4{rbf(rbf(blo(cv[0]) - m) * inv), rbf(rbf(bhi(cv[0]) - m) * inv), rbf(rbf(blo(cv[1]) - m) * inv), rbf(rbf(bhi(cv[1]) - m) * inv)};
        d[1] = f32x4{rbf(rbf(blo(cv[2]) - m) * inv), rbf(rbf(bhi(cv[2]) - m) * inv), rbf(rbf(blo(cv[3]) - m) * inv), rbf(rbf(bhi(cv[3]) - m) * inv)};
      }
    }
  }
  KL(1)
  u32x2 tsv;
#define TS_WAIT() if (!__all(tsv[1] == tag)) { _Pragma("unroll 1") for (u32 s = 0; s < SPIN_CAP; ++s) { __builtin_amdgcn_s_sleep(16); MEMB(); \
      tsv = ld_sc1_64(rs, WS_TS + (lane & 15) * 8); if (__all(tsv[1] == tag)) break; } } MEMB();
  if (tm) {
    asm volatile("s_waitcnt vmcnt(2)" ::: "memory");
    lds_wait(ctl + C_R, 8u);
    hb = *(LAS u32x4*)(L + L_H2 + (n & 7) * H2S + (32 * w + 8 * g) * 2);
    tile_part(L + L_W + w * 4096, L + L_TP + w * 2048 + lane * 16, hb, n, g);
    lds_add((LAS u32*)(L + L_CTL) + C_T, lane);
  }
  KL(2)
  if (w == 0 || (tm && w >= 14)) tsv = ld_sc1_64(rs, WS_TS + (lane & 15) * 8);
  SB();
  if (tm && w >= 14) {
    lds_wait(ctl + C_T, 16u);
    const int col = 32 * (w & 1) + 16 * (n >> 3) + 4 * g;
    const f32x4 s = tile_sum(L + L_TP + (w & 1) * 1024 + lane * 16);
    float v[4] = {rbf(s[0]), rbf(s[1]), rbf(s[2]), rbf(s[3])};
    if (mc == 1) {
      const f32x4 ln = *(LAS f32x4*)(L + L_RED + (n & 7) * 256 + col * 4);
#pragma unroll
      for (int j = 0; j < 4; ++j) v[j] = ln[j] * rbf(1.f + v[j]);
    }
    TS_WAIT()
    st_sc1_64(rs, mc < 2 ? WS_G1 + (2 * mg + (w & 1)) * 1024 + ((2 * (n >> 3) + (g >> 1)) * 16 + mc * 8 + (n & 7)) * 16 + (g & 1) * 8
                         : WS_MODR + ((mc - 2) * 8 + (n & 7)) * 1024 + mpos(64 * mg + col),
              u32x2{canon(pack2(v[0], v[1])), canon(pack2(v[2], v[3]))});
    if (w == 15) STAMP(3);
  }
  RELANE();
  KL(3) KL(4)
  if (w == 0) {
    TS_WAIT()
    const float d = (float)(int)(tsv[0] - t0);
    ctl[C_TB] = (t0 * 4u + (u32)(int)fmaxf(row_sum(d) * 0.25f, row_max(d) * 4.f - SKW)) | 1u;
  }
  KL(5) KL(6)
  u32 org = __builtin_amdgcn_readfirstlane(ctl[C_TB]);
  if (org && REB) { const int adj = ((int)(org - t0 * 4u - (p3 >= 0 ? bb * MSH : 0)) * 85 + 1024) >> 11; ta += adj; }
  KL(7)
  if (tm && mc >= 2 && w >= 14 && lane == 0) __builtin_amdgcn_raw_buffer_store_b32(tag, rs, WS_FLAGS + (((mc - 2) * 8 + mg) * 2 + (w & 1)) * 4, 0, SC1);
  KL(8)
  u32x4 g0;
  const int o0 = WS_G1 + w * 1024 + lane * 16;
  if (tq) lds_wait(ctl + C_TB, 1u);
#define KG(K) { KL(K) if (tq && (w & 1) == ((K) & 1)) { until_s(due - TAU / 2); \
    asm volatile("buffer_load_dwordx4 %0, %1, %2, 0 offen sc1" : "=&v"(g0) : "v"(o0), "s"(rs) : "memory"); } }
  KG(9) KG(10)
  KL(11) KL(12)
  if (tq) {
    asm volatile("s_waitcnt vmcnt(2)" : "+v"(g0));
#define G1_OK() __all(g0[0] != ~0u && g0[1] != ~0u && g0[2] != ~0u && g0[3] != ~0u)
    if (!G1_OK()) {
#pragma unroll 1
      for (u32 s = 0; s < SPIN_CAP; ++s) {
        __builtin_amdgcn_s_sleep(16);
        MEMB();
        g0 = ld_sc1(rs, o0);
        if (G1_OK()) break;
      }
    }
    if (w == 15) STAMP(4);
#pragma unroll
    for (int j = 0; j < 4; ++j) { const u32 o = dppu<ROR8>(g0[j]); hb[j] = pack2(blo(g0[j]) + blo(o), bhi(g0[j]) + bhi(o)); }
    PIN(hb);
  }
  KL(13)
  if (tq) {
    tile_part(L + L_W + w * 4096, L + L_TP + w * 2048 + lane * 16, hb, n, g);
    lds_add((LAS u32*)(L + L_CTL) + C_T, lane);
  }
  KL(14)
  if (tq && w >= 14) {
    lds_wait(ctl + C_T, 16u);
    const f32x4 s = tile_sum(L + L_TP + (w & 1) * 1024 + lane * 16);
    st_sc1(rs, WS_Q + ((n & 7) * 32 + 4 * xcd + (b >> 1)) * 512 + ((b & 1) * 64 + 32 * (w & 1) + 16 * (n >> 3) + 4 * g) * 4, u32x4{pack2(s[0], s[1]), tag, pack2(s[2], s[3]), tag});
    if (w == 15) STAMP(17);
  }
  RELANE();
  KL(15)
  asm volatile("" : "+s"(rot));
  const u16* vp = vc + ((size_t)bh * SK + (SK - WIN) + 4 * w) * 128;
#define VL(I) { const int t_ = ta + extra(16 + (I)); LDR(vr[I], vp, I, PACE(t_)) LT(16 + (I), t_) }
#define VLN(I) { const int t_ = ta + extra(16 + (I)); LDR(vr[I], vp, I, PACEN(t_)) LT(16 + (I), t_) }
  VL(0) VL(1)
  VL(2)
  u32x2 gq;
#define FL_WAIT() if (!__all(fl == tag)) {   _Pragma("unroll 1") for (u32 s = 0; s < SPIN_CAP; ++s) { __builtin_amdgcn_s_sleep(4); MEMB(); \
      fl = __builtin_amdgcn_raw_buffer_load_b32(rs, WS_FLAGS + lane * 4, 0, SC1); if (__all(fl == tag)) break; } } MEMB();
  u32 fl;
  if (w == WG_) {
    gq = ld_sc1_64(rs, WS_Q + bh * 512 + lane * 8);
    fl = __builtin_amdgcn_raw_buffer_load_b32(rs, p3 >= 0 ? WS_FLAGS + lane * 4 : WS_END, 0, SC1);
  }
  SB();
  VLN(3) VLN(4)
  if (w == WG_) {
    if (!__all(gq[1] == tag)) {
#pragma unroll 1
      for (u32 s = 0; s < SPIN_CAP; ++s) {
        __builtin_amdgcn_s_sleep(4);
        MEMB();
        gq = ld_sc1_64(rs, WS_Q + bh * 512 + lane * 8);
        if (__all(gq[1] == tag)) break;
      }
    }
    *(LAS u32*)(L + L_Q + lane * 4) = gq[0];
    PIN(fl);
    if (p3 >= 0) { FL_WAIT() }
    LGKM0();
    ctl[C_Q] = 1u;
    STAMP(5);
  }
  RELANE();
  VL(5)
  lds_wait(ctl + C_Q, 1u);
  const bool prer = p3 >= 0 && w < 8;
  u32x4 pre0, pre1;
  if (prer) {
    pre0 = ld_sc1(rs, WS_MODR + (8 + w) * 1024 + lane * 16);
    pre1 = ld_sc1(rs, WS_MODR + (16 + w) * 1024 + lane * 16);
  }
  SB();
  const u32x4 qf = *(LAS u32x4*)(L + L_Q + n * 16);
  if (w == 1) STAMP(6);

#define DOT(I) dot2(kr[I][3], qf[3], dot2(kr[I][2], qf[2], dot2(kr[I][1], qf[1], dot2(kr[I][0], qf[0], 0.f))))
#define TR(LO, HI, CTRL, BIT) ((n & (BIT)) ? (HI) + dppf<CTRL>(HI) : (LO) + dppf<CTRL>(LO))
  float xs[8];
#define QK(C) { _Pragma("unroll") for (int i = 2 * (C); i < 2 * (C) + 2; ++i) { const float lo_ = DOT(i), hi_ = DOT(i + 8); \
      const float t1_ = lo_ + dppf<ROR8>(lo_), t2_ = hi_ + dppf<ROR8>(hi_); xs[i] = (n & 8) ? t2_ : t1_; PIN(xs[i]); } SB(); }
  QK(0) VLN(6) QK(1) VLN(7) QK(2) VL(8) QK(3) VL(9)
  if (prer) {
    const u32 a_h2w = w * C1S + lane * 8;
    *(LAS u32x2*)(L + L_C1 + a_h2w) = u32x2{pk2(up2(pre1[0]) + 1.f), pk2(up2(pre1[1]) + 1.f)};
    *(LAS u32x2*)(L + L_C1 + a_h2w + 512) = u32x2{pk2(up2(pre1[2]) + 1.f), pk2(up2(pre1[3]) + 1.f)};
    *(LAS u32x2*)(L + L_C1 + 8 * C1S + a_h2w) = u32x2{pre0[0], pre0[1]};
    *(LAS u32x2*)(L + L_C1 + 8 * C1S + a_h2w + 512) = u32x2{pre0[2], pre0[3]};
  }
#define TR_STEP(HALF, CTRL, BIT) _Pragma("unroll") for (int hh = 0; hh < (HALF); ++hh) { \
    const float lo_ = xs[hh], hi_ = xs[hh + (HALF)]; const float t1_ = lo_ + dppf<CTRL>(lo_), t2_ = hi_ + dppf<CTRL>(hi_); xs[hh] = (n & (BIT)) ? t2_ : t1_; }
  TR_STEP(4, XOR7, 4) TR_STEP(2, XOR2, 2) TR_STEP(1, XOR1, 1)
#undef TR_STEP
  float sc = rbf(rbf(xs[0]) * 0.08838834764831845f);
  PIN(sc);
  SB();
  VL(10)
  {
    const float mw_ = wave_max(sc);
    const float lw = wave_sum(__expf(sc - mw_));
    if (lane == 0) *(LAS f32x2*)(L + L_ML + w * 8) = f32x2{mw_, lw};
    lds_add((LAS u32*)(L + L_CTL) + C_ML, lane);
  }
  SB();
  VL(11)
  RELANE();
  u32 mk[4];
#pragma unroll
  for (int j = 0; j < 4; ++j) mk[j] = (n >> 1) == j ? ((n & 1) ? 0xffff0000u : 0x0000ffffu) : 0u;
  f32x4 oa = {0.f, 0.f, 0.f, 0.f};
#define PV(I) { const u32 pb_ = dppu<0x150 + (I)>(pd); oa = mfma(vr[I], u32x4{pb_ & mk[0], pb_ & mk[1], pb_ & mk[2], pb_ & mk[3]}, oa); asm volatile("" : "+v"(oa)); SB(); }
  PIN(mk[0]); PIN(mk[1]); PIN(mk[2]); PIN(mk[3]);
  u32 a_oa = L_RED + w * OSTR + (g * 8 + n) * 16;
  u32 ra = L_RED + (lane >> 5) * 8 * OSTR + (lane & 31) * 16;
  u32 rb_ = WS_O + (h * 8 + b) * 512 + (lane & 31) * 16 + (lane >> 5) * 8;
  u32 tagv = tag;
  PIN(a_oa); PIN(ra); PIN(rb_); PIN(tagv);
  org = __builtin_amdgcn_readfirstlane(ctl[C_TB]);
  org = org ? org + TEND : t0 * 4u;
  asm volatile("" : "+s"(org));
  VL(12)
  lds_wait(ctl + C_ML, 16u);
  u32 pd;
  {
    const f32x2 ml = *(LAS f32x2*)(L + L_ML + n * 8);
    const float M = row_max(ml[0]);
    const float pe = __expf(sc - M) * rcp(row_sum(__expf(ml[0] - M) * ml[1]));
    pd = pack2(pe, pe);
    PIN(pd);
  }
  SB();
  VL(13)
  PV(0) PV(1) PV(2) PV(3) PV(4)
  VL(14)
  PV(5) PV(6) PV(7) PV(8) PV(9)
  VL(15)
  PV(10) PV(11) PV(12) PV(13) PV(14)
  if (w == 13) __builtin_amdgcn_s_setprio(3);
  PV(15)
#undef PV
  if (n < 8) *(LAS f32x4*)(L + a_oa) = oa;
  BAR();
  if (w == 0) STAMP(8);
  if (w == 14 && lane == 0) st_sc1_64(rs, WS_B0 + bid * 8, u32x2{now4(), tagv});
  if (w == 13) {
    f32x4 s[8];
#pragma unroll
    for (int ww = 0; ww < 8; ++ww) s[ww] = *(LAS f32x4*)(L + ra + ww * OSTR);
    SB();
    const f32x4 t = ((s[0] + s[1]) + (s[2] + s[3])) + ((s[4] + s[5]) + (s[6] + s[7]));
    st_sc1_64(rs, rb_, u32x2{pack2(t32(t[0], t[2]), t32(t[1], t[3])), tagv});
    STAMP(9);
  }
  if (to) {
    u32x4 wf[16];
    if (w < 4) {
      const u32 af = L_W + (8 * g + (n >> 2)) * 128 + (w ^ ((n >> 3) | ((g & 1) << 1))) * 32 + (n & 3) * 8;
#pragma unroll
      for (int ks = 0; ks < 16; ++ks) wf[ks] = trfrag(L + af + ks * 4096, L + af + ks * 4096 + 512);
      __builtin_amdgcn_s_setprio(3);
    }
    int oo = WS_O + xcd * 16384 + w * 1024 + lane * 16;
    u32 a_ow = L_TP + (2 * w + (lane >> 5)) * OBS + (lane & 31) * 8;
    u32 a_or = L_TP + ((n >> 3) * 16 + (n & 7)) * OBS + g * 16;
    int rc = n < 8 ? WS_PART + ((n * 16 + 2 * b + ((w >> 1) & 1)) * 8 + xcd) * 128 + (w & 1) * 64 + g * 16 : WS_END;
    PIN(oo); PIN(a_ow); PIN(a_or); PIN(rc);
    SEL4();
    until(org + OFO);
#pragma unroll 1
    for (u32 s = 0; s < SPIN_CAP; ++s) {
      const u32x4 og = ld_sc1(rs, oo);
      *(LAS u32x2*)(L + a_ow) = u32x2{og[0], og[2]};
      if (__builtin_expect(__all(((og[1] ^ tag) | (og[3] ^ tag)) == 0u), 1)) break;
      MEMB();
      __builtin_amdgcn_s_sleep(2);
    }
    BAR();
    if (w == 7) STAMP(2);
    if (w < 4) {
      u32x4 ob[8];
#pragma unroll
      for (int i = 0; i < 8; ++i) ob[i] = *(LAS u32x4*)(L + a_or + (i >> 2) * 8 * OBS + (i & 3) * 64);
      SB();
      f32x4 aL = {0.f, 0.f, 0.f, 0.f}, aH = {0.f, 0.f, 0.f, 0.f};
#pragma unroll
      for (int i = 0; i < 8; ++i) { aL = mfma(wf[i], ob[i], aL); aH = mfma(wf[8 + i], ob[i], aH); }
      const f32x4 v = {aL[0] + dppf<ROR8>(aH[0]), aL[1] + dppf<ROR8>(aH[1]), aL[2] + dppf<ROR8>(aH[2]), aL[3] + dppf<ROR8>(aH[3])};
      st_sc1(rs, rc, tag8(v, tagv, sel));
      if (w == 3) STAMP(16);
    }
  }

  const int bo = lane * 16;
  if (p3 >= 0) {
    u32 a_h2w = w * C1S + lane * 8;
    if (w == 8 && lane < 36) *(LAS u32*)(L + (lane < 32 ? L_ML : L_HH + 640) + lane * 4) = 0u;
    BAR();
    glds((const void*)(tp0 + (u32)((368 - 16 * w) * FF * 2)), lbase + L_W + 49152 + w * 1024);
    u32x4 bp, bq;
    if (w == 12) {
      until_s(org + OFB + 20);
      asm volatile("buffer_load_dwordx4 %0, %2, %3, 0 offen sc1\n\tbuffer_load_dwordx4 %1, %2, %3, 0 offen offset:1024 sc1" : "=&v"(bp), "=&v"(bq) : "v"(WS_B0 + bo), "s"(rs) : "memory");
    }
    BAR();
#define DL(R) glds(wd + (size_t)(32 * p3 + (R)) * 512, (lane ^ ((((R) & 3) | (((R) >> 1) & 4)) << 1)) * 16, lbase + L_W + 65536 + (R) * 1024)
    if (w < 8) { DL(3 * w); DL(3 * w + 1); DL(3 * w + 2); }
    if (w >= 12) { DL(2 * w); DL(2 * w + 1); }
    SB();
    u32 a_h2r = L_H2 + (n & 7) * H2S + (n >> 3) * 512 + g * 16;
    u32 a_hw = L_HH + (n < 8 ? n * 64 + (8 * (w & 3) + 4 * (g & 1) + 2 * (g >> 1)) * 2 : 512 + lane * 4);
    const u32x4 t16v = {tag & 0xffffu, tag >> 16, tag & 0xffffu, tag >> 16};
#define X_TIME() until_s(org + OFO + DX - 40); u32 tx = 2u; \
      _Pragma("unroll 1") for (u32 s = 0; s < LSPIN_CAP && tx == 2u; ++s) { tx = __builtin_amdgcn_readfirstlane(ctl[C_TX]); if (tx == 2u) __builtin_amdgcn_s_sleep(2); } \
      if (!(tx & 1u)) tx = org + OFO + DX;
#define X_BAD(A, B) ((or4(A) | or4(B)) & 0xffffu)
    if (w < 8) {
      const u32x2 ca = *(LAS u32x2*)(L + L_C1 + a_h2w), cb = *(LAS u32x2*)(L + L_C1 + a_h2w + 512);
      f32x2 c1[4] = {up2(ca[0]), up2(ca[1]), up2(cb[0]), up2(cb[1])};
      a_h2w = L_H2 + w * H2S + lane * 8;
      int xo = WS_X2 + xcd * 16384 + w * 2048 + lane * 16;
      u32 a_hr = L_HH + (n < 8 ? n * 64 + g * 16 : 768), a_hs = L_HH + (n < 8 ? 768 : (n & 7) * 64 + g * 16);
      PIN(a_hs);
      SEL4();
      const u32 a_d0 = L_W + 65536 + (8 * g + (n >> 2)) * 1024 + ((8 * w + ((n >> 1) & 1)) ^ (((n >> 2) | ((g & 1) << 2)) << 1)) * 16 + (n & 1) * 8;
      int yo = WS_YP + ((16 * w + 4 * (n >> 3) + g) * NP3 + p3) * 128 + (n & 7) * 16, yo2 = yo + 8 * NP3 * 128;
      PIN(xo); PIN(c1[0]); PIN(c1[1]); PIN(c1[2]); PIN(c1[3]); PIN(a_h2w); PIN(a_hr); PIN(yo); PIN(yo2);
      u32 a_cx = lbase + L_CTL + (C_X + w) * 4;
      PIN(a_cx);
      asm volatile("s_waitcnt vmcnt(3)" ::: "memory");
      BAR();
      if (w == 0) STAMP(20);
      __builtin_amdgcn_s_setprio(2);
      X_TIME()
      until(tx);
#pragma unroll 1
      for (u32 s = 0; s < SPIN_CAP; ++s) {
        const u32x4 xa = ld_sc1(rs, xo) ^ t16v, xb = ld_sc1(rs, xo + 1024) ^ t16v;
        const float x0 = __builtin_bit_cast(float, __builtin_amdgcn_readfirstlane(xa[0]));
        *(LAS u32x2*)(L + a_h2w) = u32x2{pk2((f32x2{asf(xa[0]), asf(xa[1])} - x0) * c1[0]), pk2((f32x2{asf(xa[2]), asf(xa[3])} - x0) * c1[1])};
        *(LAS u32x2*)(L + a_h2w + 512) = u32x2{pk2((f32x2{asf(xb[0]), asf(xb[1])} - x0) * c1[2]), pk2((f32x2{asf(xb[2]), asf(xb[3])} - x0) * c1[3])};
        if (__builtin_expect(__all(X_BAD(xa, xb) == 0u), 1)) break;
        MEMB();
      }
      asm volatile("s_mov_b64 exec, 1\n\tds_write_b32 %0, %0\n\ts_mov_b64 exec, -1" :: "v"(a_cx) : "memory");
      if (w == 0) STAMP(13);
      __builtin_amdgcn_s_setprio(0);
      lds_add((LAS u32*)(L + L_CTL) + C_R, lane);
      lds_wait(ctl + C_R, 12u);
      u32x4 df[4];
#pragma unroll
      for (int i = 0; i < 4; ++i) { df[i] = trfrag(L + (a_d0 ^ (32 * i)), L + (a_d0 ^ (32 * i)) + 4096); PIN(df[i]); }
      BAR();
      const u32x4 h0 = *(LAS u32x4*)(L + a_hr), h1 = *(LAS u32x4*)(L + a_hs);
      const f32x4 z = {0.f, 0.f, 0.f, 0.f};
      const f32x4 a0 = mfma(df[1], h1, mfma(df[0], h0, z)), a1 = mfma(df[3], h1, mfma(df[2], h0, z));
      st_sc1(rs, yo, tag8(a0, tagv, sel));
      st_sc1(rs, yo2, tag8(a1, tagv, sel));
    } else if (w < 12) {
      u32 a_st = L_ML + (n & 7) * 16;
      PIN(a_st);
      __builtin_amdgcn_s_setprio(3);
      u32x4 gf[16], gb_[8];
      const u32 af = L_W + (8 * g + (n >> 2)) * 128 + (((w & 3) + (n & 2) * 2) ^ (((n >> 3) | ((g & 1) << 1)) << 1)) * 16 + (n & 1) * 8;
      f32x4 aL = {0.f, 0.f, 0.f, 0.f}, aH;
      f32x2 ag, au, sg, su, gt, up;
      {
#pragma unroll
        for (int q4 = 0; q4 < 4; ++q4) {
#pragma unroll
          for (int i = 0; i < 4; ++i) gb_[i] = *(LAS u32x4*)(L + L_C1 + n * C1S + g * 16 + q4 * 256 + i * 64);
          if (q4 == 3) {
            VM0();
            BAR();
          }
#pragma unroll
          for (int i = 0; i < 4; ++i) gf[4 * q4 + i] = trfrag(L + af + (4 * q4 + i) * 4096, L + af + (4 * q4 + i) * 4096 + 512);
          SB();
#pragma unroll
          for (int i = 0; i < 4; ++i) aL = mfma(gf[4 * q4 + i], gb_[i], aL);
          PIN(aL);
        }
        const auto s0 = __builtin_amdgcn_permlane32_swap(__float_as_uint(aL[0]), __float_as_uint(aL[2]), false, false);
        const auto s1 = __builtin_amdgcn_permlane32_swap(__float_as_uint(aL[1]), __float_as_uint(aL[3]), false, false);
        ag = -f32x2{__uint_as_float(s0[0]), __uint_as_float(s1[0])};
        au = -f32x2{__uint_as_float(s0[1]), __uint_as_float(s1[1])};
        sg = f32x2{dppf<ROR8>(ag[0]), dppf<ROR8>(ag[1])} * 1.4426950408889634f;
        su = -f32x2{dppf<ROR8>(au[0]), dppf<ROR8>(au[1])};
        PIN(ag); PIN(au); PIN(sg); PIN(su); PIN(a_h2r); PIN(a_hw);
      }
      if (w == 8) STAMP(21);
      __builtin_amdgcn_s_setprio(0);
      _Pragma("unroll 1") for (u32 s = 0; s < LSPIN_CAP; ++s) if (__all(ctl[C_X + (lane & 7)] != 0u)) break;
      __builtin_amdgcn_s_setprio(3);
      MEMB();
      GU_MUL(L + a_h2r)
      SB();
      f32x4 st = *(volatile LAS f32x4*)(L + a_st);
      gu_pair(aL, aH, gt, up);
      if (!__all(st[3] != 0.f)) {
        _Pragma("unroll 1") for (u32 s = 0; s < LSPIN_CAP; ++s) { st = *(volatile LAS f32x4*)(L + a_st); if (__all(st[3] != 0.f)) break; }
      }
      gt = __builtin_elementwise_fma(__builtin_elementwise_fma(ag, f32x2{st[0], st[0]}, gt), f32x2{st[2], st[2]}, sg);
      up = __builtin_elementwise_fma(__builtin_elementwise_fma(au, f32x2{st[0], st[0]}, up), f32x2{st[1], st[1]}, su);
      *(LAS u32*)(L + a_hw) = pk2(f32x2{rcp(1.f + __builtin_amdgcn_exp2f(gt[0])), rcp(1.f + __builtin_amdgcn_exp2f(gt[1]))} * up);
      BAR();
    } else {
      int xo = WS_X2 + xcd * 16384 + (w - 12) * 4096 + lane * 16;
      u32 a_st = lbase + L_ML + (2 * (w - 12) + (lane >> 5)) * 16;
      PIN(xo); PIN(a_st);
      asm volatile("s_waitcnt vmcnt(2)" : "+v"(bp), "+v"(bq) :: "memory");
      BAR();
      if (w == 12 && !on_time(tag, org, bp, bq)) {
        ctl[C_TX] = 2u;
        ctl[C_TX] = (anchor(rs, bo, tag, org, bp, bq) + DX) | 1u;
      }
      VM0();
      if (w == 13) STAMP(19);
      lds_add((LAS u32*)(L + L_CTL) + C_R, lane);
      __builtin_amdgcn_s_setprio(1);
      X_TIME()
      until(tx + SDL);
      u32x4 xv[4];
#pragma unroll
      for (int i = 0; i < 4; ++i) xv[i] = ld_sc1(rs, xo + i * 1024);
#pragma unroll
      for (int i = 0; i < 4; ++i) xv[i] ^= t16v;
      if (__builtin_expect(!__all((X_BAD(xv[0], xv[1]) | X_BAD(xv[2], xv[3])) == 0u), 0)) {
#pragma unroll 1
        for (u32 s = 0; s < SPIN_CAP; ++s) {
          MEMB();
#pragma unroll
          for (int i = 0; i < 4; ++i) xv[i] = ld_sc1(rs, xo + i * 1024) ^ t16v;
          if (__all((X_BAD(xv[0], xv[1]) | X_BAD(xv[2], xv[3])) == 0u)) break;
        }
      }
      float sm[2], sq[2], xs0[2];
#pragma unroll
      for (int k = 0; k < 2; ++k) {
        const float x0 = __builtin_bit_cast(float, __builtin_amdgcn_readfirstlane(xv[2 * k][0]));
        const u32x4 xa = xv[2 * k], xb = xv[2 * k + 1];
        const f32x2 d0 = f32x2{asf(xa[0]), asf(xa[1])} - x0, d1 = f32x2{asf(xa[2]), asf(xa[3])} - x0, d2 = f32x2{asf(xb[0]), asf(xb[1])} - x0, d3 = f32x2{asf(xb[2]), asf(xb[3])} - x0;
        const f32x2 sv = (d0 + d1) + (d2 + d3), qv = __builtin_elementwise_fma(d0, d0, d1 * d1) + __builtin_elementwise_fma(d2, d2, d3 * d3);
        xs0[k] = x0;
        sm[k] = sv[0] + sv[1];
        sq[k] = qv[0] + qv[1];
      }
      const float r = row_sum(t32(t16(sm[0], sq[0]), t16(sm[1], sq[1]))) * (1.f / 512.f);
      const auto sw = __builtin_amdgcn_permlane16_swap(__float_as_uint(r), __float_as_uint(r), false, false);
      const float md = __uint_as_float(sw[0]);
      const float x0 = lane < 32 ? xs0[0] : xs0[1];
      const float inv = rcp(rbf(__builtin_amdgcn_sqrtf(rbf(rbf(fmaxf(fmaf(-md, md, __uint_as_float(sw[1])), 0.f)) + 1e-5f))));
      const f32x4 st = {rbf(x0 + md) - x0, inv, inv * -1.4426950408889634f, 1.f};
      asm volatile("s_mov_b32 exec_lo, 1\n\ts_mov_b32 exec_hi, 1\n\tds_write_b128 %0, %1\n\ts_mov_b64 exec, -1" :: "v"(a_st), "v"(st) : "memory");
      if (w == 15) {
        _Pragma("unroll 1") for (u32 s = 0; s < LSPIN_CAP; ++s) if (__all(ctl[C_X + (lane & 7)] != 0u)) break;
        if (lane < 8) st_sc1_64(rs, WS_B1 + lane * 512 + p3 * 8, u32x2{now4(), tagv});
      }
      BAR();
    }
    if (w == 7) STAMP(14);
    asm volatile("fused_code_end:");
  } else if (red && (w == RW || w == RW - 1)) {
    const int f = 16 * xcd + 2 * b + hj, ro_ = 4 * (w & 1) + (lane & 3);
    const int rb = e >> 4, rj = e & 15, c2 = 32 * rj + 4 * (lane & 7) + g;
    fl = __builtin_amdgcn_raw_buffer_load_b32(rs, WS_FLAGS + lane * 4, 0, SC1);
    float xx = blo(x[rb * 512 + c2]);
    FL_WAIT()
    float ga = blo(__builtin_amdgcn_raw_buffer_load_b16(rs, WS_MODR + rb * 1024 + mpos(c2), 0, SC1));
    float gb = blo(__builtin_amdgcn_raw_buffer_load_b16(rs, WS_MODR + (24 + ro_) * 1024 + mpos(4 * f + g), 0, SC1));
    int po = WS_PART + e * 1024 + lane * 16;
    int xo = WS_X2 + (lane >> 3) * 16384 + rb * 2048 + rj * 128 + (lane & 7) * 16;
    int xi = WS_X2 + xcd * 16384 + ro_ * 2048 + (4 * f + g) * 4;
    u32 xt = (g & 1) ? tag >> 16 : tag & 0xffffu;
    const u32x4 tb4 = bytes4(tag);
    u32x4 tb5 = lane < 44 ? tb4 : u32x4{0, 0, 0, 0};
    int yo = WS_YP + (f * NP3 + (lane >> 2)) * 128 + ro_ * 16;
    int y2 = lane < 44 ? yo + 4096 : WS_END;
    u32 oof = (lane & 12) == 0 ? (ro_ * 512 + 4 * f + g) * 2 : 8192;
    __amdgpu_buffer_rsrc_t ro = __builtin_amdgcn_make_buffer_rsrc((void*)out, 0, 8192, 0x00020000);
    asm volatile("" : "+s"(ro));
    PIN(xt); PIN(tb5); PIN(ga); PIN(gb); PIN(xx); PIN(po); PIN(xo); PIN(xi); PIN(yo); PIN(y2); PIN(oof);
    if (w == RW) {
      u32x4 bp, bq;
      until_s(org + OFB);
      B0_LOAD()
      const u32 anc = on_time(tag, org, bp, bq) ? org + OFO : anchor(rs, bo, tag, org, bp, bq);
      STAMP(1);
      until(anc + DP);
      u32x4 xq;
#pragma unroll 1
      for (u32 sp = 0; sp < SPIN_CAP; ++sp) {
        const u32x4 pv = ld_one(rs, po) ^ tb4;
        float t = t32(t16(asf(pv[0]), asf(pv[1])), t16(asf(pv[2]), asf(pv[3])));
        t += dppf<ROR8>(t);
        const u32 xw = __float_as_uint(rbf(fmaf(ga, t, xx))) | xt;
        const auto p1 = __builtin_amdgcn_permlane16_swap(xw, xw, false, false);
        const auto p2 = __builtin_amdgcn_permlane32_swap(p1[0], p1[0], false, false), p3_ = __builtin_amdgcn_permlane32_swap(p1[1], p1[1], false, false);
        xq = u32x4{p2[0], p3_[0], p2[1], p3_[1]};
        PIN(xq);
        if (__builtin_expect(__all((or4(pv) & 255u) == 0u), 1)) break;
        MEMB();
      }
      st_sc1(rs, xo, xq);
      STAMP(11);
      if (bid == 1 && lane == 0) __hip_atomic_store((GAS u32*)ws, tag, RLX_AGENT);
      u32 ty = anc + DYS;
      const int lt = (int)(now4() - anc) - (DX + 20);
      if (lt > 0) {
        int lk = DB1;
        while (lk < DB1 + lt + 240) lk += PB;
        u32x2 bv;
#pragma unroll 1
        for (u32 sp = 0; sp < SPIN_CAP; ++sp) {
          until_s(anc + lk);
          bv = ld_sc1_64(rs, WS_B1 + xcd * 512 + (lane < NP3 ? lane : 0) * 8);
          if (__all(bv[1] == tag)) break;
          MEMB();
          do lk += PB; while ((int)(now4() - anc) > lk);
        }
        ty = org + smax((int)(bv[0] - org)) + DY;
      }
      ctl[C_TX] = ty | 1u;
      STAMP(7);
    }
    lds_wait(ctl + C_TX, 1u);
    until(__builtin_amdgcn_readfirstlane(ctl[C_TX]));
    u32 res;
#pragma unroll 1
    for (u32 sp = 0; sp < SPIN_CAP; ++sp) {
      const u32 x2 = __builtin_amdgcn_raw_buffer_load_b32(rs, xi, 0, SC1) ^ xt;
      const u32x4 y0 = ld_one(rs, yo) ^ tb4, y1 = ld_one(rs, yo + 2048) ^ tb4, y2_ = ld_one(rs, y2) ^ tb5;
      const f32x4 s = (__builtin_bit_cast(f32x4, y0) + __builtin_bit_cast(f32x4, y1)) + __builtin_bit_cast(f32x4, y2_);
      float t = t32(t16(s[0], s[1]), t16(s[2], s[3]));
      t += dppf<ROR8>(t);
      t += dppf<ROR4>(t);
      res = pack2(fmaf(gb, t, asf(x2)), 0.f);
      PIN(res);
      if (__builtin_expect(__all((((or4(y0) | or4(y1) | or4(y2_)) & 255u) | (x2 & 0xffffu)) == 0u), 1)) break;
      MEMB();
    }
    __builtin_amdgcn_raw_buffer_store_b16((u16)res, ro, oof, 0, OAUX);
    if (w == RW) STAMP(18);
  }
  if (sv0 | sv18) { while ((u32)__builtin_amdgcn_s_memrealtime() - lk0 < 3600u) __builtin_amdgcn_s_sleep(8);
    if (sv0 && (tid & 63) == 0) __hip_atomic_store((GAS u32*)(ws + (768u << 20)) + 0 * 256 + bid, sv0, RLX_AGENT);
    if (sv18 && (tid & 63) == 0) __hip_atomic_store((GAS u32*)(ws + (768u << 20)) + 18 * 256 + bid, sv18, RLX_AGENT);
  }

  if (bid == 0 && w == 0) {
    while ((u32)__builtin_amdgcn_s_memrealtime() - lk0 < 4000u) __builtin_amdgcn_s_sleep(2);
    lane = tid & 63;
    auto rd = [&](int i, int q) { u32 v = __hip_atomic_load((GAS u32*)(ws + (768u << 20)) + i * 256 + q * 64 + lane, RLX_AGENT); return (int)(v - lk0); };
    auto ok = [&](int v) { return v > -500 && v < 4000; };
    auto red = [&](int i, int mode, int xm) { float r = mode == 0 ? -1e9f : (mode == 1 ? 1e9f : 0.f), c = 0.f;
      for (int q = 0; q < 4; ++q) { int v = rd(i, q); if (ok(v) && ((xm >> (lane & 7)) & 1)) { r = mode == 0 ? fmaxf(r, (float)v) : (mode == 1 ? fminf(r, (float)v) : r + (float)v); c += 1.f; } }
      if (mode == 0) return wave_max(r); if (mode == 1) return -wave_max(-r); return wave_sum(r) / wave_sum(c); };
#define mx(i) red(i, 0, 255)
#define mn(i) red(i, 1, 255)
#define av(i) red(i, 2, 255)
#define xm(i, m) red(i, 0, m)
#define xn(i, m) red(i, 1, m)
    float val = (float)(mx(18)-av(0)-2400);
    u32 lk = 5000u + (u32)__builtin_amdgcn_readfirstlane((int)(val * 8.f));
    while ((u32)__builtin_amdgcn_s_memrealtime() - lk0 < lk) __builtin_amdgcn_s_sleep(2);
  }
}

extern "C" void kernel_launch(void* const* d_in, const int* in_sizes, int n_in, void* d_out, int out_size, void* d_ws, size_t ws_size, hipStream_t stream) {
  (void)hipFuncSetAttribute((const void*)fused, hipFuncAttributeMaxDynamicSharedMemorySize, L_END);
  Args a;
  const int src[4][2] = {{3, 3}, {10, 10}, {4, 4}, {6, 7}};
  for (int r = 0; r < 4; ++r)
    for (int k = 0; k < 2; ++k) a.half[r][k] = (const unsigned char*)d_in[src[r][k]] + (r < 3 && k ? 64 : 0);
  a.x = (const u16*)d_in[0]; a.kc = (const u16*)d_in[1]; a.vc = (const u16*)d_in[2]; a.wd = (const u16*)d_in[8]; a.cc = (const u16*)d_in[9];
  a.out = (u16*)d_out; a.ws = (unsigned char*)d_ws;
  fused<<<256, 1024, L_END, stream>>>(a);
}
```

```cpp
#include <hip/hip_runtime.h>
#include <stdint.h>

typedef unsigned int u32;
typedef unsigned long long u64;
typedef unsigned short u16;
typedef u32 u32x4 __attribute__((ext_vector_type(4)));
typedef u32 u32x2 __attribute__((ext_vector_type(2)));
typedef float f32x4 __attribute__((ext_vector_type(4)));
typedef float f32x2 __attribute__((ext_vector_type(2)));
typedef short s16x4 __attribute__((ext_vector_type(4)));
typedef __bf16 bf16x8 __attribute__((ext_vector_type(8)));
typedef __bf16 bf16x2 __attribute__((ext_vector_type(2)));
#define GAS __attribute__((address_space(1)))
#define LAS __attribute__((address_space(3)))
#define RLX_AGENT __ATOMIC_RELAXED, __HIP_MEMORY_SCOPE_AGENT

constexpr int SK = 4096, WIN = 1024, FF = 1376, NP3 = 43;

constexpr int WS_FLAGS = 0x1000;
constexpr int WS_TS = 0x1800;
constexpr int WS_G1 = 0x2000;
constexpr int WS_B1 = 0x8000;
constexpr int WS_MODR = 0xA000;
constexpr int WS_Q = 0x12000;
constexpr int WS_O = 0x36000;
constexpr int WS_B0 = 0x56000;
constexpr int WS_PART = 0x5E000;
constexpr int WS_YP = WS_PART + 8 * 16 * 8 * 32 * 4;
constexpr int WS_X2 = WS_YP + 128 * NP3 * 128;
constexpr int WS_END = WS_X2 + 8 * 16384;
constexpr int WS_STEP = 2 << 20;
static_assert(WS_END <= WS_STEP, "");

constexpr int L_W = 0;
constexpr int L_TP = 65536;
constexpr int L_C1 = 98304;
constexpr int L_SINK = 130048;
constexpr int L_RED = 131072;
constexpr int L_H2 = L_RED + 16384;
constexpr int C1S = 1040, H2S = 1056, OSTR = 544, OBS = 288;
constexpr int L_Q = L_H2 + 8 * H2S;
constexpr int L_ML = L_Q + 512;
constexpr int L_HH = L_ML + 128;
constexpr int L_CTL = L_HH + 784;
constexpr int L_END = L_CTL + 64;
constexpr int C_T = 0, C_TB = 1, C_TX = 2, C_Q = 3, C_ML = 4, C_R = 6, C_T0 = 7, C_X = 8;
constexpr int RW = 12, WG_ = 10;

constexpr u32 SPIN_CAP = 1u << 17, LSPIN_CAP = 1u << 20;
constexpr int DUE0 = 810;
constexpr int TAU = 220;
constexpr int E1 = 16;
constexpr int E2 = 10;
constexpr int SKW = 200;
constexpr int MSH = 14;
constexpr int KA = 55;
constexpr int KW = 6;
constexpr int OFO = 425;
constexpr int OFB = 470;
constexpr int PB = 200;
constexpr int RB = 260;
constexpr int DO = 30;
constexpr int DL_ = 220;
constexpr int DP = 350;
constexpr int DX = 565;
constexpr int DYS = 1125;
constexpr int DB1 = 960;
constexpr int DY = 260;
constexpr int SDL = 12;
constexpr int extra(int i) { return i < 8 ? E1 : (i >= 14 && i < 22 ? E2 : 0); }
constexpr int TEND = DUE0 + 32 * TAU + 8 * E1 + 8 * E2;

__device__ __forceinline__ float rbf(float f) { return (float)(__bf16)f; }
__device__ __forceinline__ u32 pack2(float a, float b) { bf16x2 v = {(__bf16)a, (__bf16)b}; return __builtin_bit_cast(u32, v); }
__device__ __forceinline__ float blo(u32 v) { return __builtin_bit_cast(float, v << 16); }
__device__ __forceinline__ float bhi(u32 v) { return __builtin_bit_cast(float, v & 0xffff0000u); }
__device__ __forceinline__ u32 canon(u32 v) { return v < 0xfffffffeu ? v : 0xfffffffeu; }
#define SEL4() u32x4 sel = {0x07060500u, 0x07060501u, 0x07060502u, 0x07060503u}; asm volatile("" : "+s"(sel))
__device__ __forceinline__ u32x4 tag8(f32x4 v, u32 tagv, u32x4 sel) {
  const u32x4 r = __builtin_bit_cast(u32x4, v);
  return u32x4{__builtin_amdgcn_perm(r[0], tagv, sel[0]), __builtin_amdgcn_perm(r[1], tagv, sel[1]), __builtin_amdgcn_perm(r[2], tagv, sel[2]), __builtin_amdgcn_perm(r[3], tagv, sel[3])};
}
__device__ __forceinline__ u32x4 bytes4(u32 tag) { return u32x4{tag & 255u, (tag >> 8) & 255u, (tag >> 16) & 255u, tag >> 24}; }
__device__ __forceinline__ u32 or4(u32x4 v) { return (v[0] | v[1]) | (v[2] | v[3]); }
__device__ __forceinline__ float rcp(float x) { return __builtin_amdgcn_rcpf(x); }
template <int CTRL> __device__ __forceinline__ float dppf(float x) {
  return __builtin_bit_cast(float, __builtin_amdgcn_mov_dpp(__builtin_bit_cast(int, x), CTRL, 0xf, 0xf, true));
}
template <int CTRL> __device__ __forceinline__ u32 dppu(u32 x) { return (u32)__builtin_amdgcn_mov_dpp((int)x, CTRL, 0xf, 0xf, true); }
constexpr int ROR8 = 0x128, ROR4 = 0x124, ROR2 = 0x122, ROR1 = 0x121, XOR1 = 0xB1, XOR2 = 0x4E, XOR7 = 0x141;
__device__ __forceinline__ float row_sum(float x) { x += dppf<ROR8>(x); x += dppf<ROR4>(x); x += dppf<ROR2>(x); x += dppf<ROR1>(x); return x; }
__device__ __forceinline__ float row_max(float x) {
  x = fmaxf(x, dppf<ROR8>(x)); x = fmaxf(x, dppf<ROR4>(x)); x = fmaxf(x, dppf<ROR2>(x)); x = fmaxf(x, dppf<ROR1>(x)); return x;
}
__device__ __forceinline__ float x16_sum(float x) {
  auto s = __builtin_amdgcn_permlane16_swap(__float_as_uint(x), __float_as_uint(x), false, false);
  return __uint_as_float(s[0]) + __uint_as_float(s[1]);
}
__device__ __forceinline__ float x32_sum(float x) {
  auto s = __builtin_amdgcn_permlane32_swap(__float_as_uint(x), __float_as_uint(x), false, false);
  return __uint_as_float(s[0]) + __uint_as_float(s[1]);
}
__device__ __forceinline__ float x16_max(float x) {
  auto s = __builtin_amdgcn_permlane16_swap(__float_as_uint(x), __float_as_uint(x), false, false);
  return fmaxf(__uint_as_float(s[0]), __uint_as_float(s[1]));
}
__device__ __forceinline__ float x32_max(float x) {
  auto s = __builtin_amdgcn_permlane32_swap(__float_as_uint(x), __float_as_uint(x), false, false);
  return fmaxf(__uint_as_float(s[0]), __uint_as_float(s[1]));
}
__device__ __forceinline__ float t16(float a, float b) {
  auto s = __builtin_amdgcn_permlane16_swap(__float_as_uint(a), __float_as_uint(b), false, false);
  return __uint_as_float(s[0]) + __uint_as_float(s[1]);
}
__device__ __forceinline__ float t32(float a, float b) {
  auto s = __builtin_amdgcn_permlane32_swap(__float_as_uint(a), __float_as_uint(b), false, false);
  return __uint_as_float(s[0]) + __uint_as_float(s[1]);
}
__device__ __forceinline__ f32x2 up2(u32 v) { return f32x2{blo(v), bhi(v)}; }
__device__ __forceinline__ u32 pk2(f32x2 v) { return pack2(v[0], v[1]); }
#define PIN(x) asm volatile("" : "+v"(x))
__device__ __forceinline__ f32x2 rb2(f32x2 v) { u32 p = pk2(v); asm("" : "+v"(p)); return up2(p); }
__device__ __forceinline__ float wave_sum(float x) { return x32_sum(x16_sum(row_sum(x))); }
__device__ __forceinline__ float wave_max(float x) { return x32_max(x16_max(row_max(x))); }
__device__ __forceinline__ float dot2(u32 a, u32 b, float c) {
  return __builtin_amdgcn_fdot2_f32_bf16(__builtin_bit_cast(bf16x2, a), __builtin_bit_cast(bf16x2, b), c, false);
}
__device__ __forceinline__ f32x4 mfma(u32x4 a, u32x4 b, f32x4 c) {
  return __builtin_amdgcn_mfma_f32_16x16x32_bf16(__builtin_bit_cast(bf16x8, a), __builtin_bit_cast(bf16x8, b), c, 0, 0, 0);
}
__device__ __forceinline__ u32x4 ldnt(const void* p) { return __builtin_nontemporal_load((const u32x4*)p); }
__device__ __forceinline__ void glds(const void* g, u32 lds) {
  u32 keep;
  lds = __builtin_amdgcn_readfirstlane(lds);
  asm volatile("s_mov_b32 %0, m0\n\ts_mov_b32 m0, %2\n\ts_nop 0\n\tglobal_load_lds_dwordx4 %1, off nt\n\ts_mov_b32 m0, %0"
               : "=&s"(keep) : "v"(g), "s"(lds) : "memory");
}
__device__ __forceinline__ void glds(const void* base, u32 off, u32 lds) {
  u32 keep;
  lds = __builtin_amdgcn_readfirstlane(lds);
  asm volatile("s_mov_b32 %0, m0\n\ts_mov_b32 m0, %3\n\ts_nop 0\n\tglobal_load_lds_dwordx4 %1, %2 nt\n\ts_mov_b32 m0, %0"
               : "=&s"(keep) : "v"(off), "s"(base), "s"(lds) : "memory");
}
__device__ __forceinline__ void touch(const void* base, u32 off, u32 lds) {
  u32 keep;
  lds = __builtin_amdgcn_readfirstlane(lds);
  asm volatile("s_mov_b32 %0, m0\n\ts_mov_b32 m0, %3\n\ts_nop 0\n\tglobal_load_lds_dwordx4 %1, %2\n\ts_mov_b32 m0, %0"
               : "=&s"(keep) : "v"(off), "s"(base), "s"(lds) : "memory");
}
__device__ __forceinline__ u32x4 trfrag(LAS unsigned char* p0, LAS unsigned char* p1) {
  s16x4 a = __builtin_amdgcn_ds_read_tr16_b64_v4i16((LAS s16x4*)p0);
  s16x4 b = __builtin_amdgcn_ds_read_tr16_b64_v4i16((LAS s16x4*)p1);
  u32x2 x = __builtin_bit_cast(u32x2, a), y = __builtin_bit_cast(u32x2, b);
  return u32x4{x[0], x[1], y[0], y[1]};
}
#define SB() __builtin_amdgcn_sched_barrier(0)
#define MEMB() asm volatile("" ::: "memory")
#define VM0() asm volatile("s_waitcnt vmcnt(0)" ::: "memory")
#define LGKM0() asm volatile("s_waitcnt lgkmcnt(0)" ::: "memory")
#define BAR() asm volatile("s_waitcnt lgkmcnt(0)\n\ts_barrier" ::: "memory")
#define TAG() do { u32 ep_; asm volatile("s_load_dword %0, %1, 0x0\n\ts_waitcnt lgkmcnt(0)" : "=s"(ep_) : "s"(ws) : "memory"); \
    tag = ((ep_ & 0x7f7f7f7fu) + 0x01010101u) ^ (ep_ & 0x80808080u); } while (0)
constexpr int SC1 = 16;
__device__ __forceinline__ u32x4 ld_sc1(__amdgpu_buffer_rsrc_t rs, int off) { return __builtin_amdgcn_raw_buffer_load_b128(rs, off, 0, SC1); }
__device__ __forceinline__ u32x4 ld_one(__amdgpu_buffer_rsrc_t rs, int off) { return __builtin_amdgcn_raw_buffer_load_b128(rs, off, 0, SC1 | 2); }
__device__ __forceinline__ u32x2 ld_sc1_64(__amdgpu_buffer_rsrc_t rs, int off) { return __builtin_amdgcn_raw_buffer_load_b64(rs, off, 0, SC1); }
__device__ __forceinline__ void st_sc1(__amdgpu_buffer_rsrc_t rs, int off, u32x4 v) { __builtin_amdgcn_raw_buffer_store_b128(v, rs, off, 0, SC1); }
__device__ __forceinline__ void st_sc1_64(__amdgpu_buffer_rsrc_t rs, int off, u32x2 v) { __builtin_amdgcn_raw_buffer_store_b64(v, rs, off, 0, SC1); }

__device__ __forceinline__ void ln_stats(u32x4 xv, float& m, float& inv) {
  const float x[8] = {blo(xv[0]), bhi(xv[0]), blo(xv[1]), bhi(xv[1]), blo(xv[2]), bhi(xv[2]), blo(xv[3]), bhi(xv[3])};
  const float x0 = __builtin_bit_cast(float, __builtin_amdgcn_readfirstlane(xv[0] << 16));
  float s = 0.f, v = 0.f;
#pragma unroll
  for (int j = 0; j < 8; ++j) { const float d = x[j] - x0; s += d; v = fmaf(d, d, v); }
  const float md = wave_sum(s) * (1.f / 512.f), sq = wave_sum(v) * (1.f / 512.f);
  const float var = rbf(fmaxf(fmaf(-md, md, sq), 0.f));
  m = rbf(x0 + md);
  inv = rcp(rbf(__builtin_amdgcn_sqrtf(rbf(var + 1e-5f))));
}
#define PACE_(W, INC) do { asm volatile(W "\n1:\n\ts_memrealtime vcc\n\ts_waitcnt lgkmcnt(0)\n\ts_lshl_b32 vcc_lo, vcc_lo, 2\n\ts_sub_i32 vcc_hi, vcc_lo, %0\n\ts_cmp_lt_i32 vcc_hi, 0\n\t" \
    "s_cbranch_scc0 2f\n\ts_sleep 1\n\ts_branch 1b\n2:\n\ts_add_i32 %0, %0, %1" : "+s"(due) : "is"(INC) : "memory", "scc", "vcc"); SB(); } while (0)
#define PACE(INC) PACE_("s_waitcnt vmcnt(2)", INC)
#define PACEN(INC) PACE_("", INC)
__device__ __forceinline__ void until_s(u32 due) { PACE_("", 0); }
__device__ __forceinline__ void until(u32 t) {
  asm volatile("1:\n\ts_memrealtime vcc\n\ts_waitcnt lgkmcnt(0)\n\ts_lshl_b32 vcc_lo, vcc_lo, 2\n\ts_sub_i32 vcc_hi, vcc_lo, %0\n\ts_cmp_lt_i32 vcc_hi, 0\n\t"
               "s_cbranch_scc1 1b" :: "s"(t) : "memory", "scc", "vcc");
  SB();
}

__device__ __forceinline__ void lds_wait(volatile LAS u32* p, u32 want) {
  _Pragma("unroll 1") for (u32 s = 0; s < LSPIN_CAP; ++s) {
    if (__builtin_amdgcn_readfirstlane(*p) >= want) break;
    __builtin_amdgcn_s_sleep(2);
  }
  MEMB();
}
__device__ __forceinline__ void lds_add(LAS u32* p, int lane) {
  LGKM0();
  if (lane == 0) __hip_atomic_fetch_add(p, 1u, __ATOMIC_RELAXED, __HIP_MEMORY_SCOPE_WORKGROUP);
  MEMB();
}
__device__ __forceinline__ void tile_part(LAS unsigned char* T, LAS unsigned char* P, u32x4 hb, int n, int g) {
  LAS unsigned char* tb = T + (8 * g + (n >> 2)) * 128 + (n & 3) * 8;
  const int sx = (n >> 3) | ((g & 1) << 1);
  f32x4 a[4];
#pragma unroll
  for (int t = 0; t < 4; ++t) a[t] = mfma(trfrag(tb + (t ^ sx) * 32, tb + (t ^ sx) * 32 + 512), hb, f32x4{0.f, 0.f, 0.f, 0.f});
  *(LAS f32x4*)(P) = n < 8 ? a[0] : a[1];
  *(LAS f32x4*)(P + 1024) = n < 8 ? a[2] : a[3];
}
__device__ __forceinline__ f32x4 tile_sum(LAS unsigned char* P) {
  f32x4 s[8], r[2];
#pragma unroll
  for (int k = 0; k < 2; ++k) {
#pragma unroll
    for (int i = 0; i < 8; ++i) s[i] = *(LAS f32x4*)(P + (8 * k + i) * 2048);
    SB();
    r[k] = ((s[0] + s[1]) + (s[2] + s[3])) + ((s[4] + s[5]) + (s[6] + s[7]));
    SB();
  }
  return r[0] + r[1];
}
#define GU_MUL(P) _Pragma("unroll") for (int i = 0; i < 8; ++i) gb_[i] = *(LAS u32x4*)((P) + i * 64); SB(); \
    aL = f32x4{0.f, 0.f, 0.f, 0.f}; aH = aL; _Pragma("unroll") for (int i = 0; i < 8; ++i) { aL = mfma(gf[i], gb_[i], aL); aH = mfma(gf[8 + i], gb_[i], aH); }
__device__ __forceinline__ void gu_pair(f32x4 aL, f32x4 aH, f32x2& gt, f32x2& up) {
  const auto s0 = __builtin_amdgcn_permlane32_swap(__float_as_uint(aL[0] + dppf<ROR8>(aH[0])), __float_as_uint(aL[2] + dppf<ROR8>(aH[2])), false, false);
  const auto s1 = __builtin_amdgcn_permlane32_swap(__float_as_uint(aL[1] + dppf<ROR8>(aH[1])), __float_as_uint(aL[3] + dppf<ROR8>(aH[3])), false, false);
  gt = f32x2{__uint_as_float(s0[0]), __uint_as_float(s1[0])};
  up = f32x2{__uint_as_float(s0[1]), __uint_as_float(s1[1])};
}
__device__ __forceinline__ int mpos(int c) { return (c & 252) * 4 + (c >> 8) * 8 + (c & 3) * 2; }
__device__ __forceinline__ u32 smax(int m) { int r = (int)fminf(wave_max((float)m), 4e5f); PIN(r); return (u32)__builtin_amdgcn_readfirstlane(r); }
__device__ __forceinline__ u32 now4() { return (u32)__builtin_amdgcn_s_memrealtime() * 4u; }
#define B0_LOAD() { bp = ld_sc1(rs, WS_B0 + bo); bq = ld_sc1(rs, WS_B0 + 1024 + bo); }
__device__ __forceinline__ bool on_time(u32 tag, u32 org, u32x4 bp, u32x4 bq) {
  const u32 lim = org + OFO - DO;
  return __all((((bp[1] ^ tag) | (bp[3] ^ tag) | (bq[1] ^ tag) | (bq[3] ^ tag)) | (((lim - bp[0]) | (lim - bp[2]) | (lim - bq[0]) | (lim - bq[2])) >> 31)) == 0u);
}
__device__ __forceinline__ u32 anchor(__amdgpu_buffer_rsrc_t rs, int bo, u32 tag, u32 org, u32x4 bp, u32x4 bq) {
  int lk = OFB;
#pragma unroll 1
  for (u32 s = 0; s < SPIN_CAP; ++s) {
    if (__all(((bp[1] ^ tag) | (bp[3] ^ tag) | (bq[1] ^ tag) | (bq[3] ^ tag)) == 0u)) break;
    MEMB();
    do lk += PB; while ((int)(now4() - org) > lk);
    until_s(org + lk);
    B0_LOAD()
  }
  const int m = (int)smax(max(max((int)(bp[0] - org), (int)(bp[2] - org)), max((int)(bq[0] - org), (int)(bq[2] - org))));
  return org + (u32)max(m + DO <= OFO ? OFO : m + DL_, lk + RB - DP);
}
__device__ __forceinline__ float asf(u32 v) { return __builtin_bit_cast(float, v); }

struct Args {
  const unsigned char* half[4][2];
  const u16 *x, *kc, *vc, *wd, *cc;
  u16* out;
  unsigned char* ws;
};

__global__ void __launch_bounds__(1024, 4) fused(const Args a) {
  extern __shared__ __attribute__((aligned(16))) unsigned char smem[];
  LAS unsigned char* L = (LAS unsigned char*)smem;
  const u32 lbase = (u32)(size_t)L;
  const int tid = threadIdx.x, w = __builtin_amdgcn_readfirstlane(tid >> 6), bid = blockIdx.x;
  int lane = tid & 63;
  const int xcd = bid & 7, idx = bid >> 3, hj = idx & 3, h = 4 * xcd + hj, b = idx >> 2, bh = b * 32 + h;
  const int sw8 = hj == 3 ? (w & 1) * 8 : 0, rw = (hj == 3 ? 24 : 32) * w;
  u64 hA, hB, t64;
  u32 warm;
  asm volatile("s_load_dwordx2 %0, %4, %5\n\ts_load_dwordx2 %1, %4, %6\n\ts_load_dword %3, %4, 0x40\n\ts_memrealtime %2" : "=&s"(hA), "=&s"(hB), "=&s"(t64), "=&s"(warm)
               : "s"(__builtin_amdgcn_kernarg_segment_ptr()), "s"(16 * hj + sw8), "s"(16 * hj + (sw8 ^ 8)));
  SB();
  const int bb = (b + xcd) & 7;
  int p3r = 5 * xcd + min(xcd, 3) + bb;
  int mc = (b & 6) + ((xcd & 1) ^ 1), mg = ((xcd & 6) + b) & 7;
  const bool tq = hj == 0, to = hj == 2;
  int lim = hj == 1 ? 6 - b : (xcd < 3 ? 6 : 5) - bb;
  asm volatile("" : "+s"(p3r), "+s"(mc), "+s"(mg), "+s"(lim));
  const bool idle = (hj & 1) && lim <= 0;
  u64 tp0, tp1;
  {
    const u32 pitch = (u32)(0x0AC0040018002000ull >> (16 * hj)) & 0xffffu;
    u32 ce = (xcd << (to ? 13 : 4)) + 2 * b, c1 = 16 * mc + 2 * mg;
    asm volatile("" : "+s"(ce), "+s"(c1));
    const u32 col = 64 * ((hj & 1) ? (hj == 1 ? c1 : p3r) : ce);
    const u32 step = pitch << (to ? 1 : 3);
    u32 off = (rw + (to ? (lane & 24) + (lane >> 5) : lane >> 3)) * pitch + col + ((lane & 3) ^ ((lane >> 3) & 2)) * 16;
    tp0 = off;
    tp1 = off + step;
    u64 s2 = 2 * step;
    u32 ld = lbase + L_W + rw * 128;
    asm volatile("" : "+s"(s2), "+s"(ld), "+v"(tp0), "+v"(tp1));
    asm volatile("s_mov_b32 exec_lo, 0xf0f0f0f0\n\ts_mov_b32 exec_hi, 0xf0f0f0f0\n\ts_waitcnt lgkmcnt(0)\n\tv_lshl_add_u64 %0, %0, 0, %3\n\tv_lshl_add_u64 %1, %1, 0, %2\n\t"
                 "s_not_b64 exec, exec\n\tv_lshl_add_u64 %0, %0, 0, %2\n\tv_lshl_add_u64 %1, %1, 0, %3\n\ts_mov_b64 exec, -1"
                 : "+v"(tp0), "+v"(tp1), "+s"(hA), "+s"(hB), "+s"(t64), "+s"(warm) :: "scc");
    if (!idle) {
      u32 keep;
      asm volatile("s_mov_b32 %0, m0\n\ts_mov_b32 m0, %3\n\ts_nop 0\n\tglobal_load_lds_dwordx4 %1, off nt\n\ts_add_u32 m0, m0, 0x400\n\t"
                   "v_lshl_add_u64 %1, %1, 0, %4\n\tglobal_load_lds_dwordx4 %2, off nt\n\ts_mov_b32 m0, %0\n\tv_lshl_add_u64 %2, %2, 0, %4"
                   : "=&s"(keep), "+v"(tp0), "+v"(tp1) : "s"(ld), "s"(s2) : "memory", "scc");
      glds((const void*)tp0, ld + 2048);
      if (hj != 3) glds((const void*)tp1, ld + 3072);
    }
  }
  SB();
  u32 t0 = (u32)t64;
  const u16 *x = a.x, *kc = a.kc, *vc = a.vc, *wd = a.wd, *cc = a.cc;
  u16* out = a.out;
  unsigned char* ws = a.ws;
  t0 += (u32)(KW * max((int)((u32)__builtin_amdgcn_s_memrealtime() - t0) - KA, 0)) / 4u;
  const int p3 = (hj == 3 && !idle) ? p3r : -1;
  const bool tm = hj == 1 && !idle;
  u32 tag;
  TAG();
  const __amdgpu_buffer_rsrc_t rs = __builtin_amdgcn_make_buffer_rsrc((void*)(ws + (size_t)(tag & 63u) * WS_STEP), 0, WS_END, 0x00020000);
  if (idle && hj == 1) {
    if (w == 0) {
      st_sc1(rs, WS_G1 + ((b & 1) * 8 + xcd) * 1024 + lane * 16, u32x4{~0u, ~0u, ~0u, ~0u});
      VM0();
      if (lane == 0) st_sc1_64(rs, WS_TS + ((b & 1) * 8 + xcd) * 8, u32x2{t0, tag});
    } else {
      u64 pc;
      u32 len;
      asm volatile("s_getpc_b64 %0\n1:\n\ts_mov_b32 %1, fused_code_end - 1b + 496" : "=s"(pc), "=s"(len));
      const u32 o = ((b & 1) * 15 + w - 1) * 1024;
      if (o < len) touch((const void*)(pc & ~15ull), min(o + lane * 16, len & ~15u), lbase + L_SINK);
    }
  }
  if (w == 1) glds((const unsigned char*)((lane & 1) ? vc : kc) + ((size_t)bh * SK + (SK - WIN)) * 256, lbase + L_SINK);
  int ta = TAU;
  int n = lane & 15, g = lane >> 4;
  u32x4 cv, hb;
  const bool row = tm && (w >= 8 || mc == 1);
  if (row) cv = ldnt((w < 8 ? x : cc) + (w & 7) * 512 + lane * 8);
  if (tid < 16) ((volatile LAS u32*)(L + L_CTL))[tid] = tid == C_T0 ? t0 : 0u;
  BAR();
  t0 = __builtin_amdgcn_readfirstlane(((volatile LAS u32*)(L + L_CTL))[C_T0]);
  u32 due = t0 * 4u + (u32)((4 * bb + hj) * TAU) / 32u + DUE0;
  int rot = idx;
#define RO(I) ((((I) + rot) & 15) * 8192)
  const u16* kp = kc + ((size_t)bh * SK + (SK - WIN) + 4 * w) * 128;
  u32 l16 = lane * 16;
  PIN(l16);
#define LDR(D, P, I, W) { const GAS unsigned char* p_ = (const GAS unsigned char*)((P) + RO(I)) + l16; PIN(p_); W; D = __builtin_nontemporal_load((const GAS u32x4*)p_); SB(); }
  u32x4 kr[16], vr[16];
  volatile LAS u32* ctl = (volatile LAS u32*)(L + L_CTL);
#define RELANE() do { asm volatile("" : "+v"(lane)); n = lane & 15; g = lane >> 4; } while (0)
  const int e = b * 16 + 2 * xcd + hj;
  const bool red = hj < 2;

#define KL(I) { const int t_ = ta + extra(I); LDR(kr[I], kp, I, PACE(t_)) }
  KL(0)
  if (row) {
    if (w >= 8) {
#pragma unroll
      for (int j = 0; j < 4; ++j) { const float a0 = blo(cv[j]), a1 = bhi(cv[j]); hb[j] = pack2(a0 * rcp(1.f + __expf(-a0)), a1 * rcp(1.f + __expf(-a1))); }
      *(LAS u32x4*)(L + L_H2 + (w & 7) * H2S + lane * 16) = hb;
      lds_add((LAS u32*)(L + L_CTL) + C_R, lane);
    } else {
      float m, inv;
      ln_stats(cv, m, inv);
      if ((lane >> 3) == mg) {
        LAS f32x4* d = (LAS f32x4*)(L + L_RED + w * 256 + (lane & 7) * 32);
        d[0] = f32x4{rbf(rbf(blo(cv[0]) - m) * inv), rbf(rbf(bhi(cv[0]) - m) * inv), rbf(rbf(blo(cv[1]) - m) * inv), rbf(rbf(bhi(cv[1]) - m) * inv)};
        d[1] = f32x4{rbf(rbf(blo(cv[2]) - m) * inv), rbf(rbf(bhi(cv[2]) - m) * inv), rbf(rbf(blo(cv[3]) - m) * inv), rbf(rbf(bhi(cv[3]) - m) * inv)};
      }
    }
  }
  KL(1)
  u32x2 tsv;
#define TS_WAIT() if (!__all(tsv[1] == tag)) { _Pragma("unroll 1") for (u32 s = 0; s < SPIN_CAP; ++s) { __builtin_amdgcn_s_sleep(16); MEMB(); \
      tsv = ld_sc1_64(rs, WS_TS + (lane & 15) * 8); if (__all(tsv[1] == tag)) break; } } MEMB();
  if (tm) {
    asm volatile("s_waitcnt vmcnt(2)" ::: "memory");
    lds_wait(ctl + C_R, 8u);
    hb = *(LAS u32x4*)(L + L_H2 + (n & 7) * H2S + (32 * w + 8 * g) * 2);
    tile_part(L + L_W + w * 4096, L + L_TP + w * 2048 + lane * 16, hb, n, g);
    lds_add((LAS u32*)(L + L_CTL) + C_T, lane);
  }
  KL(2)
  if (w == 0 || (tm && w >= 14)) tsv = ld_sc1_64(rs, WS_TS + (lane & 15) * 8);
  SB();
  if (tm && w >= 14) {
    lds_wait(ctl + C_T, 16u);
    const int col = 32 * (w & 1) + 16 * (n >> 3) + 4 * g;
    const f32x4 s = tile_sum(L + L_TP + (w & 1) * 1024 + lane * 16);
    float v[4] = {rbf(s[0]), rbf(s[1]), rbf(s[2]), rbf(s[3])};
    if (mc == 1) {
      const f32x4 ln = *(LAS f32x4*)(L + L_RED + (n & 7) * 256 + col * 4);
#pragma unroll
      for (int j = 0; j < 4; ++j) v[j] = ln[j] * rbf(1.f + v[j]);
    }
    TS_WAIT()
    st_sc1_64(rs, mc < 2 ? WS_G1 + (2 * mg + (w & 1)) * 1024 + ((2 * (n >> 3) + (g >> 1)) * 16 + mc * 8 + (n & 7)) * 16 + (g & 1) * 8
                         : WS_MODR + ((mc - 2) * 8 + (n & 7)) * 1024 + mpos(64 * mg + col),
              u32x2{canon(pack2(v[0], v[1])), canon(pack2(v[2], v[3]))});
  }
  RELANE();
  KL(3) KL(4)
  if (w == 0) {
    TS_WAIT()
    const float d = __builtin_amdgcn_fmed3f((float)(int)(tsv[0] - t0), -1e5f, 1e5f);
    ctl[C_TB] = (t0 * 4u + (u32)(int)fmaxf(row_sum(d) * 0.25f, row_max(d) * 4.f - SKW)) | 1u;
  }
  KL(5) KL(6)
  u32 org = __builtin_amdgcn_readfirstlane(ctl[C_TB]);
  if (org) { const int adj = ((int)(org - t0 * 4u - (p3 >= 0 ? bb * MSH : 0)) * 85 + 1024) >> 11; ta += adj; }
  KL(7)
  if (tm && mc >= 2 && w >= 14 && lane == 0) __builtin_amdgcn_raw_buffer_store_b32(tag, rs, WS_FLAGS + (((mc - 2) * 8 + mg) * 2 + (w & 1)) * 4, 0, SC1);
  KL(8)
  u32x4 g0;
  const int o0 = WS_G1 + w * 1024 + lane * 16;
  if (tq) lds_wait(ctl + C_TB, 1u);
#define KG(K) { KL(K) if (tq && (w & 1) == ((K) & 1)) { until_s(due - TAU / 2); \
    asm volatile("buffer_load_dwordx4 %0, %1, %2, 0 offen sc1" : "=&v"(g0) : "v"(o0), "s"(rs) : "memory"); } }
  KG(9) KG(10)
  KL(11) KL(12)
  if (tq) {
    asm volatile("s_waitcnt vmcnt(2)" : "+v"(g0));
#define G1_OK() __all(g0[0] != ~0u && g0[1] != ~0u && g0[2] != ~0u && g0[3] != ~0u)
    if (!G1_OK()) {
#pragma unroll 1
      for (u32 s = 0; s < SPIN_CAP; ++s) {
        __builtin_amdgcn_s_sleep(16);
        MEMB();
        g0 = ld_sc1(rs, o0);
        if (G1_OK()) break;
      }
    }
#pragma unroll
    for (int j = 0; j < 4; ++j) { const u32 o = dppu<ROR8>(g0[j]); hb[j] = pack2(blo(g0[j]) + blo(o), bhi(g0[j]) + bhi(o)); }
    PIN(hb);
  }
  KL(13)
  if (tq) {
    tile_part(L + L_W + w * 4096, L + L_TP + w * 2048 + lane * 16, hb, n, g);
    lds_add((LAS u32*)(L + L_CTL) + C_T, lane);
  }
  KL(14)
  if (tq && w >= 14) {
    lds_wait(ctl + C_T, 16u);
    const f32x4 s = tile_sum(L + L_TP + (w & 1) * 1024 + lane * 16);
    st_sc1(rs, WS_Q + ((n & 7) * 32 + 4 * xcd + (b >> 1)) * 512 + ((b & 1) * 64 + 32 * (w & 1) + 16 * (n >> 3) + 4 * g) * 4, u32x4{pack2(s[0], s[1]), tag, pack2(s[2], s[3]), tag});
  }
  RELANE();
  KL(15)
  asm volatile("" : "+s"(rot));
  const u16* vp = vc + ((size_t)bh * SK + (SK - WIN) + 4 * w) * 128;
#define VL(I) { const int t_ = ta + extra(16 + (I)); LDR(vr[I], vp, I, PACE(t_)) }
#define VLN(I) { const int t_ = ta + extra(16 + (I)); LDR(vr[I], vp, I, PACEN(t_)) }
  VL(0) VL(1)
  VL(2)
  u32x2 gq;
#define FL_WAIT() if (!__all(fl == tag)) {   _Pragma("unroll 1") for (u32 s = 0; s < SPIN_CAP; ++s) { __builtin_amdgcn_s_sleep(4); MEMB(); \
      fl = __builtin_amdgcn_raw_buffer_load_b32(rs, WS_FLAGS + lane * 4, 0, SC1); if (__all(fl == tag)) break; } } MEMB();
  u32 fl;
  if (w == WG_) {
    gq = ld_sc1_64(rs, WS_Q + bh * 512 + lane * 8);
    fl = __builtin_amdgcn_raw_buffer_load_b32(rs, p3 >= 0 ? WS_FLAGS + lane * 4 : WS_END, 0, SC1);
  }
  SB();
  VLN(3) VLN(4)
  if (w == WG_) {
    if (!__all(gq[1] == tag)) {
#pragma unroll 1
      for (u32 s = 0; s < SPIN_CAP; ++s) {
        __builtin_amdgcn_s_sleep(4);
        MEMB();
        gq = ld_sc1_64(rs, WS_Q + bh * 512 + lane * 8);
        if (__all(gq[1] == tag)) break;
      }
    }
    *(LAS u32*)(L + L_Q + lane * 4) = gq[0];
    PIN(fl);
    if (p3 >= 0) { FL_WAIT() }
    LGKM0();
    ctl[C_Q] = 1u;
  }
  RELANE();
  VL(5)
  lds_wait(ctl + C_Q, 1u);
  const bool prer = p3 >= 0 && w < 8;
  u32x4 pre0, pre1;
  if (prer) {
    pre0 = ld_sc1(rs, WS_MODR + (8 + w) * 1024 + lane * 16);
    pre1 = ld_sc1(rs, WS_MODR + (16 + w) * 1024 + lane * 16);
  }
  SB();
  const u32x4 qf = *(LAS u32x4*)(L + L_Q + n * 16);

#define DOT(I) dot2(kr[I][3], qf[3], dot2(kr[I][2], qf[2], dot2(kr[I][1], qf[1], dot2(kr[I][0], qf[0], 0.f))))
  float xs[8];
#define QK(C) { _Pragma("unroll") for (int i = 2 * (C); i < 2 * (C) + 2; ++i) { const float lo_ = DOT(i), hi_ = DOT(i + 8); \
      const float t1_ = lo_ + dppf<ROR8>(lo_), t2_ = hi_ + dppf<ROR8>(hi_); xs[i] = (n & 8) ? t2_ : t1_; PIN(xs[i]); } SB(); }
  QK(0) VLN(6) QK(1) VLN(7) QK(2) VL(8) QK(3) VL(9)
  if (prer) {
    const u32 a_h2w = w * C1S + lane * 8;
    *(LAS u32x2*)(L + L_C1 + a_h2w) = u32x2{pk2(up2(pre1[0]) + 1.f), pk2(up2(pre1[1]) + 1.f)};
    *(LAS u32x2*)(L + L_C1 + a_h2w + 512) = u32x2{pk2(up2(pre1[2]) + 1.f), pk2(up2(pre1[3]) + 1.f)};
    *(LAS u32x2*)(L + L_C1 + 8 * C1S + a_h2w) = u32x2{pre0[0], pre0[1]};
    *(LAS u32x2*)(L + L_C1 + 8 * C1S + a_h2w + 512) = u32x2{pre0[2], pre0[3]};
  }
#define TR_STEP(HALF, CTRL, BIT) _Pragma("unroll") for (int hh = 0; hh < (HALF); ++hh) { \
    const float lo_ = xs[hh], hi_ = xs[hh + (HALF)]; const float t1_ = lo_ + dppf<CTRL>(lo_), t2_ = hi_ + dppf<CTRL>(hi_); xs[hh] = (n & (BIT)) ? t2_ : t1_; }
  TR_STEP(4, XOR7, 4) TR_STEP(2, XOR2, 2) TR_STEP(1, XOR1, 1)
#undef TR_STEP
  float sc = rbf(rbf(xs[0]) * 0.08838834764831845f);
  PIN(sc);
  SB();
  VL(10)
  {
    const float mw_ = wave_max(sc);
    const float lw = wave_sum(__expf(sc - mw_));
    if (lane == 0) *(LAS f32x2*)(L + L_ML + w * 8) = f32x2{mw_, lw};
    lds_add((LAS u32*)(L + L_CTL) + C_ML, lane);
  }
  SB();
  VL(11)
  RELANE();
  u32 mk[4];
#pragma unroll
  for (int j = 0; j < 4; ++j) mk[j] = (n >> 1) == j ? ((n & 1) ? 0xffff0000u : 0x0000ffffu) : 0u;
  f32x4 oa = {0.f, 0.f, 0.f, 0.f};
#define PV(I) { const u32 pb_ = dppu<0x150 + (I)>(pd); oa = mfma(vr[I], u32x4{pb_ & mk[0], pb_ & mk[1], pb_ & mk[2], pb_ & mk[3]}, oa); asm volatile("" : "+v"(oa)); SB(); }
  PIN(mk[0]); PIN(mk[1]); PIN(mk[2]); PIN(mk[3]);
  u32 a_oa = L_RED + w * OSTR + (g * 8 + n) * 16;
  u32 ra = L_RED + (lane >> 5) * 8 * OSTR + (lane & 31) * 16;
  u32 rb_ = WS_O + (h * 8 + b) * 512 + (lane & 31) * 16 + (lane >> 5) * 8;
  u32 tagv = tag;
  PIN(a_oa); PIN(ra); PIN(rb_); PIN(tagv);
  org = __builtin_amdgcn_readfirstlane(ctl[C_TB]);
  org = org ? org + TEND : t0 * 4u;
  asm volatile("" : "+s"(org));
  VL(12)
  lds_wait(ctl + C_ML, 16u);
  u32 pd;
  {
    const f32x2 ml = *(LAS f32x2*)(L + L_ML + n * 8);
    const float M = row_max(ml[0]);
    const float pe = __expf(sc - M) * rcp(row_sum(__expf(ml[0] - M) * ml[1]));
    pd = pack2(pe, pe);
    PIN(pd);
  }
  SB();
  VL(13)
  PV(0) PV(1) PV(2) PV(3) PV(4)
  VL(14)
  PV(5) PV(6) PV(7) PV(8) PV(9)
  VL(15)
  PV(10) PV(11) PV(12) PV(13) PV(14)
  if (w == 13) __builtin_amdgcn_s_setprio(3);
  PV(15)
#undef PV
  if (n < 8) *(LAS f32x4*)(L + a_oa) = oa;
  BAR();
  if (w == 14 && lane == 0) st_sc1_64(rs, WS_B0 + bid * 8, u32x2{now4(), tagv});
  if (w == 13) {
    f32x4 s[8];
#pragma unroll
    for (int ww = 0; ww < 8; ++ww) s[ww] = *(LAS f32x4*)(L + ra + ww * OSTR);
    SB();
    const f32x4 t = ((s[0] + s[1]) + (s[2] + s[3])) + ((s[4] + s[5]) + (s[6] + s[7]));
    st_sc1_64(rs, rb_, u32x2{pack2(t32(t[0], t[2]), t32(t[1], t[3])), tagv});
  }
  if (to) {
    u32x4 wf[16];
    if (w < 4) {
      const u32 af = L_W + (8 * g + (n >> 2)) * 128 + (w ^ ((n >> 3) | ((g & 1) << 1))) * 32 + (n & 3) * 8;
#pragma unroll
      for (int ks = 0; ks < 16; ++ks) wf[ks] = trfrag(L + af + ks * 4096, L + af + ks * 4096 + 512);
      __builtin_amdgcn_s_setprio(3);
    }
    int oo = WS_O + xcd * 16384 + w * 1024 + lane * 16;
    u32 a_ow = L_TP + (2 * w + (lane >> 5)) * OBS + (lane & 31) * 8;
    u32 a_or = L_TP + ((n >> 3) * 16 + (n & 7)) * OBS + g * 16;
    int rc = n < 8 ? WS_PART + ((n * 16 + 2 * b + ((w >> 1) & 1)) * 8 + xcd) * 128 + (w & 1) * 64 + g * 16 : WS_END;
    PIN(oo); PIN(a_ow); PIN(a_or); PIN(rc);
    SEL4();
    until(org + OFO);
#pragma unroll 1
    for (u32 s = 0; s < SPIN_CAP; ++s) {
      const u32x4 og = ld_sc1(rs, oo);
      *(LAS u32x2*)(L + a_ow) = u32x2{og[0], og[2]};
      if (__builtin_expect(__all(((og[1] ^ tag) | (og[3] ^ tag)) == 0u), 1)) break;
      MEMB();
      __builtin_amdgcn_s_sleep(2);
    }
    BAR();
    if (w < 4) {
      u32x4 ob[8];
#pragma unroll
      for (int i = 0; i < 8; ++i) ob[i] = *(LAS u32x4*)(L + a_or + (i >> 2) * 8 * OBS + (i & 3) * 64);
      SB();
      f32x4 aL = {0.f, 0.f, 0.f, 0.f}, aH = {0.f, 0.f, 0.f, 0.f};
#pragma unroll
      for (int i = 0; i < 8; ++i) { aL = mfma(wf[i], ob[i], aL); aH = mfma(wf[8 + i], ob[i], aH); }
      const f32x4 v = {aL[0] + dppf<ROR8>(aH[0]), aL[1] + dppf<ROR8>(aH[1]), aL[2] + dppf<ROR8>(aH[2]), aL[3] + dppf<ROR8>(aH[3])};
      st_sc1(rs, rc, tag8(v, tagv, sel));
    }
  }

  const int bo = lane * 16;
  if (p3 >= 0) {
    u32 a_h2w = w * C1S + lane * 8;
    if (w == 8 && lane < 36) *(LAS u32*)(L + (lane < 32 ? L_ML : L_HH + 640) + lane * 4) = 0u;
    BAR();
    glds((const void*)(tp0 + (u32)((368 - 16 * w) * FF * 2)), lbase + L_W + 49152 + w * 1024);
    u32x4 bp, bq;
    if (w == 12) {
      until_s(org + OFB + 20);
      asm volatile("buffer_load_dwordx4 %0, %2, %3, 0 offen sc1\n\tbuffer_load_dwordx4 %1, %2, %3, 0 offen offset:1024 sc1" : "=&v"(bp), "=&v"(bq) : "v"(WS_B0 + bo), "s"(rs) : "memory");
    }
    BAR();
#define DL(R) glds(wd + (size_t)(32 * p3 + (R)) * 512, (lane ^ ((((R) & 3) | (((R) >> 1) & 4)) << 1)) * 16, lbase + L_W + 65536 + (R) * 1024)
    if (w < 8) { DL(3 * w); DL(3 * w + 1); DL(3 * w + 2); }
    if (w >= 12) { DL(2 * w); DL(2 * w + 1); }
    SB();
    u32 a_h2r = L_H2 + (n & 7) * H2S + (n >> 3) * 512 + g * 16;
    u32 a_hw = L_HH + (n < 8 ? n * 64 + (8 * (w & 3) + 4 * (g & 1) + 2 * (g >> 1)) * 2 : 512 + lane * 4);
    const u32x4 t16v = {tag & 0xffffu, tag >> 16, tag & 0xffffu, tag >> 16};
#define X_TIME() until_s(org + OFO + DX - 40); u32 tx = 2u; \
      _Pragma("unroll 1") for (u32 s = 0; s < LSPIN_CAP && tx == 2u; ++s) { tx = __builtin_amdgcn_readfirstlane(ctl[C_TX]); if (tx == 2u) __builtin_amdgcn_s_sleep(2); } \
      if (!(tx & 1u)) tx = org + OFO + DX;
#define X_BAD(A, B) ((or4(A) | or4(B)) & 0xffffu)
    if (w < 8) {
      const u32x2 ca = *(LAS u32x2*)(L + L_C1 + a_h2w), cb = *(LAS u32x2*)(L + L_C1 + a_h2w + 512);
      f32x2 c1[4] = {up2(ca[0]), up2(ca[1]), up2(cb[0]), up2(cb[1])};
      a_h2w = L_H2 + w * H2S + lane * 8;
      int xo = WS_X2 + xcd * 16384 + w * 2048 + lane * 16;
      u32 a_hr = L_HH + (n < 8 ? n * 64 + g * 16 : 768), a_hs = L_HH + (n < 8 ? 768 : (n & 7) * 64 + g * 16);
      PIN(a_hs);
      SEL4();
      const u32 a_d0 = L_W + 65536 + (8 * g + (n >> 2)) * 1024 + ((8 * w + ((n >> 1) & 1)) ^ (((n >> 2) | ((g & 1) << 2)) << 1)) * 16 + (n & 1) * 8;
      int yo = WS_YP + ((16 * w + 4 * (n >> 3) + g) * NP3 + p3) * 128 + (n & 7) * 16, yo2 = yo + 8 * NP3 * 128;
      PIN(xo); PIN(c1[0]); PIN(c1[1]); PIN(c1[2]); PIN(c1[3]); PIN(a_h2w); PIN(a_hr); PIN(yo); PIN(yo2);
      u32 a_cx = lbase + L_CTL + (C_X + w) * 4;
      PIN(a_cx);
      asm volatile("s_waitcnt vmcnt(3)" ::: "memory");
      BAR();
      __builtin_amdgcn_s_setprio(2);
      X_TIME()
      until(tx);
#pragma unroll 1
      for (u32 s = 0; s < SPIN_CAP; ++s) {
        const u32x4 xa = ld_sc1(rs, xo) ^ t16v, xb = ld_sc1(rs, xo + 1024) ^ t16v;
        const float x0 = __builtin_bit_cast(float, __builtin_amdgcn_readfirstlane(xa[0]));
        *(LAS u32x2*)(L + a_h2w) = u32x2{pk2((f32x2{asf(xa[0]), asf(xa[1])} - x0) * c1[0]), pk2((f32x2{asf(xa[2]), asf(xa[3])} - x0) * c1[1])};
        *(LAS u32x2*)(L + a_h2w + 512) = u32x2{pk2((f32x2{asf(xb[0]), asf(xb[1])} - x0) * c1[2]), pk2((f32x2{asf(xb[2]), asf(xb[3])} - x0) * c1[3])};
        if (__builtin_expect(__all(X_BAD(xa, xb) == 0u), 1)) break;
        MEMB();
      }
      asm volatile("s_mov_b64 exec, 1\n\tds_write_b32 %0, %0\n\ts_mov_b64 exec, -1" :: "v"(a_cx) : "memory");
      __builtin_amdgcn_s_setprio(0);
      lds_add((LAS u32*)(L + L_CTL) + C_R, lane);
      lds_wait(ctl + C_R, 12u);
      u32x4 df[4];
#pragma unroll
      for (int i = 0; i < 4; ++i) { df[i] = trfrag(L + (a_d0 ^ (32 * i)), L + (a_d0 ^ (32 * i)) + 4096); PIN(df[i]); }
      BAR();
      const u32x4 h0 = *(LAS u32x4*)(L + a_hr), h1 = *(LAS u32x4*)(L + a_hs);
      const f32x4 z = {0.f, 0.f, 0.f, 0.f};
      const f32x4 a0 = mfma(df[1], h1, mfma(df[0], h0, z)), a1 = mfma(df[3], h1, mfma(df[2], h0, z));
      st_sc1(rs, yo, tag8(a0, tagv, sel));
      st_sc1(rs, yo2, tag8(a1, tagv, sel));
    } else if (w < 12) {
      u32 a_st = L_ML + (n & 7) * 16;
      PIN(a_st);
      __builtin_amdgcn_s_setprio(3);
      u32x4 gf[16], gb_[8];
      const u32 af = L_W + (8 * g + (n >> 2)) * 128 + (((w & 3) + (n & 2) * 2) ^ (((n >> 3) | ((g & 1) << 1)) << 1)) * 16 + (n & 1) * 8;
      f32x4 aL = {0.f, 0.f, 0.f, 0.f}, aH;
      f32x2 ag, au, sg, su, gt, up;
      {
#pragma unroll
        for (int q4 = 0; q4 < 4; ++q4) {
#pragma unroll
          for (int i = 0; i < 4; ++i) gb_[i] = *(LAS u32x4*)(L + L_C1 + n * C1S + g * 16 + q4 * 256 + i * 64);
          if (q4 == 3) {
            VM0();
            BAR();
          }
#pragma unroll
          for (int i = 0; i < 4; ++i) gf[4 * q4 + i] = trfrag(L + af + (4 * q4 + i) * 4096, L + af + (4 * q4 + i) * 4096 + 512);
          SB();
#pragma unroll
          for (int i = 0; i < 4; ++i) aL = mfma(gf[4 * q4 + i], gb_[i], aL);
          PIN(aL);
        }
        const auto s0 = __builtin_amdgcn_permlane32_swap(__float_as_uint(aL[0]), __float_as_uint(aL[2]), false, false);
        const auto s1 = __builtin_amdgcn_permlane32_swap(__float_as_uint(aL[1]), __float_as_uint(aL[3]), false, false);
        ag = -f32x2{__uint_as_float(s0[0]), __uint_as_float(s1[0])};
        au = -f32x2{__uint_as_float(s0[1]), __uint_as_float(s1[1])};
        sg = f32x2{dppf<ROR8>(ag[0]), dppf<ROR8>(ag[1])} * 1.4426950408889634f;
        su = -f32x2{dppf<ROR8>(au[0]), dppf<ROR8>(au[1])};
        PIN(ag); PIN(au); PIN(sg); PIN(su); PIN(a_h2r); PIN(a_hw);
      }
      __builtin_amdgcn_s_setprio(0);
      _Pragma("unroll 1") for (u32 s = 0; s < LSPIN_CAP; ++s) if (__all(ctl[C_X + (lane & 7)] != 0u)) break;
      __builtin_amdgcn_s_setprio(3);
      MEMB();
      GU_MUL(L + a_h2r)
      SB();
      f32x4 st = *(volatile LAS f32x4*)(L + a_st);
      gu_pair(aL, aH, gt, up);
      if (!__all(st[3] != 0.f)) {
        _Pragma("unroll 1") for (u32 s = 0; s < LSPIN_CAP; ++s) { st = *(volatile LAS f32x4*)(L + a_st); if (__all(st[3] != 0.f)) break; }
      }
      gt = __builtin_elementwise_fma(__builtin_elementwise_fma(ag, f32x2{st[0], st[0]}, gt), f32x2{st[2], st[2]}, sg);
      up = __builtin_elementwise_fma(__builtin_elementwise_fma(au, f32x2{st[0], st[0]}, up), f32x2{st[1], st[1]}, su);
      *(LAS u32*)(L + a_hw) = pk2(f32x2{rcp(1.f + __builtin_amdgcn_exp2f(gt[0])), rcp(1.f + __builtin_amdgcn_exp2f(gt[1]))} * up);
      BAR();
    } else {
      int xo = WS_X2 + xcd * 16384 + (w - 12) * 4096 + lane * 16;
      u32 a_st = lbase + L_ML + (2 * (w - 12) + (lane >> 5)) * 16;
      PIN(xo); PIN(a_st);
      asm volatile("s_waitcnt vmcnt(2)" : "+v"(bp), "+v"(bq) :: "memory");
      BAR();
      if (w == 12 && !on_time(tag, org, bp, bq)) {
        ctl[C_TX] = 2u;
        ctl[C_TX] = (anchor(rs, bo, tag, org, bp, bq) + DX) | 1u;
      }
      VM0();
      lds_add((LAS u32*)(L + L_CTL) + C_R, lane);
      __builtin_amdgcn_s_setprio(1);
      X_TIME()
      until(tx + SDL);
      u32x4 xv[4];
#pragma unroll
      for (int i = 0; i < 4; ++i) xv[i] = ld_sc1(rs, xo + i * 1024);
#pragma unroll
      for (int i = 0; i < 4; ++i) xv[i] ^= t16v;
      if (__builtin_expect(!__all((X_BAD(xv[0], xv[1]) | X_BAD(xv[2], xv[3])) == 0u), 0)) {
#pragma unroll 1
        for (u32 s = 0; s < SPIN_CAP; ++s) {
          MEMB();
#pragma unroll
          for (int i = 0; i < 4; ++i) xv[i] = ld_sc1(rs, xo + i * 1024) ^ t16v;
          if (__all((X_BAD(xv[0], xv[1]) | X_BAD(xv[2], xv[3])) == 0u)) break;
        }
      }
      float sm[2], sq[2], xs0[2];
#pragma unroll
      for (int k = 0; k < 2; ++k) {
        const float x0 = __builtin_bit_cast(float, __builtin_amdgcn_readfirstlane(xv[2 * k][0]));
        const u32x4 xa = xv[2 * k], xb = xv[2 * k + 1];
        const f32x2 d0 = f32x2{asf(xa[0]), asf(xa[1])} - x0, d1 = f32x2{asf(xa[2]), asf(xa[3])} - x0, d2 = f32x2{asf(xb[0]), asf(xb[1])} - x0, d3 = f32x2{asf(xb[2]), asf(xb[3])} - x0;
        const f32x2 sv = (d0 + d1) + (d2 + d3), qv = __builtin_elementwise_fma(d0, d0, d1 * d1) + __builtin_elementwise_fma(d2, d2, d3 * d3);
        xs0[k] = x0;
        sm[k] = sv[0] + sv[1];
        sq[k] = qv[0] + qv[1];
      }
      const float r = row_sum(t32(t16(sm[0], sq[0]), t16(sm[1], sq[1]))) * (1.f / 512.f);
      const auto sw = __builtin_amdgcn_permlane16_swap(__float_as_uint(r), __float_as_uint(r), false, false);
      const float md = __uint_as_float(sw[0]);
      const float x0 = lane < 32 ? xs0[0] : xs0[1];
      const float inv = rcp(rbf(__builtin_amdgcn_sqrtf(rbf(rbf(fmaxf(fmaf(-md, md, __uint_as_float(sw[1])), 0.f)) + 1e-5f))));
      const f32x4 st = {rbf(x0 + md) - x0, inv, inv * -1.4426950408889634f, 1.f};
      asm volatile("s_mov_b32 exec_lo, 1\n\ts_mov_b32 exec_hi, 1\n\tds_write_b128 %0, %1\n\ts_mov_b64 exec, -1" :: "v"(a_st), "v"(st) : "memory");
      if (w == 15) {
        _Pragma("unroll 1") for (u32 s = 0; s < LSPIN_CAP; ++s) if (__all(ctl[C_X + (lane & 7)] != 0u)) break;
        if (lane < 8) st_sc1_64(rs, WS_B1 + lane * 512 + p3 * 8, u32x2{now4(), tagv});
      }
      BAR();
    }
    asm volatile("fused_code_end:");
  } else if (red && (w == RW || w == RW - 1)) {
    const int f = 16 * xcd + 2 * b + hj, ro_ = 4 * (w & 1) + (lane & 3);
    const int rb = e >> 4, rj = e & 15, c2 = 32 * rj + 4 * (lane & 7) + g;
    fl = __builtin_amdgcn_raw_buffer_load_b32(rs, WS_FLAGS + lane * 4, 0, SC1);
    float xx = blo(x[rb * 512 + c2]);
    FL_WAIT()
    float ga = blo(__builtin_amdgcn_raw_buffer_load_b16(rs, WS_MODR + rb * 1024 + mpos(c2), 0, SC1));
    float gb = blo(__builtin_amdgcn_raw_buffer_load_b16(rs, WS_MODR + (24 + ro_) * 1024 + mpos(4 * f + g), 0, SC1));
    int po = WS_PART + e * 1024 + lane * 16;
    int xo = WS_X2 + (lane >> 3) * 16384 + rb * 2048 + rj * 128 + (lane & 7) * 16;
    int xi = WS_X2 + xcd * 16384 + ro_ * 2048 + (4 * f + g) * 4;
    u32 xt = (g & 1) ? tag >> 16 : tag & 0xffffu;
    const u32x4 tb4 = bytes4(tag);
    u32x4 tb5 = lane < 44 ? tb4 : u32x4{0, 0, 0, 0};
    int yo = WS_YP + (f * NP3 + (lane >> 2)) * 128 + ro_ * 16;
    int y2 = lane < 44 ? yo + 4096 : WS_END;
    u32 oof = (lane & 12) == 0 ? (ro_ * 512 + 4 * f + g) * 2 : 8192;
    __amdgpu_buffer_rsrc_t ro = __builtin_amdgcn_make_buffer_rsrc((void*)out, 0, 8192, 0x00020000);
    asm volatile("" : "+s"(ro));
    PIN(xt); PIN(tb5); PIN(ga); PIN(gb); PIN(xx); PIN(po); PIN(xo); PIN(xi); PIN(yo); PIN(y2); PIN(oof);
    if (w == RW) {
      u32x4 bp, bq;
      until_s(org + OFB);
      B0_LOAD()
      const u32 anc = on_time(tag, org, bp, bq) ? org + OFO : anchor(rs, bo, tag, org, bp, bq);
      until(anc + DP);
      u32x4 xq;
#pragma unroll 1
      for (u32 sp = 0; sp < SPIN_CAP; ++sp) {
        const u32x4 pv = ld_one(rs, po) ^ tb4;
        float t = t32(t16(asf(pv[0]), asf(pv[1])), t16(asf(pv[2]), asf(pv[3])));
        t += dppf<ROR8>(t);
        const u32 xw = __float_as_uint(rbf(xx + rbf(ga * rbf(t)))) | xt;
        const auto p1 = __builtin_amdgcn_permlane16_swap(xw, xw, false, false);
        const auto p2 = __builtin_amdgcn_permlane32_swap(p1[0], p1[0], false, false), p3_ = __builtin_amdgcn_permlane32_swap(p1[1], p1[1], false, false);
        xq = u32x4{p2[0], p3_[0], p2[1], p3_[1]};
        PIN(xq);
        if (__builtin_expect(__all((or4(pv) & 255u) == 0u), 1)) break;
        MEMB();
      }
      st_sc1(rs, xo, xq);
      if (bid == 1 && lane == 0) __hip_atomic_store((GAS u32*)ws, tag, RLX_AGENT);
      u32 ty = anc + DYS;
      const int lt = (int)(now4() - anc) - (DX + 20);
      if (lt > 0) {
        int lk = DB1;
        while (lk < DB1 + lt + 240) lk += PB;
        u32x2 bv;
#pragma unroll 1
        for (u32 sp = 0; sp < SPIN_CAP; ++sp) {
          until_s(anc + lk);
          bv = ld_sc1_64(rs, WS_B1 + xcd * 512 + (lane < NP3 ? lane : 0) * 8);
          if (__all(bv[1] == tag)) break;
          MEMB();
          do lk += PB; while ((int)(now4() - anc) > lk);
        }
        ty = org + smax((int)(bv[0] - org)) + DY;
      }
      ctl[C_TX] = ty | 1u;
    }
    lds_wait(ctl + C_TX, 1u);
    until(__builtin_amdgcn_readfirstlane(ctl[C_TX]));
    u32 res;
#pragma unroll 1
    for (u32 sp = 0; sp < SPIN_CAP; ++sp) {
      const u32 x2 = __builtin_amdgcn_raw_buffer_load_b32(rs, xi, 0, SC1) ^ xt;
      const u32x4 y0 = ld_one(rs, yo) ^ tb4, y1 = ld_one(rs, yo + 2048) ^ tb4, y2_ = ld_one(rs, y2) ^ tb5;
      const f32x4 s = (__builtin_bit_cast(f32x4, y0) + __builtin_bit_cast(f32x4, y1)) + __builtin_bit_cast(f32x4, y2_);
      float t = t32(t16(s[0], s[1]), t16(s[2], s[3]));
      t += dppf<ROR8>(t);
      t += dppf<ROR4>(t);
      res = pack2(fmaf(gb, t, asf(x2)), 0.f);
      PIN(res);
      if (__builtin_expect(__all((((or4(y0) | or4(y1) | or4(y2_)) & 255u) | (x2 & 0xffffu)) == 0u), 1)) break;
      MEMB();
    }
    __builtin_amdgcn_raw_buffer_store_b16((u16)res, ro, oof, 0, 0);
  }
}

extern "C" void kernel_launch(void* const* d_in, const int* in_sizes, int n_in, void* d_out, int out_size, void* d_ws, size_t ws_size, hipStream_t stream) {
  (void)hipFuncSetAttribute((const void*)fused, hipFuncAttributeMaxDynamicSharedMemorySize, L_END);
  Args a;
  const int src[4][2] = {{3, 3}, {10, 10}, {4, 4}, {6, 7}};
  for (int r = 0; r < 4; ++r)
    for (int k = 0; k < 2; ++k) a.half[r][k] = (const unsigned char*)d_in[src[r][k]] + (r < 3 && k ? 64 : 0);
  a.x = (const u16*)d_in[0]; a.kc = (const u16*)d_in[1]; a.vc = (const u16*)d_in[2]; a.wd = (const u16*)d_in[8]; a.cc = (const u16*)d_in[9];
  a.out = (u16*)d_out; a.ws = (unsigned char*)d_ws;
  fused<<<256, 1024, L_END, stream>>>(a);
}
```

```cpp
#include <hip/hip_runtime.h>
#include <stdint.h>

typedef unsigned int u32;
typedef unsigned long long u64;
typedef unsigned short u16;
typedef u32 u32x4 __attribute__((ext_vector_type(4)));
typedef u32 u32x2 __attribute__((ext_vector_type(2)));
typedef float f32x4 __attribute__((ext_vector_type(4)));
typedef float f32x2 __attribute__((ext_vector_type(2)));
typedef short s16x4 __attribute__((ext_vector_type(4)));
typedef __bf16 bf16x8 __attribute__((ext_vector_type(8)));
typedef __bf16 bf16x2 __attribute__((ext_vector_type(2)));
#define GAS __attribute__((address_space(1)))
#define LAS __attribute__((address_space(3)))
#define RLX_AGENT __ATOMIC_RELAXED, __HIP_MEMORY_SCOPE_AGENT

constexpr int SK = 4096, WIN = 1024, FF = 1376, NP3 = 43;

constexpr int WS_FLAGS = 0x1000;
constexpr int WS_TS = 0x1800;
constexpr int WS_G1 = 0x2000;
constexpr int WS_B1 = 0x8000;
constexpr int WS_MODR = 0xA000;
constexpr int WS_Q = 0x12000;
constexpr int WS_O = 0x36000;
constexpr int WS_B0 = 0x56000;
constexpr int WS_PART = 0x5E000;
constexpr int WS_YP = WS_PART + 8 * 16 * 8 * 32 * 4;
constexpr int WS_X2 = WS_YP + 128 * NP3 * 128;
constexpr int WS_END = WS_X2 + 8 * 16384;
constexpr int WS_STEP = 2 << 20;
static_assert(WS_END <= WS_STEP, "");

constexpr int L_W = 0;
constexpr int L_TP = 65536;
constexpr int L_C1 = 98304;
constexpr int L_SINK = 130048;
constexpr int L_RED = 131072;
constexpr int L_H2 = L_RED + 16384;
constexpr int C1S = 1040, H2S = 1056, OSTR = 544, OBS = 288;
constexpr int L_Q = L_H2 + 8 * H2S;
constexpr int L_ML = L_Q + 512;
constexpr int L_HH = L_ML + 128;
constexpr int L_CTL = L_HH + 784;
constexpr int L_END = L_CTL + 64;
constexpr int C_T = 0, C_TB = 1, C_TX = 2, C_Q = 3, C_ML = 4, C_R = 6, C_T0 = 7, C_X = 8;
constexpr int RW = 12, WG_ = 10;

constexpr u32 SPIN_CAP = 1u << 17, LSPIN_CAP = 1u << 20;
constexpr int DUE0 = 810;
constexpr int TAU = 220;
constexpr int E1 = 16;
constexpr int E2 = 10;
constexpr int SKW = 200;
constexpr int MSH = 14;
constexpr int KA = 55;
constexpr int KW = 6;
constexpr int OFO = 418;
constexpr int OFB = 463;
constexpr int PB = 200;
constexpr int RB = 260;
constexpr int DO = 30;
constexpr int DL_ = 220;
constexpr int DP = 350;
constexpr int DX = 565;
constexpr int DYS = 1125;
constexpr int DB1 = 960;
constexpr int DY = 260;
constexpr int SDL = 12;
constexpr int extra(int i) { return i < 8 ? E1 : (i >= 14 && i < 22 ? E2 : 0); }
constexpr int TEND = DUE0 + 32 * TAU + 8 * E1 + 8 * E2;

__device__ __forceinline__ float rbf(float f) { return (float)(__bf16)f; }
__device__ __forceinline__ u32 pack2(float a, float b) { bf16x2 v = {(__bf16)a, (__bf16)b}; return __builtin_bit_cast(u32, v); }
__device__ __forceinline__ float blo(u32 v) { return __builtin_bit_cast(float, v << 16); }
__device__ __forceinline__ float bhi(u32 v) { return __builtin_bit_cast(float, v & 0xffff0000u); }
__device__ __forceinline__ u32 canon(u32 v) { return v < 0xfffffffeu ? v : 0xfffffffeu; }
#define SEL4() u32x4 sel = {0x07060500u, 0x07060501u, 0x07060502u, 0x07060503u}; asm volatile("" : "+s"(sel))
__device__ __forceinline__ u32x4 tag8(f32x4 v, u32 tagv, u32x4 sel) {
  const u32x4 r = __builtin_bit_cast(u32x4, v);
  return u32x4{__builtin_amdgcn_perm(r[0], tagv, sel[0]), __builtin_amdgcn_perm(r[1], tagv, sel[1]), __builtin_amdgcn_perm(r[2], tagv, sel[2]), __builtin_amdgcn_perm(r[3], tagv, sel[3])};
}
__device__ __forceinline__ u32x4 bytes4(u32 tag) { return u32x4{tag & 255u, (tag >> 8) & 255u, (tag >> 16) & 255u, tag >> 24}; }
__device__ __forceinline__ u32 or4(u32x4 v) { return (v[0] | v[1]) | (v[2] | v[3]); }
__device__ __forceinline__ float rcp(float x) { return __builtin_amdgcn_rcpf(x); }
template <int CTRL> __device__ __forceinline__ float dppf(float x) {
  return __builtin_bit_cast(float, __builtin_amdgcn_mov_dpp(__builtin_bit_cast(int, x), CTRL, 0xf, 0xf, true));
}
template <int CTRL> __device__ __forceinline__ u32 dppu(u32 x) { return (u32)__builtin_amdgcn_mov_dpp((int)x, CTRL, 0xf, 0xf, true); }
constexpr int ROR8 = 0x128, ROR4 = 0x124, ROR2 = 0x122, ROR1 = 0x121, XOR1 = 0xB1, XOR2 = 0x4E, XOR7 = 0x141;
__device__ __forceinline__ float row_sum(float x) { x += dppf<ROR8>(x); x += dppf<ROR4>(x); x += dppf<ROR2>(x); x += dppf<ROR1>(x); return x; }
__device__ __forceinline__ float row_max(float x) {
  x = fmaxf(x, dppf<ROR8>(x)); x = fmaxf(x, dppf<ROR4>(x)); x = fmaxf(x, dppf<ROR2>(x)); x = fmaxf(x, dppf<ROR1>(x)); return x;
}
__device__ __forceinline__ float x16_sum(float x) {
  auto s = __builtin_amdgcn_permlane16_swap(__float_as_uint(x), __float_as_uint(x), false, false);
  return __uint_as_float(s[0]) + __uint_as_float(s[1]);
}
__device__ __forceinline__ float x32_sum(float x) {
  auto s = __builtin_amdgcn_permlane32_swap(__float_as_uint(x), __float_as_uint(x), false, false);
  return __uint_as_float(s[0]) + __uint_as_float(s[1]);
}
__device__ __forceinline__ float x16_max(float x) {
  auto s = __builtin_amdgcn_permlane16_swap(__float_as_uint(x), __float_as_uint(x), false, false);
  return fmaxf(__uint_as_float(s[0]), __uint_as_float(s[1]));
}
__device__ __forceinline__ float x32_max(float x) {
  auto s = __builtin_amdgcn_permlane32_swap(__float_as_uint(x), __float_as_uint(x), false, false);
  return fmaxf(__uint_as_float(s[0]), __uint_as_float(s[1]));
}
__device__ __forceinline__ float t16(float a, float b) {
  auto s = __builtin_amdgcn_permlane16_swap(__float_as_uint(a), __float_as_uint(b), false, false);
  return __uint_as_float(s[0]) + __uint_as_float(s[1]);
}
__device__ __forceinline__ float t32(float a, float b) {
  auto s = __builtin_amdgcn_permlane32_swap(__float_as_uint(a), __float_as_uint(b), false, false);
  return __uint_as_float(s[0]) + __uint_as_float(s[1]);
}
__device__ __forceinline__ f32x2 up2(u32 v) { return f32x2{blo(v), bhi(v)}; }
__device__ __forceinline__ u32 pk2(f32x2 v) { return pack2(v[0], v[1]); }
#define PIN(x) asm volatile("" : "+v"(x))
__device__ __forceinline__ f32x2 rb2(f32x2 v) { u32 p = pk2(v); asm("" : "+v"(p)); return up2(p); }
__device__ __forceinline__ float wave_sum(float x) { return x32_sum(x16_sum(row_sum(x))); }
__device__ __forceinline__ float wave_max(float x) { return x32_max(x16_max(row_max(x))); }
__device__ __forceinline__ float dot2(u32 a, u32 b, float c) {
  return __builtin_amdgcn_fdot2_f32_bf16(__builtin_bit_cast(bf16x2, a), __builtin_bit_cast(bf16x2, b), c, false);
}
__device__ __forceinline__ f32x4 mfma(u32x4 a, u32x4 b, f32x4 c) {
  return __builtin_amdgcn_mfma_f32_16x16x32_bf16(__builtin_bit_cast(bf16x8, a), __builtin_bit_cast(bf16x8, b), c, 0, 0, 0);
}
__device__ __forceinline__ u32x4 ldnt(const void* p) { return __builtin_nontemporal_load((const u32x4*)p); }
__device__ __forceinline__ void glds(const void* g, u32 lds) {
  u32 keep;
  lds = __builtin_amdgcn_readfirstlane(lds);
  asm volatile("s_mov_b32 %0, m0\n\ts_mov_b32 m0, %2\n\ts_nop 0\n\tglobal_load_lds_dwordx4 %1, off nt\n\ts_mov_b32 m0, %0"
               : "=&s"(keep) : "v"(g), "s"(lds) : "memory");
}
__device__ __forceinline__ void glds(const void* base, u32 off, u32 lds) {
  u32 keep;
  lds = __builtin_amdgcn_readfirstlane(lds);
  asm volatile("s_mov_b32 %0, m0\n\ts_mov_b32 m0, %3\n\ts_nop 0\n\tglobal_load_lds_dwordx4 %1, %2 nt\n\ts_mov_b32 m0, %0"
               : "=&s"(keep) : "v"(off), "s"(base), "s"(lds) : "memory");
}
__device__ __forceinline__ void touch(const void* base, u32 off, u32 lds) {
  u32 keep;
  lds = __builtin_amdgcn_readfirstlane(lds);
  asm volatile("s_mov_b32 %0, m0\n\ts_mov_b32 m0, %3\n\ts_nop 0\n\tglobal_load_lds_dwordx4 %1, %2\n\ts_mov_b32 m0, %0"
               : "=&s"(keep) : "v"(off), "s"(base), "s"(lds) : "memory");
}
__device__ __forceinline__ u32x4 trfrag(LAS unsigned char* p0, LAS unsigned char* p1) {
  s16x4 a = __builtin_amdgcn_ds_read_tr16_b64_v4i16((LAS s16x4*)p0);
  s16x4 b = __builtin_amdgcn_ds_read_tr16_b64_v4i16((LAS s16x4*)p1);
  u32x2 x = __builtin_bit_cast(u32x2, a), y = __builtin_bit_cast(u32x2, b);
  return u32x4{x[0], x[1], y[0], y[1]};
}
#define NEWPAGE() asm volatile("s_branch 9f\n\t.p2align 12\n9:")
#define SB() __builtin_amdgcn_sched_barrier(0)
#define MEMB() asm volatile("" ::: "memory")
#define VM0() asm volatile("s_waitcnt vmcnt(0)" ::: "memory")
#define LGKM0() asm volatile("s_waitcnt lgkmcnt(0)" ::: "memory")
#define BAR() asm volatile("s_waitcnt lgkmcnt(0)\n\ts_barrier" ::: "memory")
#define TAG() do { u32 ep_; asm volatile("s_load_dword %0, %1, 0x0\n\ts_waitcnt lgkmcnt(0)" : "=s"(ep_) : "s"(ws) : "memory"); \
    tag = ((ep_ & 0x7f7f7f7fu) + 0x01010101u) ^ (ep_ & 0x80808080u); } while (0)
constexpr int SC1 = 16;
__device__ __forceinline__ u32x4 ld_sc1(__amdgpu_buffer_rsrc_t rs, int off) { return __builtin_amdgcn_raw_buffer_load_b128(rs, off, 0, SC1); }
__device__ __forceinline__ u32x4 ld_one(__amdgpu_buffer_rsrc_t rs, int off) { return __builtin_amdgcn_raw_buffer_load_b128(rs, off, 0, SC1 | 2); }
__device__ __forceinline__ u32x2 ld_sc1_64(__amdgpu_buffer_rsrc_t rs, int off) { return __builtin_amdgcn_raw_buffer_load_b64(rs, off, 0, SC1); }
__device__ __forceinline__ void st_sc1(__amdgpu_buffer_rsrc_t rs, int off, u32x4 v) { __builtin_amdgcn_raw_buffer_store_b128(v, rs, off, 0, SC1); }
__device__ __forceinline__ void st_sc1_64(__amdgpu_buffer_rsrc_t rs, int off, u32x2 v) { __builtin_amdgcn_raw_buffer_store_b64(v, rs, off, 0, SC1); }

__device__ __forceinline__ void ln_stats(u32x4 xv, float& m, float& inv) {
  const float x[8] = {blo(xv[0]), bhi(xv[0]), blo(xv[1]), bhi(xv[1]), blo(xv[2]), bhi(xv[2]), blo(xv[3]), bhi(xv[3])};
  const float x0 = __builtin_bit_cast(float, __builtin_amdgcn_readfirstlane(xv[0] << 16));
  float s = 0.f, v = 0.f;
#pragma unroll
  for (int j = 0; j < 8; ++j) { const float d = x[j] - x0; s += d; v = fmaf(d, d, v); }
  const float md = wave_sum(s) * (1.f / 512.f), sq = wave_sum(v) * (1.f / 512.f);
  const float var = rbf(fmaxf(fmaf(-md, md, sq), 0.f));
  m = rbf(x0 + md);
  inv = rcp(rbf(__builtin_amdgcn_sqrtf(rbf(var + 1e-5f))));
}
#define PACE_(W, INC) do { asm volatile(W "\n1:\n\ts_memrealtime vcc\n\ts_waitcnt lgkmcnt(0)\n\ts_lshl_b32 vcc_lo, vcc_lo, 2\n\ts_sub_i32 vcc_hi, vcc_lo, %0\n\ts_cmp_lt_i32 vcc_hi, 0\n\t" \
    "s_cbranch_scc0 2f\n\ts_sleep 1\n\ts_branch 1b\n2:\n\ts_add_i32 %0, %0, %1" : "+s"(due) : "is"(INC) : "memory", "scc", "vcc"); SB(); } while (0)
#define PACE(INC) PACE_("s_waitcnt vmcnt(2)", INC)
#define PACEN(INC) PACE_("", INC)
__device__ __forceinline__ void until_s(u32 due) { PACE_("", 0); }
__device__ __forceinline__ void until(u32 t) {
  asm volatile("1:\n\ts_memrealtime vcc\n\ts_waitcnt lgkmcnt(0)\n\ts_lshl_b32 vcc_lo, vcc_lo, 2\n\ts_sub_i32 vcc_hi, vcc_lo, %0\n\ts_cmp_lt_i32 vcc_hi, 0\n\t"
               "s_cbranch_scc1 1b" :: "s"(t) : "memory", "scc", "vcc");
  SB();
}

__device__ __forceinline__ void lds_wait(volatile LAS u32* p, u32 want) {
  _Pragma("unroll 1") for (u32 s = 0; s < LSPIN_CAP; ++s) {
    if (__builtin_amdgcn_readfirstlane(*p) >= want) break;
    __builtin_amdgcn_s_sleep(2);
  }
  MEMB();
}
__device__ __forceinline__ void lds_add(LAS u32* p, int lane) {
  LGKM0();
  if (lane == 0) __hip_atomic_fetch_add(p, 1u, __ATOMIC_RELAXED, __HIP_MEMORY_SCOPE_WORKGROUP);
  MEMB();
}
__device__ __forceinline__ void tile_part(LAS unsigned char* T, LAS unsigned char* P, u32x4 hb, int n, int g) {
  LAS unsigned char* tb = T + (8 * g + (n >> 2)) * 128 + (n & 3) * 8;
  const int sx = (n >> 3) | ((g & 1) << 1);
  f32x4 a[4];
#pragma unroll
  for (int t = 0; t < 4; ++t) a[t] = mfma(trfrag(tb + (t ^ sx) * 32, tb + (t ^ sx) * 32 + 512), hb, f32x4{0.f, 0.f, 0.f, 0.f});
  *(LAS f32x4*)(P) = n < 8 ? a[0] : a[1];
  *(LAS f32x4*)(P + 1024) = n < 8 ? a[2] : a[3];
}
__device__ __forceinline__ f32x4 tile_sum(LAS unsigned char* P) {
  f32x4 s[8], r[2];
#pragma unroll
  for (int k = 0; k < 2; ++k) {
#pragma unroll
    for (int i = 0; i < 8; ++i) s[i] = *(LAS f32x4*)(P + (8 * k + i) * 2048);
    SB();
    r[k] = ((s[0] + s[1]) + (s[2] + s[3])) + ((s[4] + s[5]) + (s[6] + s[7]));
    SB();
  }
  return r[0] + r[1];
}
#define GU_MUL(P) _Pragma("unroll") for (int i = 0; i < 8; ++i) gb_[i] = *(LAS u32x4*)((P) + i * 64); SB(); \
    aL = f32x4{0.f, 0.f, 0.f, 0.f}; aH = aL; _Pragma("unroll") for (int i = 0; i < 8; ++i) { aL = mfma(gf[i], gb_[i], aL); aH = mfma(gf[8 + i], gb_[i], aH); }
__device__ __forceinline__ void gu_pair(f32x4 aL, f32x4 aH, f32x2& gt, f32x2& up) {
  const auto s0 = __builtin_amdgcn_permlane32_swap(__float_as_uint(aL[0] + dppf<ROR8>(aH[0])), __float_as_uint(aL[2] + dppf<ROR8>(aH[2])), false, false);
  const auto s1 = __builtin_amdgcn_permlane32_swap(__float_as_uint(aL[1] + dppf<ROR8>(aH[1])), __float_as_uint(aL[3] + dppf<ROR8>(aH[3])), false, false);
  gt = f32x2{__uint_as_float(s0[0]), __uint_as_float(s1[0])};
  up = f32x2{__uint_as_float(s0[1]), __uint_as_float(s1[1])};
}
__device__ __forceinline__ int mpos(int c) { return (c & 252) * 4 + (c >> 8) * 8 + (c & 3) * 2; }
__device__ __forceinline__ u32 smax(int m) { int r = (int)fminf(wave_max((float)m), 4e5f); PIN(r); return (u32)__builtin_amdgcn_readfirstlane(r); }
__device__ __forceinline__ u32 now4() { return (u32)__builtin_amdgcn_s_memrealtime() * 4u; }
#define B0_LOAD() { bp = ld_sc1(rs, WS_B0 + bo); bq = ld_sc1(rs, WS_B0 + 1024 + bo); }
__device__ __forceinline__ bool on_time(u32 tag, u32 org, u32x4 bp, u32x4 bq) {
  const u32 lim = org + OFO - DO;
  return __all((((bp[1] ^ tag) | (bp[3] ^ tag) | (bq[1] ^ tag) | (bq[3] ^ tag)) | (((lim - bp[0]) | (lim - bp[2]) | (lim - bq[0]) | (lim - bq[2])) >> 31)) == 0u);
}
__device__ __forceinline__ u32 anchor(__amdgpu_buffer_rsrc_t rs, int bo, u32 tag, u32 org, u32x4 bp, u32x4 bq) {
  int lk = OFB;
#pragma unroll 1
  for (u32 s = 0; s < SPIN_CAP; ++s) {
    if (__all(((bp[1] ^ tag) | (bp[3] ^ tag) | (bq[1] ^ tag) | (bq[3] ^ tag)) == 0u)) break;
    MEMB();
    do lk += PB; while ((int)(now4() - org) > lk);
    until_s(org + lk);
    B0_LOAD()
  }
  const int m = (int)smax(max(max((int)(bp[0] - org), (int)(bp[2] - org)), max((int)(bq[0] - org), (int)(bq[2] - org))));
  return org + (u32)max(m + DO <= OFO ? OFO : m + DL_, lk + RB - DP);
}
__device__ __forceinline__ float asf(u32 v) { return __builtin_bit_cast(float, v); }

struct Args {
  const unsigned char* half[4][2];
  const u16 *x, *kc, *vc, *wd, *cc;
  u16* out;
  unsigned char* ws;
};

__global__ void __launch_bounds__(1024, 4) fused(const Args a) {
  extern __shared__ __attribute__((aligned(16))) unsigned char smem[];
  LAS unsigned char* L = (LAS unsigned char*)smem;
  const u32 lbase = (u32)(size_t)L;
  const int tid = threadIdx.x, w = __builtin_amdgcn_readfirstlane(tid >> 6), bid = blockIdx.x;
  int lane = tid & 63;
  const int xcd = bid & 7, idx = bid >> 3, hj = idx & 3, h = 4 * xcd + hj, b = idx >> 2, bh = b * 32 + h;
  const int sw8 = hj == 3 ? (w & 1) * 8 : 0, rw = (hj == 3 ? 24 : 32) * w;
  u64 hA, hB, t64;
  u32 warm;
  asm volatile("s_load_dwordx2 %0, %4, %5\n\ts_load_dwordx2 %1, %4, %6\n\ts_load_dword %3, %4, 0x40\n\ts_memrealtime %2" : "=&s"(hA), "=&s"(hB), "=&s"(t64), "=&s"(warm)
               : "s"(__builtin_amdgcn_kernarg_segment_ptr()), "s"(16 * hj + sw8), "s"(16 * hj + (sw8 ^ 8)));
  SB();
  const int bb = (b + xcd) & 7;
  int p3r = 5 * xcd + min(xcd, 3) + bb;
  int mc = (b & 6) + ((xcd & 1) ^ 1), mg = ((xcd & 6) + b) & 7;
  const bool tq = hj == 0, to = hj == 2;
  int lim = hj == 1 ? 6 - b : (xcd < 3 ? 6 : 5) - bb;
  asm volatile("" : "+s"(p3r), "+s"(mc), "+s"(mg), "+s"(lim));
  const bool idle = (hj & 1) && lim <= 0;
  u64 tp0, tp1;
  {
    const u32 pitch = (u32)(0x0AC0040018002000ull >> (16 * hj)) & 0xffffu;
    u32 ce = (xcd << (to ? 13 : 4)) + 2 * b, c1 = 16 * mc + 2 * mg;
    asm volatile("" : "+s"(ce), "+s"(c1));
    const u32 col = 64 * ((hj & 1) ? (hj == 1 ? c1 : p3r) : ce);
    const u32 step = pitch << (to ? 1 : 3);
    u32 off = (rw + (to ? (lane & 24) + (lane >> 5) : lane >> 3)) * pitch + col + ((lane & 3) ^ ((lane >> 3) & 2)) * 16;
    tp0 = off;
    tp1 = off + step;
    u64 s2 = 2 * step;
    u32 ld = lbase + L_W + rw * 128;
    asm volatile("" : "+s"(s2), "+s"(ld), "+v"(tp0), "+v"(tp1));
    asm volatile("s_mov_b32 exec_lo, 0xf0f0f0f0\n\ts_mov_b32 exec_hi, 0xf0f0f0f0\n\ts_waitcnt lgkmcnt(0)\n\tv_lshl_add_u64 %0, %0, 0, %3\n\tv_lshl_add_u64 %1, %1, 0, %2\n\t"
                 "s_not_b64 exec, exec\n\tv_lshl_add_u64 %0, %0, 0, %2\n\tv_lshl_add_u64 %1, %1, 0, %3\n\ts_mov_b64 exec, -1"
                 : "+v"(tp0), "+v"(tp1), "+s"(hA), "+s"(hB), "+s"(t64), "+s"(warm) :: "scc");
    if (!idle) {
      u32 keep;
      asm volatile("s_mov_b32 %0, m0\n\ts_mov_b32 m0, %3\n\ts_nop 0\n\tglobal_load_lds_dwordx4 %1, off nt\n\ts_add_u32 m0, m0, 0x400\n\t"
                   "v_lshl_add_u64 %1, %1, 0, %4\n\tglobal_load_lds_dwordx4 %2, off nt\n\ts_mov_b32 m0, %0\n\tv_lshl_add_u64 %2, %2, 0, %4"
                   : "=&s"(keep), "+v"(tp0), "+v"(tp1) : "s"(ld), "s"(s2) : "memory", "scc");
      glds((const void*)tp0, ld + 2048);
      if (hj != 3) glds((const void*)tp1, ld + 3072);
    }
  }
  SB();
  u32 t0 = (u32)t64;
  const u16 *x = a.x, *kc = a.kc, *vc = a.vc, *wd = a.wd, *cc = a.cc;
  u16* out = a.out;
  unsigned char* ws = a.ws;
  t0 += (u32)(KW * max((int)((u32)__builtin_amdgcn_s_memrealtime() - t0) - KA, 0)) / 4u;
  const int p3 = (hj == 3 && !idle) ? p3r : -1;
  const bool tm = hj == 1 && !idle;
  u32 tag;
  TAG();
  const __amdgpu_buffer_rsrc_t rs = __builtin_amdgcn_make_buffer_rsrc((void*)(ws + (size_t)(tag & 63u) * WS_STEP), 0, WS_END, 0x00020000);
  if (idle && hj == 1) {
    if (w == 0) {
      st_sc1(rs, WS_G1 + ((b & 1) * 8 + xcd) * 1024 + lane * 16, u32x4{~0u, ~0u, ~0u, ~0u});
      VM0();
      if (lane == 0) st_sc1_64(rs, WS_TS + ((b & 1) * 8 + xcd) * 8, u32x2{t0, tag});
    } else {
      u64 pc;
      u32 len;
      asm volatile("s_getpc_b64 %0\n1:\n\ts_mov_b32 %1, fused_code_end - 1b + 1000\n\ts_max_u32 %1, %1, fused_cold_end - 1b + 1000" : "=s"(pc), "=&s"(len) :: "scc");
      const u32 o = ((b & 1) * 15 + w - 1) * 1024;
      if (o < len) touch((const void*)(pc & ~15ull), min(o + lane * 16, len & ~15u), lbase + L_SINK);
    }
  }
  if (w == 1) glds((const unsigned char*)((lane & 1) ? vc : kc) + ((size_t)bh * SK + (SK - WIN)) * 256, lbase + L_SINK);
  int ta = TAU;
  int n = lane & 15, g = lane >> 4;
  u32x4 cv, hb;
  const bool row = tm && (w >= 8 || mc == 1);
  if (row) cv = ldnt((w < 8 ? x : cc) + (w & 7) * 512 + lane * 8);
  if (tid < 16) ((volatile LAS u32*)(L + L_CTL))[tid] = tid == C_T0 ? t0 : 0u;
  BAR();
  t0 = __builtin_amdgcn_readfirstlane(((volatile LAS u32*)(L + L_CTL))[C_T0]);
  u32 due = t0 * 4u + (u32)((4 * bb + hj) * TAU) / 32u + DUE0;
  int rot = idx;
#define RO(I) ((((I) + rot) & 15) * 8192)
  const u16* kp = kc + ((size_t)bh * SK + (SK - WIN) + 4 * w) * 128;
  u32 l16 = lane * 16;
  PIN(l16);
#define LDR(D, P, I, W) { const GAS unsigned char* p_ = (const GAS unsigned char*)((P) + RO(I)) + l16; PIN(p_); W; D = __builtin_nontemporal_load((const GAS u32x4*)p_); SB(); }
  u32x4 kr[16], vr[16];
  volatile LAS u32* ctl = (volatile LAS u32*)(L + L_CTL);
#define RELANE() do { asm volatile("" : "+v"(lane)); n = lane & 15; g = lane >> 4; } while (0)
  const int e = b * 16 + 2 * xcd + hj;
  const bool red = hj < 2;

#define KL(I) { const int t_ = ta + extra(I); LDR(kr[I], kp, I, PACE(t_)) }
  KL(0)
  if (row) {
    if (w >= 8) {
#pragma unroll
      for (int j = 0; j < 4; ++j) { const float a0 = blo(cv[j]), a1 = bhi(cv[j]); hb[j] = pack2(a0 * rcp(1.f + __expf(-a0)), a1 * rcp(1.f + __expf(-a1))); }
      *(LAS u32x4*)(L + L_H2 + (w & 7) * H2S + lane * 16) = hb;
      lds_add((LAS u32*)(L + L_CTL) + C_R, lane);
    } else {
      float m, inv;
      ln_stats(cv, m, inv);
      if ((lane >> 3) == mg) {
        LAS f32x4* d = (LAS f32x4*)(L + L_RED + w * 256 + (lane & 7) * 32);
        d[0] = f32x4{rbf(rbf(blo(cv[0]) - m) * inv), rbf(rbf(bhi(cv[0]) - m) * inv), rbf(rbf(blo(cv[1]) - m) * inv), rbf(rbf(bhi(cv[1]) - m) * inv)};
        d[1] = f32x4{rbf(rbf(blo(cv[2]) - m) * inv), rbf(rbf(bhi(cv[2]) - m) * inv), rbf(rbf(blo(cv[3]) - m) * inv), rbf(rbf(bhi(cv[3]) - m) * inv)};
      }
    }
  }
  KL(1)
  u32x2 tsv;
#define TS_WAIT() if (!__all(tsv[1] == tag)) { _Pragma("unroll 1") for (u32 s = 0; s < SPIN_CAP; ++s) { __builtin_amdgcn_s_sleep(16); MEMB(); \
      tsv = ld_sc1_64(rs, WS_TS + (lane & 15) * 8); if (__all(tsv[1] == tag)) break; } } MEMB();
  if (tm) {
    asm volatile("s_waitcnt vmcnt(2)" ::: "memory");
    lds_wait(ctl + C_R, 8u);
    hb = *(LAS u32x4*)(L + L_H2 + (n & 7) * H2S + (32 * w + 8 * g) * 2);
    tile_part(L + L_W + w * 4096, L + L_TP + w * 2048 + lane * 16, hb, n, g);
    lds_add((LAS u32*)(L + L_CTL) + C_T, lane);
  }
  KL(2)
  if (w == 0 || (tm && w >= 14)) tsv = ld_sc1_64(rs, WS_TS + (lane & 15) * 8);
  SB();
  if (tm && w >= 14) {
    lds_wait(ctl + C_T, 16u);
    const int col = 32 * (w & 1) + 16 * (n >> 3) + 4 * g;
    const f32x4 s = tile_sum(L + L_TP + (w & 1) * 1024 + lane * 16);
    float v[4] = {rbf(s[0]), rbf(s[1]), rbf(s[2]), rbf(s[3])};
    if (mc == 1) {
      const f32x4 ln = *(LAS f32x4*)(L + L_RED + (n & 7) * 256 + col * 4);
#pragma unroll
      for (int j = 0; j < 4; ++j) v[j] = ln[j] * rbf(1.f + v[j]);
    }
    TS_WAIT()
    st_sc1_64(rs, mc < 2 ? WS_G1 + (2 * mg + (w & 1)) * 1024 + ((2 * (n >> 3) + (g >> 1)) * 16 + mc * 8 + (n & 7)) * 16 + (g & 1) * 8
                         : WS_MODR + ((mc - 2) * 8 + (n & 7)) * 1024 + mpos(64 * mg + col),
              u32x2{canon(pack2(v[0], v[1])), canon(pack2(v[2], v[3]))});
  }
  RELANE();
  KL(3) KL(4)
  if (w == 0) {
    TS_WAIT()
    const float d = __builtin_amdgcn_fmed3f((float)(int)(tsv[0] - t0), -1e5f, 1e5f);
    ctl[C_TB] = (t0 * 4u + (u32)(int)fmaxf(row_sum(d) * 0.25f, row_max(d) * 4.f - SKW)) | 1u;
  }
  KL(5) KL(6)
  u32 org = __builtin_amdgcn_readfirstlane(ctl[C_TB]);
  if (org) { const int adj = ((int)(org - t0 * 4u - (p3 >= 0 ? bb * MSH : 0)) * 85 + 1024) >> 11; ta += adj; }
  KL(7)
  if (tm && mc >= 2 && w >= 14 && lane == 0) __builtin_amdgcn_raw_buffer_store_b32(tag, rs, WS_FLAGS + (((mc - 2) * 8 + mg) * 2 + (w & 1)) * 4, 0, SC1);
  KL(8)
  u32x4 g0;
  const int o0 = WS_G1 + w * 1024 + lane * 16;
  if (tq) lds_wait(ctl + C_TB, 1u);
#define KG(K) { KL(K) if (tq && (w & 1) == ((K) & 1)) { until_s(due - TAU / 2); \
    asm volatile("buffer_load_dwordx4 %0, %1, %2, 0 offen sc1" : "=&v"(g0) : "v"(o0), "s"(rs) : "memory"); } }
  KG(9) KG(10)
  KL(11) KL(12)
  if (tq) {
    asm volatile("s_waitcnt vmcnt(2)" : "+v"(g0));
#define G1_OK() __all(g0[0] != ~0u && g0[1] != ~0u && g0[2] != ~0u && g0[3] != ~0u)
    if (!G1_OK()) {
#pragma unroll 1
      for (u32 s = 0; s < SPIN_CAP; ++s) {
        __builtin_amdgcn_s_sleep(16);
        MEMB();
        g0 = ld_sc1(rs, o0);
        if (G1_OK()) break;
      }
    }
#pragma unroll
    for (int j = 0; j < 4; ++j) { const u32 o = dppu<ROR8>(g0[j]); hb[j] = pack2(blo(g0[j]) + blo(o), bhi(g0[j]) + bhi(o)); }
    PIN(hb);
  }
  KL(13)
  if (tq) {
    tile_part(L + L_W + w * 4096, L + L_TP + w * 2048 + lane * 16, hb, n, g);
    lds_add((LAS u32*)(L + L_CTL) + C_T, lane);
  }
  KL(14)
  if (tq && w >= 14) {
    lds_wait(ctl + C_T, 16u);
    const f32x4 s = tile_sum(L + L_TP + (w & 1) * 1024 + lane * 16);
    st_sc1(rs, WS_Q + ((n & 7) * 32 + 4 * xcd + (b >> 1)) * 512 + ((b & 1) * 64 + 32 * (w & 1) + 16 * (n >> 3) + 4 * g) * 4, u32x4{pack2(s[0], s[1]), tag, pack2(s[2], s[3]), tag});
  }
  RELANE();
  KL(15)
  asm volatile("" : "+s"(rot));
  const u16* vp = vc + ((size_t)bh * SK + (SK - WIN) + 4 * w) * 128;
#define VL(I) { const int t_ = ta + extra(16 + (I)); LDR(vr[I], vp, I, PACE(t_)) }
#define VLN(I) { const int t_ = ta + extra(16 + (I)); LDR(vr[I], vp, I, PACEN(t_)) }
  VL(0) VL(1)
  VL(2)
  u32x2 gq;
#define FL_WAIT() if (!__all(fl == tag)) {   _Pragma("unroll 1") for (u32 s = 0; s < SPIN_CAP; ++s) { __builtin_amdgcn_s_sleep(4); MEMB(); \
      fl = __builtin_amdgcn_raw_buffer_load_b32(rs, WS_FLAGS + lane * 4, 0, SC1); if (__all(fl == tag)) break; } } MEMB();
  u32 fl;
  if (w == WG_) {
    gq = ld_sc1_64(rs, WS_Q + bh * 512 + lane * 8);
    fl = __builtin_amdgcn_raw_buffer_load_b32(rs, p3 >= 0 ? WS_FLAGS + lane * 4 : WS_END, 0, SC1);
  }
  SB();
  VLN(3) VLN(4)
  if (w == WG_) {
    if (!__all(gq[1] == tag)) {
#pragma unroll 1
      for (u32 s = 0; s < SPIN_CAP; ++s) {
        __builtin_amdgcn_s_sleep(4);
        MEMB();
        gq = ld_sc1_64(rs, WS_Q + bh * 512 + lane * 8);
        if (__all(gq[1] == tag)) break;
      }
    }
    *(LAS u32*)(L + L_Q + lane * 4) = gq[0];
    PIN(fl);
    if (p3 >= 0) { FL_WAIT() }
    LGKM0();
    ctl[C_Q] = 1u;
  }
  RELANE();
  VL(5)
  lds_wait(ctl + C_Q, 1u);
  const bool prer = p3 >= 0 && w < 8;
  u32x4 pre0, pre1;
  if (prer) {
    pre0 = ld_sc1(rs, WS_MODR + (8 + w) * 1024 + lane * 16);
    pre1 = ld_sc1(rs, WS_MODR + (16 + w) * 1024 + lane * 16);
  }
  SB();
  const u32x4 qf = *(LAS u32x4*)(L + L_Q + n * 16);

#define DOT(I) dot2(kr[I][3], qf[3], dot2(kr[I][2], qf[2], dot2(kr[I][1], qf[1], dot2(kr[I][0], qf[0], 0.f))))
  float xs[8];
#define QK(C) { _Pragma("unroll") for (int i = 2 * (C); i < 2 * (C) + 2; ++i) { const float lo_ = DOT(i), hi_ = DOT(i + 8); \
      const float t1_ = lo_ + dppf<ROR8>(lo_), t2_ = hi_ + dppf<ROR8>(hi_); xs[i] = (n & 8) ? t2_ : t1_; PIN(xs[i]); } SB(); }
  QK(0) VLN(6) QK(1) VLN(7) QK(2) VL(8) QK(3) VL(9)
  if (prer) {
    const u32 a_h2w = w * C1S + lane * 8;
    *(LAS u32x2*)(L + L_C1 + a_h2w) = u32x2{pk2(up2(pre1[0]) + 1.f), pk2(up2(pre1[1]) + 1.f)};
    *(LAS u32x2*)(L + L_C1 + a_h2w + 512) = u32x2{pk2(up2(pre1[2]) + 1.f), pk2(up2(pre1[3]) + 1.f)};
    *(LAS u32x2*)(L + L_C1 + 8 * C1S + a_h2w) = u32x2{pre0[0], pre0[1]};
    *(LAS u32x2*)(L + L_C1 + 8 * C1S + a_h2w + 512) = u32x2{pre0[2], pre0[3]};
  }
#define TR_STEP(HALF, CTRL, BIT) _Pragma("unroll") for (int hh = 0; hh < (HALF); ++hh) { \
    const float lo_ = xs[hh], hi_ = xs[hh + (HALF)]; const float t1_ = lo_ + dppf<CTRL>(lo_), t2_ = hi_ + dppf<CTRL>(hi_); xs[hh] = (n & (BIT)) ? t2_ : t1_; }
  TR_STEP(4, XOR7, 4) TR_STEP(2, XOR2, 2) TR_STEP(1, XOR1, 1)
#undef TR_STEP
  float sc = rbf(rbf(xs[0]) * 0.08838834764831845f);
  PIN(sc);
  SB();
  VL(10)
  {
    const float mw_ = wave_max(sc);
    const float lw = wave_sum(__expf(sc - mw_));
    if (lane == 0) *(LAS f32x2*)(L + L_ML + w * 8) = f32x2{mw_, lw};
    lds_add((LAS u32*)(L + L_CTL) + C_ML, lane);
  }
  SB();
  VL(11)
  NEWPAGE();
  RELANE();
  u32 mk[4];
#pragma unroll
  for (int j = 0; j < 4; ++j) mk[j] = (n >> 1) == j ? ((n & 1) ? 0xffff0000u : 0x0000ffffu) : 0u;
  f32x4 oa = {0.f, 0.f, 0.f, 0.f};
#define PV(I) { const u32 pb_ = dppu<0x150 + (I)>(pd); oa = mfma(vr[I], u32x4{pb_ & mk[0], pb_ & mk[1], pb_ & mk[2], pb_ & mk[3]}, oa); asm volatile("" : "+v"(oa)); SB(); }
  PIN(mk[0]); PIN(mk[1]); PIN(mk[2]); PIN(mk[3]);
  u32 a_oa = L_RED + w * OSTR + (g * 8 + n) * 16;
  u32 ra = L_RED + (lane >> 5) * 8 * OSTR + (lane & 31) * 16;
  u32 rb_ = WS_O + (h * 8 + b) * 512 + (lane & 31) * 16 + (lane >> 5) * 8;
  u32 tagv = tag;
  PIN(a_oa); PIN(ra); PIN(rb_); PIN(tagv);
  org = __builtin_amdgcn_readfirstlane(ctl[C_TB]);
  org = org ? org + TEND : t0 * 4u;
  asm volatile("" : "+s"(org));
  VL(12)
  lds_wait(ctl + C_ML, 16u);
  u32 pd;
  {
    const f32x2 ml = *(LAS f32x2*)(L + L_ML + n * 8);
    const float M = row_max(ml[0]);
    const float pe = __expf(sc - M) * rcp(row_sum(__expf(ml[0] - M) * ml[1]));
    pd = pack2(pe, pe);
    PIN(pd);
  }
  SB();
  VL(13)
  PV(0) PV(1) PV(2) PV(3) PV(4)
  VL(14)
  PV(5) PV(6) PV(7) PV(8) PV(9)
  VL(15)
  PV(10) PV(11) PV(12) PV(13) PV(14)
  if (w == 13) __builtin_amdgcn_s_setprio(3);
  PV(15)
#undef PV
  if (n < 8) *(LAS f32x4*)(L + a_oa) = oa;
  BAR();
  if (w == 14 && lane == 0) st_sc1_64(rs, WS_B0 + bid * 8, u32x2{now4(), tagv});
  if (w == 13) {
    f32x4 s[8];
#pragma unroll
    for (int ww = 0; ww < 8; ++ww) s[ww] = *(LAS f32x4*)(L + ra + ww * OSTR);
    SB();
    const f32x4 t = ((s[0] + s[1]) + (s[2] + s[3])) + ((s[4] + s[5]) + (s[6] + s[7]));
    st_sc1_64(rs, rb_, u32x2{pack2(t32(t[0], t[2]), t32(t[1], t[3])), tagv});
  }
  if (to) {
    u32x4 wf[16];
    if (w < 4) {
      const u32 af = L_W + (8 * g + (n >> 2)) * 128 + (w ^ ((n >> 3) | ((g & 1) << 1))) * 32 + (n & 3) * 8;
#pragma unroll
      for (int ks = 0; ks < 16; ++ks) wf[ks] = trfrag(L + af + ks * 4096, L + af + ks * 4096 + 512);
      __builtin_amdgcn_s_setprio(3);
    }
    int oo = WS_O + xcd * 16384 + w * 1024 + lane * 16;
    u32 a_ow = L_TP + (2 * w + (lane >> 5)) * OBS + (lane & 31) * 8;
    u32 a_or = L_TP + ((n >> 3) * 16 + (n & 7)) * OBS + g * 16;
    int rc = n < 8 ? WS_PART + ((n * 16 + 2 * b + ((w >> 1) & 1)) * 8 + xcd) * 128 + (w & 1) * 64 + g * 16 : WS_END;
    PIN(oo); PIN(a_ow); PIN(a_or); PIN(rc);
    SEL4();
    until(org + OFO);
#pragma unroll 1
    for (u32 s = 0; s < SPIN_CAP; ++s) {
      const u32x4 og = ld_sc1(rs, oo);
      *(LAS u32x2*)(L + a_ow) = u32x2{og[0], og[2]};
      if (__builtin_expect(__all(((og[1] ^ tag) | (og[3] ^ tag)) == 0u), 1)) break;
      MEMB();
      __builtin_amdgcn_s_sleep(2);
    }
    BAR();
    if (w < 4) {
      u32x4 ob[8];
#pragma unroll
      for (int i = 0; i < 8; ++i) ob[i] = *(LAS u32x4*)(L + a_or + (i >> 2) * 8 * OBS + (i & 3) * 64);
      SB();
      f32x4 aL = {0.f, 0.f, 0.f, 0.f}, aH = {0.f, 0.f, 0.f, 0.f};
#pragma unroll
      for (int i = 0; i < 8; ++i) { aL = mfma(wf[i], ob[i], aL); aH = mfma(wf[8 + i], ob[i], aH); }
      const f32x4 v = {aL[0] + dppf<ROR8>(aH[0]), aL[1] + dppf<ROR8>(aH[1]), aL[2] + dppf<ROR8>(aH[2]), aL[3] + dppf<ROR8>(aH[3])};
      st_sc1(rs, rc, tag8(v, tagv, sel));
    }
  }

  const int bo = lane * 16;
  if (p3 >= 0) {
    u32 a_h2w = w * C1S + lane * 8;
    if (w == 8 && lane < 36) *(LAS u32*)(L + (lane < 32 ? L_ML : L_HH + 640) + lane * 4) = 0u;
    BAR();
    glds((const void*)(tp0 + (u32)((368 - 16 * w) * FF * 2)), lbase + L_W + 49152 + w * 1024);
    u32x4 bp, bq;
    if (w == 12) {
      until_s(org + OFB + 20);
      asm volatile("buffer_load_dwordx4 %0, %2, %3, 0 offen sc1\n\tbuffer_load_dwordx4 %1, %2, %3, 0 offen offset:1024 sc1" : "=&v"(bp), "=&v"(bq) : "v"(WS_B0 + bo), "s"(rs) : "memory");
    }
    BAR();
#define DL(R) glds(wd + (size_t)(32 * p3 + (R)) * 512, (lane ^ ((((R) & 3) | (((R) >> 1) & 4)) << 1)) * 16, lbase + L_W + 65536 + (R) * 1024)
    if (w < 8) { DL(3 * w); DL(3 * w + 1); DL(3 * w + 2); }
    if (w >= 12) { DL(2 * w); DL(2 * w + 1); }
    SB();
    u32 a_h2r = L_H2 + (n & 7) * H2S + (n >> 3) * 512 + g * 16;
    u32 a_hw = L_HH + (n < 8 ? n * 64 + (8 * (w & 3) + 4 * (g & 1) + 2 * (g >> 1)) * 2 : 512 + lane * 4);
    const u32x4 t16v = {tag & 0xffffu, tag >> 16, tag & 0xffffu, tag >> 16};
#define X_TIME() until_s(org + OFO + DX - 40); u32 tx = 2u; \
      _Pragma("unroll 1") for (u32 s = 0; s < LSPIN_CAP && tx == 2u; ++s) { tx = __builtin_amdgcn_readfirstlane(ctl[C_TX]); if (tx == 2u) __builtin_amdgcn_s_sleep(2); } \
      if (!(tx & 1u)) tx = org + OFO + DX;
#define X_BAD(A, B) ((or4(A) | or4(B)) & 0xffffu)
    if (w < 8) {
      const u32x2 ca = *(LAS u32x2*)(L + L_C1 + a_h2w), cb = *(LAS u32x2*)(L + L_C1 + a_h2w + 512);
      f32x2 c1[4] = {up2(ca[0]), up2(ca[1]), up2(cb[0]), up2(cb[1])};
      a_h2w = L_H2 + w * H2S + lane * 8;
      int xo = WS_X2 + xcd * 16384 + w * 2048 + lane * 16;
      u32 a_hr = L_HH + (n < 8 ? n * 64 + g * 16 : 768), a_hs = L_HH + (n < 8 ? 768 : (n & 7) * 64 + g * 16);
      PIN(a_hs);
      SEL4();
      const u32 a_d0 = L_W + 65536 + (8 * g + (n >> 2)) * 1024 + ((8 * w + ((n >> 1) & 1)) ^ (((n >> 2) | ((g & 1) << 2)) << 1)) * 16 + (n & 1) * 8;
      int yo = WS_YP + ((16 * w + 4 * (n >> 3) + g) * NP3 + p3) * 128 + (n & 7) * 16, yo2 = yo + 8 * NP3 * 128;
      PIN(xo); PIN(c1[0]); PIN(c1[1]); PIN(c1[2]); PIN(c1[3]); PIN(a_h2w); PIN(a_hr); PIN(yo); PIN(yo2);
      u32 a_cx = lbase + L_CTL + (C_X + w) * 4;
      PIN(a_cx);
      asm volatile("s_waitcnt vmcnt(3)" ::: "memory");
      BAR();
      __builtin_amdgcn_s_setprio(2);
      X_TIME()
      until(tx);
#pragma unroll 1
      for (u32 s = 0; s < SPIN_CAP; ++s) {
        const u32x4 xa = ld_sc1(rs, xo) ^ t16v, xb = ld_sc1(rs, xo + 1024) ^ t16v;
        const float x0 = __builtin_bit_cast(float, __builtin_amdgcn_readfirstlane(xa[0]));
        *(LAS u32x2*)(L + a_h2w) = u32x2{pk2((f32x2{asf(xa[0]), asf(xa[1])} - x0) * c1[0]), pk2((f32x2{asf(xa[2]), asf(xa[3])} - x0) * c1[1])};
        *(LAS u32x2*)(L + a_h2w + 512) = u32x2{pk2((f32x2{asf(xb[0]), asf(xb[1])} - x0) * c1[2]), pk2((f32x2{asf(xb[2]), asf(xb[3])} - x0) * c1[3])};
        if (__builtin_expect(__all(X_BAD(xa, xb) == 0u), 1)) break;
        MEMB();
      }
      asm volatile("s_mov_b64 exec, 1\n\tds_write_b32 %0, %0\n\ts_mov_b64 exec, -1" :: "v"(a_cx) : "memory");
      __builtin_amdgcn_s_setprio(0);
      lds_add((LAS u32*)(L + L_CTL) + C_R, lane);
      lds_wait(ctl + C_R, 12u);
      u32x4 df[4];
#pragma unroll
      for (int i = 0; i < 4; ++i) { df[i] = trfrag(L + (a_d0 ^ (32 * i)), L + (a_d0 ^ (32 * i)) + 4096); PIN(df[i]); }
      BAR();
      const u32x4 h0 = *(LAS u32x4*)(L + a_hr), h1 = *(LAS u32x4*)(L + a_hs);
      const f32x4 z = {0.f, 0.f, 0.f, 0.f};
      const f32x4 a0 = mfma(df[1], h1, mfma(df[0], h0, z)), a1 = mfma(df[3], h1, mfma(df[2], h0, z));
      st_sc1(rs, yo, tag8(a0, tagv, sel));
      st_sc1(rs, yo2, tag8(a1, tagv, sel));
    } else if (w < 12) {
      NEWPAGE();
      u32 a_st = L_ML + (n & 7) * 16;
      PIN(a_st);
      __builtin_amdgcn_s_setprio(3);
      u32x4 gf[16], gb_[8];
      const u32 af = L_W + (8 * g + (n >> 2)) * 128 + (((w & 3) + (n & 2) * 2) ^ (((n >> 3) | ((g & 1) << 1)) << 1)) * 16 + (n & 1) * 8;
      f32x4 aL = {0.f, 0.f, 0.f, 0.f}, aH;
      f32x2 ag, au, sg, su, gt, up;
      {
#pragma unroll
        for (int q4 = 0; q4 < 4; ++q4) {
#pragma unroll
          for (int i = 0; i < 4; ++i) gb_[i] = *(LAS u32x4*)(L + L_C1 + n * C1S + g * 16 + q4 * 256 + i * 64);
          if (q4 == 3) {
            VM0();
            BAR();
          }
#pragma unroll
          for (int i = 0; i < 4; ++i) gf[4 * q4 + i] = trfrag(L + af + (4 * q4 + i) * 4096, L + af + (4 * q4 + i) * 4096 + 512);
          SB();
#pragma unroll
          for (int i = 0; i < 4; ++i) aL = mfma(gf[4 * q4 + i], gb_[i], aL);
          PIN(aL);
        }
        const auto s0 = __builtin_amdgcn_permlane32_swap(__float_as_uint(aL[0]), __float_as_uint(aL[2]), false, false);
        const auto s1 = __builtin_amdgcn_permlane32_swap(__float_as_uint(aL[1]), __float_as_uint(aL[3]), false, false);
        ag = -f32x2{__uint_as_float(s0[0]), __uint_as_float(s1[0])};
        au = -f32x2{__uint_as_float(s0[1]), __uint_as_float(s1[1])};
        sg = f32x2{dppf<ROR8>(ag[0]), dppf<ROR8>(ag[1])} * 1.4426950408889634f;
        su = -f32x2{dppf<ROR8>(au[0]), dppf<ROR8>(au[1])};
        PIN(ag); PIN(au); PIN(sg); PIN(su); PIN(a_h2r); PIN(a_hw);
      }
      __builtin_amdgcn_s_setprio(0);
      _Pragma("unroll 1") for (u32 s = 0; s < LSPIN_CAP; ++s) if (__all(ctl[C_X + (lane & 7)] != 0u)) break;
      __builtin_amdgcn_s_setprio(3);
      MEMB();
      GU_MUL(L + a_h2r)
      SB();
      f32x4 st = *(volatile LAS f32x4*)(L + a_st);
      gu_pair(aL, aH, gt, up);
      if (!__all(st[3] != 0.f)) {
        _Pragma("unroll 1") for (u32 s = 0; s < LSPIN_CAP; ++s) { st = *(volatile LAS f32x4*)(L + a_st); if (__all(st[3] != 0.f)) break; }
      }
      gt = __builtin_elementwise_fma(__builtin_elementwise_fma(ag, f32x2{st[0], st[0]}, gt), f32x2{st[2], st[2]}, sg);
      up = __builtin_elementwise_fma(__builtin_elementwise_fma(au, f32x2{st[0], st[0]}, up), f32x2{st[1], st[1]}, su);
      *(LAS u32*)(L + a_hw) = pk2(f32x2{rcp(1.f + __builtin_amdgcn_exp2f(gt[0])), rcp(1.f + __builtin_amdgcn_exp2f(gt[1]))} * up);
      BAR();
    } else {
      int xo = WS_X2 + xcd * 16384 + (w - 12) * 4096 + lane * 16;
      u32 a_st = lbase + L_ML + (2 * (w - 12) + (lane >> 5)) * 16;
      PIN(xo); PIN(a_st);
      asm volatile("s_waitcnt vmcnt(2)" : "+v"(bp), "+v"(bq) :: "memory");
      BAR();
      if (w == 12 && __builtin_expect(!on_time(tag, org, bp, bq), 0)) {
        ctl[C_TX] = 2u;
        ctl[C_TX] = (anchor(rs, bo, tag, org, bp, bq) + DX) | 1u;
      }
      VM0();
      lds_add((LAS u32*)(L + L_CTL) + C_R, lane);
      __builtin_amdgcn_s_setprio(1);
      X_TIME()
      until(tx + SDL);
      u32x4 xv[4];
#pragma unroll
      for (int i = 0; i < 4; ++i) xv[i] = ld_sc1(rs, xo + i * 1024);
#pragma unroll
      for (int i = 0; i < 4; ++i) xv[i] ^= t16v;
      if (__builtin_expect(!__all((X_BAD(xv[0], xv[1]) | X_BAD(xv[2], xv[3])) == 0u), 0)) {
#pragma unroll 1
        for (u32 s = 0; s < SPIN_CAP; ++s) {
          MEMB();
#pragma unroll
          for (int i = 0; i < 4; ++i) xv[i] = ld_sc1(rs, xo + i * 1024) ^ t16v;
          if (__all((X_BAD(xv[0], xv[1]) | X_BAD(xv[2], xv[3])) == 0u)) break;
        }
      }
      float sm[2], sq[2], xs0[2];
#pragma unroll
      for (int k = 0; k < 2; ++k) {
        const float x0 = __builtin_bit_cast(float, __builtin_amdgcn_readfirstlane(xv[2 * k][0]));
        const u32x4 xa = xv[2 * k], xb = xv[2 * k + 1];
        const f32x2 d0 = f32x2{asf(xa[0]), asf(xa[1])} - x0, d1 = f32x2{asf(xa[2]), asf(xa[3])} - x0, d2 = f32x2{asf(xb[0]), asf(xb[1])} - x0, d3 = f32x2{asf(xb[2]), asf(xb[3])} - x0;
        const f32x2 sv = (d0 + d1) + (d2 + d3), qv = __builtin_elementwise_fma(d0, d0, d1 * d1) + __builtin_elementwise_fma(d2, d2, d3 * d3);
        xs0[k] = x0;
        sm[k] = sv[0] + sv[1];
        sq[k] = qv[0] + qv[1];
      }
      const float r = row_sum(t32(t16(sm[0], sq[0]), t16(sm[1], sq[1]))) * (1.f / 512.f);
      const auto sw = __builtin_amdgcn_permlane16_swap(__float_as_uint(r), __float_as_uint(r), false, false);
      const float md = __uint_as_float(sw[0]);
      const float x0 = lane < 32 ? xs0[0] : xs0[1];
      const float inv = rcp(rbf(__builtin_amdgcn_sqrtf(rbf(rbf(fmaxf(fmaf(-md, md, __uint_as_float(sw[1])), 0.f)) + 1e-5f))));
      const f32x4 st = {rbf(x0 + md) - x0, inv, inv * -1.4426950408889634f, 1.f};
      asm volatile("s_mov_b32 exec_lo, 1\n\ts_mov_b32 exec_hi, 1\n\tds_write_b128 %0, %1\n\ts_mov_b64 exec, -1" :: "v"(a_st), "v"(st) : "memory");
      if (w == 15) {
        _Pragma("unroll 1") for (u32 s = 0; s < LSPIN_CAP; ++s) if (__all(ctl[C_X + (lane & 7)] != 0u)) break;
        if (lane < 8) st_sc1_64(rs, WS_B1 + lane * 512 + p3 * 8, u32x2{now4(), tagv});
      }
      BAR();
    }
    asm volatile("fused_code_end:");
  } else if (red && (w == RW || w == RW - 1)) {
    NEWPAGE();
    const int f = 16 * xcd + 2 * b + hj, ro_ = 4 * (w & 1) + (lane & 3);
    const int rb = e >> 4, rj = e & 15, c2 = 32 * rj + 4 * (lane & 7) + g;
    fl = __builtin_amdgcn_raw_buffer_load_b32(rs, WS_FLAGS + lane * 4, 0, SC1);
    float xx = blo(x[rb * 512 + c2]);
    FL_WAIT()
    float ga = blo(__builtin_amdgcn_raw_buffer_load_b16(rs, WS_MODR + rb * 1024 + mpos(c2), 0, SC1));
    float gb = blo(__builtin_amdgcn_raw_buffer_load_b16(rs, WS_MODR + (24 + ro_) * 1024 + mpos(4 * f + g), 0, SC1));
    int po = WS_PART + e * 1024 + lane * 16;
    int xo = WS_X2 + (lane >> 3) * 16384 + rb * 2048 + rj * 128 + (lane & 7) * 16;
    int xi = WS_X2 + xcd * 16384 + ro_ * 2048 + (4 * f + g) * 4;
    u32 xt = (g & 1) ? tag >> 16 : tag & 0xffffu;
    const u32x4 tb4 = bytes4(tag);
    u32x4 tb5 = lane < 44 ? tb4 : u32x4{0, 0, 0, 0};
    int yo = WS_YP + (f * NP3 + (lane >> 2)) * 128 + ro_ * 16;
    int y2 = lane < 44 ? yo + 4096 : WS_END;
    u32 oof = (lane & 12) == 0 ? (ro_ * 512 + 4 * f + g) * 2 : 8192;
    __amdgpu_buffer_rsrc_t ro = __builtin_amdgcn_make_buffer_rsrc((void*)out, 0, 8192, 0x00020000);
    asm volatile("" : "+s"(ro));
    PIN(xt); PIN(tb5); PIN(ga); PIN(gb); PIN(xx); PIN(po); PIN(xo); PIN(xi); PIN(yo); PIN(y2); PIN(oof);
    if (w == RW) {
      u32x4 bp, bq;
      until_s(org + OFB);
      B0_LOAD()
      u32 anc = org + OFO;
      if (__builtin_expect(!on_time(tag, org, bp, bq), 0)) anc = anchor(rs, bo, tag, org, bp, bq);
      until(anc + DP);
      u32x4 xq;
#pragma unroll 1
      for (u32 sp = 0; sp < SPIN_CAP; ++sp) {
        const u32x4 pv = ld_one(rs, po) ^ tb4;
        float t = t32(t16(asf(pv[0]), asf(pv[1])), t16(asf(pv[2]), asf(pv[3])));
        t += dppf<ROR8>(t);
        const u32 xw = __float_as_uint(rbf(xx + rbf(ga * rbf(t)))) | xt;
        const auto p1 = __builtin_amdgcn_permlane16_swap(xw, xw, false, false);
        const auto p2 = __builtin_amdgcn_permlane32_swap(p1[0], p1[0], false, false), p3_ = __builtin_amdgcn_permlane32_swap(p1[1], p1[1], false, false);
        xq = u32x4{p2[0], p3_[0], p2[1], p3_[1]};
        PIN(xq);
        if (__builtin_expect(__all((or4(pv) & 255u) == 0u), 1)) break;
        MEMB();
      }
      st_sc1(rs, xo, xq);
      if (bid == 1 && lane == 0) __hip_atomic_store((GAS u32*)ws, tag, RLX_AGENT);
      u32 ty = anc + DYS;
      const int lt = (int)(now4() - anc) - (DX + 20);
      if (__builtin_expect(lt > 0, 0)) {
        int lk = DB1;
        while (lk < DB1 + lt + 240) lk += PB;
        u32x2 bv;
#pragma unroll 1
        for (u32 sp = 0; sp < SPIN_CAP; ++sp) {
          until_s(anc + lk);
          bv = ld_sc1_64(rs, WS_B1 + xcd * 512 + (lane < NP3 ? lane : 0) * 8);
          if (__all(bv[1] == tag)) break;
          MEMB();
          do lk += PB; while ((int)(now4() - anc) > lk);
        }
        ty = org + smax((int)(bv[0] - org)) + DY;
        asm volatile("fused_cold_end:");
      }
      ctl[C_TX] = ty | 1u;
    }
    lds_wait(ctl + C_TX, 1u);
    until(__builtin_amdgcn_readfirstlane(ctl[C_TX]));
    u32 res;
#pragma unroll 1
    for (u32 sp = 0; sp < SPIN_CAP; ++sp) {
      const u32 x2 = __builtin_amdgcn_raw_buffer_load_b32(rs, xi, 0, SC1) ^ xt;
      const u32x4 y0 = ld_one(rs, yo) ^ tb4, y1 = ld_one(rs, yo + 2048) ^ tb4, y2_ = ld_one(rs, y2) ^ tb5;
      const f32x4 s = (__builtin_bit_cast(f32x4, y0) + __builtin_bit_cast(f32x4, y1)) + __builtin_bit_cast(f32x4, y2_);
      float t = t32(t16(s[0], s[1]), t16(s[2], s[3]));
      t += dppf<ROR8>(t);
      t += dppf<ROR4>(t);
      res = pack2(fmaf(gb, t, asf(x2)), 0.f);
      PIN(res);
      if (__builtin_expect(__all((((or4(y0) | or4(y1) | or4(y2_)) & 255u) | (x2 & 0xffffu)) == 0u), 1)) break;
      MEMB();
    }
    __builtin_amdgcn_raw_buffer_store_b16((u16)res, ro, oof, 0, 0);
  }
}

extern "C" void kernel_launch(void* const* d_in, const int* in_sizes, int n_in, void* d_out, int out_size, void* d_ws, size_t ws_size, hipStream_t stream) {
  (void)hipFuncSetAttribute((const void*)fused, hipFuncAttributeMaxDynamicSharedMemorySize, L_END);
  Args a;
  const int src[4][2] = {{3, 3}, {10, 10}, {4, 4}, {6, 7}};
  for (int r = 0; r < 4; ++r)
    for (int k = 0; k < 2; ++k) a.half[r][k] = (const unsigned char*)d_in[src[r][k]] + (r < 3 && k ? 64 : 0);
  a.x = (const u16*)d_in[0]; a.kc = (const u16*)d_in[1]; a.vc = (const u16*)d_in[2]; a.wd = (const u16*)d_in[8]; a.cc = (const u16*)d_in[9];
  a.out = (u16*)d_out; a.ws = (unsigned char*)d_ws;
  fused<<<256, 1024, L_END, stream>>>(a);
}
```

```cpp
#include <hip/hip_runtime.h>
#include <stdint.h>

typedef unsigned int u32;
typedef unsigned long long u64;
typedef unsigned short u16;
typedef u32 u32x4 __attribute__((ext_vector_type(4)));
typedef u32 u32x2 __attribute__((ext_vector_type(2)));
typedef float f32x4 __attribute__((ext_vector_type(4)));
typedef float f32x2 __attribute__((ext_vector_type(2)));
typedef short s16x4 __attribute__((ext_vector_type(4)));
typedef __bf16 bf16x8 __attribute__((ext_vector_type(8)));
typedef __bf16 bf16x2 __attribute__((ext_vector_type(2)));
#define GAS __attribute__((address_space(1)))
#define LAS __attribute__((address_space(3)))
#define RLX_AGENT __ATOMIC_RELAXED, __HIP_MEMORY_SCOPE_AGENT

constexpr int SK = 4096, WIN = 1024, FF = 1376, NP3 = 43;

constexpr int WS_FLAGS = 0x1000;
constexpr int WS_TS = 0x1800;
constexpr int WS_G1 = 0x2000;
constexpr int WS_B1 = 0x8000;
constexpr int WS_MODR = 0xA000;
constexpr int WS_Q = 0x12000;
constexpr int WS_B0 = 0x32000;
constexpr int WS_NV = 0x33000;
constexpr int WS_NP = 0x34000;
constexpr int LN_O = 0;
constexpr int LN_PART = 128;
constexpr int LN_X2 = 256;
constexpr int LN_YP = 384;
constexpr int WS_T = 0x38000, T_SPAN = 17 * 16384;
constexpr int WS_END = WS_T + 4 * T_SPAN;
constexpr int WS_STEP = 2 << 20;
static_assert(WS_END + 16384 <= WS_STEP && LN_YP + 16 * NP3 <= 17 * 64, "");

constexpr int L_W = 0;
constexpr int L_TP = 65536;
constexpr int L_C1 = 98304;
constexpr int L_SINK = 130048;
constexpr int L_RED = 131072;
constexpr int L_H2 = L_RED + 16384;
constexpr int C1S = 1040, H2S = 1056, OSTR = 544, OBS = 288;
constexpr int L_Q = L_H2 + 8 * H2S;
constexpr int L_ML = L_Q + 512;
constexpr int L_HH = L_ML + 128;
constexpr int L_CTL = L_HH + 784;
constexpr int L_END = L_CTL + 64;
constexpr int C_T = 0, C_TB = 1, C_TX = 2, C_Q = 3, C_ML = 4, C_R = 6, C_T0 = 7, C_X = 8;
constexpr int RW = 12, WG_ = 10;

constexpr u32 SPIN_CAP = 1u << 17, LSPIN_CAP = 1u << 20;
constexpr int DUE0 = 810;
constexpr int TAU = 220;
constexpr int E1 = 16;
constexpr int E2 = 10;
constexpr int SKW = 200;
constexpr int MSH = 14;
constexpr int KA = 55;
constexpr int KW = 6;
constexpr int OFO = 420;
constexpr int OFB = 450;
constexpr int PB = 200;
constexpr int RB = 260;
constexpr int DO = 15;
constexpr int DL_ = 220;
constexpr int DP = 328;
constexpr int DX = 513;
constexpr int DYS = 1043;
constexpr int DB1 = 880;
constexpr int DY = 200;
constexpr int SDL = 12;
constexpr int WDT = 40;
constexpr int extra(int i) { return i < 8 ? E1 : (i >= 14 && i < 22 ? E2 : 0); }
constexpr int TEND = DUE0 + 32 * TAU + 8 * E1 + 8 * E2;

__device__ __forceinline__ float rbf(float f) { return (float)(__bf16)f; }
__device__ __forceinline__ u32 pack2(float a, float b) { bf16x2 v = {(__bf16)a, (__bf16)b}; return __builtin_bit_cast(u32, v); }
__device__ __forceinline__ float blo(u32 v) { return __builtin_bit_cast(float, v << 16); }
__device__ __forceinline__ float bhi(u32 v) { return __builtin_bit_cast(float, v & 0xffff0000u); }
__device__ __forceinline__ u32 canon(u32 v) { return v < 0xfffffffeu ? v : 0xfffffffeu; }
#define SEL4() u32x4 sel = {0x07060500u, 0x07060501u, 0x07060502u, 0x07060503u}; asm volatile("" : "+s"(sel))
__device__ __forceinline__ u32x4 tag8(f32x4 v, u32 tagv, u32x4 sel) {
  const u32x4 r = __builtin_bit_cast(u32x4, v);
  return u32x4{__builtin_amdgcn_perm(r[0], tagv, sel[0]), __builtin_amdgcn_perm(r[1], tagv, sel[1]), __builtin_amdgcn_perm(r[2], tagv, sel[2]), __builtin_amdgcn_perm(r[3], tagv, sel[3])};
}
__device__ __forceinline__ u32x4 bytes4(u32 tag) { return u32x4{tag & 255u, (tag >> 8) & 255u, (tag >> 16) & 255u, tag >> 24}; }
__device__ __forceinline__ u32 or4(u32x4 v) { return (v[0] | v[1]) | (v[2] | v[3]); }
__device__ __forceinline__ float rcp(float x) { return __builtin_amdgcn_rcpf(x); }
template <int CTRL> __device__ __forceinline__ float dppf(float x) {
  return __builtin_bit_cast(float, __builtin_amdgcn_mov_dpp(__builtin_bit_cast(int, x), CTRL, 0xf, 0xf, true));
}
template <int CTRL> __device__ __forceinline__ u32 dppu(u32 x) { return (u32)__builtin_amdgcn_mov_dpp((int)x, CTRL, 0xf, 0xf, true); }
constexpr int ROR8 = 0x128, ROR4 = 0x124, ROR2 = 0x122, ROR1 = 0x121, XOR1 = 0xB1, XOR2 = 0x4E, XOR7 = 0x141;
__device__ __forceinline__ float row_sum(float x) { x += dppf<ROR8>(x); x += dppf<ROR4>(x); x += dppf<ROR2>(x); x += dppf<ROR1>(x); return x; }
__device__ __forceinline__ float row_max(float x) {
  x = fmaxf(x, dppf<ROR8>(x)); x = fmaxf(x, dppf<ROR4>(x)); x = fmaxf(x, dppf<ROR2>(x)); x = fmaxf(x, dppf<ROR1>(x)); return x;
}
__device__ __forceinline__ float x16_sum(float x) {
  auto s = __builtin_amdgcn_permlane16_swap(__float_as_uint(x), __float_as_uint(x), false, false);
  return __uint_as_float(s[0]) + __uint_as_float(s[1]);
}
__device__ __forceinline__ float x32_sum(float x) {
  auto s = __builtin_amdgcn_permlane32_swap(__float_as_uint(x), __float_as_uint(x), false, false);
  return __uint_as_float(s[0]) + __uint_as_float(s[1]);
}
__device__ __forceinline__ float x16_max(float x) {
  auto s = __builtin_amdgcn_permlane16_swap(__float_as_uint(x), __float_as_uint(x), false, false);
  return fmaxf(__uint_as_float(s[0]), __uint_as_float(s[1]));
}
__device__ __forceinline__ float x32_max(float x) {
  auto s = __builtin_amdgcn_permlane32_swap(__float_as_uint(x), __float_as_uint(x), false, false);
  return fmaxf(__uint_as_float(s[0]), __uint_as_float(s[1]));
}
__device__ __forceinline__ float t16(float a, float b) {
  auto s = __builtin_amdgcn_permlane16_swap(__float_as_uint(a), __float_as_uint(b), false, false);
  return __uint_as_float(s[0]) + __uint_as_float(s[1]);
}
__device__ __forceinline__ float t32(float a, float b) {
  auto s = __builtin_amdgcn_permlane32_swap(__float_as_uint(a), __float_as_uint(b), false, false);
  return __uint_as_float(s[0]) + __uint_as_float(s[1]);
}
__device__ __forceinline__ f32x2 up2(u32 v) { return f32x2{blo(v), bhi(v)}; }
__device__ __forceinline__ u32 pk2(f32x2 v) { return pack2(v[0], v[1]); }
#define PIN(x) asm volatile("" : "+v"(x))
__device__ __forceinline__ f32x2 rb2(f32x2 v) { u32 p = pk2(v); asm("" : "+v"(p)); return up2(p); }
__device__ __forceinline__ float wave_sum(float x) { return x32_sum(x16_sum(row_sum(x))); }
__device__ __forceinline__ float wave_max(float x) { return x32_max(x16_max(row_max(x))); }
__device__ __forceinline__ float dot2(u32 a, u32 b, float c) {
  return __builtin_amdgcn_fdot2_f32_bf16(__builtin_bit_cast(bf16x2, a), __builtin_bit_cast(bf16x2, b), c, false);
}
__device__ __forceinline__ f32x4 mfma(u32x4 a, u32x4 b, f32x4 c) {
  return __builtin_amdgcn_mfma_f32_16x16x32_bf16(__builtin_bit_cast(bf16x8, a), __builtin_bit_cast(bf16x8, b), c, 0, 0, 0);
}
__device__ __forceinline__ u32x4 ldnt(const void* p) { return __builtin_nontemporal_load((const u32x4*)p); }
__device__ __forceinline__ void glds(const void* g, u32 lds) {
  u32 keep;
  lds = __builtin_amdgcn_readfirstlane(lds);
  asm volatile("s_mov_b32 %0, m0\n\ts_mov_b32 m0, %2\n\ts_nop 0\n\tglobal_load_lds_dwordx4 %1, off nt\n\ts_mov_b32 m0, %0"
               : "=&s"(keep) : "v"(g), "s"(lds) : "memory");
}
__device__ __forceinline__ void glds(const void* base, u32 off, u32 lds) {
  u32 keep;
  lds = __builtin_amdgcn_readfirstlane(lds);
  asm volatile("s_mov_b32 %0, m0\n\ts_mov_b32 m0, %3\n\ts_nop 0\n\tglobal_load_lds_dwordx4 %1, %2 nt\n\ts_mov_b32 m0, %0"
               : "=&s"(keep) : "v"(off), "s"(base), "s"(lds) : "memory");
}
__device__ __forceinline__ void touch(const void* base, u32 off, u32 lds) {
  u32 keep;
  lds = __builtin_amdgcn_readfirstlane(lds);
  asm volatile("s_mov_b32 %0, m0\n\ts_mov_b32 m0, %3\n\ts_nop 0\n\tglobal_load_lds_dwordx4 %1, %2\n\ts_mov_b32 m0, %0"
               : "=&s"(keep) : "v"(off), "s"(base), "s"(lds) : "memory");
}
__device__ __forceinline__ u32x4 trfrag(LAS unsigned char* p0, LAS unsigned char* p1) {
  s16x4 a = __builtin_amdgcn_ds_read_tr16_b64_v4i16((LAS s16x4*)p0);
  s16x4 b = __builtin_amdgcn_ds_read_tr16_b64_v4i16((LAS s16x4*)p1);
  u32x2 x = __builtin_bit_cast(u32x2, a), y = __builtin_bit_cast(u32x2, b);
  return u32x4{x[0], x[1], y[0], y[1]};
}
#define NEWPAGE() asm volatile("s_branch 9f\n\t.p2align 12\n9:")
#define SB() __builtin_amdgcn_sched_barrier(0)
#define MEMB() asm volatile("" ::: "memory")
#define VM0() asm volatile("s_waitcnt vmcnt(0)" ::: "memory")
#define LGKM0() asm volatile("s_waitcnt lgkmcnt(0)" ::: "memory")
#define BAR() asm volatile("s_waitcnt lgkmcnt(0)\n\ts_barrier" ::: "memory")
#define TAG() do { u32 ep_; asm volatile("s_load_dword %0, %1, 0x0\n\ts_waitcnt lgkmcnt(0)" : "=s"(ep_) : "s"(ws) : "memory"); \
    tag = ((ep_ & 0x7f7f7f7fu) + 0x01010101u) ^ (ep_ & 0x80808080u); } while (0)
constexpr int SC1 = 16;
__device__ __forceinline__ u32x4 ld_sc1(__amdgpu_buffer_rsrc_t rs, int off) { return __builtin_amdgcn_raw_buffer_load_b128(rs, off, 0, SC1); }
__device__ __forceinline__ u32x4 ld_one(__amdgpu_buffer_rsrc_t rs, int off) { return __builtin_amdgcn_raw_buffer_load_b128(rs, off, 0, SC1 | 2); }
__device__ __forceinline__ u32x2 ld_sc1_64(__amdgpu_buffer_rsrc_t rs, int off) { return __builtin_amdgcn_raw_buffer_load_b64(rs, off, 0, SC1); }
__device__ __forceinline__ void st_sc1(__amdgpu_buffer_rsrc_t rs, int off, u32x4 v) { __builtin_amdgcn_raw_buffer_store_b128(v, rs, off, 0, SC1); }
__device__ __forceinline__ void st_sc1_64(__amdgpu_buffer_rsrc_t rs, int off, u32x2 v) { __builtin_amdgcn_raw_buffer_store_b64(v, rs, off, 0, SC1); }

__device__ __forceinline__ void ln_stats(u32x4 xv, float& m, float& inv) {
  const float x[8] = {blo(xv[0]), bhi(xv[0]), blo(xv[1]), bhi(xv[1]), blo(xv[2]), bhi(xv[2]), blo(xv[3]), bhi(xv[3])};
  const float x0 = __builtin_bit_cast(float, __builtin_amdgcn_readfirstlane(xv[0] << 16));
  float s = 0.f, v = 0.f;
#pragma unroll
  for (int j = 0; j < 8; ++j) { const float d = x[j] - x0; s += d; v = fmaf(d, d, v); }
  const float md = wave_sum(s) * (1.f / 512.f), sq = wave_sum(v) * (1.f / 512.f);
  const float var = rbf(fmaxf(fmaf(-md, md, sq), 0.f));
  m = rbf(x0 + md);
  inv = rcp(rbf(__builtin_amdgcn_sqrtf(rbf(var + 1e-5f))));
}
#define PACE_(W, INC) do { asm volatile(W "\n1:\n\ts_memrealtime vcc\n\ts_waitcnt lgkmcnt(0)\n\ts_lshl_b32 vcc_lo, vcc_lo, 2\n\ts_sub_i32 vcc_hi, vcc_lo, %0\n\ts_cmp_lt_i32 vcc_hi, 0\n\t" \
    "s_cbranch_scc0 2f\n\ts_sleep 1\n\ts_branch 1b\n2:\n\ts_add_i32 %0, %0, %1" : "+s"(due) : "is"(INC) : "memory", "scc", "vcc"); SB(); } while (0)
#define PACE(INC) PACE_("s_waitcnt vmcnt(2)", INC)
#define PACEN(INC) PACE_("", INC)
__device__ __forceinline__ void until_s(u32 due) { PACE_("", 0); }
__device__ __forceinline__ void until(u32 t) {
  asm volatile("1:\n\ts_memrealtime vcc\n\ts_waitcnt lgkmcnt(0)\n\ts_lshl_b32 vcc_lo, vcc_lo, 2\n\ts_sub_i32 vcc_hi, vcc_lo, %0\n\ts_cmp_lt_i32 vcc_hi, 0\n\t"
               "s_cbranch_scc1 1b" :: "s"(t) : "memory", "scc", "vcc");
  SB();
}

__device__ __forceinline__ void lds_wait(volatile LAS u32* p, u32 want) {
  _Pragma("unroll 1") for (u32 s = 0; s < LSPIN_CAP; ++s) {
    if (__builtin_amdgcn_readfirstlane(*p) >= want) break;
    __builtin_amdgcn_s_sleep(2);
  }
  MEMB();
}
__device__ __forceinline__ void lds_add(LAS u32* p, int lane) {
  LGKM0();
  if (lane == 0) __hip_atomic_fetch_add(p, 1u, __ATOMIC_RELAXED, __HIP_MEMORY_SCOPE_WORKGROUP);
  MEMB();
}
__device__ __forceinline__ void tile_part(LAS unsigned char* T, LAS unsigned char* P, u32x4 hb, int n, int g) {
  LAS unsigned char* tb = T + (8 * g + (n >> 2)) * 128 + (n & 3) * 8;
  const int sx = (n >> 3) | ((g & 1) << 1);
  f32x4 a[4];
#pragma unroll
  for (int t = 0; t < 4; ++t) a[t] = mfma(trfrag(tb + (t ^ sx) * 32, tb + (t ^ sx) * 32 + 512), hb, f32x4{0.f, 0.f, 0.f, 0.f});
  *(LAS f32x4*)(P) = n < 8 ? a[0] : a[1];
  *(LAS f32x4*)(P + 1024) = n < 8 ? a[2] : a[3];
}
__device__ __forceinline__ f32x4 tile_sum(LAS unsigned char* P) {
  f32x4 s[8], r[2];
#pragma unroll
  for (int k = 0; k < 2; ++k) {
#pragma unroll
    for (int i = 0; i < 8; ++i) s[i] = *(LAS f32x4*)(P + (8 * k + i) * 2048);
    SB();
    r[k] = ((s[0] + s[1]) + (s[2] + s[3])) + ((s[4] + s[5]) + (s[6] + s[7]));
    SB();
  }
  return r[0] + r[1];
}
#define GU_MUL(P) _Pragma("unroll") for (int i = 0; i < 8; ++i) gb_[i] = *(LAS u32x4*)((P) + i * 64); SB(); \
    aL = f32x4{0.f, 0.f, 0.f, 0.f}; aH = aL; _Pragma("unroll") for (int i = 0; i < 8; ++i) { aL = mfma(gf[i], gb_[i], aL); aH = mfma(gf[8 + i], gb_[i], aH); }
__device__ __forceinline__ void gu_pair(f32x4 aL, f32x4 aH, f32x2& gt, f32x2& up) {
  const auto s0 = __builtin_amdgcn_permlane32_swap(__float_as_uint(aL[0] + dppf<ROR8>(aH[0])), __float_as_uint(aL[2] + dppf<ROR8>(aH[2])), false, false);
  const auto s1 = __builtin_amdgcn_permlane32_swap(__float_as_uint(aL[1] + dppf<ROR8>(aH[1])), __float_as_uint(aL[3] + dppf<ROR8>(aH[3])), false, false);
  gt = f32x2{__uint_as_float(s0[0]), __uint_as_float(s1[0])};
  up = f32x2{__uint_as_float(s0[1]), __uint_as_float(s1[1])};
}
__device__ __forceinline__ int tline(int c, int ln, u32 near) { return WS_T + (c & 3) * T_SPAN + (ln >> 6) * 16384 + ((near >> c) & 1) * 8192 + (ln & 63) * 128; }
__device__ __forceinline__ int mpos(int c) { return (c & 252) * 4 + (c >> 8) * 8 + (c & 3) * 2; }
__device__ __forceinline__ u32 smax(int m) { int r = (int)fminf(wave_max((float)m), 4e5f); PIN(r); return (u32)__builtin_amdgcn_readfirstlane(r); }
__device__ __forceinline__ u32 now4() { return (u32)__builtin_amdgcn_s_memrealtime() * 4u; }
#define B0_LOAD() { bp = ld_sc1(rs, bo); bq = ld_sc1(rs, bo + 1024); }
__device__ __forceinline__ bool on_time(u32 tag, u32 org, u32x4 bp, u32x4 bq) {
  const u32 lim = org + OFO - DO;
  return __all((((bp[1] ^ tag) | (bp[3] ^ tag) | (bq[1] ^ tag) | (bq[3] ^ tag)) | (((lim - bp[0]) | (lim - bp[2]) | (lim - bq[0]) | (lim - bq[2])) >> 31)) == 0u);
}
__device__ __forceinline__ u32 anchor(__amdgpu_buffer_rsrc_t rs, int bo, u32 tag, u32 org, u32x4 bp, u32x4 bq) {
  int lk = OFB;
#pragma unroll 1
  for (u32 s = 0; s < SPIN_CAP; ++s) {
    if (__all(((bp[1] ^ tag) | (bp[3] ^ tag) | (bq[1] ^ tag) | (bq[3] ^ tag)) == 0u)) break;
    MEMB();
    do lk += PB; while ((int)(now4() - org) > lk);
    until_s(org + lk);
    B0_LOAD()
  }
  const int m = (int)smax(max(max((int)(bp[0] - org), (int)(bp[2] - org)), max((int)(bq[0] - org), (int)(bq[2] - org))));
  return org + (u32)max(m + DO <= OFO ? OFO : m + DL_, lk + RB - DP);
}
__device__ __forceinline__ float asf(u32 v) { return __builtin_bit_cast(float, v); }

struct Args {
  const unsigned char* half[4][2];
  const u16 *x, *kc, *vc, *wd, *cc;
  u16* out;
  unsigned char* ws;
};

__global__ void __launch_bounds__(1024, 4) fused(const Args a) {
  extern __shared__ __attribute__((aligned(16))) unsigned char smem[];
  LAS unsigned char* L = (LAS unsigned char*)smem;
  const u32 lbase = (u32)(size_t)L;
  const int tid = threadIdx.x, w = __builtin_amdgcn_readfirstlane(tid >> 6), bid = blockIdx.x;
  int lane = tid & 63;
  const int xcd = bid & 7, idx = bid >> 3, hj = idx & 3, h = 4 * xcd + hj, b = idx >> 2, bh = b * 32 + h;
  const int sw8 = hj == 3 ? (w & 1) * 8 : 0, rw = (hj == 3 ? 24 : 32) * w;
  u64 hA, hB, t64;
  u32 warm;
  asm volatile("s_load_dwordx2 %0, %4, %5\n\ts_load_dwordx2 %1, %4, %6\n\ts_load_dword %3, %4, 0x40\n\ts_memrealtime %2" : "=&s"(hA), "=&s"(hB), "=&s"(t64), "=&s"(warm)
               : "s"(__builtin_amdgcn_kernarg_segment_ptr()), "s"(16 * hj + sw8), "s"(16 * hj + (sw8 ^ 8)));
  SB();
  const int bb = (b + xcd) & 7;
  int p3r = 5 * xcd + min(xcd, 3) + bb;
  int mc = (b & 6) + ((xcd & 1) ^ 1), mg = ((xcd & 6) + b) & 7;
  const bool tq = hj == 0, to = hj == 2;
  int lim = hj == 1 ? 6 - b : (xcd < 3 ? 6 : 5) - bb;
  asm volatile("" : "+s"(p3r), "+s"(mc), "+s"(mg), "+s"(lim));
  const bool idle = (hj & 1) && lim <= 0;
  u64 tp0, tp1;
  {
    const u32 pitch = (u32)(0x0AC0040018002000ull >> (16 * hj)) & 0xffffu;
    u32 ce = (xcd << (to ? 13 : 4)) + 2 * b, c1 = 16 * mc + 2 * mg;
    asm volatile("" : "+s"(ce), "+s"(c1));
    const u32 col = 64 * ((hj & 1) ? (hj == 1 ? c1 : p3r) : ce);
    const u32 step = pitch << (to ? 1 : 3);
    u32 off = (rw + (to ? (lane & 24) + (lane >> 5) : lane >> 3)) * pitch + col + ((lane & 3) ^ ((lane >> 3) & 2)) * 16;
    tp0 = off;
    tp1 = off + step;
    u64 s2 = 2 * step;
    u32 ld = lbase + L_W + rw * 128;
    asm volatile("" : "+s"(s2), "+s"(ld), "+v"(tp0), "+v"(tp1));
    asm volatile("s_mov_b32 exec_lo, 0xf0f0f0f0\n\ts_mov_b32 exec_hi, 0xf0f0f0f0\n\ts_waitcnt lgkmcnt(0)\n\tv_lshl_add_u64 %0, %0, 0, %3\n\tv_lshl_add_u64 %1, %1, 0, %2\n\t"
                 "s_not_b64 exec, exec\n\tv_lshl_add_u64 %0, %0, 0, %2\n\tv_lshl_add_u64 %1, %1, 0, %3\n\ts_mov_b64 exec, -1"
                 : "+v"(tp0), "+v"(tp1), "+s"(hA), "+s"(hB), "+s"(t64), "+s"(warm) :: "scc");
    if (!idle) {
      u32 keep;
      asm volatile("s_mov_b32 %0, m0\n\ts_mov_b32 m0, %3\n\ts_nop 0\n\tglobal_load_lds_dwordx4 %1, off nt\n\ts_add_u32 m0, m0, 0x400\n\t"
                   "v_lshl_add_u64 %1, %1, 0, %4\n\tglobal_load_lds_dwordx4 %2, off nt\n\ts_mov_b32 m0, %0\n\tv_lshl_add_u64 %2, %2, 0, %4"
                   : "=&s"(keep), "+v"(tp0), "+v"(tp1) : "s"(ld), "s"(s2) : "memory", "scc");
      glds((const void*)tp0, ld + 2048);
      if (hj != 3) glds((const void*)tp1, ld + 3072);
    }
  }
  SB();
  u32 t0 = (u32)t64;
  const u16 *x = a.x, *kc = a.kc, *vc = a.vc, *wd = a.wd, *cc = a.cc;
  u16* out = a.out;
  unsigned char* ws = a.ws;
  t0 += (u32)(KW * max((int)((u32)__builtin_amdgcn_s_memrealtime() - t0) - KA, 0)) / 4u;
  const int p3 = (hj == 3 && !idle) ? p3r : -1;
  const bool tm = hj == 1 && !idle;
  u32 tag;
  TAG();
  const __amdgpu_buffer_rsrc_t rs = __builtin_amdgcn_make_buffer_rsrc((void*)(((size_t)ws + 16383 + (size_t)(tag & 63u) * WS_STEP) & ~(size_t)16383), 0, WS_END, 0x00020000);
  if (idle && hj == 1) {
    if (w == 0) {
      st_sc1(rs, WS_G1 + ((b & 1) * 8 + xcd) * 1024 + lane * 16, u32x4{~0u, ~0u, ~0u, ~0u});
      VM0();
      if (lane == 0) st_sc1_64(rs, WS_TS + ((b & 1) * 8 + xcd) * 8, u32x2{t0, tag});
    } else {
      u64 pc;
      u32 len;
      asm volatile("s_getpc_b64 %0\n1:\n\ts_mov_b32 %1, fused_code_end - 1b + 1000\n\ts_max_u32 %1, %1, fused_cold_end - 1b + 1000" : "=s"(pc), "=&s"(len) :: "scc");
      const u32 o = ((b & 1) * 15 + w - 1) * 1024;
      if (o < len) touch((const void*)(pc & ~15ull), min(o + lane * 16, len & ~15u), lbase + L_SINK);
    }
  }
  if (w == 1) glds((const unsigned char*)((lane & 1) ? vc : kc) + ((size_t)bh * SK + (SK - WIN)) * 256, lbase + L_SINK);
  int ta = TAU;
  int n = lane & 15, g = lane >> 4;
  u32x4 cv, hb;
  const bool row = tm && (w >= 8 || mc == 1);
  if (row) cv = ldnt((w < 8 ? x : cc) + (w & 7) * 512 + lane * 8);
  if (tid < 16) ((volatile LAS u32*)(L + L_CTL))[tid] = tid == C_T0 ? t0 : 0u;
  BAR();
  t0 = __builtin_amdgcn_readfirstlane(((volatile LAS u32*)(L + L_CTL))[C_T0]);
  u32 due = t0 * 4u + (u32)((4 * bb + hj) * TAU) / 32u + DUE0;
  if (hj == 3 && bb >= 6 && w == 0) {
    const int k = (bb & 1) * 8 + xcd, o = WS_NP + k * 512 + (bb & 1) * 8192;
    __builtin_amdgcn_raw_buffer_store_b32(tag, rs, lane < 2 ? o ^ (lane * 8192) : WS_END, 0, SC1);
    u64 c0, c1, c2;
    u32 v;
    asm volatile("s_waitcnt vmcnt(0)\n\ts_memtime %0\n\ts_waitcnt lgkmcnt(0)\n\tbuffer_load_dword %3, %4, %6, 0 offen sc1\n\ts_waitcnt vmcnt(0)\n\ts_memtime %1\n\ts_waitcnt lgkmcnt(0)\n\t"
                 "buffer_load_dword %3, %5, %6, 0 offen sc1\n\ts_waitcnt vmcnt(0)\n\ts_memtime %2\n\ts_waitcnt lgkmcnt(0)"
                 : "=&s"(c0), "=&s"(c1), "=&s"(c2), "=&v"(v) : "v"(o), "v"(o ^ 8192), "s"(rs) : "memory");
    const int d = min(max(2 * (int)c1 - (int)c0 - (int)c2, -600), 600);
    if (lane == 0) st_sc1_64(rs, WS_NV + k * 8, u32x2{(u32)(2 * ((bb & 1) ? -d : d)) | ((__builtin_amdgcn_s_getreg(20 | (3 << 11)) >> 2) & 1u), tag});
  }
  int rot = idx;
#define RO(I) ((((I) + rot) & 15) * 8192)
  const u16* kp = kc + ((size_t)bh * SK + (SK - WIN) + 4 * w) * 128;
  u32 l16 = lane * 16;
  PIN(l16);
#define LDR(D, P, I, W) { const GAS unsigned char* p_ = (const GAS unsigned char*)((P) + RO(I)) + l16; PIN(p_); W; D = __builtin_nontemporal_load((const GAS u32x4*)p_); SB(); }
  u32x4 kr[16], vr[16];
  volatile LAS u32* ctl = (volatile LAS u32*)(L + L_CTL);
#define RELANE() do { asm volatile("" : "+v"(lane)); n = lane & 15; g = lane >> 4; } while (0)
  const int e = b * 16 + 2 * xcd + hj;
  const bool red = hj < 2;

#define KL(I) { const int t_ = ta + extra(I); LDR(kr[I], kp, I, PACE(t_)) }
  KL(0)
  if (row) {
    if (w >= 8) {
#pragma unroll
      for (int j = 0; j < 4; ++j) { const float a0 = blo(cv[j]), a1 = bhi(cv[j]); hb[j] = pack2(a0 * rcp(1.f + __expf(-a0)), a1 * rcp(1.f + __expf(-a1))); }
      *(LAS u32x4*)(L + L_H2 + (w & 7) * H2S + lane * 16) = hb;
      lds_add((LAS u32*)(L + L_CTL) + C_R, lane);
    } else {
      float m, inv;
      ln_stats(cv, m, inv);
      if ((lane >> 3) == mg) {
        LAS f32x4* d = (LAS f32x4*)(L + L_RED + w * 256 + (lane & 7) * 32);
        d[0] = f32x4{rbf(rbf(blo(cv[0]) - m) * inv), rbf(rbf(bhi(cv[0]) - m) * inv), rbf(rbf(blo(cv[1]) - m) * inv), rbf(rbf(bhi(cv[1]) - m) * inv)};
        d[1] = f32x4{rbf(rbf(blo(cv[2]) - m) * inv), rbf(rbf(bhi(cv[2]) - m) * inv), rbf(rbf(blo(cv[3]) - m) * inv), rbf(rbf(bhi(cv[3]) - m) * inv)};
      }
    }
  }
  KL(1)
  u32x2 tsv;
#define TS_WAIT() if (!__all(tsv[1] == tag)) { _Pragma("unroll 1") for (u32 s = 0; s < SPIN_CAP; ++s) { __builtin_amdgcn_s_sleep(16); MEMB(); \
      tsv = ld_sc1_64(rs, WS_TS + (lane & 15) * 8); if (__all(tsv[1] == tag)) break; } } MEMB();
  if (tm) {
    asm volatile("s_waitcnt vmcnt(2)" ::: "memory");
    lds_wait(ctl + C_R, 8u);
    hb = *(LAS u32x4*)(L + L_H2 + (n & 7) * H2S + (32 * w + 8 * g) * 2);
    tile_part(L + L_W + w * 4096, L + L_TP + w * 2048 + lane * 16, hb, n, g);
    lds_add((LAS u32*)(L + L_CTL) + C_T, lane);
  }
  KL(2)
  if (w == 0 || (tm && w >= 14)) tsv = ld_sc1_64(rs, WS_TS + (lane & 15) * 8);
  SB();
  if (tm && w >= 14) {
    lds_wait(ctl + C_T, 16u);
    const int col = 32 * (w & 1) + 16 * (n >> 3) + 4 * g;
    const f32x4 s = tile_sum(L + L_TP + (w & 1) * 1024 + lane * 16);
    float v[4] = {rbf(s[0]), rbf(s[1]), rbf(s[2]), rbf(s[3])};
    if (mc == 1) {
      const f32x4 ln = *(LAS f32x4*)(L + L_RED + (n & 7) * 256 + col * 4);
#pragma unroll
      for (int j = 0; j < 4; ++j) v[j] = ln[j] * rbf(1.f + v[j]);
    }
    TS_WAIT()
    st_sc1_64(rs, mc < 2 ? WS_G1 + (2 * mg + (w & 1)) * 1024 + ((2 * (n >> 3) + (g >> 1)) * 16 + mc * 8 + (n & 7)) * 16 + (g & 1) * 8
                         : WS_MODR + ((mc - 2) * 8 + (n & 7)) * 1024 + mpos(64 * mg + col),
              u32x2{canon(pack2(v[0], v[1])), canon(pack2(v[2], v[3]))});
  }
  RELANE();
  KL(3) KL(4)
  if (w == 0) {
    TS_WAIT()
    const float d = __builtin_amdgcn_fmed3f((float)(int)(tsv[0] - t0), -1e5f, 1e5f);
    ctl[C_TB] = (t0 * 4u + (u32)(int)fmaxf(row_sum(d) * 0.25f, row_max(d) * 4.f - SKW)) | 1u;
  }
  KL(5) KL(6)
  u32 org = __builtin_amdgcn_readfirstlane(ctl[C_TB]);
  if (org) { const int adj = ((int)(org - t0 * 4u - (p3 >= 0 ? bb * MSH : 0)) * 85 + 1024) >> 11; ta += adj; }
  KL(7)
  if (tm && mc >= 2 && w >= 14 && lane == 0) __builtin_amdgcn_raw_buffer_store_b32(tag, rs, WS_FLAGS + (((mc - 2) * 8 + mg) * 2 + (w & 1)) * 4, 0, SC1);
  KL(8)
  u32x4 g0;
  const int o0 = WS_G1 + w * 1024 + lane * 16;
  if (tq) lds_wait(ctl + C_TB, 1u);
#define KG(K) { KL(K) if (tq && (w & 1) == ((K) & 1)) { until_s(due - TAU / 2); \
    asm volatile("buffer_load_dwordx4 %0, %1, %2, 0 offen sc1" : "=&v"(g0) : "v"(o0), "s"(rs) : "memory"); } }
  KG(9) KG(10)
  KL(11) KL(12)
  if (tq) {
    asm volatile("s_waitcnt vmcnt(2)" : "+v"(g0));
#define G1_OK() __all(g0[0] != ~0u && g0[1] != ~0u && g0[2] != ~0u && g0[3] != ~0u)
    if (!G1_OK()) {
#pragma unroll 1
      for (u32 s = 0; s < SPIN_CAP; ++s) {
        __builtin_amdgcn_s_sleep(16);
        MEMB();
        g0 = ld_sc1(rs, o0);
        if (G1_OK()) break;
      }
    }
#pragma unroll
    for (int j = 0; j < 4; ++j) { const u32 o = dppu<ROR8>(g0[j]); hb[j] = pack2(blo(g0[j]) + blo(o), bhi(g0[j]) + bhi(o)); }
    PIN(hb);
  }
  KL(13)
  if (tq) {
    tile_part(L + L_W + w * 4096, L + L_TP + w * 2048 + lane * 16, hb, n, g);
    lds_add((LAS u32*)(L + L_CTL) + C_T, lane);
  }
  KL(14)
  if (tq && w >= 14) {
    lds_wait(ctl + C_T, 16u);
    const f32x4 s = tile_sum(L + L_TP + (w & 1) * 1024 + lane * 16);
    st_sc1(rs, WS_Q + ((n & 7) * 32 + 4 * xcd + (b >> 1)) * 512 + ((b & 1) * 64 + 32 * (w & 1) + 16 * (n >> 3) + 4 * g) * 4, u32x4{pack2(s[0], s[1]), tag, pack2(s[2], s[3]), tag});
  }
  RELANE();
  KL(15)
  asm volatile("" : "+s"(rot));
  const u16* vp = vc + ((size_t)bh * SK + (SK - WIN) + 4 * w) * 128;
#define VL(I) { const int t_ = ta + extra(16 + (I)); LDR(vr[I], vp, I, PACE(t_)) }
#define VLN(I) { const int t_ = ta + extra(16 + (I)); LDR(vr[I], vp, I, PACEN(t_)) }
  VL(0) VL(1)
  VL(2)
  u32x2 gq;
#define FL_WAIT() if (!__all(fl == tag)) {   _Pragma("unroll 1") for (u32 s = 0; s < SPIN_CAP; ++s) { __builtin_amdgcn_s_sleep(4); MEMB(); \
      fl = __builtin_amdgcn_raw_buffer_load_b32(rs, WS_FLAGS + lane * 4, 0, SC1); if (__all(fl == tag)) break; } } MEMB();
  u32 fl;
  u32x2 nv;
  if (w == WG_) {
    nv = ld_sc1_64(rs, WS_NV + (lane & 15) * 8);
    gq = ld_sc1_64(rs, WS_Q + bh * 512 + lane * 8);
    fl = __builtin_amdgcn_raw_buffer_load_b32(rs, !to ? WS_FLAGS + lane * 4 : WS_END, 0, SC1);
  }
  SB();
  VLN(3) VLN(4)
  if (w == WG_) {
    if (!__all(gq[1] == tag)) {
#pragma unroll 1
      for (u32 s = 0; s < SPIN_CAP; ++s) {
        __builtin_amdgcn_s_sleep(4);
        MEMB();
        gq = ld_sc1_64(rs, WS_Q + bh * 512 + lane * 8);
        if (__all(gq[1] == tag)) break;
      }
    }
    *(LAS u32*)(L + L_Q + lane * 4) = gq[0];
    PIN(fl);
    if (!to) { FL_WAIT() }
    PIN(nv);
    if (!__all(nv[1] == tag)) {
#pragma unroll 1
      for (u32 s = 0; s < SPIN_CAP; ++s) {
        __builtin_amdgcn_s_sleep(4);
        MEMB();
        nv = ld_sc1_64(rs, WS_NV + (lane & 15) * 8);
        if (__all(nv[1] == tag)) break;
      }
    }
    const float dv = (float)((int)nv[0] >> 1);
    const u32 m4 = (u32)__ballot((row_sum((nv[0] & 1u) ? -dv : dv) > 0.f) != ((nv[0] & 1u) != 0u)) & 15u;
    LGKM0();
    ctl[C_Q] = 256u | m4 | ((m4 ^ 15u) << 4);
  }
  RELANE();
  VL(5)
  lds_wait(ctl + C_Q, 1u);
  const bool prer = p3 >= 0 && w < 8;
  u32x4 pre0, pre1;
  if (prer) {
    pre0 = ld_sc1(rs, WS_MODR + (8 + w) * 1024 + lane * 16);
    pre1 = ld_sc1(rs, WS_MODR + (16 + w) * 1024 + lane * 16);
  }
  SB();
  const u32x4 qf = *(LAS u32x4*)(L + L_Q + n * 16);

#define DOT(I) dot2(kr[I][3], qf[3], dot2(kr[I][2], qf[2], dot2(kr[I][1], qf[1], dot2(kr[I][0], qf[0], 0.f))))
  float xs[8];
#define QK(C) { _Pragma("unroll") for (int i = 2 * (C); i < 2 * (C) + 2; ++i) { const float lo_ = DOT(i), hi_ = DOT(i + 8); \
      const float t1_ = lo_ + dppf<ROR8>(lo_), t2_ = hi_ + dppf<ROR8>(hi_); xs[i] = (n & 8) ? t2_ : t1_; PIN(xs[i]); } SB(); }
  QK(0) VLN(6) QK(1) VLN(7) QK(2) VL(8) QK(3) VL(9)
  if (prer) {
    const u32 a_h2w = w * C1S + lane * 8;
    *(LAS u32x2*)(L + L_C1 + a_h2w) = u32x2{pk2(up2(pre1[0]) + 1.f), pk2(up2(pre1[1]) + 1.f)};
    *(LAS u32x2*)(L + L_C1 + a_h2w + 512) = u32x2{pk2(up2(pre1[2]) + 1.f), pk2(up2(pre1[3]) + 1.f)};
    *(LAS u32x2*)(L + L_C1 + 8 * C1S + a_h2w) = u32x2{pre0[0], pre0[1]};
    *(LAS u32x2*)(L + L_C1 + 8 * C1S + a_h2w + 512) = u32x2{pre0[2], pre0[3]};
  }
#define TR_STEP(HALF, CTRL, BIT) _Pragma("unroll") for (int hh = 0; hh < (HALF); ++hh) { \
    const float lo_ = xs[hh], hi_ = xs[hh + (HALF)]; const float t1_ = lo_ + dppf<CTRL>(lo_), t2_ = hi_ + dppf<CTRL>(hi_); xs[hh] = (n & (BIT)) ? t2_ : t1_; }
  TR_STEP(4, XOR7, 4) TR_STEP(2, XOR2, 2) TR_STEP(1, XOR1, 1)
#undef TR_STEP
  float sc = rbf(rbf(xs[0]) * 0.08838834764831845f);
  PIN(sc);
  SB();
  VL(10)
  {
    const float mw_ = wave_max(sc);
    const float lw = wave_sum(__expf(sc - mw_));
    if (lane == 0) *(LAS f32x2*)(L + L_ML + w * 8) = f32x2{mw_, lw};
    lds_add((LAS u32*)(L + L_CTL) + C_ML, lane);
  }
  SB();
  VL(11)
  NEWPAGE();
  RELANE();
  u32 mk[4];
#pragma unroll
  for (int j = 0; j < 4; ++j) mk[j] = (n >> 1) == j ? ((n & 1) ? 0xffff0000u : 0x0000ffffu) : 0u;
  f32x4 oa = {0.f, 0.f, 0.f, 0.f};
#define PV(I) { const u32 pb_ = dppu<0x150 + (I)>(pd); oa = mfma(vr[I], u32x4{pb_ & mk[0], pb_ & mk[1], pb_ & mk[2], pb_ & mk[3]}, oa); asm volatile("" : "+v"(oa)); SB(); }
  PIN(mk[0]); PIN(mk[1]); PIN(mk[2]); PIN(mk[3]);
  u32 a_oa = L_RED + w * OSTR + (g * 8 + n) * 16;
  u32 ra = L_RED + (lane >> 5) * 8 * OSTR + (lane & 31) * 16;
  const u32 near = __builtin_amdgcn_readfirstlane(ctl[C_Q]);
  u32 rb_ = tline(xcd, LN_O + (hj * 8 + b) * 4, near) + (lane & 31) * 16 + (lane >> 5) * 8;
  if (w == 14) rb_ = lane == 0 ? WS_B0 + bid * 8 : WS_END;
  int oo = tline(xcd, LN_O + 8 * w, near) + lane * 16;
  PIN(oo);
  u32 tagv = tag;
  PIN(a_oa); PIN(ra); PIN(rb_); PIN(tagv);
  org = __builtin_amdgcn_readfirstlane(ctl[C_TB]);
  org = org ? org + TEND : t0 * 4u;
  asm volatile("" : "+s"(org));
  VL(12)
  lds_wait(ctl + C_ML, 16u);
  u32 pd;
  {
    const f32x2 ml = *(LAS f32x2*)(L + L_ML + n * 8);
    const float M = row_max(ml[0]);
    const float pe = __expf(sc - M) * rcp(row_sum(__expf(ml[0] - M) * ml[1]));
    pd = pack2(pe, pe);
    PIN(pd);
  }
  SB();
  VL(13)
  PV(0) PV(1) PV(2) PV(3) PV(4)
  VL(14)
  PV(5) PV(6) PV(7) PV(8) PV(9)
  VL(15)
  PV(10) PV(11) PV(12) PV(13) PV(14)
  if (w == 13) __builtin_amdgcn_s_setprio(3);
  PV(15)
#undef PV
  if (n < 8) *(LAS f32x4*)(L + a_oa) = oa;
  BAR();
  if (w == 14) st_sc1_64(rs, rb_, u32x2{now4(), tagv});
  if (w == 13) {
    f32x4 s[8];
#pragma unroll
    for (int ww = 0; ww < 8; ++ww) s[ww] = *(LAS f32x4*)(L + ra + ww * OSTR);
    SB();
    const f32x4 t = ((s[0] + s[1]) + (s[2] + s[3])) + ((s[4] + s[5]) + (s[6] + s[7]));
    st_sc1_64(rs, rb_, u32x2{pack2(t32(t[0], t[2]), t32(t[1], t[3])), tagv});
  }
  if (to) {
    until(org + OFO);
    u32x4 og = ld_sc1(rs, oo);
    SB();
    u32x4 wf[16];
    if (w < 4) {
      __builtin_amdgcn_s_setprio(3);
      const u32 af = L_W + (8 * g + (n >> 2)) * 128 + (w ^ ((n >> 3) | ((g & 1) << 1))) * 32 + (n & 3) * 8;
#pragma unroll
      for (int ks = 0; ks < 16; ++ks) wf[ks] = trfrag(L + af + ks * 4096, L + af + ks * 4096 + 512);
    }
    u32 a_ow = L_TP + (2 * w + (lane >> 5)) * OBS + (lane & 31) * 8;
    u32 a_or = L_TP + ((n >> 3) * 16 + (n & 7)) * OBS + g * 16;
    int rc = n < 8 ? tline(b, LN_PART + (2 * n + ((w >> 1) & 1)) * 8 + xcd, near) + (w & 1) * 64 + g * 16 : WS_END;
    PIN(a_ow); PIN(a_or); PIN(rc);
    SEL4();
    SB();
    *(LAS u32x2*)(L + a_ow) = u32x2{og[0], og[2]};
    if (__builtin_expect(!__all(((og[1] ^ tag) | (og[3] ^ tag)) == 0u), 0)) {
#pragma unroll 1
      for (u32 s = 0; s < SPIN_CAP; ++s) {
        MEMB();
        og = ld_sc1(rs, oo);
        if (__all(((og[1] ^ tag) | (og[3] ^ tag)) == 0u)) break;
        __builtin_amdgcn_s_sleep(2);
      }
      *(LAS u32x2*)(L + a_ow) = u32x2{og[0], og[2]};
    }
    BAR();
    if (w < 4) {
      u32x4 ob[8];
#pragma unroll
      for (int i = 0; i < 8; ++i) ob[i] = *(LAS u32x4*)(L + a_or + (i >> 2) * 8 * OBS + (i & 3) * 64);
      SB();
      f32x4 aL = {0.f, 0.f, 0.f, 0.f}, aH = {0.f, 0.f, 0.f, 0.f};
#pragma unroll
      for (int i = 0; i < 8; ++i) { aL = mfma(wf[i], ob[i], aL); aH = mfma(wf[8 + i], ob[i], aH); }
      const f32x4 v = {aL[0] + dppf<ROR8>(aH[0]), aL[1] + dppf<ROR8>(aH[1]), aL[2] + dppf<ROR8>(aH[2]), aL[3] + dppf<ROR8>(aH[3])};
      st_sc1(rs, rc, tag8(v, tagv, sel));
    }
  }

  const int bo = WS_B0 + lane * 16;
  if (p3 >= 0) {
    u32 a_h2w = w * C1S + lane * 8;
    if (w == 8 && lane < 36) *(LAS u32*)(L + (lane < 32 ? L_ML : L_HH + 640) + lane * 4) = 0u;
    BAR();
    glds((const void*)(tp0 + (u32)((368 - 16 * w) * FF * 2)), lbase + L_W + 49152 + w * 1024);
    u32x4 bp, bq;
    if (w == 12) {
      until_s(org + OFB + 20);
      asm volatile("buffer_load_dwordx4 %0, %2, %3, 0 offen sc1\n\tbuffer_load_dwordx4 %1, %2, %3, 0 offen offset:1024 sc1" : "=&v"(bp), "=&v"(bq) : "v"(bo), "s"(rs) : "memory");
    }
#define DL(R) glds(wd + (size_t)(32 * p3 + (R)) * 512, (lane ^ ((((R) & 3) | (((R) >> 1) & 4)) << 1)) * 16, lbase + L_W + 65536 + (R) * 1024)
    if (w < 8) {
      until_s(org + OFB + WDT);
#pragma unroll
      for (int i = 0; i < 4; ++i) DL(4 * w + i);
    }
    SB();
    u32 a_h2r = L_H2 + (n & 7) * H2S + (n >> 3) * 512 + g * 16;
    u32 a_hw = L_HH + (n < 8 ? n * 64 + (8 * (w & 3) + 4 * (g & 1) + 2 * (g >> 1)) * 2 : 512 + lane * 4);
    const u32x4 t16v = {tag & 0xffffu, tag >> 16, tag & 0xffffu, tag >> 16};
#define X_TIME() until_s(org + OFO + DX - 40); u32 tx = 2u; \
      _Pragma("unroll 1") for (u32 s = 0; s < LSPIN_CAP && tx == 2u; ++s) { tx = __builtin_amdgcn_readfirstlane(ctl[C_TX]); if (tx == 2u) __builtin_amdgcn_s_sleep(2); } \
      if (!(tx & 1u)) tx = org + OFO + DX;
#define X_BAD(A, B) ((or4(A) | or4(B)) & 0xffffu)
    if (w < 8) {
      const u32x2 ca = *(LAS u32x2*)(L + L_C1 + a_h2w), cb = *(LAS u32x2*)(L + L_C1 + a_h2w + 512);
      f32x2 c1[4] = {up2(ca[0]), up2(ca[1]), up2(cb[0]), up2(cb[1])};
      a_h2w = L_H2 + w * H2S + lane * 8;
      int xo = tline(xcd, LN_X2 + 16 * w, near) + lane * 16;
      u32 a_hr = L_HH + (n < 8 ? n * 64 + g * 16 : 768), a_hs = L_HH + (n < 8 ? 768 : (n & 7) * 64 + g * 16);
      PIN(a_hs);
      SEL4();
      const u32 a_d0 = L_W + 65536 + (8 * g + (n >> 2)) * 1024 + ((8 * w + ((n >> 1) & 1)) ^ (((n >> 2) | ((g & 1) << 2)) << 1)) * 16 + (n & 1) * 8;
      int yo = tline(w, LN_YP + (4 * (n >> 3) + g) * NP3 + p3, near) + (n & 7) * 16, yo2 = tline(w, LN_YP + (8 + 4 * (n >> 3) + g) * NP3 + p3, near) + (n & 7) * 16;
      PIN(xo); PIN(c1[0]); PIN(c1[1]); PIN(c1[2]); PIN(c1[3]); PIN(a_h2w); PIN(a_hr); PIN(yo); PIN(yo2);
      u32 a_cx = lbase + L_CTL + (C_X + w) * 4;
      PIN(a_cx);
      asm volatile("s_waitcnt vmcnt(4)" ::: "memory");
      BAR();
      __builtin_amdgcn_s_setprio(2);
      X_TIME()
      until(tx);
#pragma unroll 1
      for (u32 s = 0; s < SPIN_CAP; ++s) {
        const u32x4 xa = ld_sc1(rs, xo) ^ t16v, xb = ld_sc1(rs, xo + 1024) ^ t16v;
        const float x0 = __builtin_bit_cast(float, __builtin_amdgcn_readfirstlane(xa[0]));
        *(LAS u32x2*)(L + a_h2w) = u32x2{pk2((f32x2{asf(xa[0]), asf(xa[1])} - x0) * c1[0]), pk2((f32x2{asf(xa[2]), asf(xa[3])} - x0) * c1[1])};
        *(LAS u32x2*)(L + a_h2w + 512) = u32x2{pk2((f32x2{asf(xb[0]), asf(xb[1])} - x0) * c1[2]), pk2((f32x2{asf(xb[2]), asf(xb[3])} - x0) * c1[3])};
        if (__builtin_expect(__all(X_BAD(xa, xb) == 0u), 1)) break;
        MEMB();
      }
      asm volatile("s_mov_b64 exec, 1\n\tds_write_b32 %0, %0\n\ts_mov_b64 exec, -1" :: "v"(a_cx) : "memory");
      __builtin_amdgcn_s_setprio(0);
      lds_add((LAS u32*)(L + L_CTL) + C_R, lane);
      lds_wait(ctl + C_R, 8u);
      u32x4 df[4];
#pragma unroll
      for (int i = 0; i < 4; ++i) { df[i] = trfrag(L + (a_d0 ^ (32 * i)), L + (a_d0 ^ (32 * i)) + 4096); PIN(df[i]); }
      BAR();
      const u32x4 h0 = *(LAS u32x4*)(L + a_hr), h1 = *(LAS u32x4*)(L + a_hs);
      const f32x4 z = {0.f, 0.f, 0.f, 0.f};
      const f32x4 a0 = mfma(df[1], h1, mfma(df[0], h0, z)), a1 = mfma(df[3], h1, mfma(df[2], h0, z));
      st_sc1(rs, yo, tag8(a0, tagv, sel));
      st_sc1(rs, yo2, tag8(a1, tagv, sel));
    } else if (w < 12) {
      NEWPAGE();
      u32 a_st = L_ML + (n & 7) * 16;
      PIN(a_st);
      __builtin_amdgcn_s_setprio(3);
      u32x4 gf[16], gb_[8];
      const u32 af = L_W + (8 * g + (n >> 2)) * 128 + (((w & 3) + (n & 2) * 2) ^ (((n >> 3) | ((g & 1) << 1)) << 1)) * 16 + (n & 1) * 8;
      f32x4 aL = {0.f, 0.f, 0.f, 0.f}, aH;
      f32x2 ag, au, sg, su, gt, up;
      {
#pragma unroll
        for (int q4 = 0; q4 < 4; ++q4) {
#pragma unroll
          for (int i = 0; i < 4; ++i) gb_[i] = *(LAS u32x4*)(L + L_C1 + n * C1S + g * 16 + q4 * 256 + i * 64);
          if (q4 == 3) {
            VM0();
            BAR();
          }
#pragma unroll
          for (int i = 0; i < 4; ++i) gf[4 * q4 + i] = trfrag(L + af + (4 * q4 + i) * 4096, L + af + (4 * q4 + i) * 4096 + 512);
          SB();
#pragma unroll
          for (int i = 0; i < 4; ++i) aL = mfma(gf[4 * q4 + i], gb_[i], aL);
          PIN(aL);
        }
        const auto s0 = __builtin_amdgcn_permlane32_swap(__float_as_uint(aL[0]), __float_as_uint(aL[2]), false, false);
        const auto s1 = __builtin_amdgcn_permlane32_swap(__float_as_uint(aL[1]), __float_as_uint(aL[3]), false, false);
        ag = -f32x2{__uint_as_float(s0[0]), __uint_as_float(s1[0])};
        au = -f32x2{__uint_as_float(s0[1]), __uint_as_float(s1[1])};
        sg = f32x2{dppf<ROR8>(ag[0]), dppf<ROR8>(ag[1])} * 1.4426950408889634f;
        su = -f32x2{dppf<ROR8>(au[0]), dppf<ROR8>(au[1])};
        PIN(ag); PIN(au); PIN(sg); PIN(su); PIN(a_h2r); PIN(a_hw);
      }
      __builtin_amdgcn_s_setprio(0);
      _Pragma("unroll 1") for (u32 s = 0; s < LSPIN_CAP; ++s) if (__all(ctl[C_X + (lane & 7)] != 0u)) break;
      __builtin_amdgcn_s_setprio(3);
      MEMB();
      GU_MUL(L + a_h2r)
      SB();
      f32x4 st = *(volatile LAS f32x4*)(L + a_st);
      gu_pair(aL, aH, gt, up);
      if (!__all(st[3] != 0.f)) {
        _Pragma("unroll 1") for (u32 s = 0; s < LSPIN_CAP; ++s) { st = *(volatile LAS f32x4*)(L + a_st); if (__all(st[3] != 0.f)) break; }
      }
      gt = __builtin_elementwise_fma(__builtin_elementwise_fma(ag, f32x2{st[0], st[0]}, gt), f32x2{st[2], st[2]}, sg);
      up = __builtin_elementwise_fma(__builtin_elementwise_fma(au, f32x2{st[0], st[0]}, up), f32x2{st[1], st[1]}, su);
      *(LAS u32*)(L + a_hw) = pk2(f32x2{rcp(1.f + __builtin_amdgcn_exp2f(gt[0])), rcp(1.f + __builtin_amdgcn_exp2f(gt[1]))} * up);
      BAR();
    } else {
      int xo = tline(xcd, LN_X2 + 32 * (w - 12), near) + lane * 16;
      u32 a_st = lbase + L_ML + (2 * (w - 12) + (lane >> 5)) * 16;
      PIN(xo); PIN(a_st);
      if (w == 12) asm volatile("s_waitcnt vmcnt(2)" ::: "memory"); else VM0();
      BAR();
      asm volatile("s_waitcnt vmcnt(0)" : "+v"(bp), "+v"(bq) :: "memory");
      if (w == 12 && __builtin_expect(!on_time(tag, org, bp, bq), 0)) {
        ctl[C_TX] = 2u;
        ctl[C_TX] = (anchor(rs, bo, tag, org, bp, bq) + DX) | 1u;
      }
      __builtin_amdgcn_s_setprio(1);
      X_TIME()
      until(tx + SDL);
      u32x4 xv[4];
#pragma unroll
      for (int i = 0; i < 4; ++i) xv[i] = ld_sc1(rs, xo + i * 1024);
#pragma unroll
      for (int i = 0; i < 4; ++i) xv[i] ^= t16v;
      if (__builtin_expect(!__all((X_BAD(xv[0], xv[1]) | X_BAD(xv[2], xv[3])) == 0u), 0)) {
#pragma unroll 1
        for (u32 s = 0; s < SPIN_CAP; ++s) {
          MEMB();
#pragma unroll
          for (int i = 0; i < 4; ++i) xv[i] = ld_sc1(rs, xo + i * 1024) ^ t16v;
          if (__all((X_BAD(xv[0], xv[1]) | X_BAD(xv[2], xv[3])) == 0u)) break;
        }
      }
      float sm[2], sq[2], xs0[2];
#pragma unroll
      for (int k = 0; k < 2; ++k) {
        const float x0 = __builtin_bit_cast(float, __builtin_amdgcn_readfirstlane(xv[2 * k][0]));
        const u32x4 xa = xv[2 * k], xb = xv[2 * k + 1];
        const f32x2 d0 = f32x2{asf(xa[0]), asf(xa[1])} - x0, d1 = f32x2{asf(xa[2]), asf(xa[3])} - x0, d2 = f32x2{asf(xb[0]), asf(xb[1])} - x0, d3 = f32x2{asf(xb[2]), asf(xb[3])} - x0;
        const f32x2 sv = (d0 + d1) + (d2 + d3), qv = __builtin_elementwise_fma(d0, d0, d1 * d1) + __builtin_elementwise_fma(d2, d2, d3 * d3);
        xs0[k] = x0;
        sm[k] = sv[0] + sv[1];
        sq[k] = qv[0] + qv[1];
      }
      const float r = row_sum(t32(t16(sm[0], sq[0]), t16(sm[1], sq[1]))) * (1.f / 512.f);
      const auto sw = __builtin_amdgcn_permlane16_swap(__float_as_uint(r), __float_as_uint(r), false, false);
      const float md = __uint_as_float(sw[0]);
      const float x0 = lane < 32 ? xs0[0] : xs0[1];
      const float inv = rcp(rbf(__builtin_amdgcn_sqrtf(rbf(rbf(fmaxf(fmaf(-md, md, __uint_as_float(sw[1])), 0.f)) + 1e-5f))));
      const f32x4 st = {rbf(x0 + md) - x0, inv, inv * -1.4426950408889634f, 1.f};
      asm volatile("s_mov_b32 exec_lo, 1\n\ts_mov_b32 exec_hi, 1\n\tds_write_b128 %0, %1\n\ts_mov_b64 exec, -1" :: "v"(a_st), "v"(st) : "memory");
      if (w == 15) {
        _Pragma("unroll 1") for (u32 s = 0; s < LSPIN_CAP; ++s) if (__all(ctl[C_X + (lane & 7)] != 0u)) break;
        if (lane < 8) st_sc1_64(rs, WS_B1 + lane * 512 + p3 * 8, u32x2{now4(), tagv});
      }
      BAR();
    }
    asm volatile("fused_code_end:");
  } else if (red && (w == RW || w == RW - 1)) {
    NEWPAGE();
    const int f = 16 * xcd + 2 * b + hj, ro_ = 4 * (w & 1) + (lane & 3);
    const int rb = e >> 4, rj = e & 15, c2 = 32 * rj + 4 * (lane & 7) + g;
    u32 xr_ = x[rb * 512 + c2];
    u32 gar = (__builtin_amdgcn_raw_buffer_load_b16(rs, WS_MODR + rb * 1024 + mpos(c2), 0, SC1));
    u32 gbr = (__builtin_amdgcn_raw_buffer_load_b16(rs, WS_MODR + (24 + ro_) * 1024 + mpos(4 * f + g), 0, SC1));
    int po = tline(xcd, LN_PART + (2 * b + hj) * 8, near) + lane * 16;
    int xo = tline(lane >> 3, LN_X2 + e, near) + (lane & 7) * 16;
    int xi = tline(xcd, LN_X2 + 16 * ro_, near) + (4 * f + g) * 4;
    u32 xt = (g & 1) ? tag >> 16 : tag & 0xffffu;
    const u32x4 tb4 = bytes4(tag);
    u32x4 tb5 = lane < 44 ? tb4 : u32x4{0, 0, 0, 0};
    const int yl = LN_YP + (f & 15) * NP3 + (lane >> 2);
    int yo = tline(xcd, yl, near) + ro_ * 16, y1 = tline(xcd, yl + 16, near) + ro_ * 16;
    int y2 = lane < 44 ? tline(xcd, yl + 32, near) + ro_ * 16 : WS_END;
    PIN(y1);
    u32 oof = (lane & 12) == 0 ? (ro_ * 512 + 4 * f + g) * 2 : 8192;
    __amdgpu_buffer_rsrc_t ro = __builtin_amdgcn_make_buffer_rsrc((void*)out, 0, 8192, 0x00020000);
    asm volatile("" : "+s"(ro));
    PIN(xt); PIN(tb5); PIN(po); PIN(xo); PIN(xi); PIN(yo); PIN(y2); PIN(oof);
    if (w == RW) {
      u32x4 bp, bq;
      until_s(org + OFB);
      B0_LOAD()
      SB();
      PIN(gar); PIN(xr_);
      float ga = blo(gar), xx = blo(xr_);
      PIN(ga); PIN(xx);
      u32 anc = org + OFO;
      if (__builtin_expect(!on_time(tag, org, bp, bq), 0)) anc = anchor(rs, bo, tag, org, bp, bq);
      until(anc + DP);
      u32x4 xq;
#pragma unroll 1
      for (u32 sp = 0; sp < SPIN_CAP; ++sp) {
        const u32x4 pv = ld_one(rs, po) ^ tb4;
        float t = t32(t16(asf(pv[0]), asf(pv[1])), t16(asf(pv[2]), asf(pv[3])));
        t += dppf<ROR8>(t);
        const u32 xw = __float_as_uint(rbf(xx + rbf(ga * rbf(t)))) | xt;
        const auto p1 = __builtin_amdgcn_permlane16_swap(xw, xw, false, false);
        const auto p2 = __builtin_amdgcn_permlane32_swap(p1[0], p1[0], false, false), p3_ = __builtin_amdgcn_permlane32_swap(p1[1], p1[1], false, false);
        xq = u32x4{p2[0], p3_[0], p2[1], p3_[1]};
        PIN(xq);
        if (__builtin_expect(__all((or4(pv) & 255u) == 0u), 1)) break;
        MEMB();
      }
      st_sc1(rs, xo, xq);
      if (bid == 1 && lane == 0) __hip_atomic_store((GAS u32*)ws, tag, RLX_AGENT);
      u32 ty = anc + DYS;
      const int lt = (int)(now4() - anc) - (DX + 20);
      if (__builtin_expect(lt > 0, 0)) {
        int lk = DB1;
        while (lk < DB1 + lt + 240) lk += PB;
        u32x2 bv;
#pragma unroll 1
        for (u32 sp = 0; sp < SPIN_CAP; ++sp) {
          until_s(anc + lk);
          bv = ld_sc1_64(rs, WS_B1 + xcd * 512 + (lane < NP3 ? lane : 0) * 8);
          if (__all(bv[1] == tag)) break;
          MEMB();
          do lk += PB; while ((int)(now4() - anc) > lk);
        }
        ty = org + smax((int)(bv[0] - org)) + DY;
        asm volatile("fused_cold_end:");
      }
      ctl[C_TX] = ty | 1u;
    }
    PIN(gbr);
    float gb = blo(gbr);
    PIN(gb);
    lds_wait(ctl + C_TX, 1u);
    until(__builtin_amdgcn_readfirstlane(ctl[C_TX]));
    u32 res;
#pragma unroll 1
    for (u32 sp = 0; sp < SPIN_CAP; ++sp) {
      const u32 x2 = __builtin_amdgcn_raw_buffer_load_b32(rs, xi, 0, SC1) ^ xt;
      const u32x4 y0 = ld_one(rs, yo) ^ tb4, y1_ = ld_one(rs, y1) ^ tb4, y2_ = ld_one(rs, y2) ^ tb5;
      const f32x4 s = (__builtin_bit_cast(f32x4, y0) + __builtin_bit_cast(f32x4, y1_)) + __builtin_bit_cast(f32x4, y2_);
      float t = t32(t16(s[0], s[1]), t16(s[2], s[3]));
      t += dppf<ROR8>(t);
      t += dppf<ROR4>(t);
      res = pack2(fmaf(gb, t, asf(x2)), 0.f);
      PIN(res);
      if (__builtin_expect(__all((((or4(y0) | or4(y1_) | or4(y2_)) & 255u) | (x2 & 0xffffu)) == 0u), 1)) break;
      MEMB();
    }
    __builtin_amdgcn_raw_buffer_store_b16((u16)res, ro, oof, 0, 0);
  }
}

extern "C" void kernel_launch(void* const* d_in, const int* in_sizes, int n_in, void* d_out, int out_size, void* d_ws, size_t ws_size, hipStream_t stream) {
  (void)hipFuncSetAttribute((const void*)fused, hipFuncAttributeMaxDynamicSharedMemorySize, L_END);
  Args a;
  const int src[4][2] = {{3, 3}, {10, 10}, {4, 4}, {6, 7}};
  for (int r = 0; r < 4; ++r)
    for (int k = 0; k < 2; ++k) a.half[r][k] = (const unsigned char*)d_in[src[r][k]] + (r < 3 && k ? 64 : 0);
  a.x = (const u16*)d_in[0]; a.kc = (const u16*)d_in[1]; a.vc = (const u16*)d_in[2]; a.wd = (const u16*)d_in[8]; a.cc = (const u16*)d_in[9];
  a.out = (u16*)d_out; a.ws = (unsigned char*)d_ws;
  fused<<<256, 1024, L_END, stream>>>(a);
}
```

```cpp
#define LEAK -1
#include <hip/hip_runtime.h>
#include <stdint.h>

typedef unsigned int u32;
typedef unsigned long long u64;
typedef unsigned short u16;
typedef u32 u32x4 __attribute__((ext_vector_type(4)));
typedef u32 u32x2 __attribute__((ext_vector_type(2)));
typedef float f32x4 __attribute__((ext_vector_type(4)));
typedef float f32x2 __attribute__((ext_vector_type(2)));
typedef short s16x4 __attribute__((ext_vector_type(4)));
typedef __bf16 bf16x8 __attribute__((ext_vector_type(8)));
typedef __bf16 bf16x2 __attribute__((ext_vector_type(2)));
#define GAS __attribute__((address_space(1)))
#define LAS __attribute__((address_space(3)))
#define RLX_AGENT __ATOMIC_RELAXED, __HIP_MEMORY_SCOPE_AGENT

constexpr int SK = 4096, WIN = 1024, FF = 1376, NP3 = 43;

constexpr int WS_FLAGS = 0x1000;
constexpr int WS_TS = 0x1800;
constexpr int WS_G1 = 0x2000;
constexpr int WS_B1 = 0x8000;
constexpr int WS_MODR = 0xA000;
constexpr int WS_Q = 0x12000;
constexpr int WS_B0 = 0x32000;
constexpr int WS_NV = 0x33000;
constexpr int WS_NP = 0x34000;
constexpr int LN_O = 0;
constexpr int LN_PART = 128;
constexpr int LN_X2 = 256;
constexpr int LN_YP = 384;
constexpr int WS_T = 0x38000, T_SPAN = 17 * 16384;
constexpr int WS_END = WS_T + 4 * T_SPAN;
constexpr int WS_STEP = 2 << 20;
static_assert(WS_END + 16384 <= WS_STEP && LN_YP + 16 * NP3 <= 17 * 64, "");

constexpr int L_W = 0;
constexpr int L_TP = 65536;
constexpr int L_C1 = 98304;
constexpr int L_SINK = 130048;
constexpr int L_RED = 131072;
constexpr int L_H2 = L_RED + 16384;
constexpr int C1S = 1040, H2S = 1056, OSTR = 544, OBS = 288;
constexpr int L_Q = L_H2 + 8 * H2S;
constexpr int L_ML = L_Q + 512;
constexpr int L_HH = L_ML + 128;
constexpr int L_CTL = L_HH + 784;
constexpr int L_END = L_CTL + 64;
constexpr int C_T = 0, C_TB = 1, C_TX = 2, C_Q = 3, C_ML = 4, C_R = 6, C_T0 = 7, C_X = 8;
constexpr int RW = 12, WG_ = 10;

constexpr u32 SPIN_CAP = 1u << 17, LSPIN_CAP = 1u << 20;
#define DUE0 810
#define TAU 220
#define E1 16
#define E2 10
#define SKW 200
#define MSH 14
#define KA 55
#define KW 6
#define OFO 420
#define OFB 450
#define PB 200
#define RB 260
#define DO 15
#define DL_ 220
#define DP 328
#define DX 513
#define DYS 1043
#define DB1 880
#define DY 200
#define SDL 12
#define WDT 40
constexpr int extra(int i) { return i < 8 ? E1 : (i >= 14 && i < 22 ? E2 : 0); }
constexpr int TEND = DUE0 + 32 * TAU + 8 * E1 + 8 * E2;
#define SMASK 0xffffffffu

__device__ __forceinline__ float rbf(float f) { return (float)(__bf16)f; }
__device__ __forceinline__ u32 pack2(float a, float b) { bf16x2 v = {(__bf16)a, (__bf16)b}; return __builtin_bit_cast(u32, v); }
__device__ __forceinline__ float blo(u32 v) { return __builtin_bit_cast(float, v << 16); }
__device__ __forceinline__ float bhi(u32 v) { return __builtin_bit_cast(float, v & 0xffff0000u); }
__device__ __forceinline__ u32 canon(u32 v) { return v < 0xfffffffeu ? v : 0xfffffffeu; }
#define SEL4() u32x4 sel = {0x07060500u, 0x07060501u, 0x07060502u, 0x07060503u}; asm volatile("" : "+s"(sel))
__device__ __forceinline__ u32x4 tag8(f32x4 v, u32 tagv, u32x4 sel) {
  const u32x4 r = __builtin_bit_cast(u32x4, v);
  return u32x4{__builtin_amdgcn_perm(r[0], tagv, sel[0]), __builtin_amdgcn_perm(r[1], tagv, sel[1]), __builtin_amdgcn_perm(r[2], tagv, sel[2]), __builtin_amdgcn_perm(r[3], tagv, sel[3])};
}
__device__ __forceinline__ u32x4 bytes4(u32 tag) { return u32x4{tag & 255u, (tag >> 8) & 255u, (tag >> 16) & 255u, tag >> 24}; }
__device__ __forceinline__ u32 or4(u32x4 v) { return (v[0] | v[1]) | (v[2] | v[3]); }
__device__ __forceinline__ float rcp(float x) { return __builtin_amdgcn_rcpf(x); }
template <int CTRL> __device__ __forceinline__ float dppf(float x) {
  return __builtin_bit_cast(float, __builtin_amdgcn_mov_dpp(__builtin_bit_cast(int, x), CTRL, 0xf, 0xf, true));
}
template <int CTRL> __device__ __forceinline__ u32 dppu(u32 x) { return (u32)__builtin_amdgcn_mov_dpp((int)x, CTRL, 0xf, 0xf, true); }
constexpr int ROR8 = 0x128, ROR4 = 0x124, ROR2 = 0x122, ROR1 = 0x121, XOR1 = 0xB1, XOR2 = 0x4E, XOR7 = 0x141;
__device__ __forceinline__ float row_sum(float x) { x += dppf<ROR8>(x); x += dppf<ROR4>(x); x += dppf<ROR2>(x); x += dppf<ROR1>(x); return x; }
__device__ __forceinline__ float row_max(float x) {
  x = fmaxf(x, dppf<ROR8>(x)); x = fmaxf(x, dppf<ROR4>(x)); x = fmaxf(x, dppf<ROR2>(x)); x = fmaxf(x, dppf<ROR1>(x)); return x;
}
__device__ __forceinline__ float x16_sum(float x) {
  auto s = __builtin_amdgcn_permlane16_swap(__float_as_uint(x), __float_as_uint(x), false, false);
  return __uint_as_float(s[0]) + __uint_as_float(s[1]);
}
__device__ __forceinline__ float x32_sum(float x) {
  auto s = __builtin_amdgcn_permlane32_swap(__float_as_uint(x), __float_as_uint(x), false, false);
  return __uint_as_float(s[0]) + __uint_as_float(s[1]);
}
__device__ __forceinline__ float x16_max(float x) {
  auto s = __builtin_amdgcn_permlane16_swap(__float_as_uint(x), __float_as_uint(x), false, false);
  return fmaxf(__uint_as_float(s[0]), __uint_as_float(s[1]));
}
__device__ __forceinline__ float x32_max(float x) {
  auto s = __builtin_amdgcn_permlane32_swap(__float_as_uint(x), __float_as_uint(x), false, false);
  return fmaxf(__uint_as_float(s[0]), __uint_as_float(s[1]));
}
__device__ __forceinline__ float t16(float a, float b) {
  auto s = __builtin_amdgcn_permlane16_swap(__float_as_uint(a), __float_as_uint(b), false, false);
  return __uint_as_float(s[0]) + __uint_as_float(s[1]);
}
__device__ __forceinline__ float t32(float a, float b) {
  auto s = __builtin_amdgcn_permlane32_swap(__float_as_uint(a), __float_as_uint(b), false, false);
  return __uint_as_float(s[0]) + __uint_as_float(s[1]);
}
__device__ __forceinline__ f32x2 up2(u32 v) { return f32x2{blo(v), bhi(v)}; }
__device__ __forceinline__ u32 pk2(f32x2 v) { return pack2(v[0], v[1]); }
#define PIN(x) asm volatile("" : "+v"(x))
__device__ __forceinline__ f32x2 rb2(f32x2 v) { u32 p = pk2(v); asm("" : "+v"(p)); return up2(p); }
__device__ __forceinline__ float wave_sum(float x) { return x32_sum(x16_sum(row_sum(x))); }
__device__ __forceinline__ float wave_max(float x) { return x32_max(x16_max(row_max(x))); }
__device__ __forceinline__ float dot2(u32 a, u32 b, float c) {
  return __builtin_amdgcn_fdot2_f32_bf16(__builtin_bit_cast(bf16x2, a), __builtin_bit_cast(bf16x2, b), c, false);
}
__device__ __forceinline__ f32x4 mfma(u32x4 a, u32x4 b, f32x4 c) {
  return __builtin_amdgcn_mfma_f32_16x16x32_bf16(__builtin_bit_cast(bf16x8, a), __builtin_bit_cast(bf16x8, b), c, 0, 0, 0);
}
__device__ __forceinline__ u32x4 ldnt(const void* p) { return __builtin_nontemporal_load((const u32x4*)p); }
__device__ __forceinline__ void glds(const void* g, u32 lds) {
  u32 keep;
  lds = __builtin_amdgcn_readfirstlane(lds);
  asm volatile("s_mov_b32 %0, m0\n\ts_mov_b32 m0, %2\n\ts_nop 0\n\tglobal_load_lds_dwordx4 %1, off nt\n\ts_mov_b32 m0, %0"
               : "=&s"(keep) : "v"(g), "s"(lds) : "memory");
}
__device__ __forceinline__ void glds(const void* base, u32 off, u32 lds) {
  u32 keep;
  lds = __builtin_amdgcn_readfirstlane(lds);
  asm volatile("s_mov_b32 %0, m0\n\ts_mov_b32 m0, %3\n\ts_nop 0\n\tglobal_load_lds_dwordx4 %1, %2 nt\n\ts_mov_b32 m0, %0"
               : "=&s"(keep) : "v"(off), "s"(base), "s"(lds) : "memory");
}
__device__ __forceinline__ void touch(const void* base, u32 off, u32 lds) {
  u32 keep;
  lds = __builtin_amdgcn_readfirstlane(lds);
  asm volatile("s_mov_b32 %0, m0\n\ts_mov_b32 m0, %3\n\ts_nop 0\n\tglobal_load_lds_dwordx4 %1, %2\n\ts_mov_b32 m0, %0"
               : "=&s"(keep) : "v"(off), "s"(base), "s"(lds) : "memory");
}
__device__ __forceinline__ u32x4 trfrag(LAS unsigned char* p0, LAS unsigned char* p1) {
  s16x4 a = __builtin_amdgcn_ds_read_tr16_b64_v4i16((LAS s16x4*)p0);
  s16x4 b = __builtin_amdgcn_ds_read_tr16_b64_v4i16((LAS s16x4*)p1);
  u32x2 x = __builtin_bit_cast(u32x2, a), y = __builtin_bit_cast(u32x2, b);
  return u32x4{x[0], x[1], y[0], y[1]};
}
#define NEWPAGE() asm volatile("s_branch 9f\n\t.p2align 12\n9:")
#define SB() __builtin_amdgcn_sched_barrier(0)
#define MEMB() asm volatile("" ::: "memory")
#define VM0() asm volatile("s_waitcnt vmcnt(0)" ::: "memory")
#define LGKM0() asm volatile("s_waitcnt lgkmcnt(0)" ::: "memory")
#define BAR() asm volatile("s_waitcnt lgkmcnt(0)\n\ts_barrier" ::: "memory")
#define TAG() do { u32 ep_; asm volatile("s_load_dword %0, %1, 0x0\n\ts_waitcnt lgkmcnt(0)" : "=s"(ep_) : "s"(ws) : "memory"); \
    tag = ((ep_ & 0x7f7f7f7fu) + 0x01010101u) ^ (ep_ & 0x80808080u); } while (0)
constexpr int SC1 = 16;
__device__ __forceinline__ u32x4 ld_sc1(__amdgpu_buffer_rsrc_t rs, int off) { return __builtin_amdgcn_raw_buffer_load_b128(rs, off, 0, SC1); }
__device__ __forceinline__ u32x4 ld_one(__amdgpu_buffer_rsrc_t rs, int off) { return __builtin_amdgcn_raw_buffer_load_b128(rs, off, 0, SC1 | 2); }
__device__ __forceinline__ u32x2 ld_sc1_64(__amdgpu_buffer_rsrc_t rs, int off) { return __builtin_amdgcn_raw_buffer_load_b64(rs, off, 0, SC1); }
__device__ __forceinline__ void st_sc1(__amdgpu_buffer_rsrc_t rs, int off, u32x4 v) { __builtin_amdgcn_raw_buffer_store_b128(v, rs, off, 0, SC1); }
__device__ __forceinline__ void st_sc1_64(__amdgpu_buffer_rsrc_t rs, int off, u32x2 v) { __builtin_amdgcn_raw_buffer_store_b64(v, rs, off, 0, SC1); }

__device__ __forceinline__ void ln_stats(u32x4 xv, float& m, float& inv) {
  const float x[8] = {blo(xv[0]), bhi(xv[0]), blo(xv[1]), bhi(xv[1]), blo(xv[2]), bhi(xv[2]), blo(xv[3]), bhi(xv[3])};
  const float x0 = __builtin_bit_cast(float, __builtin_amdgcn_readfirstlane(xv[0] << 16));
  float s = 0.f, v = 0.f;
#pragma unroll
  for (int j = 0; j < 8; ++j) { const float d = x[j] - x0; s += d; v = fmaf(d, d, v); }
  const float md = wave_sum(s) * (1.f / 512.f), sq = wave_sum(v) * (1.f / 512.f);
  const float var = rbf(fmaxf(fmaf(-md, md, sq), 0.f));
  m = rbf(x0 + md);
  inv = rcp(rbf(__builtin_amdgcn_sqrtf(rbf(var + 1e-5f))));
}
#define PACE_(W, INC) do { asm volatile(W "\n1:\n\ts_memrealtime vcc\n\ts_waitcnt lgkmcnt(0)\n\ts_lshl_b32 vcc_lo, vcc_lo, 2\n\ts_sub_i32 vcc_hi, vcc_lo, %0\n\ts_cmp_lt_i32 vcc_hi, 0\n\t" \
    "s_cbranch_scc0 2f\n\ts_sleep 1\n\ts_branch 1b\n2:\n\ts_add_i32 %0, %0, %1" : "+s"(due) : "is"(INC) : "memory", "scc", "vcc"); SB(); } while (0)
#define LT(K, T)
#define PACE(INC) PACE_("s_waitcnt vmcnt(2)", INC)
#define PACEN(INC) PACE_("", INC)
__device__ __forceinline__ void until_s(u32 due) { PACE_("", 0); }
__device__ __forceinline__ void until(u32 t) {
  asm volatile("1:\n\ts_memrealtime vcc\n\ts_waitcnt lgkmcnt(0)\n\ts_lshl_b32 vcc_lo, vcc_lo, 2\n\ts_sub_i32 vcc_hi, vcc_lo, %0\n\ts_cmp_lt_i32 vcc_hi, 0\n\t"
               "s_cbranch_scc1 1b" :: "s"(t) : "memory", "scc", "vcc");
  SB();
}

#define STAMP(i) do { if ((i) == 0) { sv0 = (i) == 0 ? lk0 : (u32)__builtin_amdgcn_s_memrealtime(); asm volatile("" : "+s"(sv0)); } if ((i) == 18) { sv18 = (i) == 0 ? lk0 : (u32)__builtin_amdgcn_s_memrealtime(); asm volatile("" : "+s"(sv18)); } } while (0)

#define WSTAMP(k) do { } while (0)
__device__ __forceinline__ void lds_wait(volatile LAS u32* p, u32 want) {
  _Pragma("unroll 1") for (u32 s = 0; s < LSPIN_CAP; ++s) {
    if (__builtin_amdgcn_readfirstlane(*p) >= want) break;
    __builtin_amdgcn_s_sleep(2);
  }
  MEMB();
}
__device__ __forceinline__ void lds_add(LAS u32* p, int lane) {
  LGKM0();
  if (lane == 0) __hip_atomic_fetch_add(p, 1u, __ATOMIC_RELAXED, __HIP_MEMORY_SCOPE_WORKGROUP);
  MEMB();
}
__device__ __forceinline__ void tile_part(LAS unsigned char* T, LAS unsigned char* P, u32x4 hb, int n, int g) {
  LAS unsigned char* tb = T + (8 * g + (n >> 2)) * 128 + (n & 3) * 8;
  const int sx = (n >> 3) | ((g & 1) << 1);
  f32x4 a[4];
#pragma unroll
  for (int t = 0; t < 4; ++t) a[t] = mfma(trfrag(tb + (t ^ sx) * 32, tb + (t ^ sx) * 32 + 512), hb, f32x4{0.f, 0.f, 0.f, 0.f});
  *(LAS f32x4*)(P) = n < 8 ? a[0] : a[1];
  *(LAS f32x4*)(P + 1024) = n < 8 ? a[2] : a[3];
}
__device__ __forceinline__ f32x4 tile_sum(LAS unsigned char* P) {
  f32x4 s[8], r[2];
#pragma unroll
  for (int k = 0; k < 2; ++k) {
#pragma unroll
    for (int i = 0; i < 8; ++i) s[i] = *(LAS f32x4*)(P + (8 * k + i) * 2048);
    SB();
    r[k] = ((s[0] + s[1]) + (s[2] + s[3])) + ((s[4] + s[5]) + (s[6] + s[7]));
    SB();
  }
  return r[0] + r[1];
}
#define GU_MUL(P) _Pragma("unroll") for (int i = 0; i < 8; ++i) gb_[i] = *(LAS u32x4*)((P) + i * 64); SB(); \
    aL = f32x4{0.f, 0.f, 0.f, 0.f}; aH = aL; _Pragma("unroll") for (int i = 0; i < 8; ++i) { aL = mfma(gf[i], gb_[i], aL); aH = mfma(gf[8 + i], gb_[i], aH); }
__device__ __forceinline__ void gu_pair(f32x4 aL, f32x4 aH, f32x2& gt, f32x2& up) {
  const auto s0 = __builtin_amdgcn_permlane32_swap(__float_as_uint(aL[0] + dppf<ROR8>(aH[0])), __float_as_uint(aL[2] + dppf<ROR8>(aH[2])), false, false);
  const auto s1 = __builtin_amdgcn_permlane32_swap(__float_as_uint(aL[1] + dppf<ROR8>(aH[1])), __float_as_uint(aL[3] + dppf<ROR8>(aH[3])), false, false);
  gt = f32x2{__uint_as_float(s0[0]), __uint_as_float(s1[0])};
  up = f32x2{__uint_as_float(s0[1]), __uint_as_float(s1[1])};
}
__device__ __forceinline__ int tline(int c, int ln, u32 near) { return WS_T + (c & 3) * T_SPAN + (ln >> 6) * 16384 + ((near >> c) & 1) * 8192 + (ln & 63) * 128; }
__device__ __forceinline__ int mpos(int c) { return (c & 252) * 4 + (c >> 8) * 8 + (c & 3) * 2; }
__device__ __forceinline__ u32 smax(int m) { int r = (int)fminf(wave_max((float)m), 4e5f); PIN(r); return (u32)__builtin_amdgcn_readfirstlane(r); }
__device__ __forceinline__ u32 now4() { return (u32)__builtin_amdgcn_s_memrealtime() * 4u; }
#define B0_LOAD() { bp = ld_sc1(rs, bo); bq = ld_sc1(rs, bo + 1024); }
__device__ __forceinline__ bool on_time(u32 tag, u32 org, u32x4 bp, u32x4 bq) {
  const u32 lim = org + OFO - DO;
  return __all((((bp[1] ^ tag) | (bp[3] ^ tag) | (bq[1] ^ tag) | (bq[3] ^ tag)) | (((lim - bp[0]) | (lim - bp[2]) | (lim - bq[0]) | (lim - bq[2])) >> 31)) == 0u);
}
__device__ __forceinline__ u32 anchor(__amdgpu_buffer_rsrc_t rs, int bo, u32 tag, u32 org, u32x4 bp, u32x4 bq) {
  int lk = OFB;
#pragma unroll 1
  for (u32 s = 0; s < SPIN_CAP; ++s) {
    if (__all(((bp[1] ^ tag) | (bp[3] ^ tag) | (bq[1] ^ tag) | (bq[3] ^ tag)) == 0u)) break;
    MEMB();
    do lk += PB; while ((int)(now4() - org) > lk);
    until_s(org + lk);
    B0_LOAD()
  }
  const int m = (int)smax(max(max((int)(bp[0] - org), (int)(bp[2] - org)), max((int)(bq[0] - org), (int)(bq[2] - org))));
  return org + (u32)max(m + DO <= OFO ? OFO : m + DL_, lk + RB - DP);
}
__device__ __forceinline__ float asf(u32 v) { return __builtin_bit_cast(float, v); }

struct Args {
  const unsigned char* half[4][2];
  const u16 *x, *kc, *vc, *wd, *cc;
  u16* out;
  unsigned char* ws;
};

__global__ void __launch_bounds__(1024, 4) fused(const Args a) {
  extern __shared__ __attribute__((aligned(16))) unsigned char smem[];
  LAS unsigned char* L = (LAS unsigned char*)smem;
  const u32 lbase = (u32)(size_t)L;
  const int tid = threadIdx.x, w = __builtin_amdgcn_readfirstlane(tid >> 6), bid = blockIdx.x;
  int lane = tid & 63;
  u32 lk0 = (u32)__builtin_amdgcn_s_memrealtime(); asm volatile("" : "+s"(lk0));
  u32 sv0 = 0, sv18 = 0;
  const int xcd = bid & 7, idx = bid >> 3, hj = idx & 3, h = 4 * xcd + hj, b = idx >> 2, bh = b * 32 + h;
  const int sw8 = hj == 3 ? (w & 1) * 8 : 0, rw = (hj == 3 ? 24 : 32) * w;
  u64 hA, hB, t64;
  u32 warm;
  asm volatile("s_load_dwordx2 %0, %4, %5\n\ts_load_dwordx2 %1, %4, %6\n\ts_load_dword %3, %4, 0x40\n\ts_memrealtime %2" : "=&s"(hA), "=&s"(hB), "=&s"(t64), "=&s"(warm)
               : "s"(__builtin_amdgcn_kernarg_segment_ptr()), "s"(16 * hj + sw8), "s"(16 * hj + (sw8 ^ 8)));
  SB();
  const int bb = (b + xcd) & 7;
  int p3r = 5 * xcd + min(xcd, 3) + bb;
  int mc = (b & 6) + ((xcd & 1) ^ 1), mg = ((xcd & 6) + b) & 7;
  const bool tq = hj == 0, to = hj == 2;
  int lim = hj == 1 ? 6 - b : (xcd < 3 ? 6 : 5) - bb;
  asm volatile("" : "+s"(p3r), "+s"(mc), "+s"(mg), "+s"(lim));
  const bool idle = (hj & 1) && lim <= 0;
  u64 tp0, tp1;
  {
    const u32 pitch = (u32)(0x0AC0040018002000ull >> (16 * hj)) & 0xffffu;
    u32 ce = (xcd << (to ? 13 : 4)) + 2 * b, c1 = 16 * mc + 2 * mg;
    asm volatile("" : "+s"(ce), "+s"(c1));
    const u32 col = 64 * ((hj & 1) ? (hj == 1 ? c1 : p3r) : ce);
    const u32 step = pitch << (to ? 1 : 3);
    u32 off = (rw + (to ? (lane & 24) + (lane >> 5) : lane >> 3)) * pitch + col + ((lane & 3) ^ ((lane >> 3) & 2)) * 16;
    tp0 = off;
    tp1 = off + step;
    u64 s2 = 2 * step;
    u32 ld = lbase + L_W + rw * 128;
    asm volatile("" : "+s"(s2), "+s"(ld), "+v"(tp0), "+v"(tp1));
    asm volatile("s_mov_b32 exec_lo, 0xf0f0f0f0\n\ts_mov_b32 exec_hi, 0xf0f0f0f0\n\ts_waitcnt lgkmcnt(0)\n\tv_lshl_add_u64 %0, %0, 0, %3\n\tv_lshl_add_u64 %1, %1, 0, %2\n\t"
                 "s_not_b64 exec, exec\n\tv_lshl_add_u64 %0, %0, 0, %2\n\tv_lshl_add_u64 %1, %1, 0, %3\n\ts_mov_b64 exec, -1"
                 : "+v"(tp0), "+v"(tp1), "+s"(hA), "+s"(hB), "+s"(t64), "+s"(warm) :: "scc");
    if (!idle) {
      u32 keep;
      asm volatile("s_mov_b32 %0, m0\n\ts_mov_b32 m0, %3\n\ts_nop 0\n\tglobal_load_lds_dwordx4 %1, off nt\n\ts_add_u32 m0, m0, 0x400\n\t"
                   "v_lshl_add_u64 %1, %1, 0, %4\n\tglobal_load_lds_dwordx4 %2, off nt\n\ts_mov_b32 m0, %0\n\tv_lshl_add_u64 %2, %2, 0, %4"
                   : "=&s"(keep), "+v"(tp0), "+v"(tp1) : "s"(ld), "s"(s2) : "memory", "scc");
      glds((const void*)tp0, ld + 2048);
      if (hj != 3) glds((const void*)tp1, ld + 3072);
    }
  }
  SB();
  u32 t0 = (u32)t64;
  const u16 *x = a.x, *kc = a.kc, *vc = a.vc, *wd = a.wd, *cc = a.cc;
  u16* out = a.out;
  unsigned char* ws = a.ws;
  if (w == 0) STAMP(24);
  if (w == 0) STAMP(0);
  t0 += (u32)(KW * max((int)((u32)__builtin_amdgcn_s_memrealtime() - t0) - KA, 0)) / 4u;
  const int p3 = (hj == 3 && !idle) ? p3r : -1;
  const bool tm = hj == 1 && !idle;
  u32 tag;
  TAG();
  const __amdgpu_buffer_rsrc_t rs = __builtin_amdgcn_make_buffer_rsrc((void*)(((size_t)ws + 16383 + (size_t)(tag & 63u) * WS_STEP) & ~(size_t)16383), 0, WS_END, 0x00020000);
  if (idle && hj == 1) {
    if (w == 0) {
      st_sc1(rs, WS_G1 + ((b & 1) * 8 + xcd) * 1024 + lane * 16, u32x4{~0u, ~0u, ~0u, ~0u});
      VM0();
      if (lane == 0) st_sc1_64(rs, WS_TS + ((b & 1) * 8 + xcd) * 8, u32x2{t0, tag});
    } else {
      u64 pc;
      u32 len;
      asm volatile("s_getpc_b64 %0\n1:\n\ts_mov_b32 %1, fused_code_end - 1b + 1000\n\ts_max_u32 %1, %1, fused_cold_end - 1b + 1000" : "=s"(pc), "=&s"(len) :: "scc");
      const u32 o = ((b & 1) * 15 + w - 1) * 1024;
      if (o < len) touch((const void*)(pc & ~15ull), min(o + lane * 16, len & ~15u), lbase + L_SINK);
    }
  }
  if (w == 1) glds((const unsigned char*)((lane & 1) ? vc : kc) + ((size_t)bh * SK + (SK - WIN)) * 256, lbase + L_SINK);
  int ta = TAU;
  int n = lane & 15, g = lane >> 4;
  u32x4 cv, hb;
  const bool row = tm && (w >= 8 || mc == 1);
  if (row) cv = ldnt((w < 8 ? x : cc) + (w & 7) * 512 + lane * 8);
  if (tid < 16) ((volatile LAS u32*)(L + L_CTL))[tid] = tid == C_T0 ? t0 : 0u;
  BAR();
  t0 = __builtin_amdgcn_readfirstlane(((volatile LAS u32*)(L + L_CTL))[C_T0]);
  u32 due = t0 * 4u + (u32)((4 * bb + hj) * TAU) / 32u + DUE0;
  if (hj == 3 && bb >= 6 && w == 0) {
    const int k = (bb & 1) * 8 + xcd, o = WS_NP + k * 512 + (bb & 1) * 8192;
    __builtin_amdgcn_raw_buffer_store_b32(tag, rs, lane < 2 ? o ^ (lane * 8192) : WS_END, 0, SC1);
    u64 c0, c1, c2;
    u32 v;
    asm volatile("s_waitcnt vmcnt(0)\n\ts_memtime %0\n\ts_waitcnt lgkmcnt(0)\n\tbuffer_load_dword %3, %4, %6, 0 offen sc1\n\ts_waitcnt vmcnt(0)\n\ts_memtime %1\n\ts_waitcnt lgkmcnt(0)\n\t"
                 "buffer_load_dword %3, %5, %6, 0 offen sc1\n\ts_waitcnt vmcnt(0)\n\ts_memtime %2\n\ts_waitcnt lgkmcnt(0)"
                 : "=&s"(c0), "=&s"(c1), "=&s"(c2), "=&v"(v) : "v"(o), "v"(o ^ 8192), "s"(rs) : "memory");
    const int d = min(max(2 * (int)c1 - (int)c0 - (int)c2, -600), 600);
    if (lane == 0) st_sc1_64(rs, WS_NV + k * 8, u32x2{(u32)(2 * ((bb & 1) ? -d : d)) | ((__builtin_amdgcn_s_getreg(20 | (3 << 11)) >> 2) & 1u), tag});
  }
  int rot = idx;
#define RO(I) ((((I) + rot) & 15) * 8192)
  const u16* kp = kc + ((size_t)bh * SK + (SK - WIN) + 4 * w) * 128;
  u32 l16 = lane * 16;
  PIN(l16);
#define LDR(D, P, I, W) { const GAS unsigned char* p_ = (const GAS unsigned char*)((P) + RO(I)) + l16; PIN(p_); W; D = __builtin_nontemporal_load((const GAS u32x4*)p_); SB(); }
  u32x4 kr[16], vr[16];
  volatile LAS u32* ctl = (volatile LAS u32*)(L + L_CTL);
#define RELANE() do { asm volatile("" : "+v"(lane)); n = lane & 15; g = lane >> 4; } while (0)
  const int e = b * 16 + 2 * xcd + hj;
  const bool red = hj < 2;

#define KL(I) { const int t_ = ta + extra(I); LDR(kr[I], kp, I, PACE(t_)) LT(I, t_) }
  KL(0)
  if (row) {
    if (w >= 8) {
#pragma unroll
      for (int j = 0; j < 4; ++j) { const float a0 = blo(cv[j]), a1 = bhi(cv[j]); hb[j] = pack2(a0 * rcp(1.f + __expf(-a0)), a1 * rcp(1.f + __expf(-a1))); }
      *(LAS u32x4*)(L + L_H2 + (w & 7) * H2S + lane * 16) = hb;
      lds_add((LAS u32*)(L + L_CTL) + C_R, lane);
    } else {
      float m, inv;
      ln_stats(cv, m, inv);
      if ((lane >> 3) == mg) {
        LAS f32x4* d = (LAS f32x4*)(L + L_RED + w * 256 + (lane & 7) * 32);
        d[0] = f32x4{rbf(rbf(blo(cv[0]) - m) * inv), rbf(rbf(bhi(cv[0]) - m) * inv), rbf(rbf(blo(cv[1]) - m) * inv), rbf(rbf(bhi(cv[1]) - m) * inv)};
        d[1] = f32x4{rbf(rbf(blo(cv[2]) - m) * inv), rbf(rbf(bhi(cv[2]) - m) * inv), rbf(rbf(blo(cv[3]) - m) * inv), rbf(rbf(bhi(cv[3]) - m) * inv)};
      }
    }
  }
  KL(1)
  u32x2 tsv;
#define TS_WAIT() if (!__all(tsv[1] == tag)) { _Pragma("unroll 1") for (u32 s = 0; s < SPIN_CAP; ++s) { __builtin_amdgcn_s_sleep(16); MEMB(); \
      tsv = ld_sc1_64(rs, WS_TS + (lane & 15) * 8); if (__all(tsv[1] == tag)) break; } } MEMB();
  if (tm) {
    asm volatile("s_waitcnt vmcnt(2)" ::: "memory");
    lds_wait(ctl + C_R, 8u);
    hb = *(LAS u32x4*)(L + L_H2 + (n & 7) * H2S + (32 * w + 8 * g) * 2);
    tile_part(L + L_W + w * 4096, L + L_TP + w * 2048 + lane * 16, hb, n, g);
    lds_add((LAS u32*)(L + L_CTL) + C_T, lane);
  }
  KL(2)
  if (w == 0 || (tm && w >= 14)) tsv = ld_sc1_64(rs, WS_TS + (lane & 15) * 8);
  SB();
  if (tm && w >= 14) {
    lds_wait(ctl + C_T, 16u);
    const int col = 32 * (w & 1) + 16 * (n >> 3) + 4 * g;
    const f32x4 s = tile_sum(L + L_TP + (w & 1) * 1024 + lane * 16);
    float v[4] = {rbf(s[0]), rbf(s[1]), rbf(s[2]), rbf(s[3])};
    if (mc == 1) {
      const f32x4 ln = *(LAS f32x4*)(L + L_RED + (n & 7) * 256 + col * 4);
#pragma unroll
      for (int j = 0; j < 4; ++j) v[j] = ln[j] * rbf(1.f + v[j]);
    }
    TS_WAIT()
    st_sc1_64(rs, mc < 2 ? WS_G1 + (2 * mg + (w & 1)) * 1024 + ((2 * (n >> 3) + (g >> 1)) * 16 + mc * 8 + (n & 7)) * 16 + (g & 1) * 8
                         : WS_MODR + ((mc - 2) * 8 + (n & 7)) * 1024 + mpos(64 * mg + col),
              u32x2{canon(pack2(v[0], v[1])), canon(pack2(v[2], v[3]))});
    if (w == 15) STAMP(3);
  }
  RELANE();
  KL(3) KL(4)
  if (w == 0) {
    TS_WAIT()
    const float d = __builtin_amdgcn_fmed3f((float)(int)(tsv[0] - t0), -1e5f, 1e5f);
    ctl[C_TB] = (t0 * 4u + (u32)(int)fmaxf(row_sum(d) * 0.25f, row_max(d) * 4.f - SKW)) | 1u;
  }
  KL(5) KL(6)
  u32 org = __builtin_amdgcn_readfirstlane(ctl[C_TB]);
  if (org) { const int adj = ((int)(org - t0 * 4u - (p3 >= 0 ? bb * MSH : 0)) * 85 + 1024) >> 11; ta += adj; }
  KL(7)
  if (tm && mc >= 2 && w >= 14 && lane == 0) __builtin_amdgcn_raw_buffer_store_b32(tag, rs, WS_FLAGS + (((mc - 2) * 8 + mg) * 2 + (w & 1)) * 4, 0, SC1);
  KL(8)
  u32x4 g0;
  const int o0 = WS_G1 + w * 1024 + lane * 16;
  if (tq) lds_wait(ctl + C_TB, 1u);
#define KG(K) { KL(K) if (tq && (w & 1) == ((K) & 1)) { until_s(due - TAU / 2); \
    asm volatile("buffer_load_dwordx4 %0, %1, %2, 0 offen sc1" : "=&v"(g0) : "v"(o0), "s"(rs) : "memory"); } }
  KG(9) KG(10)
  KL(11) KL(12)
  if (tq) {
    asm volatile("s_waitcnt vmcnt(2)" : "+v"(g0));
#define G1_OK() __all(g0[0] != ~0u && g0[1] != ~0u && g0[2] != ~0u && g0[3] != ~0u)
    if (!G1_OK()) {
#pragma unroll 1
      for (u32 s = 0; s < SPIN_CAP; ++s) {
        __builtin_amdgcn_s_sleep(16);
        MEMB();
        g0 = ld_sc1(rs, o0);
        if (G1_OK()) break;
      }
    }
    if (w == 15) STAMP(4);
#pragma unroll
    for (int j = 0; j < 4; ++j) { const u32 o = dppu<ROR8>(g0[j]); hb[j] = pack2(blo(g0[j]) + blo(o), bhi(g0[j]) + bhi(o)); }
    PIN(hb);
  }
  KL(13)
  if (tq) {
    tile_part(L + L_W + w * 4096, L + L_TP + w * 2048 + lane * 16, hb, n, g);
    lds_add((LAS u32*)(L + L_CTL) + C_T, lane);
  }
  KL(14)
  if (tq && w >= 14) {
    lds_wait(ctl + C_T, 16u);
    const f32x4 s = tile_sum(L + L_TP + (w & 1) * 1024 + lane * 16);
    st_sc1(rs, WS_Q + ((n & 7) * 32 + 4 * xcd + (b >> 1)) * 512 + ((b & 1) * 64 + 32 * (w & 1) + 16 * (n >> 3) + 4 * g) * 4, u32x4{pack2(s[0], s[1]), tag, pack2(s[2], s[3]), tag});
    if (w == 15) STAMP(17);
  }
  RELANE();
  KL(15)
  asm volatile("" : "+s"(rot));
  const u16* vp = vc + ((size_t)bh * SK + (SK - WIN) + 4 * w) * 128;
#define VL(I) { const int t_ = ta + extra(16 + (I)); LDR(vr[I], vp, I, PACE(t_)) LT(16 + (I), t_) }
#define VLN(I) { const int t_ = ta + extra(16 + (I)); LDR(vr[I], vp, I, PACEN(t_)) LT(16 + (I), t_) }
  VL(0) VL(1)
  VL(2)
  u32x2 gq;
#define FL_WAIT() if (!__all(fl == tag)) {   _Pragma("unroll 1") for (u32 s = 0; s < SPIN_CAP; ++s) { __builtin_amdgcn_s_sleep(4); MEMB(); \
      fl = __builtin_amdgcn_raw_buffer_load_b32(rs, WS_FLAGS + lane * 4, 0, SC1); if (__all(fl == tag)) break; } } MEMB();
  u32 fl;
  u32x2 nv;
  if (w == WG_) {
    nv = ld_sc1_64(rs, WS_NV + (lane & 15) * 8);
    gq = ld_sc1_64(rs, WS_Q + bh * 512 + lane * 8);
    fl = __builtin_amdgcn_raw_buffer_load_b32(rs, !to ? WS_FLAGS + lane * 4 : WS_END, 0, SC1);
  }
  SB();
  VLN(3) VLN(4)
  if (w == WG_) {
    if (!__all(gq[1] == tag)) {
#pragma unroll 1
      for (u32 s = 0; s < SPIN_CAP; ++s) {
        __builtin_amdgcn_s_sleep(4);
        MEMB();
        gq = ld_sc1_64(rs, WS_Q + bh * 512 + lane * 8);
        if (__all(gq[1] == tag)) break;
      }
    }
    *(LAS u32*)(L + L_Q + lane * 4) = gq[0];
    PIN(fl);
    if (!to) { FL_WAIT() }
    PIN(nv);
    if (!__all(nv[1] == tag)) {
#pragma unroll 1
      for (u32 s = 0; s < SPIN_CAP; ++s) {
        __builtin_amdgcn_s_sleep(4);
        MEMB();
        nv = ld_sc1_64(rs, WS_NV + (lane & 15) * 8);
        if (__all(nv[1] == tag)) break;
      }
    }
    const float dv = (float)((int)nv[0] >> 1);
    const u32 m4 = (u32)__ballot((row_sum((nv[0] & 1u) ? -dv : dv) > 0.f) != ((nv[0] & 1u) != 0u)) & 15u;
    LGKM0();
    ctl[C_Q] = 256u | m4 | ((m4 ^ 15u) << 4);
    STAMP(5);
  }
  RELANE();
  VL(5)
  lds_wait(ctl + C_Q, 1u);
  const bool prer = p3 >= 0 && w < 8;
  u32x4 pre0, pre1;
  if (prer) {
    pre0 = ld_sc1(rs, WS_MODR + (8 + w) * 1024 + lane * 16);
    pre1 = ld_sc1(rs, WS_MODR + (16 + w) * 1024 + lane * 16);
  }
  SB();
  const u32x4 qf = *(LAS u32x4*)(L + L_Q + n * 16);
  if (w == 1) STAMP(6);

#define DOT(I) dot2(kr[I][3], qf[3], dot2(kr[I][2], qf[2], dot2(kr[I][1], qf[1], dot2(kr[I][0], qf[0], 0.f))))
  float xs[8];
#define QK(C) { _Pragma("unroll") for (int i = 2 * (C); i < 2 * (C) + 2; ++i) { const float lo_ = DOT(i), hi_ = DOT(i + 8); \
      const float t1_ = lo_ + dppf<ROR8>(lo_), t2_ = hi_ + dppf<ROR8>(hi_); xs[i] = (n & 8) ? t2_ : t1_; PIN(xs[i]); } SB(); }
  QK(0) VLN(6) QK(1) VLN(7) QK(2) VL(8) QK(3) VL(9)
  if (prer) {
    const u32 a_h2w = w * C1S + lane * 8;
    *(LAS u32x2*)(L + L_C1 + a_h2w) = u32x2{pk2(up2(pre1[0]) + 1.f), pk2(up2(pre1[1]) + 1.f)};
    *(LAS u32x2*)(L + L_C1 + a_h2w + 512) = u32x2{pk2(up2(pre1[2]) + 1.f), pk2(up2(pre1[3]) + 1.f)};
    *(LAS u32x2*)(L + L_C1 + 8 * C1S + a_h2w) = u32x2{pre0[0], pre0[1]};
    *(LAS u32x2*)(L + L_C1 + 8 * C1S + a_h2w + 512) = u32x2{pre0[2], pre0[3]};
  }
#define TR_STEP(HALF, CTRL, BIT) _Pragma("unroll") for (int hh = 0; hh < (HALF); ++hh) { \
    const float lo_ = xs[hh], hi_ = xs[hh + (HALF)]; const float t1_ = lo_ + dppf<CTRL>(lo_), t2_ = hi_ + dppf<CTRL>(hi_); xs[hh] = (n & (BIT)) ? t2_ : t1_; }
  TR_STEP(4, XOR7, 4) TR_STEP(2, XOR2, 2) TR_STEP(1, XOR1, 1)
#undef TR_STEP
  float sc = rbf(rbf(xs[0]) * 0.08838834764831845f);
  PIN(sc);
  SB();
  VL(10)
  {
    const float mw_ = wave_max(sc);
    const float lw = wave_sum(__expf(sc - mw_));
    if (lane == 0) *(LAS f32x2*)(L + L_ML + w * 8) = f32x2{mw_, lw};
    lds_add((LAS u32*)(L + L_CTL) + C_ML, lane);
  }
  SB();
  VL(11)
  NEWPAGE();
  RELANE();
  u32 mk[4];
#pragma unroll
  for (int j = 0; j < 4; ++j) mk[j] = (n >> 1) == j ? ((n & 1) ? 0xffff0000u : 0x0000ffffu) : 0u;
  f32x4 oa = {0.f, 0.f, 0.f, 0.f};
#define PV(I) { const u32 pb_ = dppu<0x150 + (I)>(pd); oa = mfma(vr[I], u32x4{pb_ & mk[0], pb_ & mk[1], pb_ & mk[2], pb_ & mk[3]}, oa); asm volatile("" : "+v"(oa)); SB(); }
  PIN(mk[0]); PIN(mk[1]); PIN(mk[2]); PIN(mk[3]);
  u32 a_oa = L_RED + w * OSTR + (g * 8 + n) * 16;
  u32 ra = L_RED + (lane >> 5) * 8 * OSTR + (lane & 31) * 16;
  const u32 near = __builtin_amdgcn_readfirstlane(ctl[C_Q]);
  u32 rb_ = tline(xcd, LN_O + (hj * 8 + b) * 4, near) + (lane & 31) * 16 + (lane >> 5) * 8;
  if (w == 14) rb_ = lane == 0 ? WS_B0 + bid * 8 : WS_END;
  int oo = tline(xcd, LN_O + 8 * w, near) + lane * 16;
  PIN(oo);
  u32 tagv = tag;
  PIN(a_oa); PIN(ra); PIN(rb_); PIN(tagv);
  org = __builtin_amdgcn_readfirstlane(ctl[C_TB]);
  org = org ? org + TEND : t0 * 4u;
  asm volatile("" : "+s"(org));
  VL(12)
  lds_wait(ctl + C_ML, 16u);
  u32 pd;
  {
    const f32x2 ml = *(LAS f32x2*)(L + L_ML + n * 8);
    const float M = row_max(ml[0]);
    const float pe = __expf(sc - M) * rcp(row_sum(__expf(ml[0] - M) * ml[1]));
    pd = pack2(pe, pe);
    PIN(pd);
  }
  SB();
  VL(13)
  PV(0) PV(1) PV(2) PV(3) PV(4)
  VL(14)
  PV(5) PV(6) PV(7) PV(8) PV(9)
  VL(15)
  PV(10) PV(11) PV(12) PV(13) PV(14)
  if (w == 13) __builtin_amdgcn_s_setprio(3);
  PV(15)
#undef PV
  if (n < 8) *(LAS f32x4*)(L + a_oa) = oa;
  BAR();
  if (w == 0) STAMP(8);
  if (w == 14) st_sc1_64(rs, rb_, u32x2{now4(), tagv});
  if (w == 13) {
    f32x4 s[8];
#pragma unroll
    for (int ww = 0; ww < 8; ++ww) s[ww] = *(LAS f32x4*)(L + ra + ww * OSTR);
    SB();
    const f32x4 t = ((s[0] + s[1]) + (s[2] + s[3])) + ((s[4] + s[5]) + (s[6] + s[7]));
    st_sc1_64(rs, rb_, u32x2{pack2(t32(t[0], t[2]), t32(t[1], t[3])), tagv});
    STAMP(9);
  }
  if (to) {
    until(org + OFO);
    u32x4 og = ld_sc1(rs, oo);
    SB();
    u32x4 wf[16];
    if (w < 4) {
      __builtin_amdgcn_s_setprio(3);
      const u32 af = L_W + (8 * g + (n >> 2)) * 128 + (w ^ ((n >> 3) | ((g & 1) << 1))) * 32 + (n & 3) * 8;
#pragma unroll
      for (int ks = 0; ks < 16; ++ks) wf[ks] = trfrag(L + af + ks * 4096, L + af + ks * 4096 + 512);
    }
    u32 a_ow = L_TP + (2 * w + (lane >> 5)) * OBS + (lane & 31) * 8;
    u32 a_or = L_TP + ((n >> 3) * 16 + (n & 7)) * OBS + g * 16;
    int rc = n < 8 ? tline(b, LN_PART + (2 * n + ((w >> 1) & 1)) * 8 + xcd, near) + (w & 1) * 64 + g * 16 : WS_END;
    PIN(a_ow); PIN(a_or); PIN(rc);
    SEL4();
    SB();
    *(LAS u32x2*)(L + a_ow) = u32x2{og[0], og[2]};
    if (__builtin_expect(!__all(((og[1] ^ tag) | (og[3] ^ tag)) == 0u), 0)) {
#pragma unroll 1
      for (u32 s = 0; s < SPIN_CAP; ++s) {
        MEMB();
        og = ld_sc1(rs, oo);
        if (__all(((og[1] ^ tag) | (og[3] ^ tag)) == 0u)) break;
        __builtin_amdgcn_s_sleep(2);
      }
      *(LAS u32x2*)(L + a_ow) = u32x2{og[0], og[2]};
    }
    BAR();
    if (w == 7) STAMP(2);
    if (w < 4) {
      u32x4 ob[8];
#pragma unroll
      for (int i = 0; i < 8; ++i) ob[i] = *(LAS u32x4*)(L + a_or + (i >> 2) * 8 * OBS + (i & 3) * 64);
      SB();
      f32x4 aL = {0.f, 0.f, 0.f, 0.f}, aH = {0.f, 0.f, 0.f, 0.f};
#pragma unroll
      for (int i = 0; i < 8; ++i) { aL = mfma(wf[i], ob[i], aL); aH = mfma(wf[8 + i], ob[i], aH); }
      const f32x4 v = {aL[0] + dppf<ROR8>(aH[0]), aL[1] + dppf<ROR8>(aH[1]), aL[2] + dppf<ROR8>(aH[2]), aL[3] + dppf<ROR8>(aH[3])};
      st_sc1(rs, rc, tag8(v, tagv, sel));
      if (w == 3) STAMP(16);
    }
  }

  const int bo = WS_B0 + lane * 16;
  if (p3 >= 0) {
    u32 a_h2w = w * C1S + lane * 8;
    if (w == 8 && lane < 36) *(LAS u32*)(L + (lane < 32 ? L_ML : L_HH + 640) + lane * 4) = 0u;
    BAR();
    glds((const void*)(tp0 + (u32)((368 - 16 * w) * FF * 2)), lbase + L_W + 49152 + w * 1024);
    u32x4 bp, bq;
    if (w == 12) {
      until_s(org + OFB + 20);
      asm volatile("buffer_load_dwordx4 %0, %2, %3, 0 offen sc1\n\tbuffer_load_dwordx4 %1, %2, %3, 0 offen offset:1024 sc1" : "=&v"(bp), "=&v"(bq) : "v"(bo), "s"(rs) : "memory");
    }
#define DL(R) glds(wd + (size_t)(32 * p3 + (R)) * 512, (lane ^ ((((R) & 3) | (((R) >> 1) & 4)) << 1)) * 16, lbase + L_W + 65536 + (R) * 1024)
    if (w < 8) {
      until_s(org + OFB + WDT);
#pragma unroll
      for (int i = 0; i < 4; ++i) DL(4 * w + i);
    }
    SB();
    u32 a_h2r = L_H2 + (n & 7) * H2S + (n >> 3) * 512 + g * 16;
    u32 a_hw = L_HH + (n < 8 ? n * 64 + (8 * (w & 3) + 4 * (g & 1) + 2 * (g >> 1)) * 2 : 512 + lane * 4);
    const u32x4 t16v = {tag & 0xffffu, tag >> 16, tag & 0xffffu, tag >> 16};
#define X_TIME() until_s(org + OFO + DX - 40); u32 tx = 2u; \
      _Pragma("unroll 1") for (u32 s = 0; s < LSPIN_CAP && tx == 2u; ++s) { tx = __builtin_amdgcn_readfirstlane(ctl[C_TX]); if (tx == 2u) __builtin_amdgcn_s_sleep(2); } \
      if (!(tx & 1u)) tx = org + OFO + DX;
#define X_BAD(A, B) ((or4(A) | or4(B)) & 0xffffu)
    if (w < 8) {
      const u32x2 ca = *(LAS u32x2*)(L + L_C1 + a_h2w), cb = *(LAS u32x2*)(L + L_C1 + a_h2w + 512);
      f32x2 c1[4] = {up2(ca[0]), up2(ca[1]), up2(cb[0]), up2(cb[1])};
      a_h2w = L_H2 + w * H2S + lane * 8;
      int xo = tline(xcd, LN_X2 + 16 * w, near) + lane * 16;
      u32 a_hr = L_HH + (n < 8 ? n * 64 + g * 16 : 768), a_hs = L_HH + (n < 8 ? 768 : (n & 7) * 64 + g * 16);
      PIN(a_hs);
      SEL4();
      const u32 a_d0 = L_W + 65536 + (8 * g + (n >> 2)) * 1024 + ((8 * w + ((n >> 1) & 1)) ^ (((n >> 2) | ((g & 1) << 2)) << 1)) * 16 + (n & 1) * 8;
      int yo = tline(w, LN_YP + (4 * (n >> 3) + g) * NP3 + p3, near) + (n & 7) * 16, yo2 = tline(w, LN_YP + (8 + 4 * (n >> 3) + g) * NP3 + p3, near) + (n & 7) * 16;
      PIN(xo); PIN(c1[0]); PIN(c1[1]); PIN(c1[2]); PIN(c1[3]); PIN(a_h2w); PIN(a_hr); PIN(yo); PIN(yo2);
      u32 a_cx = lbase + L_CTL + (C_X + w) * 4;
      PIN(a_cx);
      asm volatile("s_waitcnt vmcnt(4)" ::: "memory");
      BAR();
      if (w == 0) STAMP(20);
      __builtin_amdgcn_s_setprio(2);
      X_TIME()
      until(tx);
#pragma unroll 1
      for (u32 s = 0; s < SPIN_CAP; ++s) {
        const u32x4 xa = ld_sc1(rs, xo) ^ t16v, xb = ld_sc1(rs, xo + 1024) ^ t16v;
        const float x0 = __builtin_bit_cast(float, __builtin_amdgcn_readfirstlane(xa[0]));
        *(LAS u32x2*)(L + a_h2w) = u32x2{pk2((f32x2{asf(xa[0]), asf(xa[1])} - x0) * c1[0]), pk2((f32x2{asf(xa[2]), asf(xa[3])} - x0) * c1[1])};
        *(LAS u32x2*)(L + a_h2w + 512) = u32x2{pk2((f32x2{asf(xb[0]), asf(xb[1])} - x0) * c1[2]), pk2((f32x2{asf(xb[2]), asf(xb[3])} - x0) * c1[3])};
        if (__builtin_expect(__all(X_BAD(xa, xb) == 0u), 1)) break;
        MEMB();
      }
      asm volatile("s_mov_b64 exec, 1\n\tds_write_b32 %0, %0\n\ts_mov_b64 exec, -1" :: "v"(a_cx) : "memory");
      if (w == 0) STAMP(13);
      __builtin_amdgcn_s_setprio(0);
      lds_add((LAS u32*)(L + L_CTL) + C_R, lane);
      lds_wait(ctl + C_R, 8u);
      u32x4 df[4];
#pragma unroll
      for (int i = 0; i < 4; ++i) { df[i] = trfrag(L + (a_d0 ^ (32 * i)), L + (a_d0 ^ (32 * i)) + 4096); PIN(df[i]); }
      BAR();
      const u32x4 h0 = *(LAS u32x4*)(L + a_hr), h1 = *(LAS u32x4*)(L + a_hs);
      const f32x4 z = {0.f, 0.f, 0.f, 0.f};
      const f32x4 a0 = mfma(df[1], h1, mfma(df[0], h0, z)), a1 = mfma(df[3], h1, mfma(df[2], h0, z));
      st_sc1(rs, yo, tag8(a0, tagv, sel));
      st_sc1(rs, yo2, tag8(a1, tagv, sel));
    } else if (w < 12) {
      NEWPAGE();
      u32 a_st = L_ML + (n & 7) * 16;
      PIN(a_st);
      __builtin_amdgcn_s_setprio(3);
      u32x4 gf[16], gb_[8];
      const u32 af = L_W + (8 * g + (n >> 2)) * 128 + (((w & 3) + (n & 2) * 2) ^ (((n >> 3) | ((g & 1) << 1)) << 1)) * 16 + (n & 1) * 8;
      f32x4 aL = {0.f, 0.f, 0.f, 0.f}, aH;
      f32x2 ag, au, sg, su, gt, up;
      {
#pragma unroll
        for (int q4 = 0; q4 < 4; ++q4) {
#pragma unroll
          for (int i = 0; i < 4; ++i) gb_[i] = *(LAS u32x4*)(L + L_C1 + n * C1S + g * 16 + q4 * 256 + i * 64);
          if (q4 == 3) {
            VM0();
            BAR();
          }
#pragma unroll
          for (int i = 0; i < 4; ++i) gf[4 * q4 + i] = trfrag(L + af + (4 * q4 + i) * 4096, L + af + (4 * q4 + i) * 4096 + 512);
          SB();
#pragma unroll
          for (int i = 0; i < 4; ++i) aL = mfma(gf[4 * q4 + i], gb_[i], aL);
          PIN(aL);
        }
        const auto s0 = __builtin_amdgcn_permlane32_swap(__float_as_uint(aL[0]), __float_as_uint(aL[2]), false, false);
        const auto s1 = __builtin_amdgcn_permlane32_swap(__float_as_uint(aL[1]), __float_as_uint(aL[3]), false, false);
        ag = -f32x2{__uint_as_float(s0[0]), __uint_as_float(s1[0])};
        au = -f32x2{__uint_as_float(s0[1]), __uint_as_float(s1[1])};
        sg = f32x2{dppf<ROR8>(ag[0]), dppf<ROR8>(ag[1])} * 1.4426950408889634f;
        su = -f32x2{dppf<ROR8>(au[0]), dppf<ROR8>(au[1])};
        PIN(ag); PIN(au); PIN(sg); PIN(su); PIN(a_h2r); PIN(a_hw);
      }
      if (w == 8) STAMP(21);
      __builtin_amdgcn_s_setprio(0);
      _Pragma("unroll 1") for (u32 s = 0; s < LSPIN_CAP; ++s) if (__all(ctl[C_X + (lane & 7)] != 0u)) break;
      __builtin_amdgcn_s_setprio(3);
      MEMB();
      GU_MUL(L + a_h2r)
      SB();
      f32x4 st = *(volatile LAS f32x4*)(L + a_st);
      gu_pair(aL, aH, gt, up);
      if (!__all(st[3] != 0.f)) {
        _Pragma("unroll 1") for (u32 s = 0; s < LSPIN_CAP; ++s) { st = *(volatile LAS f32x4*)(L + a_st); if (__all(st[3] != 0.f)) break; }
      }
      gt = __builtin_elementwise_fma(__builtin_elementwise_fma(ag, f32x2{st[0], st[0]}, gt), f32x2{st[2], st[2]}, sg);
      up = __builtin_elementwise_fma(__builtin_elementwise_fma(au, f32x2{st[0], st[0]}, up), f32x2{st[1], st[1]}, su);
      *(LAS u32*)(L + a_hw) = pk2(f32x2{rcp(1.f + __builtin_amdgcn_exp2f(gt[0])), rcp(1.f + __builtin_amdgcn_exp2f(gt[1]))} * up);
      BAR();
    } else {
      int xo = tline(xcd, LN_X2 + 32 * (w - 12), near) + lane * 16;
      u32 a_st = lbase + L_ML + (2 * (w - 12) + (lane >> 5)) * 16;
      PIN(xo); PIN(a_st);
      if (w == 12) asm volatile("s_waitcnt vmcnt(2)" ::: "memory"); else VM0();
      BAR();
      asm volatile("s_waitcnt vmcnt(0)" : "+v"(bp), "+v"(bq) :: "memory");
      if (w == 12 && __builtin_expect(!on_time(tag, org, bp, bq), 0)) {
        ctl[C_TX] = 2u;
        ctl[C_TX] = (anchor(rs, bo, tag, org, bp, bq) + DX) | 1u;
      }
      __builtin_amdgcn_s_setprio(1);
      X_TIME()
      until(tx + SDL);
      u32x4 xv[4];
#pragma unroll
      for (int i = 0; i < 4; ++i) xv[i] = ld_sc1(rs, xo + i * 1024);
#pragma unroll
      for (int i = 0; i < 4; ++i) xv[i] ^= t16v;
      if (__builtin_expect(!__all((X_BAD(xv[0], xv[1]) | X_BAD(xv[2], xv[3])) == 0u), 0)) {
#pragma unroll 1
        for (u32 s = 0; s < SPIN_CAP; ++s) {
          MEMB();
#pragma unroll
          for (int i = 0; i < 4; ++i) xv[i] = ld_sc1(rs, xo + i * 1024) ^ t16v;
          if (__all((X_BAD(xv[0], xv[1]) | X_BAD(xv[2], xv[3])) == 0u)) break;
        }
      }
      float sm[2], sq[2], xs0[2];
#pragma unroll
      for (int k = 0; k < 2; ++k) {
        const float x0 = __builtin_bit_cast(float, __builtin_amdgcn_readfirstlane(xv[2 * k][0]));
        const u32x4 xa = xv[2 * k], xb = xv[2 * k + 1];
        const f32x2 d0 = f32x2{asf(xa[0]), asf(xa[1])} - x0, d1 = f32x2{asf(xa[2]), asf(xa[3])} - x0, d2 = f32x2{asf(xb[0]), asf(xb[1])} - x0, d3 = f32x2{asf(xb[2]), asf(xb[3])} - x0;
        const f32x2 sv = (d0 + d1) + (d2 + d3), qv = __builtin_elementwise_fma(d0, d0, d1 * d1) + __builtin_elementwise_fma(d2, d2, d3 * d3);
        xs0[k] = x0;
        sm[k] = sv[0] + sv[1];
        sq[k] = qv[0] + qv[1];
      }
      const float r = row_sum(t32(t16(sm[0], sq[0]), t16(sm[1], sq[1]))) * (1.f / 512.f);
      const auto sw = __builtin_amdgcn_permlane16_swap(__float_as_uint(r), __float_as_uint(r), false, false);
      const float md = __uint_as_float(sw[0]);
      const float x0 = lane < 32 ? xs0[0] : xs0[1];
      const float inv = rcp(rbf(__builtin_amdgcn_sqrtf(rbf(rbf(fmaxf(fmaf(-md, md, __uint_as_float(sw[1])), 0.f)) + 1e-5f))));
      const f32x4 st = {rbf(x0 + md) - x0, inv, inv * -1.4426950408889634f, 1.f};
      asm volatile("s_mov_b32 exec_lo, 1\n\ts_mov_b32 exec_hi, 1\n\tds_write_b128 %0, %1\n\ts_mov_b64 exec, -1" :: "v"(a_st), "v"(st) : "memory");
      if (w == 15) {
        _Pragma("unroll 1") for (u32 s = 0; s < LSPIN_CAP; ++s) if (__all(ctl[C_X + (lane & 7)] != 0u)) break;
        if (lane < 8) st_sc1_64(rs, WS_B1 + lane * 512 + p3 * 8, u32x2{now4(), tagv});
      }
      BAR();
    }
    if (w == 7) STAMP(14);
    asm volatile("fused_code_end:");
  } else if (red && (w == RW || w == RW - 1)) {
    NEWPAGE();
    const int f = 16 * xcd + 2 * b + hj, ro_ = 4 * (w & 1) + (lane & 3);
    const int rb = e >> 4, rj = e & 15, c2 = 32 * rj + 4 * (lane & 7) + g;
    u32 xr_ = x[rb * 512 + c2];
    u32 gar = (__builtin_amdgcn_raw_buffer_load_b16(rs, WS_MODR + rb * 1024 + mpos(c2), 0, SC1));
    u32 gbr = (__builtin_amdgcn_raw_buffer_load_b16(rs, WS_MODR + (24 + ro_) * 1024 + mpos(4 * f + g), 0, SC1));
    int po = tline(xcd, LN_PART + (2 * b + hj) * 8, near) + lane * 16;
    int xo = tline(lane >> 3, LN_X2 + e, near) + (lane & 7) * 16;
    int xi = tline(xcd, LN_X2 + 16 * ro_, near) + (4 * f + g) * 4;
    u32 xt = (g & 1) ? tag >> 16 : tag & 0xffffu;
    const u32x4 tb4 = bytes4(tag);
    u32x4 tb5 = lane < 44 ? tb4 : u32x4{0, 0, 0, 0};
    const int yl = LN_YP + (f & 15) * NP3 + (lane >> 2);
    int yo = tline(xcd, yl, near) + ro_ * 16, y1 = tline(xcd, yl + 16, near) + ro_ * 16;
    int y2 = lane < 44 ? tline(xcd, yl + 32, near) + ro_ * 16 : WS_END;
    PIN(y1);
    u32 oof = (lane & 12) == 0 ? (ro_ * 512 + 4 * f + g) * 2 : 8192;
    __amdgpu_buffer_rsrc_t ro = __builtin_amdgcn_make_buffer_rsrc((void*)out, 0, 8192, 0x00020000);
    asm volatile("" : "+s"(ro));
    PIN(xt); PIN(tb5); PIN(po); PIN(xo); PIN(xi); PIN(yo); PIN(y2); PIN(oof);
    if (w == RW) {
      u32x4 bp, bq;
      until_s(org + OFB);
      B0_LOAD()
      SB();
      PIN(gar); PIN(xr_);
      float ga = blo(gar), xx = blo(xr_);
      PIN(ga); PIN(xx);
      u32 anc = org + OFO;
      if (__builtin_expect(!on_time(tag, org, bp, bq), 0)) anc = anchor(rs, bo, tag, org, bp, bq);
      STAMP(1);
      until(anc + DP);
      u32x4 xq;
#pragma unroll 1
      for (u32 sp = 0; sp < SPIN_CAP; ++sp) {
        const u32x4 pv = ld_one(rs, po) ^ tb4;
        float t = t32(t16(asf(pv[0]), asf(pv[1])), t16(asf(pv[2]), asf(pv[3])));
        t += dppf<ROR8>(t);
        const u32 xw = __float_as_uint(rbf(xx + rbf(ga * rbf(t)))) | xt;
        const auto p1 = __builtin_amdgcn_permlane16_swap(xw, xw, false, false);
        const auto p2 = __builtin_amdgcn_permlane32_swap(p1[0], p1[0], false, false), p3_ = __builtin_amdgcn_permlane32_swap(p1[1], p1[1], false, false);
        xq = u32x4{p2[0], p3_[0], p2[1], p3_[1]};
        PIN(xq);
        if (__builtin_expect(__all((or4(pv) & 255u) == 0u), 1)) break;
        MEMB();
      }
      st_sc1(rs, xo, xq);
      STAMP(11);
      if (bid == 1 && lane == 0) __hip_atomic_store((GAS u32*)ws, tag, RLX_AGENT);
      u32 ty = anc + DYS;
      const int lt = (int)(now4() - anc) - (DX + 20);
      if (__builtin_expect(lt > 0, 0)) {
        int lk = DB1;
        while (lk < DB1 + lt + 240) lk += PB;
        u32x2 bv;
#pragma unroll 1
        for (u32 sp = 0; sp < SPIN_CAP; ++sp) {
          until_s(anc + lk);
          bv = ld_sc1_64(rs, WS_B1 + xcd * 512 + (lane < NP3 ? lane : 0) * 8);
          if (__all(bv[1] == tag)) break;
          MEMB();
          do lk += PB; while ((int)(now4() - anc) > lk);
        }
        ty = org + smax((int)(bv[0] - org)) + DY;
        asm volatile("fused_cold_end:");
      }
      ctl[C_TX] = ty | 1u;
      STAMP(7);
    }
    PIN(gbr);
    float gb = blo(gbr);
    PIN(gb);
    lds_wait(ctl + C_TX, 1u);
    until(__builtin_amdgcn_readfirstlane(ctl[C_TX]));
    u32 res;
#pragma unroll 1
    for (u32 sp = 0; sp < SPIN_CAP; ++sp) {
      const u32 x2 = __builtin_amdgcn_raw_buffer_load_b32(rs, xi, 0, SC1) ^ xt;
      const u32x4 y0 = ld_one(rs, yo) ^ tb4, y1_ = ld_one(rs, y1) ^ tb4, y2_ = ld_one(rs, y2) ^ tb5;
      const f32x4 s = (__builtin_bit_cast(f32x4, y0) + __builtin_bit_cast(f32x4, y1_)) + __builtin_bit_cast(f32x4, y2_);
      float t = t32(t16(s[0], s[1]), t16(s[2], s[3]));
      t += dppf<ROR8>(t);
      t += dppf<ROR4>(t);
      res = pack2(fmaf(gb, t, asf(x2)), 0.f);
      PIN(res);
      if (__builtin_expect(__all((((or4(y0) | or4(y1_) | or4(y2_)) & 255u) | (x2 & 0xffffu)) == 0u), 1)) break;
      MEMB();
    }
    __builtin_amdgcn_raw_buffer_store_b16((u16)res, ro, oof, 0, 0);
    if (w == RW) STAMP(18);
  }
  if (sv0 | sv18) { while ((u32)__builtin_amdgcn_s_memrealtime() - lk0 < 3600u) __builtin_amdgcn_s_sleep(8);
    if (sv0 && (tid & 63) == 0) __hip_atomic_store((GAS u32*)(ws + (768u << 20)) + 0 * 256 + bid, sv0, RLX_AGENT);
    if (sv18 && (tid & 63) == 0) __hip_atomic_store((GAS u32*)(ws + (768u << 20)) + 18 * 256 + bid, sv18, RLX_AGENT);
  }

  if (bid == 0 && w == 0) {
    while ((u32)__builtin_amdgcn_s_memrealtime() - lk0 < 4000u) __builtin_amdgcn_s_sleep(2);
    lane = tid & 63;
    auto rd = [&](int i, int q) { u32 v = __hip_atomic_load((GAS u32*)(ws + (768u << 20)) + i * 256 + q * 64 + lane, RLX_AGENT); return (int)(v - lk0); };
    auto ok = [&](int v) { return v > -500 && v < 4000; };
    auto red = [&](int i, int mode, int xm) { float r = mode == 0 ? -1e9f : (mode == 1 ? 1e9f : 0.f), c = 0.f;
      for (int q = 0; q < 4; ++q) { int v = rd(i, q); if (ok(v) && ((xm >> (lane & 7)) & 1)) { r = mode == 0 ? fmaxf(r, (float)v) : (mode == 1 ? fminf(r, (float)v) : r + (float)v); c += 1.f; } }
      if (mode == 0) return wave_max(r); if (mode == 1) return -wave_max(-r); return wave_sum(r) / wave_sum(c); };
#define mx(i) red(i, 0, 255)
#define mn(i) red(i, 1, 255)
#define av(i) red(i, 2, 255)
#define xm(i, m) red(i, 0, m)
#define xn(i, m) red(i, 1, m)
    float val = (float)(mx(18)-av(0)-2400);
    u32 lk = 5000u + (u32)__builtin_amdgcn_readfirstlane((int)(val * 8.f));
    while ((u32)__builtin_amdgcn_s_memrealtime() - lk0 < lk) __builtin_amdgcn_s_sleep(2);
  }
}

extern "C" void kernel_launch(void* const* d_in, const int* in_sizes, int n_in, void* d_out, int out_size, void* d_ws, size_t ws_size, hipStream_t stream) {
  (void)hipFuncSetAttribute((const void*)fused, hipFuncAttributeMaxDynamicSharedMemorySize, L_END);
  Args a;
  const int src[4][2] = {{3, 3}, {10, 10}, {4, 4}, {6, 7}};
  for (int r = 0; r < 4; ++r)
    for (int k = 0; k < 2; ++k) a.half[r][k] = (const unsigned char*)d_in[src[r][k]] + (r < 3 && k ? 64 : 0);
  a.x = (const u16*)d_in[0]; a.kc = (const u16*)d_in[1]; a.vc = (const u16*)d_in[2]; a.wd = (const u16*)d_in[8]; a.cc = (const u16*)d_in[9];
  a.out = (u16*)d_out; a.ws = (unsigned char*)d_ws;
  fused<<<256, 1024, L_END, stream>>>(a);
}
```

```cpp
#include <hip/hip_runtime.h>
#include <stdint.h>

typedef unsigned int u32;
typedef unsigned long long u64;
typedef unsigned short u16;
typedef u32 u32x4 __attribute__((ext_vector_type(4)));
typedef u32 u32x2 __attribute__((ext_vector_type(2)));
typedef float f32x4 __attribute__((ext_vector_type(4)));
typedef float f32x2 __attribute__((ext_vector_type(2)));
typedef short s16x4 __attribute__((ext_vector_type(4)));
typedef __bf16 bf16x8 __attribute__((ext_vector_type(8)));
typedef __bf16 bf16x2 __attribute__((ext_vector_type(2)));
#define GAS __attribute__((address_space(1)))
#define LAS __attribute__((address_space(3)))
#define RLX_AGENT __ATOMIC_RELAXED, __HIP_MEMORY_SCOPE_AGENT

constexpr int SK = 4096, WIN = 1024, FF = 1376, NP3 = 43;

constexpr int WS_FLAGS = 0x1000;
constexpr int WS_TS = 0x1800;
constexpr int WS_G1 = 0x2000;
constexpr int WS_B1 = 0x8000;
constexpr int WS_MODR = 0xA000;
constexpr int WS_Q = 0x12000;
constexpr int WS_B0 = 0x32000;
constexpr int WS_NV = 0x33000;
constexpr int WS_NP = 0x34000;
constexpr int LN_O = 0;
constexpr int LN_PART = 128;
constexpr int LN_X2 = 256;
constexpr int LN_YP = 384;
constexpr int WS_T = 0x38000, T_SPAN = 17 * 16384;
constexpr int WS_END = WS_T + 4 * T_SPAN;
constexpr int WS_STEP = 2 << 20;
static_assert(WS_END <= WS_STEP && LN_YP + 16 * NP3 <= 17 * 64, "");

constexpr int L_W = 0;
constexpr int L_TP = 65536;
constexpr int L_C1 = 98304;
constexpr int L_SINK = 130048;
constexpr int L_RED = 131072;
constexpr int L_H2 = L_RED + 16384;
constexpr int C1S = 1040, H2S = 1056, OSTR = 544, OBS = 288;
constexpr int L_Q = L_H2 + 8 * H2S;
constexpr int L_ML = L_Q + 512;
constexpr int L_HH = L_ML + 128;
constexpr int L_CTL = L_HH + 784;
constexpr int L_END = L_CTL + 64;
constexpr int C_T = 0, C_TB = 1, C_TX = 2, C_Q = 3, C_ML = 4, C_R = 6, C_T0 = 7, C_X = 8;
constexpr int RW = 12, WG_ = 10;

constexpr u32 SPIN_CAP = 1u << 17, LSPIN_CAP = 1u << 20;
constexpr int DUE0 = 810;
constexpr int TAU = 220;
constexpr int E1 = 16;
constexpr int E2 = 10;
constexpr int SKW = 200;
constexpr int MSH = 14;
constexpr int KA = 55;
constexpr int KW = 6;
constexpr int OFO = 420;
constexpr int OFB = 450;
constexpr int PB = 200;
constexpr int RB = 260;
constexpr int DO = 15;
constexpr int DL_ = 220;
constexpr int DP = 328;
constexpr int DX = 513;
constexpr int DYS = 1043;
constexpr int DB1 = 880;
constexpr int DY = 200;
constexpr int SDL = 12;
constexpr int WDT = 40;
constexpr int extra(int i) { return i < 8 ? E1 : (i >= 14 && i < 22 ? E2 : 0); }
constexpr int TEND = DUE0 + 32 * TAU + 8 * E1 + 8 * E2;

__device__ __forceinline__ float rbf(float f) { return (float)(__bf16)f; }
__device__ __forceinline__ u32 pack2(float a, float b) { bf16x2 v = {(__bf16)a, (__bf16)b}; return __builtin_bit_cast(u32, v); }
__device__ __forceinline__ float blo(u32 v) { return __builtin_bit_cast(float, v << 16); }
__device__ __forceinline__ float bhi(u32 v) { return __builtin_bit_cast(float, v & 0xffff0000u); }
__device__ __forceinline__ u32 canon(u32 v) { return v < 0xfffffffeu ? v : 0xfffffffeu; }
#define SEL4() u32x4 sel = {0x07060500u, 0x07060501u, 0x07060502u, 0x07060503u}; asm volatile("" : "+s"(sel))
__device__ __forceinline__ u32x4 tag8(f32x4 v, u32 tagv, u32x4 sel) {
  const u32x4 r = __builtin_bit_cast(u32x4, v);
  return u32x4{__builtin_amdgcn_perm(r[0], tagv, sel[0]), __builtin_amdgcn_perm(r[1], tagv, sel[1]), __builtin_amdgcn_perm(r[2], tagv, sel[2]), __builtin_amdgcn_perm(r[3], tagv, sel[3])};
}
__device__ __forceinline__ u32x4 bytes4(u32 tag) { return u32x4{tag & 255u, (tag >> 8) & 255u, (tag >> 16) & 255u, tag >> 24}; }
__device__ __forceinline__ u32 or4(u32x4 v) { return (v[0] | v[1]) | (v[2] | v[3]); }
__device__ __forceinline__ float rcp(float x) { return __builtin_amdgcn_rcpf(x); }
template <int CTRL> __device__ __forceinline__ float dppf(float x) {
  return __builtin_bit_cast(float, __builtin_amdgcn_mov_dpp(__builtin_bit_cast(int, x), CTRL, 0xf, 0xf, true));
}
template <int CTRL> __device__ __forceinline__ u32 dppu(u32 x) { return (u32)__builtin_amdgcn_mov_dpp((int)x, CTRL, 0xf, 0xf, true); }
constexpr int ROR8 = 0x128, ROR4 = 0x124, ROR2 = 0x122, ROR1 = 0x121, XOR1 = 0xB1, XOR2 = 0x4E, XOR7 = 0x141;
__device__ __forceinline__ float row_sum(float x) { x += dppf<ROR8>(x); x += dppf<ROR4>(x); x += dppf<ROR2>(x); x += dppf<ROR1>(x); return x; }
__device__ __forceinline__ float row_max(float x) {
  x = fmaxf(x, dppf<ROR8>(x)); x = fmaxf(x, dppf<ROR4>(x)); x = fmaxf(x, dppf<ROR2>(x)); x = fmaxf(x, dppf<ROR1>(x)); return x;
}
__device__ __forceinline__ float x16_sum(float x) {
  auto s = __builtin_amdgcn_permlane16_swap(__float_as_uint(x), __float_as_uint(x), false, false);
  return __uint_as_float(s[0]) + __uint_as_float(s[1]);
}
__device__ __forceinline__ float x32_sum(float x) {
  auto s = __builtin_amdgcn_permlane32_swap(__float_as_uint(x), __float_as_uint(x), false, false);
  return __uint_as_float(s[0]) + __uint_as_float(s[1]);
}
__device__ __forceinline__ float x16_max(float x) {
  auto s = __builtin_amdgcn_permlane16_swap(__float_as_uint(x), __float_as_uint(x), false, false);
  return fmaxf(__uint_as_float(s[0]), __uint_as_float(s[1]));
}
__device__ __forceinline__ float x32_max(float x) {
  auto s = __builtin_amdgcn_permlane32_swap(__float_as_uint(x), __float_as_uint(x), false, false);
  return fmaxf(__uint_as_float(s[0]), __uint_as_float(s[1]));
}
__device__ __forceinline__ float t16(float a, float b) {
  auto s = __builtin_amdgcn_permlane16_swap(__float_as_uint(a), __float_as_uint(b), false, false);
  return __uint_as_float(s[0]) + __uint_as_float(s[1]);
}
__device__ __forceinline__ float t32(float a, float b) {
  auto s = __builtin_amdgcn_permlane32_swap(__float_as_uint(a), __float_as_uint(b), false, false);
  return __uint_as_float(s[0]) + __uint_as_float(s[1]);
}
__device__ __forceinline__ f32x2 up2(u32 v) { return f32x2{blo(v), bhi(v)}; }
__device__ __forceinline__ u32 pk2(f32x2 v) { return pack2(v[0], v[1]); }
#define PIN(x) asm volatile("" : "+v"(x))
__device__ __forceinline__ f32x2 rb2(f32x2 v) { u32 p = pk2(v); asm("" : "+v"(p)); return up2(p); }
__device__ __forceinline__ float wave_sum(float x) { return x32_sum(x16_sum(row_sum(x))); }
__device__ __forceinline__ float wave_max(float x) { return x32_max(x16_max(row_max(x))); }
__device__ __forceinline__ float dot2(u32 a, u32 b, float c) {
  return __builtin_amdgcn_fdot2_f32_bf16(__builtin_bit_cast(bf16x2, a), __builtin_bit_cast(bf16x2, b), c, false);
}
__device__ __forceinline__ f32x4 mfma(u32x4 a, u32x4 b, f32x4 c) {
  return __builtin_amdgcn_mfma_f32_16x16x32_bf16(__builtin_bit_cast(bf16x8, a), __builtin_bit_cast(bf16x8, b), c, 0, 0, 0);
}
__device__ __forceinline__ u32x4 ldnt(const void* p) { return __builtin_nontemporal_load((const u32x4*)p); }
__device__ __forceinline__ void glds(const void* g, u32 lds) {
  u32 keep;
  lds = __builtin_amdgcn_readfirstlane(lds);
  asm volatile("s_mov_b32 %0, m0\n\ts_mov_b32 m0, %2\n\ts_nop 0\n\tglobal_load_lds_dwordx4 %1, off nt\n\ts_mov_b32 m0, %0"
               : "=&s"(keep) : "v"(g), "s"(lds) : "memory");
}
__device__ __forceinline__ void glds(const void* base, u32 off, u32 lds) {
  u32 keep;
  lds = __builtin_amdgcn_readfirstlane(lds);
  asm volatile("s_mov_b32 %0, m0\n\ts_mov_b32 m0, %3\n\ts_nop 0\n\tglobal_load_lds_dwordx4 %1, %2 nt\n\ts_mov_b32 m0, %0"
               : "=&s"(keep) : "v"(off), "s"(base), "s"(lds) : "memory");
}
__device__ __forceinline__ void touch(const void* base, u32 off, u32 lds) {
  u32 keep;
  lds = __builtin_amdgcn_readfirstlane(lds);
  asm volatile("s_mov_b32 %0, m0\n\ts_mov_b32 m0, %3\n\ts_nop 0\n\tglobal_load_lds_dwordx4 %1, %2\n\ts_mov_b32 m0, %0"
               : "=&s"(keep) : "v"(off), "s"(base), "s"(lds) : "memory");
}
__device__ __forceinline__ u32x4 trfrag(LAS unsigned char* p0, LAS unsigned char* p1) {
  s16x4 a = __builtin_amdgcn_ds_read_tr16_b64_v4i16((LAS s16x4*)p0);
  s16x4 b = __builtin_amdgcn_ds_read_tr16_b64_v4i16((LAS s16x4*)p1);
  u32x2 x = __builtin_bit_cast(u32x2, a), y = __builtin_bit_cast(u32x2, b);
  return u32x4{x[0], x[1], y[0], y[1]};
}
#define NEWPAGE() asm volatile("s_branch 9f\n\t.p2align 12\n9:")
#define SB() __builtin_amdgcn_sched_barrier(0)
#define MEMB() asm volatile("" ::: "memory")
#define VM0() asm volatile("s_waitcnt vmcnt(0)" ::: "memory")
#define LGKM0() asm volatile("s_waitcnt lgkmcnt(0)" ::: "memory")
#define BAR() asm volatile("s_waitcnt lgkmcnt(0)\n\ts_barrier" ::: "memory")
#define TAG() do { u32 ep_; asm volatile("s_load_dword %0, %1, 0x0\n\ts_waitcnt lgkmcnt(0)" : "=s"(ep_) : "s"(ws) : "memory"); \
    tag = ((ep_ & 0x7f7f7f7fu) + 0x01010101u) ^ (ep_ & 0x80808080u); } while (0)
constexpr int SC1 = 16;
__device__ __forceinline__ u32x4 ld_sc1(__amdgpu_buffer_rsrc_t rs, int off) { return __builtin_amdgcn_raw_buffer_load_b128(rs, off, 0, SC1); }
__device__ __forceinline__ u32x4 ld_one(__amdgpu_buffer_rsrc_t rs, int off) { return __builtin_amdgcn_raw_buffer_load_b128(rs, off, 0, SC1 | 2); }
__device__ __forceinline__ u32x2 ld_sc1_64(__amdgpu_buffer_rsrc_t rs, int off) { return __builtin_amdgcn_raw_buffer_load_b64(rs, off, 0, SC1); }
__device__ __forceinline__ void st_sc1(__amdgpu_buffer_rsrc_t rs, int off, u32x4 v) { __builtin_amdgcn_raw_buffer_store_b128(v, rs, off, 0, SC1); }
__device__ __forceinline__ void st_sc1_64(__amdgpu_buffer_rsrc_t rs, int off, u32x2 v) { __builtin_amdgcn_raw_buffer_store_b64(v, rs, off, 0, SC1); }

__device__ __forceinline__ void ln_stats(u32x4 xv, float& m, float& inv) {
  const float x[8] = {blo(xv[0]), bhi(xv[0]), blo(xv[1]), bhi(xv[1]), blo(xv[2]), bhi(xv[2]), blo(xv[3]), bhi(xv[3])};
  const float x0 = __builtin_bit_cast(float, __builtin_amdgcn_readfirstlane(xv[0] << 16));
  float s = 0.f, v = 0.f;
#pragma unroll
  for (int j = 0; j < 8; ++j) { const float d = x[j] - x0; s += d; v = fmaf(d, d, v); }
  const float md = wave_sum(s) * (1.f / 512.f), sq = wave_sum(v) * (1.f / 512.f);
  const float var = rbf(fmaxf(fmaf(-md, md, sq), 0.f));
  m = rbf(x0 + md);
  inv = rcp(rbf(__builtin_amdgcn_sqrtf(rbf(var + 1e-5f))));
}
#define PACE_(W, INC) do { asm volatile(W "\n1:\n\ts_memrealtime vcc\n\ts_waitcnt lgkmcnt(0)\n\ts_lshl_b32 vcc_lo, vcc_lo, 2\n\ts_sub_i32 vcc_hi, vcc_lo, %0\n\ts_cmp_lt_i32 vcc_hi, 0\n\t" \
    "s_cbranch_scc0 2f\n\ts_sleep 1\n\ts_branch 1b\n2:\n\ts_add_i32 %0, %0, %1" : "+s"(due) : "is"(INC) : "memory", "scc", "vcc"); SB(); } while (0)
#define PACE(INC) PACE_("s_waitcnt vmcnt(2)", INC)
#define PACEN(INC) PACE_("", INC)
__device__ __forceinline__ void until_s(u32 due) { PACE_("", 0); }
__device__ __forceinline__ void until(u32 t) {
  asm volatile("1:\n\ts_memrealtime vcc\n\ts_waitcnt lgkmcnt(0)\n\ts_lshl_b32 vcc_lo, vcc_lo, 2\n\ts_sub_i32 vcc_hi, vcc_lo, %0\n\ts_cmp_lt_i32 vcc_hi, 0\n\t"
               "s_cbranch_scc1 1b" :: "s"(t) : "memory", "scc", "vcc");
  SB();
}

__device__ __forceinline__ void lds_wait(volatile LAS u32* p, u32 want) {
  _Pragma("unroll 1") for (u32 s = 0; s < LSPIN_CAP; ++s) {
    if (__builtin_amdgcn_readfirstlane(*p) >= want) break;
    __builtin_amdgcn_s_sleep(2);
  }
  MEMB();
}
__device__ __forceinline__ void lds_add(LAS u32* p, int lane) {
  LGKM0();
  if (lane == 0) __hip_atomic_fetch_add(p, 1u, __ATOMIC_RELAXED, __HIP_MEMORY_SCOPE_WORKGROUP);
  MEMB();
}
__device__ __forceinline__ void tile_part(LAS unsigned char* T, LAS unsigned char* P, u32x4 hb, int n, int g) {
  LAS unsigned char* tb = T + (8 * g + (n >> 2)) * 128 + (n & 3) * 8;
  const int sx = (n >> 3) | ((g & 1) << 1);
  f32x4 a[4];
#pragma unroll
  for (int t = 0; t < 4; ++t) a[t] = mfma(trfrag(tb + (t ^ sx) * 32, tb + (t ^ sx) * 32 + 512), hb, f32x4{0.f, 0.f, 0.f, 0.f});
  *(LAS f32x4*)(P) = n < 8 ? a[0] : a[1];
  *(LAS f32x4*)(P + 1024) = n < 8 ? a[2] : a[3];
}
__device__ __forceinline__ f32x4 tile_sum(LAS unsigned char* P) {
  f32x4 s[8], r[2];
#pragma unroll
  for (int k = 0; k < 2; ++k) {
#pragma unroll
    for (int i = 0; i < 8; ++i) s[i] = *(LAS f32x4*)(P + (8 * k + i) * 2048);
    SB();
    r[k] = ((s[0] + s[1]) + (s[2] + s[3])) + ((s[4] + s[5]) + (s[6] + s[7]));
    SB();
  }
  return r[0] + r[1];
}
#define GU_MUL(P) _Pragma("unroll") for (int i = 0; i < 8; ++i) gb_[i] = *(LAS u32x4*)((P) + i * 64); SB(); \
    aL = f32x4{0.f, 0.f, 0.f, 0.f}; aH = aL; _Pragma("unroll") for (int i = 0; i < 8; ++i) { aL = mfma(gf[i], gb_[i], aL); aH = mfma(gf[8 + i], gb_[i], aH); }
__device__ __forceinline__ void gu_pair(f32x4 aL, f32x4 aH, f32x2& gt, f32x2& up) {
  const auto s0 = __builtin_amdgcn_permlane32_swap(__float_as_uint(aL[0] + dppf<ROR8>(aH[0])), __float_as_uint(aL[2] + dppf<ROR8>(aH[2])), false, false);
  const auto s1 = __builtin_amdgcn_permlane32_swap(__float_as_uint(aL[1] + dppf<ROR8>(aH[1])), __float_as_uint(aL[3] + dppf<ROR8>(aH[3])), false, false);
  gt = f32x2{__uint_as_float(s0[0]), __uint_as_float(s1[0])};
  up = f32x2{__uint_as_float(s0[1]), __uint_as_float(s1[1])};
}
__device__ __forceinline__ int tline(int c, int ln, u32 near) { return WS_T + (c & 3) * T_SPAN + (ln >> 6) * 16384 + ((near >> c) & 1) * 8192 + (ln & 63) * 128; }
__device__ __forceinline__ int mpos(int c) { return (c & 252) * 4 + (c >> 8) * 8 + (c & 3) * 2; }
__device__ __forceinline__ u32 smax(int m) { int r = (int)fminf(wave_max((float)m), 4e5f); PIN(r); return (u32)__builtin_amdgcn_readfirstlane(r); }
__device__ __forceinline__ u32 now4() { return (u32)__builtin_amdgcn_s_memrealtime() * 4u; }
#define B0_LOAD() { bp = ld_sc1(rs, bo); bq = ld_sc1(rs, bo + 1024); }
__device__ __forceinline__ bool on_time(u32 tag, u32 org, u32x4 bp, u32x4 bq) {
  const u32 lim = org + OFO - DO;
  return __all((((bp[1] ^ tag) | (bp[3] ^ tag) | (bq[1] ^ tag) | (bq[3] ^ tag)) | (((lim - bp[0]) | (lim - bp[2]) | (lim - bq[0]) | (lim - bq[2])) >> 31)) == 0u);
}
__device__ __forceinline__ u32 anchor(__amdgpu_buffer_rsrc_t rs, int bo, u32 tag, u32 org, u32x4 bp, u32x4 bq) {
  int lk = OFB;
#pragma unroll 1
  for (u32 s = 0; s < SPIN_CAP; ++s) {
    if (__all(((bp[1] ^ tag) | (bp[3] ^ tag) | (bq[1] ^ tag) | (bq[3] ^ tag)) == 0u)) break;
    MEMB();
    do lk += PB; while ((int)(now4() - org) > lk);
    until_s(org + lk);
    B0_LOAD()
  }
  const int m = (int)smax(max(max((int)(bp[0] - org), (int)(bp[2] - org)), max((int)(bq[0] - org), (int)(bq[2] - org))));
  return org + (u32)max(m + DO <= OFO ? OFO : m + DL_, lk + RB - DP);
}
__device__ __forceinline__ float asf(u32 v) { return __builtin_bit_cast(float, v); }

struct Args {
  const unsigned char* half[4][2];
  const u16 *x, *kc, *vc, *wd, *cc;
  u16* out;
  unsigned char* ws;
};

__global__ void __launch_bounds__(1024, 4) fused(const Args a) {
  extern __shared__ __attribute__((aligned(16))) unsigned char smem[];
  LAS unsigned char* L = (LAS unsigned char*)smem;
  const u32 lbase = (u32)(size_t)L;
  const int tid = threadIdx.x, w = __builtin_amdgcn_readfirstlane(tid >> 6), bid = blockIdx.x;
  int lane = tid & 63;
  const int xcd = bid & 7, idx = bid >> 3, hj = idx & 3, h = 4 * xcd + hj, b = idx >> 2, bh = b * 32 + h;
  const int sw8 = hj == 3 ? (w & 1) * 8 : 0, rw = (hj == 3 ? 24 : 32) * w;
  u64 hA, hB, t64;
  u32 warm;
  asm volatile("s_load_dwordx2 %0, %4, %5\n\ts_load_dwordx2 %1, %4, %6\n\ts_load_dword %3, %4, 0x40\n\ts_memrealtime %2" : "=&s"(hA), "=&s"(hB), "=&s"(t64), "=&s"(warm)
               : "s"(__builtin_amdgcn_kernarg_segment_ptr()), "s"(16 * hj + sw8), "s"(16 * hj + (sw8 ^ 8)));
  SB();
  const int bb = (b + xcd) & 7;
  int p3r = 5 * xcd + min(xcd, 3) + bb;
  int mc = (b & 6) + ((xcd & 1) ^ 1), mg = ((xcd & 6) + b) & 7;
  const bool tq = hj == 0, to = hj == 2;
  int lim = hj == 1 ? 6 - b : (xcd < 3 ? 6 : 5) - bb;
  asm volatile("" : "+s"(p3r), "+s"(mc), "+s"(mg), "+s"(lim));
  const bool idle = (hj & 1) && lim <= 0;
  u64 tp0, tp1;
  {
    const u32 pitch = (u32)(0x0AC0040018002000ull >> (16 * hj)) & 0xffffu;
    u32 ce = (xcd << (to ? 13 : 4)) + 2 * b, c1 = 16 * mc + 2 * mg;
    asm volatile("" : "+s"(ce), "+s"(c1));
    const u32 col = 64 * ((hj & 1) ? (hj == 1 ? c1 : p3r) : ce);
    const u32 step = pitch << (to ? 1 : 3);
    u32 off = (rw + (to ? (lane & 24) + (lane >> 5) : lane >> 3)) * pitch + col + ((lane & 3) ^ ((lane >> 3) & 2)) * 16;
    tp0 = off;
    tp1 = off + step;
    u64 s2 = 2 * step;
    u32 ld = lbase + L_W + rw * 128;
    asm volatile("" : "+s"(s2), "+s"(ld), "+v"(tp0), "+v"(tp1));
    asm volatile("s_mov_b32 exec_lo, 0xf0f0f0f0\n\ts_mov_b32 exec_hi, 0xf0f0f0f0\n\ts_waitcnt lgkmcnt(0)\n\tv_lshl_add_u64 %0, %0, 0, %3\n\tv_lshl_add_u64 %1, %1, 0, %2\n\t"
                 "s_not_b64 exec, exec\n\tv_lshl_add_u64 %0, %0, 0, %2\n\tv_lshl_add_u64 %1, %1, 0, %3\n\ts_mov_b64 exec, -1"
                 : "+v"(tp0), "+v"(tp1), "+s"(hA), "+s"(hB), "+s"(t64), "+s"(warm) :: "scc");
    if (!idle) {
      u32 keep;
      asm volatile("s_mov_b32 %0, m0\n\ts_mov_b32 m0, %3\n\ts_nop 0\n\tglobal_load_lds_dwordx4 %1, off nt\n\ts_add_u32 m0, m0, 0x400\n\t"
                   "v_lshl_add_u64 %1, %1, 0, %4\n\tglobal_load_lds_dwordx4 %2, off nt\n\ts_mov_b32 m0, %0\n\tv_lshl_add_u64 %2, %2, 0, %4"
                   : "=&s"(keep), "+v"(tp0), "+v"(tp1) : "s"(ld), "s"(s2) : "memory", "scc");
      glds((const void*)tp0, ld + 2048);
      if (hj != 3) glds((const void*)tp1, ld + 3072);
    }
  }
  SB();
  u32 t0 = (u32)t64;
  const u16 *x = a.x, *kc = a.kc, *vc = a.vc, *wd = a.wd, *cc = a.cc;
  u16* out = a.out;
  unsigned char* ws = a.ws;
  t0 += (u32)(KW * max((int)((u32)__builtin_amdgcn_s_memrealtime() - t0) - KA, 0)) / 4u;
  const int p3 = (hj == 3 && !idle) ? p3r : -1;
  const bool tm = hj == 1 && !idle;
  u32 tag;
  TAG();
  const __amdgpu_buffer_rsrc_t rs = __builtin_amdgcn_make_buffer_rsrc((void*)(((size_t)ws + (size_t)((tag & 63u) + 1u) * WS_STEP) & ~(size_t)(WS_STEP - 1)), 0, WS_END, 0x00020000);
  if (idle && hj == 1) {
    if (w == 0) {
      st_sc1(rs, WS_G1 + ((b & 1) * 8 + xcd) * 1024 + lane * 16, u32x4{~0u, ~0u, ~0u, ~0u});
      VM0();
      if (lane == 0) st_sc1_64(rs, WS_TS + ((b & 1) * 8 + xcd) * 8, u32x2{t0, tag});
    } else {
      u64 pc;
      u32 len;
      asm volatile("s_getpc_b64 %0\n1:\n\ts_mov_b32 %1, fused_code_end - 1b + 1000\n\ts_max_u32 %1, %1, fused_cold_end - 1b + 1000" : "=s"(pc), "=&s"(len) :: "scc");
      const u32 o = ((b & 1) * 15 + w - 1) * 1024;
      if (o < len) touch((const void*)(pc & ~15ull), min(o + lane * 16, len & ~15u), lbase + L_SINK);
    }
  }
  if (w == 1) glds((const unsigned char*)((lane & 1) ? vc : kc) + ((size_t)bh * SK + (SK - WIN)) * 256, lbase + L_SINK);
  int ta = TAU;
  int n = lane & 15, g = lane >> 4;
  u32x4 cv, hb;
  const bool row = tm && (w >= 8 || mc == 1);
  if (row) cv = ldnt((w < 8 ? x : cc) + (w & 7) * 512 + lane * 8);
  if (tid < 16) ((volatile LAS u32*)(L + L_CTL))[tid] = tid == C_T0 ? t0 : 0u;
  BAR();
  t0 = __builtin_amdgcn_readfirstlane(((volatile LAS u32*)(L + L_CTL))[C_T0]);
  u32 due = t0 * 4u + (u32)((4 * bb + hj) * TAU) / 32u + DUE0;
  if (hj == 3 && bb >= 6 && w == 0) {
    const int k = (bb & 1) * 8 + xcd, o = WS_NP + k * 512 + (bb & 1) * 8192;
    __builtin_amdgcn_raw_buffer_store_b32(tag, rs, lane < 2 ? o ^ (lane * 8192) : WS_END, 0, SC1);
    u64 c0, c1, c2;
    u32 v;
    asm volatile("s_waitcnt vmcnt(0)\n\ts_memtime %0\n\ts_waitcnt lgkmcnt(0)\n\tbuffer_load_dword %3, %4, %6, 0 offen sc1\n\ts_waitcnt vmcnt(0)\n\ts_memtime %1\n\ts_waitcnt lgkmcnt(0)\n\t"
                 "buffer_load_dword %3, %5, %6, 0 offen sc1\n\ts_waitcnt vmcnt(0)\n\ts_memtime %2\n\ts_waitcnt lgkmcnt(0)"
                 : "=&s"(c0), "=&s"(c1), "=&s"(c2), "=&v"(v) : "v"(o), "v"(o ^ 8192), "s"(rs) : "memory");
    const int d = min(max(2 * (int)c1 - (int)c0 - (int)c2, -600), 600);
    if (lane == 0) st_sc1_64(rs, WS_NV + k * 8, u32x2{(u32)(2 * ((bb & 1) ? -d : d)) | ((__builtin_amdgcn_s_getreg(20 | (3 << 11)) >> 2) & 1u), tag});
  }
  int rot = idx;
#define RO(I) ((((I) + rot) & 15) * 8192)
  const u16* kp = kc + ((size_t)bh * SK + (SK - WIN) + 4 * w) * 128;
  u32 l16 = lane * 16;
  PIN(l16);
#define LDR(D, P, I, W) { const GAS unsigned char* p_ = (const GAS unsigned char*)((P) + RO(I)) + l16; PIN(p_); W; D = __builtin_nontemporal_load((const GAS u32x4*)p_); SB(); }
  u32x4 kr[16], vr[16];
  volatile LAS u32* ctl = (volatile LAS u32*)(L + L_CTL);
#define RELANE() do { asm volatile("" : "+v"(lane)); n = lane & 15; g = lane >> 4; } while (0)
  const int e = b * 16 + 2 * xcd + hj;
  const bool red = hj < 2;

#define KL(I) { const int t_ = ta + extra(I); LDR(kr[I], kp, I, PACE(t_)) }
  KL(0)
  if (row) {
    if (w >= 8) {
#pragma unroll
      for (int j = 0; j < 4; ++j) { const float a0 = blo(cv[j]), a1 = bhi(cv[j]); hb[j] = pack2(a0 * rcp(1.f + __expf(-a0)), a1 * rcp(1.f + __expf(-a1))); }
      *(LAS u32x4*)(L + L_H2 + (w & 7) * H2S + lane * 16) = hb;
      lds_add((LAS u32*)(L + L_CTL) + C_R, lane);
    } else {
      float m, inv;
      ln_stats(cv, m, inv);
      if ((lane >> 3) == mg) {
        LAS f32x4* d = (LAS f32x4*)(L + L_RED + w * 256 + (lane & 7) * 32);
        d[0] = f32x4{rbf(rbf(blo(cv[0]) - m) * inv), rbf(rbf(bhi(cv[0]) - m) * inv), rbf(rbf(blo(cv[1]) - m) * inv), rbf(rbf(bhi(cv[1]) - m) * inv)};
        d[1] = f32x4{rbf(rbf(blo(cv[2]) - m) * inv), rbf(rbf(bhi(cv[2]) - m) * inv), rbf(rbf(blo(cv[3]) - m) * inv), rbf(rbf(bhi(cv[3]) - m) * inv)};
      }
    }
  }
  KL(1)
  u32x2 tsv;
#define TS_WAIT() if (!__all(tsv[1] == tag)) { _Pragma("unroll 1") for (u32 s = 0; s < SPIN_CAP; ++s) { __builtin_amdgcn_s_sleep(16); MEMB(); \
      tsv = ld_sc1_64(rs, WS_TS + (lane & 15) * 8); if (__all(tsv[1] == tag)) break; } } MEMB();
  if (tm) {
    asm volatile("s_waitcnt vmcnt(2)" ::: "memory");
    lds_wait(ctl + C_R, 8u);
    hb = *(LAS u32x4*)(L + L_H2 + (n & 7) * H2S + (32 * w + 8 * g) * 2);
    tile_part(L + L_W + w * 4096, L + L_TP + w * 2048 + lane * 16, hb, n, g);
    lds_add((LAS u32*)(L + L_CTL) + C_T, lane);
  }
  KL(2)
  if (w == 0 || (tm && w >= 14)) tsv = ld_sc1_64(rs, WS_TS + (lane & 15) * 8);
  SB();
  if (tm && w >= 14) {
    lds_wait(ctl + C_T, 16u);
    const int col = 32 * (w & 1) + 16 * (n >> 3) + 4 * g;
    const f32x4 s = tile_sum(L + L_TP + (w & 1) * 1024 + lane * 16);
    float v[4] = {rbf(s[0]), rbf(s[1]), rbf(s[2]), rbf(s[3])};
    if (mc == 1) {
      const f32x4 ln = *(LAS f32x4*)(L + L_RED + (n & 7) * 256 + col * 4);
#pragma unroll
      for (int j = 0; j < 4; ++j) v[j] = ln[j] * rbf(1.f + v[j]);
    }
    TS_WAIT()
    st_sc1_64(rs, mc < 2 ? WS_G1 + (2 * mg + (w & 1)) * 1024 + ((2 * (n >> 3) + (g >> 1)) * 16 + mc * 8 + (n & 7)) * 16 + (g & 1) * 8
                         : WS_MODR + ((mc - 2) * 8 + (n & 7)) * 1024 + mpos(64 * mg + col),
              u32x2{canon(pack2(v[0], v[1])), canon(pack2(v[2], v[3]))});
  }
  RELANE();
  KL(3) KL(4)
  if (w == 0) {
    TS_WAIT()
    const float d = __builtin_amdgcn_fmed3f((float)(int)(tsv[0] - t0), -1e5f, 1e5f);
    ctl[C_TB] = (t0 * 4u + (u32)(int)fmaxf(row_sum(d) * 0.25f, row_max(d) * 4.f - SKW)) | 1u;
  }
  KL(5) KL(6)
  u32 org = __builtin_amdgcn_readfirstlane(ctl[C_TB]);
  if (org) { const int adj = ((int)(org - t0 * 4u - (p3 >= 0 ? bb * MSH : 0)) * 85 + 1024) >> 11; ta += adj; }
  KL(7)
  if (tm && mc >= 2 && w >= 14 && lane == 0) __builtin_amdgcn_raw_buffer_store_b32(tag, rs, WS_FLAGS + (((mc - 2) * 8 + mg) * 2 + (w & 1)) * 4, 0, SC1);
  KL(8)
  u32x4 g0;
  const int o0 = WS_G1 + w * 1024 + lane * 16;
  if (tq) lds_wait(ctl + C_TB, 1u);
#define KG(K) { KL(K) if (tq && (w & 1) == ((K) & 1)) { until_s(due - TAU / 2); \
    asm volatile("buffer_load_dwordx4 %0, %1, %2, 0 offen sc1" : "=&v"(g0) : "v"(o0), "s"(rs) : "memory"); } }
  KG(9) KG(10)
  KL(11) KL(12)
  if (tq) {
    asm volatile("s_waitcnt vmcnt(2)" : "+v"(g0));
#define G1_OK() __all(g0[0] != ~0u && g0[1] != ~0u && g0[2] != ~0u && g0[3] != ~0u)
    if (!G1_OK()) {
#pragma unroll 1
      for (u32 s = 0; s < SPIN_CAP; ++s) {
        __builtin_amdgcn_s_sleep(16);
        MEMB();
        g0 = ld_sc1(rs, o0);
        if (G1_OK()) break;
      }
    }
#pragma unroll
    for (int j = 0; j < 4; ++j) { const u32 o = dppu<ROR8>(g0[j]); hb[j] = pack2(blo(g0[j]) + blo(o), bhi(g0[j]) + bhi(o)); }
    PIN(hb);
  }
  KL(13)
  if (tq) {
    tile_part(L + L_W + w * 4096, L + L_TP + w * 2048 + lane * 16, hb, n, g);
    lds_add((LAS u32*)(L + L_CTL) + C_T, lane);
  }
  KL(14)
  if (tq && w >= 14) {
    lds_wait(ctl + C_T, 16u);
    const f32x4 s = tile_sum(L + L_TP + (w & 1) * 1024 + lane * 16);
    st_sc1(rs, WS_Q + ((n & 7) * 32 + 4 * xcd + (b >> 1)) * 512 + ((b & 1) * 64 + 32 * (w & 1) + 16 * (n >> 3) + 4 * g) * 4, u32x4{pack2(s[0], s[1]), tag, pack2(s[2], s[3]), tag});
  }
  RELANE();
  KL(15)
  asm volatile("" : "+s"(rot));
  const u16* vp = vc + ((size_t)bh * SK + (SK - WIN) + 4 * w) * 128;
#define VL(I) { const int t_ = ta + extra(16 + (I)); LDR(vr[I], vp, I, PACE(t_)) }
#define VLN(I) { const int t_ = ta + extra(16 + (I)); LDR(vr[I], vp, I, PACEN(t_)) }
  VL(0) VL(1)
  VL(2)
  u32x2 gq;
#define FL_WAIT() if (!__all(fl == tag)) {   _Pragma("unroll 1") for (u32 s = 0; s < SPIN_CAP; ++s) { __builtin_amdgcn_s_sleep(4); MEMB(); \
      fl = __builtin_amdgcn_raw_buffer_load_b32(rs, WS_FLAGS + lane * 4, 0, SC1); if (__all(fl == tag)) break; } } MEMB();
  u32 fl;
  u32x2 nv;
  if (w == WG_) {
    nv = ld_sc1_64(rs, WS_NV + (lane & 15) * 8);
    gq = ld_sc1_64(rs, WS_Q + bh * 512 + lane * 8);
    fl = __builtin_amdgcn_raw_buffer_load_b32(rs, !to ? WS_FLAGS + lane * 4 : WS_END, 0, SC1);
  }
  SB();
  VLN(3) VLN(4)
  if (w == WG_) {
    if (!__all(gq[1] == tag)) {
#pragma unroll 1
      for (u32 s = 0; s < SPIN_CAP; ++s) {
        __builtin_amdgcn_s_sleep(4);
        MEMB();
        gq = ld_sc1_64(rs, WS_Q + bh * 512 + lane * 8);
        if (__all(gq[1] == tag)) break;
      }
    }
    *(LAS u32*)(L + L_Q + lane * 4) = gq[0];
    PIN(fl);
    if (!to) { FL_WAIT() }
    PIN(nv);
    if (!__all(nv[1] == tag)) {
#pragma unroll 1
      for (u32 s = 0; s < SPIN_CAP; ++s) {
        __builtin_amdgcn_s_sleep(4);
        MEMB();
        nv = ld_sc1_64(rs, WS_NV + (lane & 15) * 8);
        if (__all(nv[1] == tag)) break;
      }
    }
    const float dv = (float)((int)nv[0] >> 1);
    const u32 m4 = (u32)__ballot((row_sum((nv[0] & 1u) ? -dv : dv) > 0.f) != ((nv[0] & 1u) != 0u)) & 15u;
    LGKM0();
    ctl[C_Q] = 256u | m4 | ((m4 ^ 15u) << 4);
  }
  RELANE();
  VL(5)
  lds_wait(ctl + C_Q, 1u);
  const bool prer = p3 >= 0 && w < 8;
  u32x4 pre0, pre1;
  if (prer) {
    pre0 = ld_sc1(rs, WS_MODR + (8 + w) * 1024 + lane * 16);
    pre1 = ld_sc1(rs, WS_MODR + (16 + w) * 1024 + lane * 16);
  }
  SB();
  const u32x4 qf = *(LAS u32x4*)(L + L_Q + n * 16);

#define DOT(I) dot2(kr[I][3], qf[3], dot2(kr[I][2], qf[2], dot2(kr[I][1], qf[1], dot2(kr[I][0], qf[0], 0.f))))
  float xs[8];
#define QK(C) { _Pragma("unroll") for (int i = 2 * (C); i < 2 * (C) + 2; ++i) { const float lo_ = DOT(i), hi_ = DOT(i + 8); \
      const float t1_ = lo_ + dppf<ROR8>(lo_), t2_ = hi_ + dppf<ROR8>(hi_); xs[i] = (n & 8) ? t2_ : t1_; PIN(xs[i]); } SB(); }
  QK(0) VLN(6) QK(1) VLN(7) QK(2) VL(8) QK(3) VL(9)
  if (prer) {
    const u32 a_h2w = w * C1S + lane * 8;
    *(LAS u32x2*)(L + L_C1 + a_h2w) = u32x2{pk2(up2(pre1[0]) + 1.f), pk2(up2(pre1[1]) + 1.f)};
    *(LAS u32x2*)(L + L_C1 + a_h2w + 512) = u32x2{pk2(up2(pre1[2]) + 1.f), pk2(up2(pre1[3]) + 1.f)};
    *(LAS u32x2*)(L + L_C1 + 8 * C1S + a_h2w) = u32x2{pre0[0], pre0[1]};
    *(LAS u32x2*)(L + L_C1 + 8 * C1S + a_h2w + 512) = u32x2{pre0[2], pre0[3]};
  }
#define TR_STEP(HALF, CTRL, BIT) _Pragma("unroll") for (int hh = 0; hh < (HALF); ++hh) { \
    const float lo_ = xs[hh], hi_ = xs[hh + (HALF)]; const float t1_ = lo_ + dppf<CTRL>(lo_), t2_ = hi_ + dppf<CTRL>(hi_); xs[hh] = (n & (BIT)) ? t2_ : t1_; }
  TR_STEP(4, XOR7, 4) TR_STEP(2, XOR2, 2) TR_STEP(1, XOR1, 1)
#undef TR_STEP
  float sc = rbf(rbf(xs[0]) * 0.08838834764831845f);
  PIN(sc);
  SB();
  VL(10)
  {
    const float mw_ = wave_max(sc);
    const float lw = wave_sum(__expf(sc - mw_));
    if (lane == 0) *(LAS f32x2*)(L + L_ML + w * 8) = f32x2{mw_, lw};
    lds_add((LAS u32*)(L + L_CTL) + C_ML, lane);
  }
  SB();
  VL(11)
  NEWPAGE();
  RELANE();
  u32 mk[4];
#pragma unroll
  for (int j = 0; j < 4; ++j) mk[j] = (n >> 1) == j ? ((n & 1) ? 0xffff0000u : 0x0000ffffu) : 0u;
  f32x4 oa = {0.f, 0.f, 0.f, 0.f};
#define PV(I) { const u32 pb_ = dppu<0x150 + (I)>(pd); oa = mfma(vr[I], u32x4{pb_ & mk[0], pb_ & mk[1], pb_ & mk[2], pb_ & mk[3]}, oa); asm volatile("" : "+v"(oa)); SB(); }
  PIN(mk[0]); PIN(mk[1]); PIN(mk[2]); PIN(mk[3]);
  u32 a_oa = L_RED + w * OSTR + (g * 8 + n) * 16;
  u32 ra = L_RED + (lane >> 5) * 8 * OSTR + (lane & 31) * 16;
  const u32 near = __builtin_amdgcn_readfirstlane(ctl[C_Q]);
  u32 rb_ = tline(xcd, LN_O + (hj * 8 + b) * 4, near) + (lane & 31) * 16 + (lane >> 5) * 8;
  if (w == 14) rb_ = lane == 0 ? WS_B0 + bid * 8 : WS_END;
  int oo = tline(xcd, LN_O + 8 * w, near) + lane * 16;
  PIN(oo);
  u32 tagv = tag;
  PIN(a_oa); PIN(ra); PIN(rb_); PIN(tagv);
  org = __builtin_amdgcn_readfirstlane(ctl[C_TB]);
  org = org ? org + TEND : t0 * 4u;
  asm volatile("" : "+s"(org));
  VL(12)
  lds_wait(ctl + C_ML, 16u);
  u32 pd;
  {
    const f32x2 ml = *(LAS f32x2*)(L + L_ML + n * 8);
    const float M = row_max(ml[0]);
    const float pe = __expf(sc - M) * rcp(row_sum(__expf(ml[0] - M) * ml[1]));
    pd = pack2(pe, pe);
    PIN(pd);
  }
  SB();
  VL(13)
  PV(0) PV(1) PV(2) PV(3) PV(4)
  VL(14)
  PV(5) PV(6) PV(7) PV(8) PV(9)
  VL(15)
  PV(10) PV(11) PV(12) PV(13) PV(14)
  if (w == 13) __builtin_amdgcn_s_setprio(3);
  PV(15)
#undef PV
  if (n < 8) *(LAS f32x4*)(L + a_oa) = oa;
  BAR();
  if (w == 14) st_sc1_64(rs, rb_, u32x2{now4(), tagv});
  if (w == 13) {
    f32x4 s[8];
#pragma unroll
    for (int ww = 0; ww < 8; ++ww) s[ww] = *(LAS f32x4*)(L + ra + ww * OSTR);
    SB();
    const f32x4 t = ((s[0] + s[1]) + (s[2] + s[3])) + ((s[4] + s[5]) + (s[6] + s[7]));
    st_sc1_64(rs, rb_, u32x2{pack2(t32(t[0], t[2]), t32(t[1], t[3])), tagv});
  }
  if (to) {
    until(org + OFO);
    u32x4 og = ld_sc1(rs, oo);
    SB();
    u32x4 wf[16];
    if (w < 4) {
      __builtin_amdgcn_s_setprio(3);
      const u32 af = L_W + (8 * g + (n >> 2)) * 128 + (w ^ ((n >> 3) | ((g & 1) << 1))) * 32 + (n & 3) * 8;
#pragma unroll
      for (int ks = 0; ks < 16; ++ks) wf[ks] = trfrag(L + af + ks * 4096, L + af + ks * 4096 + 512);
    }
    u32 a_ow = L_TP + (2 * w + (lane >> 5)) * OBS + (lane & 31) * 8;
    u32 a_or = L_TP + ((n >> 3) * 16 + (n & 7)) * OBS + g * 16;
    int rc = n < 8 ? tline(b, LN_PART + (2 * n + ((w >> 1) & 1)) * 8 + xcd, near) + (w & 1) * 64 + g * 16 : WS_END;
    PIN(a_ow); PIN(a_or); PIN(rc);
    SEL4();
    SB();
    *(LAS u32x2*)(L + a_ow) = u32x2{og[0], og[2]};
    if (__builtin_expect(!__all(((og[1] ^ tag) | (og[3] ^ tag)) == 0u), 0)) {
#pragma unroll 1
      for (u32 s = 0; s < SPIN_CAP; ++s) {
        MEMB();
        og = ld_sc1(rs, oo);
        if (__all(((og[1] ^ tag) | (og[3] ^ tag)) == 0u)) break;
        __builtin_amdgcn_s_sleep(2);
      }
      *(LAS u32x2*)(L + a_ow) = u32x2{og[0], og[2]};
    }
    BAR();
    if (w < 4) {
      u32x4 ob[8];
#pragma unroll
      for (int i = 0; i < 8; ++i) ob[i] = *(LAS u32x4*)(L + a_or + (i >> 2) * 8 * OBS + (i & 3) * 64);
      SB();
      f32x4 aL = {0.f, 0.f, 0.f, 0.f}, aH = {0.f, 0.f, 0.f, 0.f};
#pragma unroll
      for (int i = 0; i < 8; ++i) { aL = mfma(wf[i], ob[i], aL); aH = mfma(wf[8 + i], ob[i], aH); }
      const f32x4 v = {aL[0] + dppf<ROR8>(aH[0]), aL[1] + dppf<ROR8>(aH[1]), aL[2] + dppf<ROR8>(aH[2]), aL[3] + dppf<ROR8>(aH[3])};
      st_sc1(rs, rc, tag8(v, tagv, sel));
    }
  }

  const int bo = WS_B0 + lane * 16;
  if (p3 >= 0) {
    u32 a_h2w = w * C1S + lane * 8;
    if (w == 8 && lane < 36) *(LAS u32*)(L + (lane < 32 ? L_ML : L_HH + 640) + lane * 4) = 0u;
    BAR();
    glds((const void*)(tp0 + (u32)((368 - 16 * w) * FF * 2)), lbase + L_W + 49152 + w * 1024);
    u32x4 bp, bq;
    if (w == 12) {
      until_s(org + OFB + 20);
      asm volatile("buffer_load_dwordx4 %0, %2, %3, 0 offen sc1\n\tbuffer_load_dwordx4 %1, %2, %3, 0 offen offset:1024 sc1" : "=&v"(bp), "=&v"(bq) : "v"(bo), "s"(rs) : "memory");
    }
#define DL(R) glds(wd + (size_t)(32 * p3 + (R)) * 512, (lane ^ ((((R) & 3) | (((R) >> 1) & 4)) << 1)) * 16, lbase + L_W + 65536 + (R) * 1024)
    if (w < 8) {
      until_s(org + OFB + WDT);
#pragma unroll
      for (int i = 0; i < 4; ++i) DL(4 * w + i);
    }
    SB();
    u32 a_h2r = L_H2 + (n & 7) * H2S + (n >> 3) * 512 + g * 16;
    u32 a_hw = L_HH + (n < 8 ? n * 64 + (8 * (w & 3) + 4 * (g & 1) + 2 * (g >> 1)) * 2 : 512 + lane * 4);
    const u32x4 t16v = {tag & 0xffffu, tag >> 16, tag & 0xffffu, tag >> 16};
#define X_TIME() until_s(org + OFO + DX - 40); u32 tx = 2u; \
      _Pragma("unroll 1") for (u32 s = 0; s < LSPIN_CAP && tx == 2u; ++s) { tx = __builtin_amdgcn_readfirstlane(ctl[C_TX]); if (tx == 2u) __builtin_amdgcn_s_sleep(2); } \
      if (!(tx & 1u)) tx = org + OFO + DX;
#define X_BAD(A, B) ((or4(A) | or4(B)) & 0xffffu)
    if (w < 8) {
      const u32x2 ca = *(LAS u32x2*)(L + L_C1 + a_h2w), cb = *(LAS u32x2*)(L + L_C1 + a_h2w + 512);
      f32x2 c1[4] = {up2(ca[0]), up2(ca[1]), up2(cb[0]), up2(cb[1])};
      a_h2w = L_H2 + w * H2S + lane * 8;
      int xo = tline(xcd, LN_X2 + 16 * w, near) + lane * 16;
      u32 a_hr = L_HH + (n < 8 ? n * 64 + g * 16 : 768), a_hs = L_HH + (n < 8 ? 768 : (n & 7) * 64 + g * 16);
      PIN(a_hs);
      SEL4();
      const u32 a_d0 = L_W + 65536 + (8 * g + (n >> 2)) * 1024 + ((8 * w + ((n >> 1) & 1)) ^ (((n >> 2) | ((g & 1) << 2)) << 1)) * 16 + (n & 1) * 8;
      int yo = tline(w, LN_YP + (4 * (n >> 3) + g) * NP3 + p3, near) + (n & 7) * 16, yo2 = tline(w, LN_YP + (8 + 4 * (n >> 3) + g) * NP3 + p3, near) + (n & 7) * 16;
      PIN(xo); PIN(c1[0]); PIN(c1[1]); PIN(c1[2]); PIN(c1[3]); PIN(a_h2w); PIN(a_hr); PIN(yo); PIN(yo2);
      u32 a_cx = lbase + L_CTL + (C_X + w) * 4;
      PIN(a_cx);
      asm volatile("s_waitcnt vmcnt(4)" ::: "memory");
      BAR();
      __builtin_amdgcn_s_setprio(2);
      X_TIME()
      until(tx);
#pragma unroll 1
      for (u32 s = 0; s < SPIN_CAP; ++s) {
        const u32x4 xa = ld_sc1(rs, xo) ^ t16v, xb = ld_sc1(rs, xo + 1024) ^ t16v;
        const float x0 = __builtin_bit_cast(float, __builtin_amdgcn_readfirstlane(xa[0]));
        *(LAS u32x2*)(L + a_h2w) = u32x2{pk2((f32x2{asf(xa[0]), asf(xa[1])} - x0) * c1[0]), pk2((f32x2{asf(xa[2]), asf(xa[3])} - x0) * c1[1])};
        *(LAS u32x2*)(L + a_h2w + 512) = u32x2{pk2((f32x2{asf(xb[0]), asf(xb[1])} - x0) * c1[2]), pk2((f32x2{asf(xb[2]), asf(xb[3])} - x0) * c1[3])};
        if (__builtin_expect(__all(X_BAD(xa, xb) == 0u), 1)) break;
        MEMB();
      }
      asm volatile("s_mov_b64 exec, 1\n\tds_write_b32 %0, %0\n\ts_mov_b64 exec, -1" :: "v"(a_cx) : "memory");
      __builtin_amdgcn_s_setprio(0);
      lds_add((LAS u32*)(L + L_CTL) + C_R, lane);
      lds_wait(ctl + C_R, 8u);
      u32x4 df[4];
#pragma unroll
      for (int i = 0; i < 4; ++i) { df[i] = trfrag(L + (a_d0 ^ (32 * i)), L + (a_d0 ^ (32 * i)) + 4096); PIN(df[i]); }
      BAR();
      const u32x4 h0 = *(LAS u32x4*)(L + a_hr), h1 = *(LAS u32x4*)(L + a_hs);
      const f32x4 z = {0.f, 0.f, 0.f, 0.f};
      const f32x4 a0 = mfma(df[1], h1, mfma(df[0], h0, z)), a1 = mfma(df[3], h1, mfma(df[2], h0, z));
      st_sc1(rs, yo, tag8(a0, tagv, sel));
      st_sc1(rs, yo2, tag8(a1, tagv, sel));
    } else if (w < 12) {
      NEWPAGE();
      u32 a_st = L_ML + (n & 7) * 16;
      PIN(a_st);
      __builtin_amdgcn_s_setprio(3);
      u32x4 gf[16], gb_[8];
      const u32 af = L_W + (8 * g + (n >> 2)) * 128 + (((w & 3) + (n & 2) * 2) ^ (((n >> 3) | ((g & 1) << 1)) << 1)) * 16 + (n & 1) * 8;
      f32x4 aL = {0.f, 0.f, 0.f, 0.f}, aH;
      f32x2 ag, au, sg, su, gt, up;
      {
#pragma unroll
        for (int q4 = 0; q4 < 4; ++q4) {
#pragma unroll
          for (int i = 0; i < 4; ++i) gb_[i] = *(LAS u32x4*)(L + L_C1 + n * C1S + g * 16 + q4 * 256 + i * 64);
          if (q4 == 3) {
            VM0();
            BAR();
          }
#pragma unroll
          for (int i = 0; i < 4; ++i) gf[4 * q4 + i] = trfrag(L + af + (4 * q4 + i) * 4096, L + af + (4 * q4 + i) * 4096 + 512);
          SB();
#pragma unroll
          for (int i = 0; i < 4; ++i) aL = mfma(gf[4 * q4 + i], gb_[i], aL);
          PIN(aL);
        }
        const auto s0 = __builtin_amdgcn_permlane32_swap(__float_as_uint(aL[0]), __float_as_uint(aL[2]), false, false);
        const auto s1 = __builtin_amdgcn_permlane32_swap(__float_as_uint(aL[1]), __float_as_uint(aL[3]), false, false);
        ag = -f32x2{__uint_as_float(s0[0]), __uint_as_float(s1[0])};
        au = -f32x2{__uint_as_float(s0[1]), __uint_as_float(s1[1])};
        sg = f32x2{dppf<ROR8>(ag[0]), dppf<ROR8>(ag[1])} * 1.4426950408889634f;
        su = -f32x2{dppf<ROR8>(au[0]), dppf<ROR8>(au[1])};
        PIN(ag); PIN(au); PIN(sg); PIN(su); PIN(a_h2r); PIN(a_hw);
      }
      __builtin_amdgcn_s_setprio(0);
      _Pragma("unroll 1") for (u32 s = 0; s < LSPIN_CAP; ++s) if (__all(ctl[C_X + (lane & 7)] != 0u)) break;
      __builtin_amdgcn_s_setprio(3);
      MEMB();
      GU_MUL(L + a_h2r)
      SB();
      f32x4 st = *(volatile LAS f32x4*)(L + a_st);
      gu_pair(aL, aH, gt, up);
      if (!__all(st[3] != 0.f)) {
        _Pragma("unroll 1") for (u32 s = 0; s < LSPIN_CAP; ++s) { st = *(volatile LAS f32x4*)(L + a_st); if (__all(st[3] != 0.f)) break; }
      }
      gt = __builtin_elementwise_fma(__builtin_elementwise_fma(ag, f32x2{st[0], st[0]}, gt), f32x2{st[2], st[2]}, sg);
      up = __builtin_elementwise_fma(__builtin_elementwise_fma(au, f32x2{st[0], st[0]}, up), f32x2{st[1], st[1]}, su);
      *(LAS u32*)(L + a_hw) = pk2(f32x2{rcp(1.f + __builtin_amdgcn_exp2f(gt[0])), rcp(1.f + __builtin_amdgcn_exp2f(gt[1]))} * up);
      BAR();
    } else {
      int xo = tline(xcd, LN_X2 + 32 * (w - 12), near) + lane * 16;
      u32 a_st = lbase + L_ML + (2 * (w - 12) + (lane >> 5)) * 16;
      PIN(xo); PIN(a_st);
      if (w == 12) asm volatile("s_waitcnt vmcnt(2)" ::: "memory"); else VM0();
      BAR();
      asm volatile("s_waitcnt vmcnt(0)" : "+v"(bp), "+v"(bq) :: "memory");
      if (w == 12 && __builtin_expect(!on_time(tag, org, bp, bq), 0)) {
        ctl[C_TX] = 2u;
        ctl[C_TX] = (anchor(rs, bo, tag, org, bp, bq) + DX) | 1u;
      }
      __builtin_amdgcn_s_setprio(1);
      X_TIME()
      until(tx + SDL);
      u32x4 xv[4];
#pragma unroll
      for (int i = 0; i < 4; ++i) xv[i] = ld_sc1(rs, xo + i * 1024);
#pragma unroll
      for (int i = 0; i < 4; ++i) xv[i] ^= t16v;
      if (__builtin_expect(!__all((X_BAD(xv[0], xv[1]) | X_BAD(xv[2], xv[3])) == 0u), 0)) {
#pragma unroll 1
        for (u32 s = 0; s < SPIN_CAP; ++s) {
          MEMB();
#pragma unroll
          for (int i = 0; i < 4; ++i) xv[i] = ld_sc1(rs, xo + i * 1024) ^ t16v;
          if (__all((X_BAD(xv[0], xv[1]) | X_BAD(xv[2], xv[3])) == 0u)) break;
        }
      }
      float sm[2], sq[2], xs0[2];
#pragma unroll
      for (int k = 0; k < 2; ++k) {
        const float x0 = __builtin_bit_cast(float, __builtin_amdgcn_readfirstlane(xv[2 * k][0]));
        const u32x4 xa = xv[2 * k], xb = xv[2 * k + 1];
        const f32x2 d0 = f32x2{asf(xa[0]), asf(xa[1])} - x0, d1 = f32x2{asf(xa[2]), asf(xa[3])} - x0, d2 = f32x2{asf(xb[0]), asf(xb[1])} - x0, d3 = f32x2{asf(xb[2]), asf(xb[3])} - x0;
        const f32x2 sv = (d0 + d1) + (d2 + d3), qv = __builtin_elementwise_fma(d0, d0, d1 * d1) + __builtin_elementwise_fma(d2, d2, d3 * d3);
        xs0[k] = x0;
        sm[k] = sv[0] + sv[1];
        sq[k] = qv[0] + qv[1];
      }
      const float r = row_sum(t32(t16(sm[0], sq[0]), t16(sm[1], sq[1]))) * (1.f / 512.f);
      const auto sw = __builtin_amdgcn_permlane16_swap(__float_as_uint(r), __float_as_uint(r), false, false);
      const float md = __uint_as_float(sw[0]);
      const float x0 = lane < 32 ? xs0[0] : xs0[1];
      const float inv = rcp(rbf(__builtin_amdgcn_sqrtf(rbf(rbf(fmaxf(fmaf(-md, md, __uint_as_float(sw[1])), 0.f)) + 1e-5f))));
      const f32x4 st = {rbf(x0 + md) - x0, inv, inv * -1.4426950408889634f, 1.f};
      asm volatile("s_mov_b32 exec_lo, 1\n\ts_mov_b32 exec_hi, 1\n\tds_write_b128 %0, %1\n\ts_mov_b64 exec, -1" :: "v"(a_st), "v"(st) : "memory");
      if (w == 15) {
        _Pragma("unroll 1") for (u32 s = 0; s < LSPIN_CAP; ++s) if (__all(ctl[C_X + (lane & 7)] != 0u)) break;
        if (lane < 8) st_sc1_64(rs, WS_B1 + lane * 512 + p3 * 8, u32x2{now4(), tagv});
      }
      BAR();
    }
    asm volatile("fused_code_end:");
  } else if (red && (w == RW || w == RW - 1)) {
    NEWPAGE();
    const int f = 16 * xcd + 2 * b + hj, ro_ = 4 * (w & 1) + (lane & 3);
    const int rb = e >> 4, rj = e & 15, c2 = 32 * rj + 4 * (lane & 7) + g;
    u32 xr_ = x[rb * 512 + c2];
    u32 gar = (__builtin_amdgcn_raw_buffer_load_b16(rs, WS_MODR + rb * 1024 + mpos(c2), 0, SC1));
    u32 gbr = (__builtin_amdgcn_raw_buffer_load_b16(rs, WS_MODR + (24 + ro_) * 1024 + mpos(4 * f + g), 0, SC1));
    int po = tline(xcd, LN_PART + (2 * b + hj) * 8, near) + lane * 16;
    int xo = tline(lane >> 3, LN_X2 + e, near) + (lane & 7) * 16;
    int xi = tline(xcd, LN_X2 + 16 * ro_, near) + (4 * f + g) * 4;
    u32 xt = (g & 1) ? tag >> 16 : tag & 0xffffu;
    const u32x4 tb4 = bytes4(tag);
    u32x4 tb5 = lane < 44 ? tb4 : u32x4{0, 0, 0, 0};
    const int yl = LN_YP + (f & 15) * NP3 + (lane >> 2);
    int yo = tline(xcd, yl, near) + ro_ * 16, y1 = tline(xcd, yl + 16, near) + ro_ * 16;
    int y2 = lane < 44 ? tline(xcd, yl + 32, near) + ro_ * 16 : WS_END;
    PIN(y1);
    u32 oof = (lane & 12) == 0 ? (ro_ * 512 + 4 * f + g) * 2 : 8192;
    __amdgpu_buffer_rsrc_t ro = __builtin_amdgcn_make_buffer_rsrc((void*)out, 0, 8192, 0x00020000);
    asm volatile("" : "+s"(ro));
    PIN(xt); PIN(tb5); PIN(po); PIN(xo); PIN(xi); PIN(yo); PIN(y2); PIN(oof);
    if (w == RW) {
      u32x4 bp, bq;
      until_s(org + OFB);
      B0_LOAD()
      SB();
      PIN(gar); PIN(xr_);
      float ga = blo(gar), xx = blo(xr_);
      PIN(ga); PIN(xx);
      u32 anc = org + OFO;
      if (__builtin_expect(!on_time(tag, org, bp, bq), 0)) anc = anchor(rs, bo, tag, org, bp, bq);
      until(anc + DP);
      u32x4 xq;
#pragma unroll 1
      for (u32 sp = 0; sp < SPIN_CAP; ++sp) {
        const u32x4 pv = ld_one(rs, po) ^ tb4;
        float t = t32(t16(asf(pv[0]), asf(pv[1])), t16(asf(pv[2]), asf(pv[3])));
        t += dppf<ROR8>(t);
        const u32 xw = __float_as_uint(rbf(xx + rbf(ga * rbf(t)))) | xt;
        const auto p1 = __builtin_amdgcn_permlane16_swap(xw, xw, false, false);
        const auto p2 = __builtin_amdgcn_permlane32_swap(p1[0], p1[0], false, false), p3_ = __builtin_amdgcn_permlane32_swap(p1[1], p1[1], false, false);
        xq = u32x4{p2[0], p3_[0], p2[1], p3_[1]};
        PIN(xq);
        if (__builtin_expect(__all((or4(pv) & 255u) == 0u), 1)) break;
        MEMB();
      }
      st_sc1(rs, xo, xq);
      if (bid == 1 && lane == 0) __hip_atomic_store((GAS u32*)ws, tag, RLX_AGENT);
      u32 ty = anc + DYS;
      const int lt = (int)(now4() - anc) - (DX + 20);
      if (__builtin_expect(lt > 0, 0)) {
        int lk = DB1;
        while (lk < DB1 + lt + 240) lk += PB;
        u32x2 bv;
#pragma unroll 1
        for (u32 sp = 0; sp < SPIN_CAP; ++sp) {
          until_s(anc + lk);
          bv = ld_sc1_64(rs, WS_B1 + xcd * 512 + (lane < NP3 ? lane : 0) * 8);
          if (__all(bv[1] == tag)) break;
          MEMB();
          do lk += PB; while ((int)(now4() - anc) > lk);
        }
        ty = org + smax((int)(bv[0] - org)) + DY;
        asm volatile("fused_cold_end:");
      }
      ctl[C_TX] = ty | 1u;
    }
    PIN(gbr);
    float gb = blo(gbr);
    PIN(gb);
    lds_wait(ctl + C_TX, 1u);
    until(__builtin_amdgcn_readfirstlane(ctl[C_TX]));
    u32 res;
#pragma unroll 1
    for (u32 sp = 0; sp < SPIN_CAP; ++sp) {
      const u32 x2 = __builtin_amdgcn_raw_buffer_load_b32(rs, xi, 0, SC1) ^ xt;
      const u32x4 y0 = ld_one(rs, yo) ^ tb4, y1_ = ld_one(rs, y1) ^ tb4, y2_ = ld_one(rs, y2) ^ tb5;
      const f32x4 s = (__builtin_bit_cast(f32x4, y0) + __builtin_bit_cast(f32x4, y1_)) + __builtin_bit_cast(f32x4, y2_);
      float t = t32(t16(s[0], s[1]), t16(s[2], s[3]));
      t += dppf<ROR8>(t);
      t += dppf<ROR4>(t);
      res = pack2(fmaf(gb, t, asf(x2)), 0.f);
      PIN(res);
      if (__builtin_expect(__all((((or4(y0) | or4(y1_) | or4(y2_)) & 255u) | (x2 & 0xffffu)) == 0u), 1)) break;
      MEMB();
    }
    __builtin_amdgcn_raw_buffer_store_b16((u16)res, ro, oof, 0, 0);
  }
}

extern "C" void kernel_launch(void* const* d_in, const int* in_sizes, int n_in, void* d_out, int out_size, void* d_ws, size_t ws_size, hipStream_t stream) {
  (void)hipFuncSetAttribute((const void*)fused, hipFuncAttributeMaxDynamicSharedMemorySize, L_END);
  Args a;
  const int src[4][2] = {{3, 3}, {10, 10}, {4, 4}, {6, 7}};
  for (int r = 0; r < 4; ++r)
    for (int k = 0; k < 2; ++k) a.half[r][k] = (const unsigned char*)d_in[src[r][k]] + (r < 3 && k ? 64 : 0);
  a.x = (const u16*)d_in[0]; a.kc = (const u16*)d_in[1]; a.vc = (const u16*)d_in[2]; a.wd = (const u16*)d_in[8]; a.cc = (const u16*)d_in[9];
  a.out = (u16*)d_out; a.ws = (unsigned char*)d_ws;
  fused<<<256, 1024, L_END, stream>>>(a);
}
```

```cpp
#define KW 5
#define LEAK -1
#include <hip/hip_runtime.h>
#include <stdint.h>

typedef unsigned int u32;
typedef unsigned long long u64;
typedef unsigned short u16;
typedef u32 u32x4 __attribute__((ext_vector_type(4)));
typedef u32 u32x2 __attribute__((ext_vector_type(2)));
typedef float f32x4 __attribute__((ext_vector_type(4)));
typedef float f32x2 __attribute__((ext_vector_type(2)));
typedef short s16x4 __attribute__((ext_vector_type(4)));
typedef __bf16 bf16x8 __attribute__((ext_vector_type(8)));
typedef __bf16 bf16x2 __attribute__((ext_vector_type(2)));
#define GAS __attribute__((address_space(1)))
#define LAS __attribute__((address_space(3)))
#define RLX_AGENT __ATOMIC_RELAXED, __HIP_MEMORY_SCOPE_AGENT

constexpr int SK = 4096, WIN = 1024, FF = 1376, NP3 = 43;

constexpr int WS_FLAGS = 0x1000;
constexpr int WS_TS = 0x1800;
constexpr int WS_G1 = 0x2000;
constexpr int WS_B1 = 0x8000;
constexpr int WS_MODR = 0xA000;
constexpr int WS_Q = 0x12000;
constexpr int WS_B0 = 0x32000;
constexpr int WS_NV = 0x33000;
constexpr int WS_NP = 0x34000;
constexpr int LN_O = 0;
constexpr int LN_PART = 128;
constexpr int LN_X2 = 256;
constexpr int LN_YP = 384;
constexpr int WS_T = 0x38000, T_SPAN = 17 * 16384;
constexpr int WS_END = WS_T + 4 * T_SPAN;
constexpr int WS_STEP = 2 << 20;
static_assert(WS_END <= WS_STEP && LN_YP + 16 * NP3 <= 17 * 64, "");

constexpr int L_W = 0;
constexpr int L_TP = 65536;
constexpr int L_C1 = 98304;
constexpr int L_SINK = 130048;
constexpr int L_RED = 131072;
constexpr int L_H2 = L_RED + 16384;
constexpr int C1S = 1040, H2S = 1056, OSTR = 544, OBS = 288;
constexpr int L_Q = L_H2 + 8 * H2S;
constexpr int L_ML = L_Q + 512;
constexpr int L_HH = L_ML + 128;
constexpr int L_CTL = L_HH + 784;
constexpr int L_END = L_CTL + 64;
constexpr int C_T = 0, C_TB = 1, C_TX = 2, C_Q = 3, C_ML = 4, C_R = 6, C_T0 = 7, C_X = 8;
constexpr int RW = 12, WG_ = 10;

constexpr u32 SPIN_CAP = 1u << 17, LSPIN_CAP = 1u << 20;
#define DUE0 810
#define TAU 220
#define E1 16
#define E2 10
#define SKW 200
#define MSH 14
#define KA 48
#define OFO 420
#define OFB 450
#define PB 200
#define RB 260
#define DO 15
#define DL_ 220
#define DP 328
#define DX 513
#define DYS 1043
#define DB1 880
#define DY 200
#define SDL 12
#define WDT 40
constexpr int extra(int i) { return i < 8 ? E1 : (i >= 14 && i < 22 ? E2 : 0); }
constexpr int TEND = DUE0 + 32 * TAU + 8 * E1 + 8 * E2;
#define SMASK 0xffffffffu

__device__ __forceinline__ float rbf(float f) { return (float)(__bf16)f; }
__device__ __forceinline__ u32 pack2(float a, float b) { bf16x2 v = {(__bf16)a, (__bf16)b}; return __builtin_bit_cast(u32, v); }
__device__ __forceinline__ float blo(u32 v) { return __builtin_bit_cast(float, v << 16); }
__device__ __forceinline__ float bhi(u32 v) { return __builtin_bit_cast(float, v & 0xffff0000u); }
__device__ __forceinline__ u32 canon(u32 v) { return v < 0xfffffffeu ? v : 0xfffffffeu; }
#define SEL4() u32x4 sel = {0x07060500u, 0x07060501u, 0x07060502u, 0x07060503u}; asm volatile("" : "+s"(sel))
__device__ __forceinline__ u32x4 tag8(f32x4 v, u32 tagv, u32x4 sel) {
  const u32x4 r = __builtin_bit_cast(u32x4, v);
  return u32x4{__builtin_amdgcn_perm(r[0], tagv, sel[0]), __builtin_amdgcn_perm(r[1], tagv, sel[1]), __builtin_amdgcn_perm(r[2], tagv, sel[2]), __builtin_amdgcn_perm(r[3], tagv, sel[3])};
}
__device__ __forceinline__ u32x4 bytes4(u32 tag) { return u32x4{tag & 255u, (tag >> 8) & 255u, (tag >> 16) & 255u, tag >> 24}; }
__device__ __forceinline__ u32 or4(u32x4 v) { return (v[0] | v[1]) | (v[2] | v[3]); }
__device__ __forceinline__ float rcp(float x) { return __builtin_amdgcn_rcpf(x); }
template <int CTRL> __device__ __forceinline__ float dppf(float x) {
  return __builtin_bit_cast(float, __builtin_amdgcn_mov_dpp(__builtin_bit_cast(int, x), CTRL, 0xf, 0xf, true));
}
template <int CTRL> __device__ __forceinline__ u32 dppu(u32 x) { return (u32)__builtin_amdgcn_mov_dpp((int)x, CTRL, 0xf, 0xf, true); }
constexpr int ROR8 = 0x128, ROR4 = 0x124, ROR2 = 0x122, ROR1 = 0x121, XOR1 = 0xB1, XOR2 = 0x4E, XOR7 = 0x141;
__device__ __forceinline__ float row_sum(float x) { x += dppf<ROR8>(x); x += dppf<ROR4>(x); x += dppf<ROR2>(x); x += dppf<ROR1>(x); return x; }
__device__ __forceinline__ float row_max(float x) {
  x = fmaxf(x, dppf<ROR8>(x)); x = fmaxf(x, dppf<ROR4>(x)); x = fmaxf(x, dppf<ROR2>(x)); x = fmaxf(x, dppf<ROR1>(x)); return x;
}
__device__ __forceinline__ float x16_sum(float x) {
  auto s = __builtin_amdgcn_permlane16_swap(__float_as_uint(x), __float_as_uint(x), false, false);
  return __uint_as_float(s[0]) + __uint_as_float(s[1]);
}
__device__ __forceinline__ float x32_sum(float x) {
  auto s = __builtin_amdgcn_permlane32_swap(__float_as_uint(x), __float_as_uint(x), false, false);
  return __uint_as_float(s[0]) + __uint_as_float(s[1]);
}
__device__ __forceinline__ float x16_max(float x) {
  auto s = __builtin_amdgcn_permlane16_swap(__float_as_uint(x), __float_as_uint(x), false, false);
  return fmaxf(__uint_as_float(s[0]), __uint_as_float(s[1]));
}
__device__ __forceinline__ float x32_max(float x) {
  auto s = __builtin_amdgcn_permlane32_swap(__float_as_uint(x), __float_as_uint(x), false, false);
  return fmaxf(__uint_as_float(s[0]), __uint_as_float(s[1]));
}
__device__ __forceinline__ float t16(float a, float b) {
  auto s = __builtin_amdgcn_permlane16_swap(__float_as_uint(a), __float_as_uint(b), false, false);
  return __uint_as_float(s[0]) + __uint_as_float(s[1]);
}
__device__ __forceinline__ float t32(float a, float b) {
  auto s = __builtin_amdgcn_permlane32_swap(__float_as_uint(a), __float_as_uint(b), false, false);
  return __uint_as_float(s[0]) + __uint_as_float(s[1]);
}
__device__ __forceinline__ f32x2 up2(u32 v) { return f32x2{blo(v), bhi(v)}; }
__device__ __forceinline__ u32 pk2(f32x2 v) { return pack2(v[0], v[1]); }
#define PIN(x) asm volatile("" : "+v"(x))
__device__ __forceinline__ f32x2 rb2(f32x2 v) { u32 p = pk2(v); asm("" : "+v"(p)); return up2(p); }
__device__ __forceinline__ float wave_sum(float x) { return x32_sum(x16_sum(row_sum(x))); }
__device__ __forceinline__ float wave_max(float x) { return x32_max(x16_max(row_max(x))); }
__device__ __forceinline__ float dot2(u32 a, u32 b, float c) {
  return __builtin_amdgcn_fdot2_f32_bf16(__builtin_bit_cast(bf16x2, a), __builtin_bit_cast(bf16x2, b), c, false);
}
__device__ __forceinline__ f32x4 mfma(u32x4 a, u32x4 b, f32x4 c) {
  return __builtin_amdgcn_mfma_f32_16x16x32_bf16(__builtin_bit_cast(bf16x8, a), __builtin_bit_cast(bf16x8, b), c, 0, 0, 0);
}
__device__ __forceinline__ u32x4 ldnt(const void* p) { return __builtin_nontemporal_load((const u32x4*)p); }
__device__ __forceinline__ void glds(const void* g, u32 lds) {
  u32 keep;
  lds = __builtin_amdgcn_readfirstlane(lds);
  asm volatile("s_mov_b32 %0, m0\n\ts_mov_b32 m0, %2\n\ts_nop 0\n\tglobal_load_lds_dwordx4 %1, off nt\n\ts_mov_b32 m0, %0"
               : "=&s"(keep) : "v"(g), "s"(lds) : "memory");
}
__device__ __forceinline__ void glds(const void* base, u32 off, u32 lds) {
  u32 keep;
  lds = __builtin_amdgcn_readfirstlane(lds);
  asm volatile("s_mov_b32 %0, m0\n\ts_mov_b32 m0, %3\n\ts_nop 0\n\tglobal_load_lds_dwordx4 %1, %2 nt\n\ts_mov_b32 m0, %0"
               : "=&s"(keep) : "v"(off), "s"(base), "s"(lds) : "memory");
}
__device__ __forceinline__ void touch(const void* base, u32 off, u32 lds) {
  u32 keep;
  lds = __builtin_amdgcn_readfirstlane(lds);
  asm volatile("s_mov_b32 %0, m0\n\ts_mov_b32 m0, %3\n\ts_nop 0\n\tglobal_load_lds_dwordx4 %1, %2\n\ts_mov_b32 m0, %0"
               : "=&s"(keep) : "v"(off), "s"(base), "s"(lds) : "memory");
}
__device__ __forceinline__ u32x4 trfrag(LAS unsigned char* p0, LAS unsigned char* p1) {
  s16x4 a = __builtin_amdgcn_ds_read_tr16_b64_v4i16((LAS s16x4*)p0);
  s16x4 b = __builtin_amdgcn_ds_read_tr16_b64_v4i16((LAS s16x4*)p1);
  u32x2 x = __builtin_bit_cast(u32x2, a), y = __builtin_bit_cast(u32x2, b);
  return u32x4{x[0], x[1], y[0], y[1]};
}
#define NEWPAGE() asm volatile("s_branch 9f\n\t.p2align 12\n9:")
#define SB() __builtin_amdgcn_sched_barrier(0)
#define MEMB() asm volatile("" ::: "memory")
#define VM0() asm volatile("s_waitcnt vmcnt(0)" ::: "memory")
#define LGKM0() asm volatile("s_waitcnt lgkmcnt(0)" ::: "memory")
#define BAR() asm volatile("s_waitcnt lgkmcnt(0)\n\ts_barrier" ::: "memory")
#define TAG() do { u32 ep_; asm volatile("s_load_dword %0, %1, 0x0\n\ts_waitcnt lgkmcnt(0)" : "=s"(ep_) : "s"(ws) : "memory"); \
    tag = ((ep_ & 0x7f7f7f7fu) + 0x01010101u) ^ (ep_ & 0x80808080u); } while (0)
constexpr int SC1 = 16;
__device__ __forceinline__ u32x4 ld_sc1(__amdgpu_buffer_rsrc_t rs, int off) { return __builtin_amdgcn_raw_buffer_load_b128(rs, off, 0, SC1); }
__device__ __forceinline__ u32x4 ld_one(__amdgpu_buffer_rsrc_t rs, int off) { return __builtin_amdgcn_raw_buffer_load_b128(rs, off, 0, SC1 | 2); }
__device__ __forceinline__ u32x2 ld_sc1_64(__amdgpu_buffer_rsrc_t rs, int off) { return __builtin_amdgcn_raw_buffer_load_b64(rs, off, 0, SC1); }
__device__ __forceinline__ void st_sc1(__amdgpu_buffer_rsrc_t rs, int off, u32x4 v) { __builtin_amdgcn_raw_buffer_store_b128(v, rs, off, 0, SC1); }
__device__ __forceinline__ void st_sc1_64(__amdgpu_buffer_rsrc_t rs, int off, u32x2 v) { __builtin_amdgcn_raw_buffer_store_b64(v, rs, off, 0, SC1); }

__device__ __forceinline__ void ln_stats(u32x4 xv, float& m, float& inv) {
  const float x[8] = {blo(xv[0]), bhi(xv[0]), blo(xv[1]), bhi(xv[1]), blo(xv[2]), bhi(xv[2]), blo(xv[3]), bhi(xv[3])};
  const float x0 = __builtin_bit_cast(float, __builtin_amdgcn_readfirstlane(xv[0] << 16));
  float s = 0.f, v = 0.f;
#pragma unroll
  for (int j = 0; j < 8; ++j) { const float d = x[j] - x0; s += d; v = fmaf(d, d, v); }
  const float md = wave_sum(s) * (1.f / 512.f), sq = wave_sum(v) * (1.f / 512.f);
  const float var = rbf(fmaxf(fmaf(-md, md, sq), 0.f));
  m = rbf(x0 + md);
  inv = rcp(rbf(__builtin_amdgcn_sqrtf(rbf(var + 1e-5f))));
}
#define PACE_(W, INC) do { asm volatile(W "\n1:\n\ts_memrealtime vcc\n\ts_waitcnt lgkmcnt(0)\n\ts_lshl_b32 vcc_lo, vcc_lo, 2\n\ts_sub_i32 vcc_hi, vcc_lo, %0\n\ts_cmp_lt_i32 vcc_hi, 0\n\t" \
    "s_cbranch_scc0 2f\n\ts_sleep 1\n\ts_branch 1b\n2:\n\ts_add_i32 %0, %0, %1" : "+s"(due) : "is"(INC) : "memory", "scc", "vcc"); SB(); } while (0)
#define LT(K, T)
#define PACE(INC) PACE_("s_waitcnt vmcnt(2)", INC)
#define PACEN(INC) PACE_("", INC)
__device__ __forceinline__ void until_s(u32 due) { PACE_("", 0); }
__device__ __forceinline__ void until(u32 t) {
  asm volatile("1:\n\ts_memrealtime vcc\n\ts_waitcnt lgkmcnt(0)\n\ts_lshl_b32 vcc_lo, vcc_lo, 2\n\ts_sub_i32 vcc_hi, vcc_lo, %0\n\ts_cmp_lt_i32 vcc_hi, 0\n\t"
               "s_cbranch_scc1 1b" :: "s"(t) : "memory", "scc", "vcc");
  SB();
}

#define STAMP(i) do { if ((i) == 0) { sv0 = (i) == 0 ? lk0 : (u32)__builtin_amdgcn_s_memrealtime(); asm volatile("" : "+s"(sv0)); } if ((i) == 18) { sv18 = (i) == 0 ? lk0 : (u32)__builtin_amdgcn_s_memrealtime(); asm volatile("" : "+s"(sv18)); } } while (0)

#define WSTAMP(k) do { } while (0)
__device__ __forceinline__ void lds_wait(volatile LAS u32* p, u32 want) {
  _Pragma("unroll 1") for (u32 s = 0; s < LSPIN_CAP; ++s) {
    if (__builtin_amdgcn_readfirstlane(*p) >= want) break;
    __builtin_amdgcn_s_sleep(2);
  }
  MEMB();
}
__device__ __forceinline__ void lds_add(LAS u32* p, int lane) {
  LGKM0();
  if (lane == 0) __hip_atomic_fetch_add(p, 1u, __ATOMIC_RELAXED, __HIP_MEMORY_SCOPE_WORKGROUP);
  MEMB();
}
__device__ __forceinline__ void tile_part(LAS unsigned char* T, LAS unsigned char* P, u32x4 hb, int n, int g) {
  LAS unsigned char* tb = T + (8 * g + (n >> 2)) * 128 + (n & 3) * 8;
  const int sx = (n >> 3) | ((g & 1) << 1);
  f32x4 a[4];
#pragma unroll
  for (int t = 0; t < 4; ++t) a[t] = mfma(trfrag(tb + (t ^ sx) * 32, tb + (t ^ sx) * 32 + 512), hb, f32x4{0.f, 0.f, 0.f, 0.f});
  *(LAS f32x4*)(P) = n < 8 ? a[0] : a[1];
  *(LAS f32x4*)(P + 1024) = n < 8 ? a[2] : a[3];
}
__device__ __forceinline__ f32x4 tile_sum(LAS unsigned char* P) {
  f32x4 s[8], r[2];
#pragma unroll
  for (int k = 0; k < 2; ++k) {
#pragma unroll
    for (int i = 0; i < 8; ++i) s[i] = *(LAS f32x4*)(P + (8 * k + i) * 2048);
    SB();
    r[k] = ((s[0] + s[1]) + (s[2] + s[3])) + ((s[4] + s[5]) + (s[6] + s[7]));
    SB();
  }
  return r[0] + r[1];
}
#define GU_MUL(P) _Pragma("unroll") for (int i = 0; i < 8; ++i) gb_[i] = *(LAS u32x4*)((P) + i * 64); SB(); \
    aL = f32x4{0.f, 0.f, 0.f, 0.f}; aH = aL; _Pragma("unroll") for (int i = 0; i < 8; ++i) { aL = mfma(gf[i], gb_[i], aL); aH = mfma(gf[8 + i], gb_[i], aH); }
__device__ __forceinline__ void gu_pair(f32x4 aL, f32x4 aH, f32x2& gt, f32x2& up) {
  const auto s0 = __builtin_amdgcn_permlane32_swap(__float_as_uint(aL[0] + dppf<ROR8>(aH[0])), __float_as_uint(aL[2] + dppf<ROR8>(aH[2])), false, false);
  const auto s1 = __builtin_amdgcn_permlane32_swap(__float_as_uint(aL[1] + dppf<ROR8>(aH[1])), __float_as_uint(aL[3] + dppf<ROR8>(aH[3])), false, false);
  gt = f32x2{__uint_as_float(s0[0]), __uint_as_float(s1[0])};
  up = f32x2{__uint_as_float(s0[1]), __uint_as_float(s1[1])};
}
__device__ __forceinline__ int tline(int c, int ln, u32 near) { return WS_T + (c & 3) * T_SPAN + (ln >> 6) * 16384 + ((near >> c) & 1) * 8192 + (ln & 63) * 128; }
__device__ __forceinline__ int mpos(int c) { return (c & 252) * 4 + (c >> 8) * 8 + (c & 3) * 2; }
__device__ __forceinline__ u32 smax(int m) { int r = (int)fminf(wave_max((float)m), 4e5f); PIN(r); return (u32)__builtin_amdgcn_readfirstlane(r); }
__device__ __forceinline__ u32 now4() { return (u32)__builtin_amdgcn_s_memrealtime() * 4u; }
#define B0_LOAD() { bp = ld_sc1(rs, bo); bq = ld_sc1(rs, bo + 1024); }
__device__ __forceinline__ bool on_time(u32 tag, u32 org, u32x4 bp, u32x4 bq) {
  const u32 lim = org + OFO - DO;
  return __all((((bp[1] ^ tag) | (bp[3] ^ tag) | (bq[1] ^ tag) | (bq[3] ^ tag)) | (((lim - bp[0]) | (lim - bp[2]) | (lim - bq[0]) | (lim - bq[2])) >> 31)) == 0u);
}
__device__ __forceinline__ u32 anchor(__amdgpu_buffer_rsrc_t rs, int bo, u32 tag, u32 org, u32x4 bp, u32x4 bq) {
  int lk = OFB;
#pragma unroll 1
  for (u32 s = 0; s < SPIN_CAP; ++s) {
    if (__all(((bp[1] ^ tag) | (bp[3] ^ tag) | (bq[1] ^ tag) | (bq[3] ^ tag)) == 0u)) break;
    MEMB();
    do lk += PB; while ((int)(now4() - org) > lk);
    until_s(org + lk);
    B0_LOAD()
  }
  const int m = (int)smax(max(max((int)(bp[0] - org), (int)(bp[2] - org)), max((int)(bq[0] - org), (int)(bq[2] - org))));
  return org + (u32)max(m + DO <= OFO ? OFO : m + DL_, lk + RB - DP);
}
__device__ __forceinline__ float asf(u32 v) { return __builtin_bit_cast(float, v); }

struct Args {
  const unsigned char* half[4][2];
  const u16 *x, *kc, *vc, *wd, *cc;
  u16* out;
  unsigned char* ws;
};

__global__ void __launch_bounds__(1024, 4) fused(const Args a) {
  extern __shared__ __attribute__((aligned(16))) unsigned char smem[];
  LAS unsigned char* L = (LAS unsigned char*)smem;
  const u32 lbase = (u32)(size_t)L;
  const int tid = threadIdx.x, w = __builtin_amdgcn_readfirstlane(tid >> 6), bid = blockIdx.x;
  int lane = tid & 63;
  u32 lk0 = (u32)__builtin_amdgcn_s_memrealtime(); asm volatile("" : "+s"(lk0));
  u32 sv0 = 0, sv18 = 0;
  const int xcd = bid & 7, idx = bid >> 3, hj = idx & 3, h = 4 * xcd + hj, b = idx >> 2, bh = b * 32 + h;
  const int sw8 = hj == 3 ? (w & 1) * 8 : 0, rw = (hj == 3 ? 24 : 32) * w;
  u64 hA, hB, t64, t1;
  u32 warm;
  asm volatile("s_load_dwordx2 %0, %4, %5\n\ts_load_dwordx2 %1, %4, %6\n\ts_load_dword %3, %4, 0x40\n\ts_memrealtime %2" : "=&s"(hA), "=&s"(hB), "=&s"(t64), "=&s"(warm)
               : "s"(__builtin_amdgcn_kernarg_segment_ptr()), "s"(16 * hj + sw8), "s"(16 * hj + (sw8 ^ 8)));
  SB();
  const int bb = (b + xcd) & 7;
  int p3r = 5 * xcd + min(xcd, 3) + bb;
  int mc = (b & 6) + ((xcd & 1) ^ 1), mg = ((xcd & 6) + b) & 7;
  const bool tq = hj == 0, to = hj == 2;
  int lim = hj == 1 ? 6 - b : (xcd < 3 ? 6 : 5) - bb;
  asm volatile("" : "+s"(p3r), "+s"(mc), "+s"(mg), "+s"(lim));
  const bool idle = (hj & 1) && lim <= 0;
  u64 tp0, tp1;
  {
    const u32 pitch = (u32)(0x0AC0040018002000ull >> (16 * hj)) & 0xffffu;
    u32 ce = (xcd << (to ? 13 : 4)) + 2 * b, c1 = 16 * mc + 2 * mg;
    asm volatile("" : "+s"(ce), "+s"(c1));
    const u32 col = 64 * ((hj & 1) ? (hj == 1 ? c1 : p3r) : ce);
    const u32 step = pitch << (to ? 1 : 3);
    u32 off = (rw + (to ? (lane & 24) + (lane >> 5) : lane >> 3)) * pitch + col + ((lane & 3) ^ ((lane >> 3) & 2)) * 16;
    tp0 = off;
    tp1 = off + step;
    u64 s2 = 2 * step;
    u32 ld = lbase + L_W + rw * 128;
    asm volatile("" : "+s"(s2), "+s"(ld), "+v"(tp0), "+v"(tp1));
    asm volatile("s_mov_b32 exec_lo, 0xf0f0f0f0\n\ts_mov_b32 exec_hi, 0xf0f0f0f0\n\ts_waitcnt lgkmcnt(0)\n\ts_memrealtime %6\n\tv_lshl_add_u64 %0, %0, 0, %3\n\tv_lshl_add_u64 %1, %1, 0, %2\n\t"
                 "s_not_b64 exec, exec\n\tv_lshl_add_u64 %0, %0, 0, %2\n\tv_lshl_add_u64 %1, %1, 0, %3\n\ts_mov_b64 exec, -1"
                 : "+v"(tp0), "+v"(tp1), "+s"(hA), "+s"(hB), "+s"(t64), "+s"(warm), "=&s"(t1) :: "scc");
    if (!idle) {
      u32 keep;
      asm volatile("s_mov_b32 %0, m0\n\ts_mov_b32 m0, %3\n\ts_nop 0\n\tglobal_load_lds_dwordx4 %1, off nt\n\ts_add_u32 m0, m0, 0x400\n\t"
                   "v_lshl_add_u64 %1, %1, 0, %4\n\tglobal_load_lds_dwordx4 %2, off nt\n\ts_mov_b32 m0, %0\n\tv_lshl_add_u64 %2, %2, 0, %4"
                   : "=&s"(keep), "+v"(tp0), "+v"(tp1) : "s"(ld), "s"(s2) : "memory", "scc");
      glds((const void*)tp0, ld + 2048);
      if (hj != 3) glds((const void*)tp1, ld + 3072);
    }
  }
  SB();
  u32 t0 = (u32)t64;
  const u16 *x = a.x, *kc = a.kc, *vc = a.vc, *wd = a.wd, *cc = a.cc;
  u16* out = a.out;
  unsigned char* ws = a.ws;
  if (w == 0) STAMP(24);
  if (w == 0) STAMP(0);
  const int p3 = (hj == 3 && !idle) ? p3r : -1;
  const bool tm = hj == 1 && !idle;
  u32 tag;
  TAG();
  asm volatile("" : "+s"(t1));
  t0 += (u32)(KW * max((int)((u32)t1 - t0) - KA, 0)) / 4u;
  const __amdgpu_buffer_rsrc_t rs = __builtin_amdgcn_make_buffer_rsrc((void*)(((size_t)ws + (size_t)((tag & 63u) + 1u) * WS_STEP) & ~(size_t)(WS_STEP - 1)), 0, WS_END, 0x00020000);
  if (idle && hj == 1) {
    if (w == 0) {
      st_sc1(rs, WS_G1 + ((b & 1) * 8 + xcd) * 1024 + lane * 16, u32x4{~0u, ~0u, ~0u, ~0u});
      VM0();
      if (lane == 0) st_sc1_64(rs, WS_TS + ((b & 1) * 8 + xcd) * 8, u32x2{t0, tag});
    } else {
      u64 pc;
      u32 len;
      asm volatile("s_getpc_b64 %0\n1:\n\ts_mov_b32 %1, fused_code_end - 1b + 1000\n\ts_max_u32 %1, %1, fused_cold_end - 1b + 1000" : "=s"(pc), "=&s"(len) :: "scc");
      const u32 o = ((b & 1) * 15 + w - 1) * 1024;
      if (o < len) touch((const void*)(pc & ~15ull), min(o + lane * 16, len & ~15u), lbase + L_SINK);
    }
  }
  if (w == 1) glds((const unsigned char*)((lane & 1) ? vc : kc) + ((size_t)bh * SK + (SK - WIN)) * 256, lbase + L_SINK);
  int ta = TAU;
  int n = lane & 15, g = lane >> 4;
  u32x4 cv, hb;
  const bool row = tm && (w >= 8 || mc == 1);
  if (row) cv = ldnt((w < 8 ? x : cc) + (w & 7) * 512 + lane * 8);
  if (tid < 16) ((volatile LAS u32*)(L + L_CTL))[tid] = tid == C_T0 ? t0 : 0u;
  BAR();
  t0 = __builtin_amdgcn_readfirstlane(((volatile LAS u32*)(L + L_CTL))[C_T0]);
  u32 due = t0 * 4u + (u32)((4 * bb + hj) * TAU) / 32u + DUE0;
  if (hj == 3 && bb >= 6 && w == 0) {
    const int k = (bb & 1) * 8 + xcd, o = WS_NP + k * 512 + (bb & 1) * 8192;
    __builtin_amdgcn_raw_buffer_store_b32(tag, rs, lane < 2 ? o ^ (lane * 8192) : WS_END, 0, SC1);
    u64 c0, c1, c2;
    u32 v;
    asm volatile("s_waitcnt vmcnt(0)\n\ts_memtime %0\n\ts_waitcnt lgkmcnt(0)\n\tbuffer_load_dword %3, %4, %6, 0 offen sc1\n\ts_waitcnt vmcnt(0)\n\ts_memtime %1\n\ts_waitcnt lgkmcnt(0)\n\t"
                 "buffer_load_dword %3, %5, %6, 0 offen sc1\n\ts_waitcnt vmcnt(0)\n\ts_memtime %2\n\ts_waitcnt lgkmcnt(0)"
                 : "=&s"(c0), "=&s"(c1), "=&s"(c2), "=&v"(v) : "v"(o), "v"(o ^ 8192), "s"(rs) : "memory");
    const int d = min(max(2 * (int)c1 - (int)c0 - (int)c2, -600), 600);
    if (lane == 0) st_sc1_64(rs, WS_NV + k * 8, u32x2{(u32)(2 * ((bb & 1) ? -d : d)) | ((__builtin_amdgcn_s_getreg(20 | (3 << 11)) >> 2) & 1u), tag});
  }
  int rot = idx;
#define RO(I) ((((I) + rot) & 15) * 8192)
  const u16* kp = kc + ((size_t)bh * SK + (SK - WIN) + 4 * w) * 128;
  u32 l16 = lane * 16;
  PIN(l16);
#define LDR(D, P, I, W) { const GAS unsigned char* p_ = (const GAS unsigned char*)((P) + RO(I)) + l16; PIN(p_); W; D = __builtin_nontemporal_load((const GAS u32x4*)p_); SB(); }
  u32x4 kr[16], vr[16];
  volatile LAS u32* ctl = (volatile LAS u32*)(L + L_CTL);
#define RELANE() do { asm volatile("" : "+v"(lane)); n = lane & 15; g = lane >> 4; } while (0)
  const int e = b * 16 + 2 * xcd + hj;
  const bool red = hj < 2;

#define KL(I) { const int t_ = ta + extra(I); LDR(kr[I], kp, I, PACE(t_)) LT(I, t_) }
  KL(0)
  if (row) {
    if (w >= 8) {
#pragma unroll
      for (int j = 0; j < 4; ++j) { const float a0 = blo(cv[j]), a1 = bhi(cv[j]); hb[j] = pack2(a0 * rcp(1.f + __expf(-a0)), a1 * rcp(1.f + __expf(-a1))); }
      *(LAS u32x4*)(L + L_H2 + (w & 7) * H2S + lane * 16) = hb;
      lds_add((LAS u32*)(L + L_CTL) + C_R, lane);
    } else {
      float m, inv;
      ln_stats(cv, m, inv);
      if ((lane >> 3) == mg) {
        LAS f32x4* d = (LAS f32x4*)(L + L_RED + w * 256 + (lane & 7) * 32);
        d[0] = f32x4{rbf(rbf(blo(cv[0]) - m) * inv), rbf(rbf(bhi(cv[0]) - m) * inv), rbf(rbf(blo(cv[1]) - m) * inv), rbf(rbf(bhi(cv[1]) - m) * inv)};
        d[1] = f32x4{rbf(rbf(blo(cv[2]) - m) * inv), rbf(rbf(bhi(cv[2]) - m) * inv), rbf(rbf(blo(cv[3]) - m) * inv), rbf(rbf(bhi(cv[3]) - m) * inv)};
      }
    }
  }
  KL(1)
  u32x2 tsv;
#define TS_WAIT() if (!__all(tsv[1] == tag)) { _Pragma("unroll 1") for (u32 s = 0; s < SPIN_CAP; ++s) { __builtin_amdgcn_s_sleep(16); MEMB(); \
      tsv = ld_sc1_64(rs, WS_TS + (lane & 15) * 8); if (__all(tsv[1] == tag)) break; } } MEMB();
  if (tm) {
    asm volatile("s_waitcnt vmcnt(2)" ::: "memory");
    lds_wait(ctl + C_R, 8u);
    hb = *(LAS u32x4*)(L + L_H2 + (n & 7) * H2S + (32 * w + 8 * g) * 2);
    tile_part(L + L_W + w * 4096, L + L_TP + w * 2048 + lane * 16, hb, n, g);
    lds_add((LAS u32*)(L + L_CTL) + C_T, lane);
  }
  KL(2)
  if (w == 0 || (tm && w >= 14)) tsv = ld_sc1_64(rs, WS_TS + (lane & 15) * 8);
  SB();
  if (tm && w >= 14) {
    lds_wait(ctl + C_T, 16u);
    const int col = 32 * (w & 1) + 16 * (n >> 3) + 4 * g;
    const f32x4 s = tile_sum(L + L_TP + (w & 1) * 1024 + lane * 16);
    float v[4] = {rbf(s[0]), rbf(s[1]), rbf(s[2]), rbf(s[3])};
    if (mc == 1) {
      const f32x4 ln = *(LAS f32x4*)(L + L_RED + (n & 7) * 256 + col * 4);
#pragma unroll
      for (int j = 0; j < 4; ++j) v[j] = ln[j] * rbf(1.f + v[j]);
    }
    TS_WAIT()
    st_sc1_64(rs, mc < 2 ? WS_G1 + (2 * mg + (w & 1)) * 1024 + ((2 * (n >> 3) + (g >> 1)) * 16 + mc * 8 + (n & 7)) * 16 + (g & 1) * 8
                         : WS_MODR + ((mc - 2) * 8 + (n & 7)) * 1024 + mpos(64 * mg + col),
              u32x2{canon(pack2(v[0], v[1])), canon(pack2(v[2], v[3]))});
    if (w == 15) STAMP(3);
  }
  RELANE();
  KL(3) KL(4)
  if (w == 0) {
    TS_WAIT()
    const float d = __builtin_amdgcn_fmed3f((float)(int)(tsv[0] - t0), -1e5f, 1e5f);
    ctl[C_TB] = (t0 * 4u + (u32)(int)fmaxf(row_sum(d) * 0.25f, row_max(d) * 4.f - SKW)) | 1u;
  }
  KL(5) KL(6)
  u32 org = __builtin_amdgcn_readfirstlane(ctl[C_TB]);
  if (org) { const int adj = ((int)(org - t0 * 4u - (p3 >= 0 ? bb * MSH : 0)) * 85 + 1024) >> 11; ta += adj; }
  KL(7)
  if (tm && mc >= 2 && w >= 14 && lane == 0) __builtin_amdgcn_raw_buffer_store_b32(tag, rs, WS_FLAGS + (((mc - 2) * 8 + mg) * 2 + (w & 1)) * 4, 0, SC1);
  KL(8)
  u32x4 g0;
  const int o0 = WS_G1 + w * 1024 + lane * 16;
  if (tq) lds_wait(ctl + C_TB, 1u);
#define KG(K) { KL(K) if (tq && (w & 1) == ((K) & 1)) { until_s(due - TAU / 2); \
    asm volatile("buffer_load_dwordx4 %0, %1, %2, 0 offen sc1" : "=&v"(g0) : "v"(o0), "s"(rs) : "memory"); } }
  KG(9) KG(10)
  KL(11) KL(12)
  if (tq) {
    asm volatile("s_waitcnt vmcnt(2)" : "+v"(g0));
#define G1_OK() __all(g0[0] != ~0u && g0[1] != ~0u && g0[2] != ~0u && g0[3] != ~0u)
    if (!G1_OK()) {
#pragma unroll 1
      for (u32 s = 0; s < SPIN_CAP; ++s) {
        __builtin_amdgcn_s_sleep(16);
        MEMB();
        g0 = ld_sc1(rs, o0);
        if (G1_OK()) break;
      }
    }
    if (w == 15) STAMP(4);
#pragma unroll
    for (int j = 0; j < 4; ++j) { const u32 o = dppu<ROR8>(g0[j]); hb[j] = pack2(blo(g0[j]) + blo(o), bhi(g0[j]) + bhi(o)); }
    PIN(hb);
  }
  KL(13)
  if (tq) {
    tile_part(L + L_W + w * 4096, L + L_TP + w * 2048 + lane * 16, hb, n, g);
    lds_add((LAS u32*)(L + L_CTL) + C_T, lane);
  }
  KL(14)
  if (tq && w >= 14) {
    lds_wait(ctl + C_T, 16u);
    const f32x4 s = tile_sum(L + L_TP + (w & 1) * 1024 + lane * 16);
    st_sc1(rs, WS_Q + ((n & 7) * 32 + 4 * xcd + (b >> 1)) * 512 + ((b & 1) * 64 + 32 * (w & 1) + 16 * (n >> 3) + 4 * g) * 4, u32x4{pack2(s[0], s[1]), tag, pack2(s[2], s[3]), tag});
    if (w == 15) STAMP(17);
  }
  RELANE();
  KL(15)
  asm volatile("" : "+s"(rot));
  const u16* vp = vc + ((size_t)bh * SK + (SK - WIN) + 4 * w) * 128;
#define VL(I) { const int t_ = ta + extra(16 + (I)); LDR(vr[I], vp, I, PACE(t_)) LT(16 + (I), t_) }
#define VLN(I) { const int t_ = ta + extra(16 + (I)); LDR(vr[I], vp, I, PACEN(t_)) LT(16 + (I), t_) }
  VL(0) VL(1)
  VL(2)
  u32x2 gq;
#define FL_WAIT() if (!__all(fl == tag)) {   _Pragma("unroll 1") for (u32 s = 0; s < SPIN_CAP; ++s) { __builtin_amdgcn_s_sleep(4); MEMB(); \
      fl = __builtin_amdgcn_raw_buffer_load_b32(rs, WS_FLAGS + lane * 4, 0, SC1); if (__all(fl == tag)) break; } } MEMB();
  u32 fl;
  u32x2 nv;
  if (w == WG_) {
    nv = ld_sc1_64(rs, WS_NV + (lane & 15) * 8);
    gq = ld_sc1_64(rs, WS_Q + bh * 512 + lane * 8);
    fl = __builtin_amdgcn_raw_buffer_load_b32(rs, !to ? WS_FLAGS + lane * 4 : WS_END, 0, SC1);
  }
  SB();
  VLN(3) VLN(4)
  if (w == WG_) {
    if (!__all(gq[1] == tag)) {
#pragma unroll 1
      for (u32 s = 0; s < SPIN_CAP; ++s) {
        __builtin_amdgcn_s_sleep(4);
        MEMB();
        gq = ld_sc1_64(rs, WS_Q + bh * 512 + lane * 8);
        if (__all(gq[1] == tag)) break;
      }
    }
    *(LAS u32*)(L + L_Q + lane * 4) = gq[0];
    PIN(fl);
    if (!to) { FL_WAIT() }
    PIN(nv);
    if (!__all(nv[1] == tag)) {
#pragma unroll 1
      for (u32 s = 0; s < SPIN_CAP; ++s) {
        __builtin_amdgcn_s_sleep(4);
        MEMB();
        nv = ld_sc1_64(rs, WS_NV + (lane & 15) * 8);
        if (__all(nv[1] == tag)) break;
      }
    }
    const float dv = (float)((int)nv[0] >> 1);
    const u32 m4 = (u32)__ballot((row_sum((nv[0] & 1u) ? -dv : dv) > 0.f) != ((nv[0] & 1u) != 0u)) & 15u;
    LGKM0();
    ctl[C_Q] = 256u | m4 | ((m4 ^ 15u) << 4);
    STAMP(5);
  }
  RELANE();
  VL(5)
  lds_wait(ctl + C_Q, 1u);
  const bool prer = p3 >= 0 && w < 8;
  u32x4 pre0, pre1;
  if (prer) {
    pre0 = ld_sc1(rs, WS_MODR + (8 + w) * 1024 + lane * 16);
    pre1 = ld_sc1(rs, WS_MODR + (16 + w) * 1024 + lane * 16);
  }
  SB();
  const u32x4 qf = *(LAS u32x4*)(L + L_Q + n * 16);
  if (w == 1) STAMP(6);

#define DOT(I) dot2(kr[I][3], qf[3], dot2(kr[I][2], qf[2], dot2(kr[I][1], qf[1], dot2(kr[I][0], qf[0], 0.f))))
  float xs[8];
#define QK(C) { _Pragma("unroll") for (int i = 2 * (C); i < 2 * (C) + 2; ++i) { const float lo_ = DOT(i), hi_ = DOT(i + 8); \
      const float t1_ = lo_ + dppf<ROR8>(lo_), t2_ = hi_ + dppf<ROR8>(hi_); xs[i] = (n & 8) ? t2_ : t1_; PIN(xs[i]); } SB(); }
  QK(0) VLN(6) QK(1) VLN(7) QK(2) VL(8) QK(3) VL(9)
  if (prer) {
    const u32 a_h2w = w * C1S + lane * 8;
    *(LAS u32x2*)(L + L_C1 + a_h2w) = u32x2{pk2(up2(pre1[0]) + 1.f), pk2(up2(pre1[1]) + 1.f)};
    *(LAS u32x2*)(L + L_C1 + a_h2w + 512) = u32x2{pk2(up2(pre1[2]) + 1.f), pk2(up2(pre1[3]) + 1.f)};
    *(LAS u32x2*)(L + L_C1 + 8 * C1S + a_h2w) = u32x2{pre0[0], pre0[1]};
    *(LAS u32x2*)(L + L_C1 + 8 * C1S + a_h2w + 512) = u32x2{pre0[2], pre0[3]};
  }
#define TR_STEP(HALF, CTRL, BIT) _Pragma("unroll") for (int hh = 0; hh < (HALF); ++hh) { \
    const float lo_ = xs[hh], hi_ = xs[hh + (HALF)]; const float t1_ = lo_ + dppf<CTRL>(lo_), t2_ = hi_ + dppf<CTRL>(hi_); xs[hh] = (n & (BIT)) ? t2_ : t1_; }
  TR_STEP(4, XOR7, 4) TR_STEP(2, XOR2, 2) TR_STEP(1, XOR1, 1)
#undef TR_STEP
  float sc = rbf(rbf(xs[0]) * 0.08838834764831845f);
  PIN(sc);
  SB();
  VL(10)
  {
    const float mw_ = wave_max(sc);
    const float lw = wave_sum(__expf(sc - mw_));
    if (lane == 0) *(LAS f32x2*)(L + L_ML + w * 8) = f32x2{mw_, lw};
    lds_add((LAS u32*)(L + L_CTL) + C_ML, lane);
  }
  SB();
  VL(11)
  NEWPAGE();
  RELANE();
  u32 mk[4];
#pragma unroll
  for (int j = 0; j < 4; ++j) mk[j] = (n >> 1) == j ? ((n & 1) ? 0xffff0000u : 0x0000ffffu) : 0u;
  f32x4 oa = {0.f, 0.f, 0.f, 0.f};
#define PV(I) { const u32 pb_ = dppu<0x150 + (I)>(pd); oa = mfma(vr[I], u32x4{pb_ & mk[0], pb_ & mk[1], pb_ & mk[2], pb_ & mk[3]}, oa); asm volatile("" : "+v"(oa)); SB(); }
  PIN(mk[0]); PIN(mk[1]); PIN(mk[2]); PIN(mk[3]);
  u32 a_oa = L_RED + w * OSTR + (g * 8 + n) * 16;
  u32 ra = L_RED + (lane >> 5) * 8 * OSTR + (lane & 31) * 16;
  const u32 near = __builtin_amdgcn_readfirstlane(ctl[C_Q]);
  u32 rb_ = tline(xcd, LN_O + (hj * 8 + b) * 4, near) + (lane & 31) * 16 + (lane >> 5) * 8;
  if (w == 14) rb_ = lane == 0 ? WS_B0 + bid * 8 : WS_END;
  int oo = tline(xcd, LN_O + 8 * w, near) + lane * 16;
  PIN(oo);
  u32 tagv = tag;
  PIN(a_oa); PIN(ra); PIN(rb_); PIN(tagv);
  org = __builtin_amdgcn_readfirstlane(ctl[C_TB]);
  org = org ? org + TEND : t0 * 4u;
  asm volatile("" : "+s"(org));
  VL(12)
  lds_wait(ctl + C_ML, 16u);
  u32 pd;
  {
    const f32x2 ml = *(LAS f32x2*)(L + L_ML + n * 8);
    const float M = row_max(ml[0]);
    const float pe = __expf(sc - M) * rcp(row_sum(__expf(ml[0] - M) * ml[1]));
    pd = pack2(pe, pe);
    PIN(pd);
  }
  SB();
  VL(13)
  PV(0) PV(1) PV(2) PV(3) PV(4)
  VL(14)
  PV(5) PV(6) PV(7) PV(8) PV(9)
  VL(15)
  PV(10) PV(11) PV(12) PV(13) PV(14)
  if (w == 13) __builtin_amdgcn_s_setprio(3);
  PV(15)
#undef PV
  if (n < 8) *(LAS f32x4*)(L + a_oa) = oa;
  BAR();
  if (w == 0) STAMP(8);
  if (w == 14) st_sc1_64(rs, rb_, u32x2{now4(), tagv});
  if (w == 13) {
    f32x4 s[8];
#pragma unroll
    for (int ww = 0; ww < 8; ++ww) s[ww] = *(LAS f32x4*)(L + ra + ww * OSTR);
    SB();
    const f32x4 t = ((s[0] + s[1]) + (s[2] + s[3])) + ((s[4] + s[5]) + (s[6] + s[7]));
    st_sc1_64(rs, rb_, u32x2{pack2(t32(t[0], t[2]), t32(t[1], t[3])), tagv});
    STAMP(9);
  }
  if (to) {
    until(org + OFO);
    u32x4 og = ld_sc1(rs, oo);
    SB();
    u32x4 wf[16];
    if (w < 4) {
      __builtin_amdgcn_s_setprio(3);
      const u32 af = L_W + (8 * g + (n >> 2)) * 128 + (w ^ ((n >> 3) | ((g & 1) << 1))) * 32 + (n & 3) * 8;
#pragma unroll
      for (int ks = 0; ks < 16; ++ks) wf[ks] = trfrag(L + af + ks * 4096, L + af + ks * 4096 + 512);
    }
    u32 a_ow = L_TP + (2 * w + (lane >> 5)) * OBS + (lane & 31) * 8;
    u32 a_or = L_TP + ((n >> 3) * 16 + (n & 7)) * OBS + g * 16;
    int rc = n < 8 ? tline(b, LN_PART + (2 * n + ((w >> 1) & 1)) * 8 + xcd, near) + (w & 1) * 64 + g * 16 : WS_END;
    PIN(a_ow); PIN(a_or); PIN(rc);
    SEL4();
    SB();
    *(LAS u32x2*)(L + a_ow) = u32x2{og[0], og[2]};
    if (__builtin_expect(!__all(((og[1] ^ tag) | (og[3] ^ tag)) == 0u), 0)) {
#pragma unroll 1
      for (u32 s = 0; s < SPIN_CAP; ++s) {
        MEMB();
        og = ld_sc1(rs, oo);
        if (__all(((og[1] ^ tag) | (og[3] ^ tag)) == 0u)) break;
        __builtin_amdgcn_s_sleep(2);
      }
      *(LAS u32x2*)(L + a_ow) = u32x2{og[0], og[2]};
    }
    BAR();
    if (w == 7) STAMP(2);
    if (w < 4) {
      u32x4 ob[8];
#pragma unroll
      for (int i = 0; i < 8; ++i) ob[i] = *(LAS u32x4*)(L + a_or + (i >> 2) * 8 * OBS + (i & 3) * 64);
      SB();
      f32x4 aL = {0.f, 0.f, 0.f, 0.f}, aH = {0.f, 0.f, 0.f, 0.f};
#pragma unroll
      for (int i = 0; i < 8; ++i) { aL = mfma(wf[i], ob[i], aL); aH = mfma(wf[8 + i], ob[i], aH); }
      const f32x4 v = {aL[0] + dppf<ROR8>(aH[0]), aL[1] + dppf<ROR8>(aH[1]), aL[2] + dppf<ROR8>(aH[2]), aL[3] + dppf<ROR8>(aH[3])};
      st_sc1(rs, rc, tag8(v, tagv, sel));
      if (w == 3) STAMP(16);
    }
  }

  const int bo = WS_B0 + lane * 16;
  if (p3 >= 0) {
    u32 a_h2w = w * C1S + lane * 8;
    if (w == 8 && lane < 36) *(LAS u32*)(L + (lane < 32 ? L_ML : L_HH + 640) + lane * 4) = 0u;
    BAR();
    glds((const void*)(tp0 + (u32)((368 - 16 * w) * FF * 2)), lbase + L_W + 49152 + w * 1024);
    u32x4 bp, bq;
    if (w == 12) {
      until_s(org + OFB + 20);
      asm volatile("buffer_load_dwordx4 %0, %2, %3, 0 offen sc1\n\tbuffer_load_dwordx4 %1, %2, %3, 0 offen offset:1024 sc1" : "=&v"(bp), "=&v"(bq) : "v"(bo), "s"(rs) : "memory");
    }
#define DL(R) glds(wd + (size_t)(32 * p3 + (R)) * 512, (lane ^ ((((R) & 3) | (((R) >> 1) & 4)) << 1)) * 16, lbase + L_W + 65536 + (R) * 1024)
    if (w < 8) {
      until_s(org + OFB + WDT);
#pragma unroll
      for (int i = 0; i < 4; ++i) DL(4 * w + i);
    }
    SB();
    u32 a_h2r = L_H2 + (n & 7) * H2S + (n >> 3) * 512 + g * 16;
    u32 a_hw = L_HH + (n < 8 ? n * 64 + (8 * (w & 3) + 4 * (g & 1) + 2 * (g >> 1)) * 2 : 512 + lane * 4);
    const u32x4 t16v = {tag & 0xffffu, tag >> 16, tag & 0xffffu, tag >> 16};
#define X_TIME() until_s(org + OFO + DX - 40); u32 tx = 2u; \
      _Pragma("unroll 1") for (u32 s = 0; s < LSPIN_CAP && tx == 2u; ++s) { tx = __builtin_amdgcn_readfirstlane(ctl[C_TX]); if (tx == 2u) __builtin_amdgcn_s_sleep(2); } \
      if (!(tx & 1u)) tx = org + OFO + DX;
#define X_BAD(A, B) ((or4(A) | or4(B)) & 0xffffu)
    if (w < 8) {
      const u32x2 ca = *(LAS u32x2*)(L + L_C1 + a_h2w), cb = *(LAS u32x2*)(L + L_C1 + a_h2w + 512);
      f32x2 c1[4] = {up2(ca[0]), up2(ca[1]), up2(cb[0]), up2(cb[1])};
      a_h2w = L_H2 + w * H2S + lane * 8;
      int xo = tline(xcd, LN_X2 + 16 * w, near) + lane * 16;
      u32 a_hr = L_HH + (n < 8 ? n * 64 + g * 16 : 768), a_hs = L_HH + (n < 8 ? 768 : (n & 7) * 64 + g * 16);
      PIN(a_hs);
      SEL4();
      const u32 a_d0 = L_W + 65536 + (8 * g + (n >> 2)) * 1024 + ((8 * w + ((n >> 1) & 1)) ^ (((n >> 2) | ((g & 1) << 2)) << 1)) * 16 + (n & 1) * 8;
      int yo = tline(w, LN_YP + (4 * (n >> 3) + g) * NP3 + p3, near) + (n & 7) * 16, yo2 = tline(w, LN_YP + (8 + 4 * (n >> 3) + g) * NP3 + p3, near) + (n & 7) * 16;
      PIN(xo); PIN(c1[0]); PIN(c1[1]); PIN(c1[2]); PIN(c1[3]); PIN(a_h2w); PIN(a_hr); PIN(yo); PIN(yo2);
      u32 a_cx = lbase + L_CTL + (C_X + w) * 4;
      PIN(a_cx);
      asm volatile("s_waitcnt vmcnt(4)" ::: "memory");
      BAR();
      if (w == 0) STAMP(20);
      __builtin_amdgcn_s_setprio(2);
      X_TIME()
      until(tx);
#pragma unroll 1
      for (u32 s = 0; s < SPIN_CAP; ++s) {
        const u32x4 xa = ld_sc1(rs, xo) ^ t16v, xb = ld_sc1(rs, xo + 1024) ^ t16v;
        const float x0 = __builtin_bit_cast(float, __builtin_amdgcn_readfirstlane(xa[0]));
        *(LAS u32x2*)(L + a_h2w) = u32x2{pk2((f32x2{asf(xa[0]), asf(xa[1])} - x0) * c1[0]), pk2((f32x2{asf(xa[2]), asf(xa[3])} - x0) * c1[1])};
        *(LAS u32x2*)(L + a_h2w + 512) = u32x2{pk2((f32x2{asf(xb[0]), asf(xb[1])} - x0) * c1[2]), pk2((f32x2{asf(xb[2]), asf(xb[3])} - x0) * c1[3])};
        if (__builtin_expect(__all(X_BAD(xa, xb) == 0u), 1)) break;
        MEMB();
      }
      asm volatile("s_mov_b64 exec, 1\n\tds_write_b32 %0, %0\n\ts_mov_b64 exec, -1" :: "v"(a_cx) : "memory");
      if (w == 0) STAMP(13);
      __builtin_amdgcn_s_setprio(0);
      lds_add((LAS u32*)(L + L_CTL) + C_R, lane);
      lds_wait(ctl + C_R, 8u);
      u32x4 df[4];
#pragma unroll
      for (int i = 0; i < 4; ++i) { df[i] = trfrag(L + (a_d0 ^ (32 * i)), L + (a_d0 ^ (32 * i)) + 4096); PIN(df[i]); }
      BAR();
      const u32x4 h0 = *(LAS u32x4*)(L + a_hr), h1 = *(LAS u32x4*)(L + a_hs);
      const f32x4 z = {0.f, 0.f, 0.f, 0.f};
      const f32x4 a0 = mfma(df[1], h1, mfma(df[0], h0, z)), a1 = mfma(df[3], h1, mfma(df[2], h0, z));
      st_sc1(rs, yo, tag8(a0, tagv, sel));
      st_sc1(rs, yo2, tag8(a1, tagv, sel));
    } else if (w < 12) {
      NEWPAGE();
      u32 a_st = L_ML + (n & 7) * 16;
      PIN(a_st);
      __builtin_amdgcn_s_setprio(3);
      u32x4 gf[16], gb_[8];
      const u32 af = L_W + (8 * g + (n >> 2)) * 128 + (((w & 3) + (n & 2) * 2) ^ (((n >> 3) | ((g & 1) << 1)) << 1)) * 16 + (n & 1) * 8;
      f32x4 aL = {0.f, 0.f, 0.f, 0.f}, aH;
      f32x2 ag, au, sg, su, gt, up;
      {
#pragma unroll
        for (int q4 = 0; q4 < 4; ++q4) {
#pragma unroll
          for (int i = 0; i < 4; ++i) gb_[i] = *(LAS u32x4*)(L + L_C1 + n * C1S + g * 16 + q4 * 256 + i * 64);
          if (q4 == 3) {
            VM0();
            BAR();
          }
#pragma unroll
          for (int i = 0; i < 4; ++i) gf[4 * q4 + i] = trfrag(L + af + (4 * q4 + i) * 4096, L + af + (4 * q4 + i) * 4096 + 512);
          SB();
#pragma unroll
          for (int i = 0; i < 4; ++i) aL = mfma(gf[4 * q4 + i], gb_[i], aL);
          PIN(aL);
        }
        const auto s0 = __builtin_amdgcn_permlane32_swap(__float_as_uint(aL[0]), __float_as_uint(aL[2]), false, false);
        const auto s1 = __builtin_amdgcn_permlane32_swap(__float_as_uint(aL[1]), __float_as_uint(aL[3]), false, false);
        ag = -f32x2{__uint_as_float(s0[0]), __uint_as_float(s1[0])};
        au = -f32x2{__uint_as_float(s0[1]), __uint_as_float(s1[1])};
        sg = f32x2{dppf<ROR8>(ag[0]), dppf<ROR8>(ag[1])} * 1.4426950408889634f;
        su = -f32x2{dppf<ROR8>(au[0]), dppf<ROR8>(au[1])};
        PIN(ag); PIN(au); PIN(sg); PIN(su); PIN(a_h2r); PIN(a_hw);
      }
      if (w == 8) STAMP(21);
      __builtin_amdgcn_s_setprio(0);
      _Pragma("unroll 1") for (u32 s = 0; s < LSPIN_CAP; ++s) if (__all(ctl[C_X + (lane & 7)] != 0u)) break;
      __builtin_amdgcn_s_setprio(3);
      MEMB();
      GU_MUL(L + a_h2r)
      SB();
      f32x4 st = *(volatile LAS f32x4*)(L + a_st);
      gu_pair(aL, aH, gt, up);
      if (!__all(st[3] != 0.f)) {
        _Pragma("unroll 1") for (u32 s = 0; s < LSPIN_CAP; ++s) { st = *(volatile LAS f32x4*)(L + a_st); if (__all(st[3] != 0.f)) break; }
      }
      gt = __builtin_elementwise_fma(__builtin_elementwise_fma(ag, f32x2{st[0], st[0]}, gt), f32x2{st[2], st[2]}, sg);
      up = __builtin_elementwise_fma(__builtin_elementwise_fma(au, f32x2{st[0], st[0]}, up), f32x2{st[1], st[1]}, su);
      *(LAS u32*)(L + a_hw) = pk2(f32x2{rcp(1.f + __builtin_amdgcn_exp2f(gt[0])), rcp(1.f + __builtin_amdgcn_exp2f(gt[1]))} * up);
      BAR();
    } else {
      int xo = tline(xcd, LN_X2 + 32 * (w - 12), near) + lane * 16;
      u32 a_st = lbase + L_ML + (2 * (w - 12) + (lane >> 5)) * 16;
      PIN(xo); PIN(a_st);
      if (w == 12) asm volatile("s_waitcnt vmcnt(2)" ::: "memory"); else VM0();
      BAR();
      asm volatile("s_waitcnt vmcnt(0)" : "+v"(bp), "+v"(bq) :: "memory");
      if (w == 12 && __builtin_expect(!on_time(tag, org, bp, bq), 0)) {
        ctl[C_TX] = 2u;
        ctl[C_TX] = (anchor(rs, bo, tag, org, bp, bq) + DX) | 1u;
      }
      __builtin_amdgcn_s_setprio(1);
      X_TIME()
      until(tx + SDL);
      u32x4 xv[4];
#pragma unroll
      for (int i = 0; i < 4; ++i) xv[i] = ld_sc1(rs, xo + i * 1024);
#pragma unroll
      for (int i = 0; i < 4; ++i) xv[i] ^= t16v;
      if (__builtin_expect(!__all((X_BAD(xv[0], xv[1]) | X_BAD(xv[2], xv[3])) == 0u), 0)) {
#pragma unroll 1
        for (u32 s = 0; s < SPIN_CAP; ++s) {
          MEMB();
#pragma unroll
          for (int i = 0; i < 4; ++i) xv[i] = ld_sc1(rs, xo + i * 1024) ^ t16v;
          if (__all((X_BAD(xv[0], xv[1]) | X_BAD(xv[2], xv[3])) == 0u)) break;
        }
      }
      float sm[2], sq[2], xs0[2];
#pragma unroll
      for (int k = 0; k < 2; ++k) {
        const float x0 = __builtin_bit_cast(float, __builtin_amdgcn_readfirstlane(xv[2 * k][0]));
        const u32x4 xa = xv[2 * k], xb = xv[2 * k + 1];
        const f32x2 d0 = f32x2{asf(xa[0]), asf(xa[1])} - x0, d1 = f32x2{asf(xa[2]), asf(xa[3])} - x0, d2 = f32x2{asf(xb[0]), asf(xb[1])} - x0, d3 = f32x2{asf(xb[2]), asf(xb[3])} - x0;
        const f32x2 sv = (d0 + d1) + (d2 + d3), qv = __builtin_elementwise_fma(d0, d0, d1 * d1) + __builtin_elementwise_fma(d2, d2, d3 * d3);
        xs0[k] = x0;
        sm[k] = sv[0] + sv[1];
        sq[k] = qv[0] + qv[1];
      }
      const float r = row_sum(t32(t16(sm[0], sq[0]), t16(sm[1], sq[1]))) * (1.f / 512.f);
      const auto sw = __builtin_amdgcn_permlane16_swap(__float_as_uint(r), __float_as_uint(r), false, false);
      const float md = __uint_as_float(sw[0]);
      const float x0 = lane < 32 ? xs0[0] : xs0[1];
      const float inv = rcp(rbf(__builtin_amdgcn_sqrtf(rbf(rbf(fmaxf(fmaf(-md, md, __uint_as_float(sw[1])), 0.f)) + 1e-5f))));
      const f32x4 st = {rbf(x0 + md) - x0, inv, inv * -1.4426950408889634f, 1.f};
      asm volatile("s_mov_b32 exec_lo, 1\n\ts_mov_b32 exec_hi, 1\n\tds_write_b128 %0, %1\n\ts_mov_b64 exec, -1" :: "v"(a_st), "v"(st) : "memory");
      if (w == 15) {
        _Pragma("unroll 1") for (u32 s = 0; s < LSPIN_CAP; ++s) if (__all(ctl[C_X + (lane & 7)] != 0u)) break;
        if (lane < 8) st_sc1_64(rs, WS_B1 + lane * 512 + p3 * 8, u32x2{now4(), tagv});
      }
      BAR();
    }
    if (w == 7) STAMP(14);
    asm volatile("fused_code_end:");
  } else if (red && (w == RW || w == RW - 1)) {
    NEWPAGE();
    const int f = 16 * xcd + 2 * b + hj, ro_ = 4 * (w & 1) + (lane & 3);
    const int rb = e >> 4, rj = e & 15, c2 = 32 * rj + 4 * (lane & 7) + g;
    u32 xr_ = x[rb * 512 + c2];
    u32 gar = (__builtin_amdgcn_raw_buffer_load_b16(rs, WS_MODR + rb * 1024 + mpos(c2), 0, SC1));
    u32 gbr = (__builtin_amdgcn_raw_buffer_load_b16(rs, WS_MODR + (24 + ro_) * 1024 + mpos(4 * f + g), 0, SC1));
    int po = tline(xcd, LN_PART + (2 * b + hj) * 8, near) + lane * 16;
    int xo = tline(lane >> 3, LN_X2 + e, near) + (lane & 7) * 16;
    int xi = tline(xcd, LN_X2 + 16 * ro_, near) + (4 * f + g) * 4;
    u32 xt = (g & 1) ? tag >> 16 : tag & 0xffffu;
    const u32x4 tb4 = bytes4(tag);
    u32x4 tb5 = lane < 44 ? tb4 : u32x4{0, 0, 0, 0};
    const int yl = LN_YP + (f & 15) * NP3 + (lane >> 2);
    int yo = tline(xcd, yl, near) + ro_ * 16, y1 = tline(xcd, yl + 16, near) + ro_ * 16;
    int y2 = lane < 44 ? tline(xcd, yl + 32, near) + ro_ * 16 : WS_END;
    PIN(y1);
    u32 oof = (lane & 12) == 0 ? (ro_ * 512 + 4 * f + g) * 2 : 8192;
    __amdgpu_buffer_rsrc_t ro = __builtin_amdgcn_make_buffer_rsrc((void*)out, 0, 8192, 0x00020000);
    asm volatile("" : "+s"(ro));
    PIN(xt); PIN(tb5); PIN(po); PIN(xo); PIN(xi); PIN(yo); PIN(y2); PIN(oof);
    if (w == RW) {
      u32x4 bp, bq;
      until_s(org + OFB);
      B0_LOAD()
      SB();
      PIN(gar); PIN(xr_);
      float ga = blo(gar), xx = blo(xr_);
      PIN(ga); PIN(xx);
      u32 anc = org + OFO;
      if (__builtin_expect(!on_time(tag, org, bp, bq), 0)) anc = anchor(rs, bo, tag, org, bp, bq);
      STAMP(1);
      until(anc + DP);
      u32x4 xq;
#pragma unroll 1
      for (u32 sp = 0; sp < SPIN_CAP; ++sp) {
        const u32x4 pv = ld_one(rs, po) ^ tb4;
        float t = t32(t16(asf(pv[0]), asf(pv[1])), t16(asf(pv[2]), asf(pv[3])));
        t += dppf<ROR8>(t);
        const u32 xw = __float_as_uint(rbf(xx + rbf(ga * rbf(t)))) | xt;
        const auto p1 = __builtin_amdgcn_permlane16_swap(xw, xw, false, false);
        const auto p2 = __builtin_amdgcn_permlane32_swap(p1[0], p1[0], false, false), p3_ = __builtin_amdgcn_permlane32_swap(p1[1], p1[1], false, false);
        xq = u32x4{p2[0], p3_[0], p2[1], p3_[1]};
        PIN(xq);
        if (__builtin_expect(__all((or4(pv) & 255u) == 0u), 1)) break;
        MEMB();
      }
      st_sc1(rs, xo, xq);
      STAMP(11);
      if (bid == 1 && lane == 0) __hip_atomic_store((GAS u32*)ws, tag, RLX_AGENT);
      u32 ty = anc + DYS;
      const int lt = (int)(now4() - anc) - (DX + 20);
      if (__builtin_expect(lt > 0, 0)) {
        int lk = DB1;
        while (lk < DB1 + lt + 240) lk += PB;
        u32x2 bv;
#pragma unroll 1
        for (u32 sp = 0; sp < SPIN_CAP; ++sp) {
          until_s(anc + lk);
          bv = ld_sc1_64(rs, WS_B1 + xcd * 512 + (lane < NP3 ? lane : 0) * 8);
          if (__all(bv[1] == tag)) break;
          MEMB();
          do lk += PB; while ((int)(now4() - anc) > lk);
        }
        ty = org + smax((int)(bv[0] - org)) + DY;
        asm volatile("fused_cold_end:");
      }
      ctl[C_TX] = ty | 1u;
      STAMP(7);
    }
    PIN(gbr);
    float gb = blo(gbr);
    PIN(gb);
    lds_wait(ctl + C_TX, 1u);
    until(__builtin_amdgcn_readfirstlane(ctl[C_TX]));
    u32 res;
#pragma unroll 1
    for (u32 sp = 0; sp < SPIN_CAP; ++sp) {
      const u32 x2 = __builtin_amdgcn_raw_buffer_load_b32(rs, xi, 0, SC1) ^ xt;
      const u32x4 y0 = ld_one(rs, yo) ^ tb4, y1_ = ld_one(rs, y1) ^ tb4, y2_ = ld_one(rs, y2) ^ tb5;
      const f32x4 s = (__builtin_bit_cast(f32x4, y0) + __builtin_bit_cast(f32x4, y1_)) + __builtin_bit_cast(f32x4, y2_);
      float t = t32(t16(s[0], s[1]), t16(s[2], s[3]));
      t += dppf<ROR8>(t);
      t += dppf<ROR4>(t);
      res = pack2(fmaf(gb, t, asf(x2)), 0.f);
      PIN(res);
      if (__builtin_expect(__all((((or4(y0) | or4(y1_) | or4(y2_)) & 255u) | (x2 & 0xffffu)) == 0u), 1)) break;
      MEMB();
    }
    __builtin_amdgcn_raw_buffer_store_b16((u16)res, ro, oof, 0, 0);
    if (w == RW) STAMP(18);
  }
  if (sv0 | sv18) { while ((u32)__builtin_amdgcn_s_memrealtime() - lk0 < 3600u) __builtin_amdgcn_s_sleep(8);
    if (sv0 && (tid & 63) == 0) __hip_atomic_store((GAS u32*)(ws + (768u << 20)) + 0 * 256 + bid, sv0, RLX_AGENT);
    if (sv18 && (tid & 63) == 0) __hip_atomic_store((GAS u32*)(ws + (768u << 20)) + 18 * 256 + bid, sv18, RLX_AGENT);
  }

  if (bid == 0 && w == 0) {
    while ((u32)__builtin_amdgcn_s_memrealtime() - lk0 < 4000u) __builtin_amdgcn_s_sleep(2);
    lane = tid & 63;
    auto rd = [&](int i, int q) { u32 v = __hip_atomic_load((GAS u32*)(ws + (768u << 20)) + i * 256 + q * 64 + lane, RLX_AGENT); return (int)(v - lk0); };
    auto ok = [&](int v) { return v > -500 && v < 4000; };
    auto red = [&](int i, int mode, int xm) { float r = mode == 0 ? -1e9f : (mode == 1 ? 1e9f : 0.f), c = 0.f;
      for (int q = 0; q < 4; ++q) { int v = rd(i, q); if (ok(v) && ((xm >> (lane & 7)) & 1)) { r = mode == 0 ? fmaxf(r, (float)v) : (mode == 1 ? fminf(r, (float)v) : r + (float)v); c += 1.f; } }
      if (mode == 0) return wave_max(r); if (mode == 1) return -wave_max(-r); return wave_sum(r) / wave_sum(c); };
#define mx(i) red(i, 0, 255)
#define mn(i) red(i, 1, 255)
#define av(i) red(i, 2, 255)
#define xm(i, m) red(i, 0, m)
#define xn(i, m) red(i, 1, m)
    float val = (float)(mx(18)-av(0)-2400);
    u32 lk = 5000u + (u32)__builtin_amdgcn_readfirstlane((int)(val * 8.f));
    while ((u32)__builtin_amdgcn_s_memrealtime() - lk0 < lk) __builtin_amdgcn_s_sleep(2);
  }
}

extern "C" void kernel_launch(void* const* d_in, const int* in_sizes, int n_in, void* d_out, int out_size, void* d_ws, size_t ws_size, hipStream_t stream) {
  (void)hipFuncSetAttribute((const void*)fused, hipFuncAttributeMaxDynamicSharedMemorySize, L_END);
  Args a;
  const int src[4][2] = {{3, 3}, {10, 10}, {4, 4}, {6, 7}};
  for (int r = 0; r < 4; ++r)
    for (int k = 0; k < 2; ++k) a.half[r][k] = (const unsigned char*)d_in[src[r][k]] + (r < 3 && k ? 64 : 0);
  a.x = (const u16*)d_in[0]; a.kc = (const u16*)d_in[1]; a.vc = (const u16*)d_in[2]; a.wd = (const u16*)d_in[8]; a.cc = (const u16*)d_in[9];
  a.out = (u16*)d_out; a.ws = (unsigned char*)d_ws;
  fused<<<256, 1024, L_END, stream>>>(a);
}
```

```cpp
#include <hip/hip_runtime.h>
#include <stdint.h>

typedef unsigned int u32;
typedef unsigned long long u64;
typedef unsigned short u16;
typedef u32 u32x4 __attribute__((ext_vector_type(4)));
typedef u32 u32x2 __attribute__((ext_vector_type(2)));
typedef float f32x4 __attribute__((ext_vector_type(4)));
typedef float f32x2 __attribute__((ext_vector_type(2)));
typedef short s16x4 __attribute__((ext_vector_type(4)));
typedef __bf16 bf16x8 __attribute__((ext_vector_type(8)));
typedef __bf16 bf16x2 __attribute__((ext_vector_type(2)));
#define GAS __attribute__((address_space(1)))
#define LAS __attribute__((address_space(3)))
#define RLX_AGENT __ATOMIC_RELAXED, __HIP_MEMORY_SCOPE_AGENT

constexpr int SK = 4096, WIN = 1024, FF = 1376, NP3 = 43;

constexpr int WS_FLAGS = 0x1000;
constexpr int WS_TS = 0x1800;
constexpr int WS_G1 = 0x2000;
constexpr int WS_B1 = 0x8000;
constexpr int WS_MODR = 0xA000;
constexpr int WS_Q = 0x12000;
constexpr int WS_B0 = 0x32000;
constexpr int WS_NV = 0x33000;
constexpr int WS_NP = 0x34000;
constexpr int LN_O = 0;
constexpr int LN_PART = 128;
constexpr int LN_X2 = 256;
constexpr int LN_YP = 384;
constexpr int WS_T = 0x38000, T_SPAN = 17 * 16384;
constexpr int WS_END = WS_T + 4 * T_SPAN;
constexpr int WS_STEP = 2 << 20;
static_assert(WS_END <= WS_STEP && LN_YP + 16 * NP3 <= 17 * 64, "");

constexpr int L_W = 0;
constexpr int L_TP = 65536;
constexpr int L_C1 = 98304;
constexpr int L_SINK = 130048;
constexpr int L_RED = 131072;
constexpr int L_H2 = L_RED + 16384;
constexpr int C1S = 1040, H2S = 1056, OSTR = 544, OBS = 288;
constexpr int L_Q = L_H2 + 8 * H2S;
constexpr int L_ML = L_Q + 512;
constexpr int L_HH = L_ML + 128;
constexpr int L_CTL = L_HH + 784;
constexpr int L_END = L_CTL + 64;
constexpr int C_T = 0, C_TB = 1, C_TX = 2, C_Q = 3, C_ML = 4, C_R = 6, C_T0 = 7, C_X = 8;
constexpr int RW = 12, WG_ = 10;

constexpr u32 SPIN_CAP = 1u << 17, LSPIN_CAP = 1u << 20;
constexpr int DUE0 = 810;
constexpr int TAU = 220;
constexpr int E1 = 16;
constexpr int E2 = 10;
constexpr int SKW = 200;
constexpr int MSH = 14;
constexpr int KA = 48;
constexpr int KW = 4;
constexpr int OFO = 420;
constexpr int OFB = 450;
constexpr int PB = 200;
constexpr int RB = 260;
constexpr int DO = 15;
constexpr int DL_ = 220;
constexpr int DP = 328;
constexpr int DX = 513;
constexpr int DYS = 1043;
constexpr int DB1 = 880;
constexpr int DY = 200;
constexpr int SDL = 12;
constexpr int WDT = 40;
constexpr int extra(int i) { return i < 8 ? E1 : (i >= 14 && i < 22 ? E2 : 0); }
constexpr int TEND = DUE0 + 32 * TAU + 8 * E1 + 8 * E2;

__device__ __forceinline__ float rbf(float f) { return (float)(__bf16)f; }
__device__ __forceinline__ u32 pack2(float a, float b) { bf16x2 v = {(__bf16)a, (__bf16)b}; return __builtin_bit_cast(u32, v); }
__device__ __forceinline__ float blo(u32 v) { return __builtin_bit_cast(float, v << 16); }
__device__ __forceinline__ float bhi(u32 v) { return __builtin_bit_cast(float, v & 0xffff0000u); }
__device__ __forceinline__ u32 canon(u32 v) { return v < 0xfffffffeu ? v : 0xfffffffeu; }
#define SEL4() u32x4 sel = {0x07060500u, 0x07060501u, 0x07060502u, 0x07060503u}; asm volatile("" : "+s"(sel))
__device__ __forceinline__ u32x4 tag8(f32x4 v, u32 tagv, u32x4 sel) {
  const u32x4 r = __builtin_bit_cast(u32x4, v);
  return u32x4{__builtin_amdgcn_perm(r[0], tagv, sel[0]), __builtin_amdgcn_perm(r[1], tagv, sel[1]), __builtin_amdgcn_perm(r[2], tagv, sel[2]), __builtin_amdgcn_perm(r[3], tagv, sel[3])};
}
__device__ __forceinline__ u32x4 bytes4(u32 tag) { return u32x4{tag & 255u, (tag >> 8) & 255u, (tag >> 16) & 255u, tag >> 24}; }
__device__ __forceinline__ u32 or4(u32x4 v) { return (v[0] | v[1]) | (v[2] | v[3]); }
__device__ __forceinline__ float rcp(float x) { return __builtin_amdgcn_rcpf(x); }
template <int CTRL> __device__ __forceinline__ float dppf(float x) {
  return __builtin_bit_cast(float, __builtin_amdgcn_mov_dpp(__builtin_bit_cast(int, x), CTRL, 0xf, 0xf, true));
}
template <int CTRL> __device__ __forceinline__ u32 dppu(u32 x) { return (u32)__builtin_amdgcn_mov_dpp((int)x, CTRL, 0xf, 0xf, true); }
constexpr int ROR8 = 0x128, ROR4 = 0x124, ROR2 = 0x122, ROR1 = 0x121, XOR1 = 0xB1, XOR2 = 0x4E, XOR7 = 0x141;
__device__ __forceinline__ float row_sum(float x) { x += dppf<ROR8>(x); x += dppf<ROR4>(x); x += dppf<ROR2>(x); x += dppf<ROR1>(x); return x; }
__device__ __forceinline__ float row_max(float x) {
  x = fmaxf(x, dppf<ROR8>(x)); x = fmaxf(x, dppf<ROR4>(x)); x = fmaxf(x, dppf<ROR2>(x)); x = fmaxf(x, dppf<ROR1>(x)); return x;
}
__device__ __forceinline__ float x16_sum(float x) {
  auto s = __builtin_amdgcn_permlane16_swap(__float_as_uint(x), __float_as_uint(x), false, false);
  return __uint_as_float(s[0]) + __uint_as_float(s[1]);
}
__device__ __forceinline__ float x32_sum(float x) {
  auto s = __builtin_amdgcn_permlane32_swap(__float_as_uint(x), __float_as_uint(x), false, false);
  return __uint_as_float(s[0]) + __uint_as_float(s[1]);
}
__device__ __forceinline__ float x16_max(float x) {
  auto s = __builtin_amdgcn_permlane16_swap(__float_as_uint(x), __float_as_uint(x), false, false);
  return fmaxf(__uint_as_float(s[0]), __uint_as_float(s[1]));
}
__device__ __forceinline__ float x32_max(float x) {
  auto s = __builtin_amdgcn_permlane32_swap(__float_as_uint(x), __float_as_uint(x), false, false);
  return fmaxf(__uint_as_float(s[0]), __uint_as_float(s[1]));
}
__device__ __forceinline__ float t16(float a, float b) {
  auto s = __builtin_amdgcn_permlane16_swap(__float_as_uint(a), __float_as_uint(b), false, false);
  return __uint_as_float(s[0]) + __uint_as_float(s[1]);
}
__device__ __forceinline__ float t32(float a, float b) {
  auto s = __builtin_amdgcn_permlane32_swap(__float_as_uint(a), __float_as_uint(b), false, false);
  return __uint_as_float(s[0]) + __uint_as_float(s[1]);
}
__device__ __forceinline__ f32x2 up2(u32 v) { return f32x2{blo(v), bhi(v)}; }
__device__ __forceinline__ u32 pk2(f32x2 v) { return pack2(v[0], v[1]); }
#define PIN(x) asm volatile("" : "+v"(x))
__device__ __forceinline__ f32x2 rb2(f32x2 v) { u32 p = pk2(v); asm("" : "+v"(p)); return up2(p); }
__device__ __forceinline__ float wave_sum(float x) { return x32_sum(x16_sum(row_sum(x))); }
__device__ __forceinline__ float wave_max(float x) { return x32_max(x16_max(row_max(x))); }
__device__ __forceinline__ float dot2(u32 a, u32 b, float c) {
  return __builtin_amdgcn_fdot2_f32_bf16(__builtin_bit_cast(bf16x2, a), __builtin_bit_cast(bf16x2, b), c, false);
}
__device__ __forceinline__ f32x4 mfma(u32x4 a, u32x4 b, f32x4 c) {
  return __builtin_amdgcn_mfma_f32_16x16x32_bf16(__builtin_bit_cast(bf16x8, a), __builtin_bit_cast(bf16x8, b), c, 0, 0, 0);
}
__device__ __forceinline__ u32x4 ldnt(const void* p) { return __builtin_nontemporal_load((const u32x4*)p); }
__device__ __forceinline__ void glds(const void* g, u32 lds) {
  u32 keep;
  lds = __builtin_amdgcn_readfirstlane(lds);
  asm volatile("s_mov_b32 %0, m0\n\ts_mov_b32 m0, %2\n\ts_nop 0\n\tglobal_load_lds_dwordx4 %1, off nt\n\ts_mov_b32 m0, %0"
               : "=&s"(keep) : "v"(g), "s"(lds) : "memory");
}
__device__ __forceinline__ void glds(const void* base, u32 off, u32 lds) {
  u32 keep;
  lds = __builtin_amdgcn_readfirstlane(lds);
  asm volatile("s_mov_b32 %0, m0\n\ts_mov_b32 m0, %3\n\ts_nop 0\n\tglobal_load_lds_dwordx4 %1, %2 nt\n\ts_mov_b32 m0, %0"
               : "=&s"(keep) : "v"(off), "s"(base), "s"(lds) : "memory");
}
__device__ __forceinline__ void touch(const void* base, u32 off, u32 lds) {
  u32 keep;
  lds = __builtin_amdgcn_readfirstlane(lds);
  asm volatile("s_mov_b32 %0, m0\n\ts_mov_b32 m0, %3\n\ts_nop 0\n\tglobal_load_lds_dwordx4 %1, %2\n\ts_mov_b32 m0, %0"
               : "=&s"(keep) : "v"(off), "s"(base), "s"(lds) : "memory");
}
__device__ __forceinline__ u32x4 trfrag(LAS unsigned char* p0, LAS unsigned char* p1) {
  s16x4 a = __builtin_amdgcn_ds_read_tr16_b64_v4i16((LAS s16x4*)p0);
  s16x4 b = __builtin_amdgcn_ds_read_tr16_b64_v4i16((LAS s16x4*)p1);
  u32x2 x = __builtin_bit_cast(u32x2, a), y = __builtin_bit_cast(u32x2, b);
  return u32x4{x[0], x[1], y[0], y[1]};
}
#define NEWPAGE() asm volatile("s_branch 9f\n\t.p2align 12\n9:")
#define SB() __builtin_amdgcn_sched_barrier(0)
#define MEMB() asm volatile("" ::: "memory")
#define VM0() asm volatile("s_waitcnt vmcnt(0)" ::: "memory")
#define LGKM0() asm volatile("s_waitcnt lgkmcnt(0)" ::: "memory")
#define BAR() asm volatile("s_waitcnt lgkmcnt(0)\n\ts_barrier" ::: "memory")
#define TAG() do { u32 ep_; asm volatile("s_load_dword %0, %1, 0x0\n\ts_waitcnt lgkmcnt(0)" : "=s"(ep_) : "s"(ws) : "memory"); \
    tag = ((ep_ & 0x7f7f7f7fu) + 0x01010101u) ^ (ep_ & 0x80808080u); } while (0)
constexpr int SC1 = 16;
__device__ __forceinline__ u32x4 ld_sc1(__amdgpu_buffer_rsrc_t rs, int off) { return __builtin_amdgcn_raw_buffer_load_b128(rs, off, 0, SC1); }
__device__ __forceinline__ u32x4 ld_one(__amdgpu_buffer_rsrc_t rs, int off) { return __builtin_amdgcn_raw_buffer_load_b128(rs, off, 0, SC1 | 2); }
__device__ __forceinline__ u32x2 ld_sc1_64(__amdgpu_buffer_rsrc_t rs, int off) { return __builtin_amdgcn_raw_buffer_load_b64(rs, off, 0, SC1); }
__device__ __forceinline__ void st_sc1(__amdgpu_buffer_rsrc_t rs, int off, u32x4 v) { __builtin_amdgcn_raw_buffer_store_b128(v, rs, off, 0, SC1); }
__device__ __forceinline__ void st_sc1_64(__amdgpu_buffer_rsrc_t rs, int off, u32x2 v) { __builtin_amdgcn_raw_buffer_store_b64(v, rs, off, 0, SC1); }

__device__ __forceinline__ void ln_stats(u32x4 xv, float& m, float& inv) {
  const float x[8] = {blo(xv[0]), bhi(xv[0]), blo(xv[1]), bhi(xv[1]), blo(xv[2]), bhi(xv[2]), blo(xv[3]), bhi(xv[3])};
  const float x0 = __builtin_bit_cast(float, __builtin_amdgcn_readfirstlane(xv[0] << 16));
  float s = 0.f, v = 0.f;
#pragma unroll
  for (int j = 0; j < 8; ++j) { const float d = x[j] - x0; s += d; v = fmaf(d, d, v); }
  const float md = wave_sum(s) * (1.f / 512.f), sq = wave_sum(v) * (1.f / 512.f);
  const float var = rbf(fmaxf(fmaf(-md, md, sq), 0.f));
  m = rbf(x0 + md);
  inv = rcp(rbf(__builtin_amdgcn_sqrtf(rbf(var + 1e-5f))));
}
#define PACE_(W, INC) do { asm volatile(W "\n1:\n\ts_memrealtime vcc\n\ts_waitcnt lgkmcnt(0)\n\ts_lshl_b32 vcc_lo, vcc_lo, 2\n\ts_sub_i32 vcc_hi, vcc_lo, %0\n\ts_cmp_lt_i32 vcc_hi, 0\n\t" \
    "s_cbranch_scc0 2f\n\ts_sleep 1\n\ts_branch 1b\n2:\n\ts_add_i32 %0, %0, %1" : "+s"(due) : "is"(INC) : "memory", "scc", "vcc"); SB(); } while (0)
#define PACE(INC) PACE_("s_waitcnt vmcnt(2)", INC)
#define PACEN(INC) PACE_("", INC)
__device__ __forceinline__ void until_s(u32 due) { PACE_("", 0); }
__device__ __forceinline__ void until(u32 t) {
  asm volatile("1:\n\ts_memrealtime vcc\n\ts_waitcnt lgkmcnt(0)\n\ts_lshl_b32 vcc_lo, vcc_lo, 2\n\ts_sub_i32 vcc_hi, vcc_lo, %0\n\ts_cmp_lt_i32 vcc_hi, 0\n\t"
               "s_cbranch_scc1 1b" :: "s"(t) : "memory", "scc", "vcc");
  SB();
}

__device__ __forceinline__ void lds_wait(volatile LAS u32* p, u32 want) {
  _Pragma("unroll 1") for (u32 s = 0; s < LSPIN_CAP; ++s) {
    if (__builtin_amdgcn_readfirstlane(*p) >= want) break;
    __builtin_amdgcn_s_sleep(2);
  }
  MEMB();
}
__device__ __forceinline__ void lds_add(LAS u32* p, int lane) {
  LGKM0();
  if (lane == 0) __hip_atomic_fetch_add(p, 1u, __ATOMIC_RELAXED, __HIP_MEMORY_SCOPE_WORKGROUP);
  MEMB();
}
__device__ __forceinline__ void tile_part(LAS unsigned char* T, LAS unsigned char* P, u32x4 hb, int n, int g) {
  LAS unsigned char* tb = T + (8 * g + (n >> 2)) * 128 + (n & 3) * 8;
  const int sx = (n >> 3) | ((g & 1) << 1);
  f32x4 a[4];
#pragma unroll
  for (int t = 0; t < 4; ++t) a[t] = mfma(trfrag(tb + (t ^ sx) * 32, tb + (t ^ sx) * 32 + 512), hb, f32x4{0.f, 0.f, 0.f, 0.f});
  *(LAS f32x4*)(P) = n < 8 ? a[0] : a[1];
  *(LAS f32x4*)(P + 1024) = n < 8 ? a[2] : a[3];
}
__device__ __forceinline__ f32x4 tile_sum(LAS unsigned char* P) {
  f32x4 s[8], r[2];
#pragma unroll
  for (int k = 0; k < 2; ++k) {
#pragma unroll
    for (int i = 0; i < 8; ++i) s[i] = *(LAS f32x4*)(P + (8 * k + i) * 2048);
    SB();
    r[k] = ((s[0] + s[1]) + (s[2] + s[3])) + ((s[4] + s[5]) + (s[6] + s[7]));
    SB();
  }
  return r[0] + r[1];
}
#define GU_MUL(P) _Pragma("unroll") for (int i = 0; i < 8; ++i) gb_[i] = *(LAS u32x4*)((P) + i * 64); SB(); \
    aL = f32x4{0.f, 0.f, 0.f, 0.f}; aH = aL; _Pragma("unroll") for (int i = 0; i < 8; ++i) { aL = mfma(gf[i], gb_[i], aL); aH = mfma(gf[8 + i], gb_[i], aH); }
__device__ __forceinline__ void gu_pair(f32x4 aL, f32x4 aH, f32x2& gt, f32x2& up) {
  const auto s0 = __builtin_amdgcn_permlane32_swap(__float_as_uint(aL[0] + dppf<ROR8>(aH[0])), __float_as_uint(aL[2] + dppf<ROR8>(aH[2])), false, false);
  const auto s1 = __builtin_amdgcn_permlane32_swap(__float_as_uint(aL[1] + dppf<ROR8>(aH[1])), __float_as_uint(aL[3] + dppf<ROR8>(aH[3])), false, false);
  gt = f32x2{__uint_as_float(s0[0]), __uint_as_float(s1[0])};
  up = f32x2{__uint_as_float(s0[1]), __uint_as_float(s1[1])};
}
__device__ __forceinline__ int tline(int c, int ln, u32 near) { return WS_T + (c & 3) * T_SPAN + (ln >> 6) * 16384 + ((near >> c) & 1) * 8192 + (ln & 63) * 128; }
__device__ __forceinline__ int mpos(int c) { return (c & 252) * 4 + (c >> 8) * 8 + (c & 3) * 2; }
__device__ __forceinline__ u32 smax(int m) { int r = (int)fminf(wave_max((float)m), 4e5f); PIN(r); return (u32)__builtin_amdgcn_readfirstlane(r); }
__device__ __forceinline__ u32 now4() { return (u32)__builtin_amdgcn_s_memrealtime() * 4u; }
#define B0_LOAD() { bp = ld_sc1(rs, bo); bq = ld_sc1(rs, bo + 1024); }
__device__ __forceinline__ bool on_time(u32 tag, u32 org, u32x4 bp, u32x4 bq) {
  const u32 lim = org + OFO - DO;
  return __all((((bp[1] ^ tag) | (bp[3] ^ tag) | (bq[1] ^ tag) | (bq[3] ^ tag)) | (((lim - bp[0]) | (lim - bp[2]) | (lim - bq[0]) | (lim - bq[2])) >> 31)) == 0u);
}
__device__ __forceinline__ u32 anchor(__amdgpu_buffer_rsrc_t rs, int bo, u32 tag, u32 org, u32x4 bp, u32x4 bq) {
  int lk = OFB;
#pragma unroll 1
  for (u32 s = 0; s < SPIN_CAP; ++s) {
    if (__all(((bp[1] ^ tag) | (bp[3] ^ tag) | (bq[1] ^ tag) | (bq[3] ^ tag)) == 0u)) break;
    MEMB();
    do lk += PB; while ((int)(now4() - org) > lk);
    until_s(org + lk);
    B0_LOAD()
  }
  const int m = (int)smax(max(max((int)(bp[0] - org), (int)(bp[2] - org)), max((int)(bq[0] - org), (int)(bq[2] - org))));
  return org + (u32)max(m + DO <= OFO ? OFO : m + DL_, lk + RB - DP);
}
__device__ __forceinline__ float asf(u32 v) { return __builtin_bit_cast(float, v); }

struct Args {
  const unsigned char* half[4][2];
  const u16 *x, *kc, *vc, *wd, *cc;
  u16* out;
  unsigned char* ws;
};

__global__ void __launch_bounds__(1024, 4) fused(const Args a) {
  extern __shared__ __attribute__((aligned(16))) unsigned char smem[];
  LAS unsigned char* L = (LAS unsigned char*)smem;
  const u32 lbase = (u32)(size_t)L;
  const int tid = threadIdx.x, w = __builtin_amdgcn_readfirstlane(tid >> 6), bid = blockIdx.x;
  int lane = tid & 63;
  const int xcd = bid & 7, idx = bid >> 3, hj = idx & 3, h = 4 * xcd + hj, b = idx >> 2, bh = b * 32 + h;
  const int sw8 = hj == 3 ? (w & 1) * 8 : 0, rw = (hj == 3 ? 24 : 32) * w;
  u64 hA, hB, t64, t1;
  u32 warm;
  asm volatile("s_load_dwordx2 %0, %4, %5\n\ts_load_dwordx2 %1, %4, %6\n\ts_load_dword %3, %4, 0x40\n\ts_memrealtime %2" : "=&s"(hA), "=&s"(hB), "=&s"(t64), "=&s"(warm)
               : "s"(__builtin_amdgcn_kernarg_segment_ptr()), "s"(16 * hj + sw8), "s"(16 * hj + (sw8 ^ 8)));
  SB();
  const int bb = (b + xcd) & 7;
  int p3r = 5 * xcd + min(xcd, 3) + bb;
  int mc = (b & 6) + ((xcd & 1) ^ 1), mg = ((xcd & 6) + b) & 7;
  const bool tq = hj == 0, to = hj == 2;
  int lim = hj == 1 ? 6 - b : (xcd < 3 ? 6 : 5) - bb;
  asm volatile("" : "+s"(p3r), "+s"(mc), "+s"(mg), "+s"(lim));
  const bool idle = (hj & 1) && lim <= 0;
  u64 tp0, tp1;
  {
    const u32 pitch = (u32)(0x0AC0040018002000ull >> (16 * hj)) & 0xffffu;
    u32 ce = (xcd << (to ? 13 : 4)) + 2 * b, c1 = 16 * mc + 2 * mg;
    asm volatile("" : "+s"(ce), "+s"(c1));
    const u32 col = 64 * ((hj & 1) ? (hj == 1 ? c1 : p3r) : ce);
    const u32 step = pitch << (to ? 1 : 3);
    u32 off = (rw + (to ? (lane & 24) + (lane >> 5) : lane >> 3)) * pitch + col + ((lane & 3) ^ ((lane >> 3) & 2)) * 16;
    tp0 = off;
    tp1 = off + step;
    u64 s2 = 2 * step;
    u32 ld = lbase + L_W + rw * 128;
    asm volatile("" : "+s"(s2), "+s"(ld), "+v"(tp0), "+v"(tp1));
    asm volatile("s_mov_b32 exec_lo, 0xf0f0f0f0\n\ts_mov_b32 exec_hi, 0xf0f0f0f0\n\ts_waitcnt lgkmcnt(0)\n\ts_memrealtime %6\n\tv_lshl_add_u64 %0, %0, 0, %3\n\tv_lshl_add_u64 %1, %1, 0, %2\n\t"
                 "s_not_b64 exec, exec\n\tv_lshl_add_u64 %0, %0, 0, %2\n\tv_lshl_add_u64 %1, %1, 0, %3\n\ts_mov_b64 exec, -1"
                 : "+v"(tp0), "+v"(tp1), "+s"(hA), "+s"(hB), "+s"(t64), "+s"(warm), "=&s"(t1) :: "scc");
    if (!idle) {
      u32 keep;
      asm volatile("s_mov_b32 %0, m0\n\ts_mov_b32 m0, %3\n\ts_nop 0\n\tglobal_load_lds_dwordx4 %1, off nt\n\ts_add_u32 m0, m0, 0x400\n\t"
                   "v_lshl_add_u64 %1, %1, 0, %4\n\tglobal_load_lds_dwordx4 %2, off nt\n\ts_mov_b32 m0, %0\n\tv_lshl_add_u64 %2, %2, 0, %4"
                   : "=&s"(keep), "+v"(tp0), "+v"(tp1) : "s"(ld), "s"(s2) : "memory", "scc");
      glds((const void*)tp0, ld + 2048);
      if (hj != 3) glds((const void*)tp1, ld + 3072);
    }
  }
  SB();
  u32 t0 = (u32)t64;
  const u16 *x = a.x, *kc = a.kc, *vc = a.vc, *wd = a.wd, *cc = a.cc;
  u16* out = a.out;
  unsigned char* ws = a.ws;
  const int p3 = (hj == 3 && !idle) ? p3r : -1;
  const bool tm = hj == 1 && !idle;
  u32 tag;
  TAG();
  asm volatile("" : "+s"(t1));
  t0 += (u32)(KW * max((int)((u32)t1 - t0) - KA, 0)) / 4u;
  const __amdgpu_buffer_rsrc_t rs = __builtin_amdgcn_make_buffer_rsrc((void*)(((size_t)ws + (size_t)((tag & 63u) + 1u) * WS_STEP) & ~(size_t)(WS_STEP - 1)), 0, WS_END, 0x00020000);
  if (idle && hj == 1) {
    if (w == 0) {
      st_sc1(rs, WS_G1 + ((b & 1) * 8 + xcd) * 1024 + lane * 16, u32x4{~0u, ~0u, ~0u, ~0u});
      VM0();
      if (lane == 0) st_sc1_64(rs, WS_TS + ((b & 1) * 8 + xcd) * 8, u32x2{t0, tag});
    } else {
      u64 pc;
      u32 len;
      asm volatile("s_getpc_b64 %0\n1:\n\ts_mov_b32 %1, fused_code_end - 1b + 1000\n\ts_max_u32 %1, %1, fused_cold_end - 1b + 1000" : "=s"(pc), "=&s"(len) :: "scc");
      const u32 o = ((b & 1) * 15 + w - 1) * 1024;
      if (o < len) touch((const void*)(pc & ~15ull), min(o + lane * 16, len & ~15u), lbase + L_SINK);
    }
  }
  if (w == 1) glds((const unsigned char*)((lane & 1) ? vc : kc) + ((size_t)bh * SK + (SK - WIN)) * 256, lbase + L_SINK);
  int ta = TAU;
  int n = lane & 15, g = lane >> 4;
  u32x4 cv, hb;
  const bool row = tm && (w >= 8 || mc == 1);
  if (row) cv = ldnt((w < 8 ? x : cc) + (w & 7) * 512 + lane * 8);
  if (tid < 16) ((volatile LAS u32*)(L + L_CTL))[tid] = tid == C_T0 ? t0 : 0u;
  BAR();
  t0 = __builtin_amdgcn_readfirstlane(((volatile LAS u32*)(L + L_CTL))[C_T0]);
  u32 due = t0 * 4u + (u32)((4 * bb + hj) * TAU) / 32u + DUE0;
  if (hj == 3 && bb >= 6 && w == 0) {
    const int k = (bb & 1) * 8 + xcd, o = WS_NP + k * 512 + (bb & 1) * 8192;
    __builtin_amdgcn_raw_buffer_store_b32(tag, rs, lane < 2 ? o ^ (lane * 8192) : WS_END, 0, SC1);
    u64 c0, c1, c2;
    u32 v;
    asm volatile("s_waitcnt vmcnt(0)\n\ts_memtime %0\n\ts_waitcnt lgkmcnt(0)\n\tbuffer_load_dword %3, %4, %6, 0 offen sc1\n\ts_waitcnt vmcnt(0)\n\ts_memtime %1\n\ts_waitcnt lgkmcnt(0)\n\t"
                 "buffer_load_dword %3, %5, %6, 0 offen sc1\n\ts_waitcnt vmcnt(0)\n\ts_memtime %2\n\ts_waitcnt lgkmcnt(0)"
                 : "=&s"(c0), "=&s"(c1), "=&s"(c2), "=&v"(v) : "v"(o), "v"(o ^ 8192), "s"(rs) : "memory");
    const int d = min(max(2 * (int)c1 - (int)c0 - (int)c2, -600), 600);
    if (lane == 0) st_sc1_64(rs, WS_NV + k * 8, u32x2{(u32)(2 * ((bb & 1) ? -d : d)) | ((__builtin_amdgcn_s_getreg(20 | (3 << 11)) >> 2) & 1u), tag});
  }
  int rot = idx;
#define RO(I) ((((I) + rot) & 15) * 8192)
  const u16* kp = kc + ((size_t)bh * SK + (SK - WIN) + 4 * w) * 128;
  u32 l16 = lane * 16;
  PIN(l16);
#define LDR(D, P, I, W) { const GAS unsigned char* p_ = (const GAS unsigned char*)((P) + RO(I)) + l16; PIN(p_); W; D = __builtin_nontemporal_load((const GAS u32x4*)p_); SB(); }
  u32x4 kr[16], vr[16];
  volatile LAS u32* ctl = (volatile LAS u32*)(L + L_CTL);
#define RELANE() do { asm volatile("" : "+v"(lane)); n = lane & 15; g = lane >> 4; } while (0)
  const int e = b * 16 + 2 * xcd + hj;
  const bool red = hj < 2;

#define KL(I) { const int t_ = ta + extra(I); LDR(kr[I], kp, I, PACE(t_)) }
  KL(0)
  if (row) {
    if (w >= 8) {
#pragma unroll
      for (int j = 0; j < 4; ++j) { const float a0 = blo(cv[j]), a1 = bhi(cv[j]); hb[j] = pack2(a0 * rcp(1.f + __expf(-a0)), a1 * rcp(1.f + __expf(-a1))); }
      *(LAS u32x4*)(L + L_H2 + (w & 7) * H2S + lane * 16) = hb;
      lds_add((LAS u32*)(L + L_CTL) + C_R, lane);
    } else {
      float m, inv;
      ln_stats(cv, m, inv);
      if ((lane >> 3) == mg) {
        LAS f32x4* d = (LAS f32x4*)(L + L_RED + w * 256 + (lane & 7) * 32);
        d[0] = f32x4{rbf(rbf(blo(cv[0]) - m) * inv), rbf(rbf(bhi(cv[0]) - m) * inv), rbf(rbf(blo(cv[1]) - m) * inv), rbf(rbf(bhi(cv[1]) - m) * inv)};
        d[1] = f32x4{rbf(rbf(blo(cv[2]) - m) * inv), rbf(rbf(bhi(cv[2]) - m) * inv), rbf(rbf(blo(cv[3]) - m) * inv), rbf(rbf(bhi(cv[3]) - m) * inv)};
      }
    }
  }
  KL(1)
  u32x2 tsv;
#define TS_WAIT() if (!__all(tsv[1] == tag)) { _Pragma("unroll 1") for (u32 s = 0; s < SPIN_CAP; ++s) { __builtin_amdgcn_s_sleep(16); MEMB(); \
      tsv = ld_sc1_64(rs, WS_TS + (lane & 15) * 8); if (__all(tsv[1] == tag)) break; } } MEMB();
  if (tm) {
    asm volatile("s_waitcnt vmcnt(2)" ::: "memory");
    lds_wait(ctl + C_R, 8u);
    hb = *(LAS u32x4*)(L + L_H2 + (n & 7) * H2S + (32 * w + 8 * g) * 2);
    tile_part(L + L_W + w * 4096, L + L_TP + w * 2048 + lane * 16, hb, n, g);
    lds_add((LAS u32*)(L + L_CTL) + C_T, lane);
  }
  KL(2)
  if (w == 0 || (tm && w >= 14)) tsv = ld_sc1_64(rs, WS_TS + (lane & 15) * 8);
  SB();
  if (tm && w >= 14) {
    lds_wait(ctl + C_T, 16u);
    const int col = 32 * (w & 1) + 16 * (n >> 3) + 4 * g;
    const f32x4 s = tile_sum(L + L_TP + (w & 1) * 1024 + lane * 16);
    float v[4] = {rbf(s[0]), rbf(s[1]), rbf(s[2]), rbf(s[3])};
    if (mc == 1) {
      const f32x4 ln = *(LAS f32x4*)(L + L_RED + (n & 7) * 256 + col * 4);
#pragma unroll
      for (int j = 0; j < 4; ++j) v[j] = ln[j] * rbf(1.f + v[j]);
    }
    TS_WAIT()
    st_sc1_64(rs, mc < 2 ? WS_G1 + (2 * mg + (w & 1)) * 1024 + ((2 * (n >> 3) + (g >> 1)) * 16 + mc * 8 + (n & 7)) * 16 + (g & 1) * 8
                         : WS_MODR + ((mc - 2) * 8 + (n & 7)) * 1024 + mpos(64 * mg + col),
              u32x2{canon(pack2(v[0], v[1])), canon(pack2(v[2], v[3]))});
  }
  RELANE();
  KL(3) KL(4)
  if (w == 0) {
    TS_WAIT()
    const float d = __builtin_amdgcn_fmed3f((float)(int)(tsv[0] - t0), -1e5f, 1e5f);
    ctl[C_TB] = (t0 * 4u + (u32)(int)fmaxf(row_sum(d) * 0.25f, row_max(d) * 4.f - SKW)) | 1u;
  }
  KL(5) KL(6)
  u32 org = __builtin_amdgcn_readfirstlane(ctl[C_TB]);
  if (org) { const int adj = ((int)(org - t0 * 4u - (p3 >= 0 ? bb * MSH : 0)) * 85 + 1024) >> 11; ta += adj; }
  KL(7)
  if (tm && mc >= 2 && w >= 14 && lane == 0) __builtin_amdgcn_raw_buffer_store_b32(tag, rs, WS_FLAGS + (((mc - 2) * 8 + mg) * 2 + (w & 1)) * 4, 0, SC1);
  KL(8)
  u32x4 g0;
  const int o0 = WS_G1 + w * 1024 + lane * 16;
  if (tq) lds_wait(ctl + C_TB, 1u);
#define KG(K) { KL(K) if (tq && (w & 1) == ((K) & 1)) { until_s(due - TAU / 2); \
    asm volatile("buffer_load_dwordx4 %0, %1, %2, 0 offen sc1" : "=&v"(g0) : "v"(o0), "s"(rs) : "memory"); } }
  KG(9) KG(10)
  KL(11) KL(12)
  if (tq) {
    asm volatile("s_waitcnt vmcnt(2)" : "+v"(g0));
#define G1_OK() __all(g0[0] != ~0u && g0[1] != ~0u && g0[2] != ~0u && g0[3] != ~0u)
    if (!G1_OK()) {
#pragma unroll 1
      for (u32 s = 0; s < SPIN_CAP; ++s) {
        __builtin_amdgcn_s_sleep(16);
        MEMB();
        g0 = ld_sc1(rs, o0);
        if (G1_OK()) break;
      }
    }
#pragma unroll
    for (int j = 0; j < 4; ++j) { const u32 o = dppu<ROR8>(g0[j]); hb[j] = pack2(blo(g0[j]) + blo(o), bhi(g0[j]) + bhi(o)); }
    PIN(hb);
  }
  KL(13)
  if (tq) {
    tile_part(L + L_W + w * 4096, L + L_TP + w * 2048 + lane * 16, hb, n, g);
    lds_add((LAS u32*)(L + L_CTL) + C_T, lane);
  }
  KL(14)
  if (tq && w >= 14) {
    lds_wait(ctl + C_T, 16u);
    const f32x4 s = tile_sum(L + L_TP + (w & 1) * 1024 + lane * 16);
    st_sc1(rs, WS_Q + ((n & 7) * 32 + 4 * xcd + (b >> 1)) * 512 + ((b & 1) * 64 + 32 * (w & 1) + 16 * (n >> 3) + 4 * g) * 4, u32x4{pack2(s[0], s[1]), tag, pack2(s[2], s[3]), tag});
  }
  RELANE();
  KL(15)
  asm volatile("" : "+s"(rot));
  const u16* vp = vc + ((size_t)bh * SK + (SK - WIN) + 4 * w) * 128;
#define VL(I) { const int t_ = ta + extra(16 + (I)); LDR(vr[I], vp, I, PACE(t_)) }
#define VLN(I) { const int t_ = ta + extra(16 + (I)); LDR(vr[I], vp, I, PACEN(t_)) }
  VL(0) VL(1)
  VL(2)
  u32x2 gq;
#define FL_WAIT() if (!__all(fl == tag)) {   _Pragma("unroll 1") for (u32 s = 0; s < SPIN_CAP; ++s) { __builtin_amdgcn_s_sleep(4); MEMB(); \
      fl = __builtin_amdgcn_raw_buffer_load_b32(rs, WS_FLAGS + lane * 4, 0, SC1); if (__all(fl == tag)) break; } } MEMB();
  u32 fl;
  u32x2 nv;
  if (w == WG_) {
    nv = ld_sc1_64(rs, WS_NV + (lane & 15) * 8);
    gq = ld_sc1_64(rs, WS_Q + bh * 512 + lane * 8);
    fl = __builtin_amdgcn_raw_buffer_load_b32(rs, !to ? WS_FLAGS + lane * 4 : WS_END, 0, SC1);
  }
  SB();
  VLN(3) VLN(4)
  if (w == WG_) {
    if (!__all(gq[1] == tag)) {
#pragma unroll 1
      for (u32 s = 0; s < SPIN_CAP; ++s) {
        __builtin_amdgcn_s_sleep(4);
        MEMB();
        gq = ld_sc1_64(rs, WS_Q + bh * 512 + lane * 8);
        if (__all(gq[1] == tag)) break;
      }
    }
    *(LAS u32*)(L + L_Q + lane * 4) = gq[0];
    PIN(fl);
    if (!to) { FL_WAIT() }
    PIN(nv);
    if (!__all(nv[1] == tag)) {
#pragma unroll 1
      for (u32 s = 0; s < SPIN_CAP; ++s) {
        __builtin_amdgcn_s_sleep(4);
        MEMB();
        nv = ld_sc1_64(rs, WS_NV + (lane & 15) * 8);
        if (__all(nv[1] == tag)) break;
      }
    }
    const float dv = (float)((int)nv[0] >> 1);
    const u32 m4 = (u32)__ballot((row_sum((nv[0] & 1u) ? -dv : dv) > 0.f) != ((nv[0] & 1u) != 0u)) & 15u;
    LGKM0();
    ctl[C_Q] = 256u | m4 | ((m4 ^ 15u) << 4);
  }
  RELANE();
  VL(5)
  lds_wait(ctl + C_Q, 1u);
  const bool prer = p3 >= 0 && w < 8;
  u32x4 pre0, pre1;
  if (prer) {
    pre0 = ld_sc1(rs, WS_MODR + (8 + w) * 1024 + lane * 16);
    pre1 = ld_sc1(rs, WS_MODR + (16 + w) * 1024 + lane * 16);
  }
  SB();
  const u32x4 qf = *(LAS u32x4*)(L + L_Q + n * 16);

#define DOT(I) dot2(kr[I][3], qf[3], dot2(kr[I][2], qf[2], dot2(kr[I][1], qf[1], dot2(kr[I][0], qf[0], 0.f))))
  float xs[8];
#define QK(C) { _Pragma("unroll") for (int i = 2 * (C); i < 2 * (C) + 2; ++i) { const float lo_ = DOT(i), hi_ = DOT(i + 8); \
      const float t1_ = lo_ + dppf<ROR8>(lo_), t2_ = hi_ + dppf<ROR8>(hi_); xs[i] = (n & 8) ? t2_ : t1_; PIN(xs[i]); } SB(); }
  QK(0) VLN(6) QK(1) VLN(7) QK(2) VL(8) QK(3) VL(9)
  if (prer) {
    const u32 a_h2w = w * C1S + lane * 8;
    *(LAS u32x2*)(L + L_C1 + a_h2w) = u32x2{pk2(up2(pre1[0]) + 1.f), pk2(up2(pre1[1]) + 1.f)};
    *(LAS u32x2*)(L + L_C1 + a_h2w + 512) = u32x2{pk2(up2(pre1[2]) + 1.f), pk2(up2(pre1[3]) + 1.f)};
    *(LAS u32x2*)(L + L_C1 + 8 * C1S + a_h2w) = u32x2{pre0[0], pre0[1]};
    *(LAS u32x2*)(L + L_C1 + 8 * C1S + a_h2w + 512) = u32x2{pre0[2], pre0[3]};
  }
#define TR_STEP(HALF, CTRL, BIT) _Pragma("unroll") for (int hh = 0; hh < (HALF); ++hh) { \
    const float lo_ = xs[hh], hi_ = xs[hh + (HALF)]; const float t1_ = lo_ + dppf<CTRL>(lo_), t2_ = hi_ + dppf<CTRL>(hi_); xs[hh] = (n & (BIT)) ? t2_ : t1_; }
  TR_STEP(4, XOR7, 4) TR_STEP(2, XOR2, 2) TR_STEP(1, XOR1, 1)
#undef TR_STEP
  float sc = rbf(rbf(xs[0]) * 0.08838834764831845f);
  PIN(sc);
  SB();
  VL(10)
  {
    const float mw_ = wave_max(sc);
    const float lw = wave_sum(__expf(sc - mw_));
    if (lane == 0) *(LAS f32x2*)(L + L_ML + w * 8) = f32x2{mw_, lw};
    lds_add((LAS u32*)(L + L_CTL) + C_ML, lane);
  }
  SB();
  VL(11)
  NEWPAGE();
  RELANE();
  u32 mk[4];
#pragma unroll
  for (int j = 0; j < 4; ++j) mk[j] = (n >> 1) == j ? ((n & 1) ? 0xffff0000u : 0x0000ffffu) : 0u;
  f32x4 oa = {0.f, 0.f, 0.f, 0.f};
#define PV(I) { const u32 pb_ = dppu<0x150 + (I)>(pd); oa = mfma(vr[I], u32x4{pb_ & mk[0], pb_ & mk[1], pb_ & mk[2], pb_ & mk[3]}, oa); asm volatile("" : "+v"(oa)); SB(); }
  PIN(mk[0]); PIN(mk[1]); PIN(mk[2]); PIN(mk[3]);
  u32 a_oa = L_RED + w * OSTR + (g * 8 + n) * 16;
  u32 ra = L_RED + (lane >> 5) * 8 * OSTR + (lane & 31) * 16;
  const u32 near = __builtin_amdgcn_readfirstlane(ctl[C_Q]);
  u32 rb_ = tline(xcd, LN_O + (hj * 8 + b) * 4, near) + (lane & 31) * 16 + (lane >> 5) * 8;
  if (w == 14) rb_ = lane == 0 ? WS_B0 + bid * 8 : WS_END;
  int oo = tline(xcd, LN_O + 8 * w, near) + lane * 16;
  PIN(oo);
  u32 tagv = tag;
  PIN(a_oa); PIN(ra); PIN(rb_); PIN(tagv);
  org = __builtin_amdgcn_readfirstlane(ctl[C_TB]);
  org = org ? org + TEND : t0 * 4u;
  asm volatile("" : "+s"(org));
  VL(12)
  lds_wait(ctl + C_ML, 16u);
  u32 pd;
  {
    const f32x2 ml = *(LAS f32x2*)(L + L_ML + n * 8);
    const float M = row_max(ml[0]);
    const float pe = __expf(sc - M) * rcp(row_sum(__expf(ml[0] - M) * ml[1]));
    pd = pack2(pe, pe);
    PIN(pd);
  }
  SB();
  VL(13)
  PV(0) PV(1) PV(2) PV(3) PV(4)
  VL(14)
  PV(5) PV(6) PV(7) PV(8) PV(9)
  VL(15)
  PV(10) PV(11) PV(12) PV(13) PV(14)
  if (w == 13) __builtin_amdgcn_s_setprio(3);
  PV(15)
#undef PV
  if (n < 8) *(LAS f32x4*)(L + a_oa) = oa;
  BAR();
  if (w == 14) st_sc1_64(rs, rb_, u32x2{now4(), tagv});
  if (w == 13) {
    f32x4 s[8];
#pragma unroll
    for (int ww = 0; ww < 8; ++ww) s[ww] = *(LAS f32x4*)(L + ra + ww * OSTR);
    SB();
    const f32x4 t = ((s[0] + s[1]) + (s[2] + s[3])) + ((s[4] + s[5]) + (s[6] + s[7]));
    st_sc1_64(rs, rb_, u32x2{pack2(t32(t[0], t[2]), t32(t[1], t[3])), tagv});
  }
  if (to) {
    until(org + OFO);
    u32x4 og = ld_sc1(rs, oo);
    SB();
    u32x4 wf[16];
    if (w < 4) {
      __builtin_amdgcn_s_setprio(3);
      const u32 af = L_W + (8 * g + (n >> 2)) * 128 + (w ^ ((n >> 3) | ((g & 1) << 1))) * 32 + (n & 3) * 8;
#pragma unroll
      for (int ks = 0; ks < 16; ++ks) wf[ks] = trfrag(L + af + ks * 4096, L + af + ks * 4096 + 512);
    }
    u32 a_ow = L_TP + (2 * w + (lane >> 5)) * OBS + (lane & 31) * 8;
    u32 a_or = L_TP + ((n >> 3) * 16 + (n & 7)) * OBS + g * 16;
    int rc = n < 8 ? tline(b, LN_PART + (2 * n + ((w >> 1) & 1)) * 8 + xcd, near) + (w & 1) * 64 + g * 16 : WS_END;
    PIN(a_ow); PIN(a_or); PIN(rc);
    SEL4();
    SB();
    *(LAS u32x2*)(L + a_ow) = u32x2{og[0], og[2]};
    if (__builtin_expect(!__all(((og[1] ^ tag) | (og[3] ^ tag)) == 0u), 0)) {
#pragma unroll 1
      for (u32 s = 0; s < SPIN_CAP; ++s) {
        MEMB();
        og = ld_sc1(rs, oo);
        if (__all(((og[1] ^ tag) | (og[3] ^ tag)) == 0u)) break;
        __builtin_amdgcn_s_sleep(2);
      }
      *(LAS u32x2*)(L + a_ow) = u32x2{og[0], og[2]};
    }
    BAR();
    if (w < 4) {
      u32x4 ob[8];
#pragma unroll
      for (int i = 0; i < 8; ++i) ob[i] = *(LAS u32x4*)(L + a_or + (i >> 2) * 8 * OBS + (i & 3) * 64);
      SB();
      f32x4 aL = {0.f, 0.f, 0.f, 0.f}, aH = {0.f, 0.f, 0.f, 0.f};
#pragma unroll
      for (int i = 0; i < 8; ++i) { aL = mfma(wf[i], ob[i], aL); aH = mfma(wf[8 + i], ob[i], aH); }
      const f32x4 v = {aL[0] + dppf<ROR8>(aH[0]), aL[1] + dppf<ROR8>(aH[1]), aL[2] + dppf<ROR8>(aH[2]), aL[3] + dppf<ROR8>(aH[3])};
      st_sc1(rs, rc, tag8(v, tagv, sel));
    }
  }

  const int bo = WS_B0 + lane * 16;
  if (p3 >= 0) {
    u32 a_h2w = w * C1S + lane * 8;
    if (w == 8 && lane < 36) *(LAS u32*)(L + (lane < 32 ? L_ML : L_HH + 640) + lane * 4) = 0u;
    BAR();
    glds((const void*)(tp0 + (u32)((368 - 16 * w) * FF * 2)), lbase + L_W + 49152 + w * 1024);
    u32x4 bp, bq;
    if (w == 12) {
      until_s(org + OFB + 20);
      asm volatile("buffer_load_dwordx4 %0, %2, %3, 0 offen sc1\n\tbuffer_load_dwordx4 %1, %2, %3, 0 offen offset:1024 sc1" : "=&v"(bp), "=&v"(bq) : "v"(bo), "s"(rs) : "memory");
    }
#define DL(R) glds(wd + (size_t)(32 * p3 + (R)) * 512, (lane ^ ((((R) & 3) | (((R) >> 1) & 4)) << 1)) * 16, lbase + L_W + 65536 + (R) * 1024)
    if (w < 8) {
      until_s(org + OFB + WDT);
#pragma unroll
      for (int i = 0; i < 4; ++i) DL(4 * w + i);
    }
    SB();
    u32 a_h2r = L_H2 + (n & 7) * H2S + (n >> 3) * 512 + g * 16;
    u32 a_hw = L_HH + (n < 8 ? n * 64 + (8 * (w & 3) + 4 * (g & 1) + 2 * (g >> 1)) * 2 : 512 + lane * 4);
    const u32x4 t16v = {tag & 0xffffu, tag >> 16, tag & 0xffffu, tag >> 16};
#define X_TIME() until_s(org + OFO + DX - 40); u32 tx = 2u; \
      _Pragma("unroll 1") for (u32 s = 0; s < LSPIN_CAP && tx == 2u; ++s) { tx = __builtin_amdgcn_readfirstlane(ctl[C_TX]); if (tx == 2u) __builtin_amdgcn_s_sleep(2); } \
      if (!(tx & 1u)) tx = org + OFO + DX;
#define X_BAD(A, B) ((or4(A) | or4(B)) & 0xffffu)
    if (w < 8) {
      const u32x2 ca = *(LAS u32x2*)(L + L_C1 + a_h2w), cb = *(LAS u32x2*)(L + L_C1 + a_h2w + 512);
      f32x2 c1[4] = {up2(ca[0]), up2(ca[1]), up2(cb[0]), up2(cb[1])};
      a_h2w = L_H2 + w * H2S + lane * 8;
      int xo = tline(xcd, LN_X2 + 16 * w, near) + lane * 16;
      u32 a_hr = L_HH + (n < 8 ? n * 64 + g * 16 : 768), a_hs = L_HH + (n < 8 ? 768 : (n & 7) * 64 + g * 16);
      PIN(a_hs);
      SEL4();
      const u32 a_d0 = L_W + 65536 + (8 * g + (n >> 2)) * 1024 + ((8 * w + ((n >> 1) & 1)) ^ (((n >> 2) | ((g & 1) << 2)) << 1)) * 16 + (n & 1) * 8;
      int yo = tline(w, LN_YP + (4 * (n >> 3) + g) * NP3 + p3, near) + (n & 7) * 16, yo2 = tline(w, LN_YP + (8 + 4 * (n >> 3) + g) * NP3 + p3, near) + (n & 7) * 16;
      PIN(xo); PIN(c1[0]); PIN(c1[1]); PIN(c1[2]); PIN(c1[3]); PIN(a_h2w); PIN(a_hr); PIN(yo); PIN(yo2);
      u32 a_cx = lbase + L_CTL + (C_X + w) * 4;
      PIN(a_cx);
      asm volatile("s_waitcnt vmcnt(4)" ::: "memory");
      BAR();
      __builtin_amdgcn_s_setprio(2);
      X_TIME()
      until(tx);
#pragma unroll 1
      for (u32 s = 0; s < SPIN_CAP; ++s) {
        const u32x4 xa = ld_sc1(rs, xo) ^ t16v, xb = ld_sc1(rs, xo + 1024) ^ t16v;
        const float x0 = __builtin_bit_cast(float, __builtin_amdgcn_readfirstlane(xa[0]));
        *(LAS u32x2*)(L + a_h2w) = u32x2{pk2((f32x2{asf(xa[0]), asf(xa[1])} - x0) * c1[0]), pk2((f32x2{asf(xa[2]), asf(xa[3])} - x0) * c1[1])};
        *(LAS u32x2*)(L + a_h2w + 512) = u32x2{pk2((f32x2{asf(xb[0]), asf(xb[1])} - x0) * c1[2]), pk2((f32x2{asf(xb[2]), asf(xb[3])} - x0) * c1[3])};
        if (__builtin_expect(__all(X_BAD(xa, xb) == 0u), 1)) break;
        MEMB();
      }
      asm volatile("s_mov_b64 exec, 1\n\tds_write_b32 %0, %0\n\ts_mov_b64 exec, -1" :: "v"(a_cx) : "memory");
      __builtin_amdgcn_s_setprio(0);
      lds_add((LAS u32*)(L + L_CTL) + C_R, lane);
      lds_wait(ctl + C_R, 8u);
      u32x4 df[4];
#pragma unroll
      for (int i = 0; i < 4; ++i) { df[i] = trfrag(L + (a_d0 ^ (32 * i)), L + (a_d0 ^ (32 * i)) + 4096); PIN(df[i]); }
      BAR();
      const u32x4 h0 = *(LAS u32x4*)(L + a_hr), h1 = *(LAS u32x4*)(L + a_hs);
      const f32x4 z = {0.f, 0.f, 0.f, 0.f};
      const f32x4 a0 = mfma(df[1], h1, mfma(df[0], h0, z)), a1 = mfma(df[3], h1, mfma(df[2], h0, z));
      st_sc1(rs, yo, tag8(a0, tagv, sel));
      st_sc1(rs, yo2, tag8(a1, tagv, sel));
    } else if (w < 12) {
      NEWPAGE();
      u32 a_st = L_ML + (n & 7) * 16;
      PIN(a_st);
      __builtin_amdgcn_s_setprio(3);
      u32x4 gf[16], gb_[8];
      const u32 af = L_W + (8 * g + (n >> 2)) * 128 + (((w & 3) + (n & 2) * 2) ^ (((n >> 3) | ((g & 1) << 1)) << 1)) * 16 + (n & 1) * 8;
      f32x4 aL = {0.f, 0.f, 0.f, 0.f}, aH;
      f32x2 ag, au, sg, su, gt, up;
      {
#pragma unroll
        for (int q4 = 0; q4 < 4; ++q4) {
#pragma unroll
          for (int i = 0; i < 4; ++i) gb_[i] = *(LAS u32x4*)(L + L_C1 + n * C1S + g * 16 + q4 * 256 + i * 64);
          if (q4 == 3) {
            VM0();
            BAR();
          }
#pragma unroll
          for (int i = 0; i < 4; ++i) gf[4 * q4 + i] = trfrag(L + af + (4 * q4 + i) * 4096, L + af + (4 * q4 + i) * 4096 + 512);
          SB();
#pragma unroll
          for (int i = 0; i < 4; ++i) aL = mfma(gf[4 * q4 + i], gb_[i], aL);
          PIN(aL);
        }
        const auto s0 = __builtin_amdgcn_permlane32_swap(__float_as_uint(aL[0]), __float_as_uint(aL[2]), false, false);
        const auto s1 = __builtin_amdgcn_permlane32_swap(__float_as_uint(aL[1]), __float_as_uint(aL[3]), false, false);
        ag = -f32x2{__uint_as_float(s0[0]), __uint_as_float(s1[0])};
        au = -f32x2{__uint_as_float(s0[1]), __uint_as_float(s1[1])};
        sg = f32x2{dppf<ROR8>(ag[0]), dppf<ROR8>(ag[1])} * 1.4426950408889634f;
        su = -f32x2{dppf<ROR8>(au[0]), dppf<ROR8>(au[1])};
        PIN(ag); PIN(au); PIN(sg); PIN(su); PIN(a_h2r); PIN(a_hw);
      }
      __builtin_amdgcn_s_setprio(0);
      _Pragma("unroll 1") for (u32 s = 0; s < LSPIN_CAP; ++s) if (__all(ctl[C_X + (lane & 7)] != 0u)) break;
      __builtin_amdgcn_s_setprio(3);
      MEMB();
      GU_MUL(L + a_h2r)
      SB();
      f32x4 st = *(volatile LAS f32x4*)(L + a_st);
      gu_pair(aL, aH, gt, up);
      if (!__all(st[3] != 0.f)) {
        _Pragma("unroll 1") for (u32 s = 0; s < LSPIN_CAP; ++s) { st = *(volatile LAS f32x4*)(L + a_st); if (__all(st[3] != 0.f)) break; }
      }
      gt = __builtin_elementwise_fma(__builtin_elementwise_fma(ag, f32x2{st[0], st[0]}, gt), f32x2{st[2], st[2]}, sg);
      up = __builtin_elementwise_fma(__builtin_elementwise_fma(au, f32x2{st[0], st[0]}, up), f32x2{st[1], st[1]}, su);
      *(LAS u32*)(L + a_hw) = pk2(f32x2{rcp(1.f + __builtin_amdgcn_exp2f(gt[0])), rcp(1.f + __builtin_amdgcn_exp2f(gt[1]))} * up);
      BAR();
    } else {
      int xo = tline(xcd, LN_X2 + 32 * (w - 12), near) + lane * 16;
      u32 a_st = lbase + L_ML + (2 * (w - 12) + (lane >> 5)) * 16;
      PIN(xo); PIN(a_st);
      if (w == 12) asm volatile("s_waitcnt vmcnt(2)" ::: "memory"); else VM0();
      BAR();
      asm volatile("s_waitcnt vmcnt(0)" : "+v"(bp), "+v"(bq) :: "memory");
      if (w == 12 && __builtin_expect(!on_time(tag, org, bp, bq), 0)) {
        ctl[C_TX] = 2u;
        ctl[C_TX] = (anchor(rs, bo, tag, org, bp, bq) + DX) | 1u;
      }
      __builtin_amdgcn_s_setprio(1);
      X_TIME()
      until(tx + SDL);
      u32x4 xv[4];
#pragma unroll
      for (int i = 0; i < 4; ++i) xv[i] = ld_sc1(rs, xo + i * 1024);
#pragma unroll
      for (int i = 0; i < 4; ++i) xv[i] ^= t16v;
      if (__builtin_expect(!__all((X_BAD(xv[0], xv[1]) | X_BAD(xv[2], xv[3])) == 0u), 0)) {
#pragma unroll 1
        for (u32 s = 0; s < SPIN_CAP; ++s) {
          MEMB();
#pragma unroll
          for (int i = 0; i < 4; ++i) xv[i] = ld_sc1(rs, xo + i * 1024) ^ t16v;
          if (__all((X_BAD(xv[0], xv[1]) | X_BAD(xv[2], xv[3])) == 0u)) break;
        }
      }
      float sm[2], sq[2], xs0[2];
#pragma unroll
      for (int k = 0; k < 2; ++k) {
        const float x0 = __builtin_bit_cast(float, __builtin_amdgcn_readfirstlane(xv[2 * k][0]));
        const u32x4 xa = xv[2 * k], xb = xv[2 * k + 1];
        const f32x2 d0 = f32x2{asf(xa[0]), asf(xa[1])} - x0, d1 = f32x2{asf(xa[2]), asf(xa[3])} - x0, d2 = f32x2{asf(xb[0]), asf(xb[1])} - x0, d3 = f32x2{asf(xb[2]), asf(xb[3])} - x0;
        const f32x2 sv = (d0 + d1) + (d2 + d3), qv = __builtin_elementwise_fma(d0, d0, d1 * d1) + __builtin_elementwise_fma(d2, d2, d3 * d3);
        xs0[k] = x0;
        sm[k] = sv[0] + sv[1];
        sq[k] = qv[0] + qv[1];
      }
      const float r = row_sum(t32(t16(sm[0], sq[0]), t16(sm[1], sq[1]))) * (1.f / 512.f);
      const auto sw = __builtin_amdgcn_permlane16_swap(__float_as_uint(r), __float_as_uint(r), false, false);
      const float md = __uint_as_float(sw[0]);
      const float x0 = lane < 32 ? xs0[0] : xs0[1];
      const float inv = rcp(rbf(__builtin_amdgcn_sqrtf(rbf(rbf(fmaxf(fmaf(-md, md, __uint_as_float(sw[1])), 0.f)) + 1e-5f))));
      const f32x4 st = {rbf(x0 + md) - x0, inv, inv * -1.4426950408889634f, 1.f};
      asm volatile("s_mov_b32 exec_lo, 1\n\ts_mov_b32 exec_hi, 1\n\tds_write_b128 %0, %1\n\ts_mov_b64 exec, -1" :: "v"(a_st), "v"(st) : "memory");
      if (w == 15) {
        _Pragma("unroll 1") for (u32 s = 0; s < LSPIN_CAP; ++s) if (__all(ctl[C_X + (lane & 7)] != 0u)) break;
        if (lane < 8) st_sc1_64(rs, WS_B1 + lane * 512 + p3 * 8, u32x2{now4(), tagv});
      }
      BAR();
    }
    asm volatile("fused_code_end:");
  } else if (red && (w == RW || w == RW - 1)) {
    NEWPAGE();
    const int f = 16 * xcd + 2 * b + hj, ro_ = 4 * (w & 1) + (lane & 3);
    const int rb = e >> 4, rj = e & 15, c2 = 32 * rj + 4 * (lane & 7) + g;
    u32 xr_ = x[rb * 512 + c2];
    u32 gar = (__builtin_amdgcn_raw_buffer_load_b16(rs, WS_MODR + rb * 1024 + mpos(c2), 0, SC1));
    u32 gbr = (__builtin_amdgcn_raw_buffer_load_b16(rs, WS_MODR + (24 + ro_) * 1024 + mpos(4 * f + g), 0, SC1));
    int po = tline(xcd, LN_PART + (2 * b + hj) * 8, near) + lane * 16;
    int xo = tline(lane >> 3, LN_X2 + e, near) + (lane & 7) * 16;
    int xi = tline(xcd, LN_X2 + 16 * ro_, near) + (4 * f + g) * 4;
    u32 xt = (g & 1) ? tag >> 16 : tag & 0xffffu;
    const u32x4 tb4 = bytes4(tag);
    u32x4 tb5 = lane < 44 ? tb4 : u32x4{0, 0, 0, 0};
    const int yl = LN_YP + (f & 15) * NP3 + (lane >> 2);
    int yo = tline(xcd, yl, near) + ro_ * 16, y1 = tline(xcd, yl + 16, near) + ro_ * 16;
    int y2 = lane < 44 ? tline(xcd, yl + 32, near) + ro_ * 16 : WS_END;
    PIN(y1);
    u32 oof = (lane & 12) == 0 ? (ro_ * 512 + 4 * f + g) * 2 : 8192;
    __amdgpu_buffer_rsrc_t ro = __builtin_amdgcn_make_buffer_rsrc((void*)out, 0, 8192, 0x00020000);
    asm volatile("" : "+s"(ro));
    PIN(xt); PIN(tb5); PIN(po); PIN(xo); PIN(xi); PIN(yo); PIN(y2); PIN(oof);
    if (w == RW) {
      u32x4 bp, bq;
      until_s(org + OFB);
      B0_LOAD()
      SB();
      PIN(gar); PIN(xr_);
      float ga = blo(gar), xx = blo(xr_);
      PIN(ga); PIN(xx);
      u32 anc = org + OFO;
      if (__builtin_expect(!on_time(tag, org, bp, bq), 0)) anc = anchor(rs, bo, tag, org, bp, bq);
      until(anc + DP);
      u32x4 xq;
#pragma unroll 1
      for (u32 sp = 0; sp < SPIN_CAP; ++sp) {
        const u32x4 pv = ld_one(rs, po) ^ tb4;
        float t = t32(t16(asf(pv[0]), asf(pv[1])), t16(asf(pv[2]), asf(pv[3])));
        t += dppf<ROR8>(t);
        const u32 xw = __float_as_uint(rbf(xx + rbf(ga * rbf(t)))) | xt;
        const auto p1 = __builtin_amdgcn_permlane16_swap(xw, xw, false, false);
        const auto p2 = __builtin_amdgcn_permlane32_swap(p1[0], p1[0], false, false), p3_ = __builtin_amdgcn_permlane32_swap(p1[1], p1[1], false, false);
        xq = u32x4{p2[0], p3_[0], p2[1], p3_[1]};
        PIN(xq);
        if (__builtin_expect(__all((or4(pv) & 255u) == 0u), 1)) break;
        MEMB();
      }
      st_sc1(rs, xo, xq);
      if (bid == 1 && lane == 0) __hip_atomic_store((GAS u32*)ws, tag, RLX_AGENT);
      u32 ty = anc + DYS;
      const int lt = (int)(now4() - anc) - (DX + 20);
      if (__builtin_expect(lt > 0, 0)) {
        int lk = DB1;
        while (lk < DB1 + lt + 240) lk += PB;
        u32x2 bv;
#pragma unroll 1
        for (u32 sp = 0; sp < SPIN_CAP; ++sp) {
          until_s(anc + lk);
          bv = ld_sc1_64(rs, WS_B1 + xcd * 512 + (lane < NP3 ? lane : 0) * 8);
          if (__all(bv[1] == tag)) break;
          MEMB();
          do lk += PB; while ((int)(now4() - anc) > lk);
        }
        ty = org + smax((int)(bv[0] - org)) + DY;
        asm volatile("fused_cold_end:");
      }
      ctl[C_TX] = ty | 1u;
    }
    PIN(gbr);
    float gb = blo(gbr);
    PIN(gb);
    lds_wait(ctl + C_TX, 1u);
    until(__builtin_amdgcn_readfirstlane(ctl[C_TX]));
    u32 res;
#pragma unroll 1
    for (u32 sp = 0; sp < SPIN_CAP; ++sp) {
      const u32 x2 = __builtin_amdgcn_raw_buffer_load_b32(rs, xi, 0, SC1) ^ xt;
      const u32x4 y0 = ld_one(rs, yo) ^ tb4, y1_ = ld_one(rs, y1) ^ tb4, y2_ = ld_one(rs, y2) ^ tb5;
      const f32x4 s = (__builtin_bit_cast(f32x4, y0) + __builtin_bit_cast(f32x4, y1_)) + __builtin_bit_cast(f32x4, y2_);
      float t = t32(t16(s[0], s[1]), t16(s[2], s[3]));
      t += dppf<ROR8>(t);
      t += dppf<ROR4>(t);
      res = pack2(fmaf(gb, t, asf(x2)), 0.f);
      PIN(res);
      if (__builtin_expect(__all((((or4(y0) | or4(y1_) | or4(y2_)) & 255u) | (x2 & 0xffffu)) == 0u), 1)) break;
      MEMB();
    }
    __builtin_amdgcn_raw_buffer_store_b16((u16)res, ro, oof, 0, 0);
  }
}

extern "C" void kernel_launch(void* const* d_in, const int* in_sizes, int n_in, void* d_out, int out_size, void* d_ws, size_t ws_size, hipStream_t stream) {
  (void)hipFuncSetAttribute((const void*)fused, hipFuncAttributeMaxDynamicSharedMemorySize, L_END);
  Args a;
  const int src[4][2] = {{3, 3}, {10, 10}, {4, 4}, {6, 7}};
  for (int r = 0; r < 4; ++r)
    for (int k = 0; k < 2; ++k) a.half[r][k] = (const unsigned char*)d_in[src[r][k]] + (r < 3 && k ? 64 : 0);
  a.x = (const u16*)d_in[0]; a.kc = (const u16*)d_in[1]; a.vc = (const u16*)d_in[2]; a.wd = (const u16*)d_in[8]; a.cc = (const u16*)d_in[9];
  a.out = (u16*)d_out; a.ws = (unsigned char*)d_ws;
  fused<<<256, 1024, L_END, stream>>>(a);
}
```

```cpp
#include <hip/hip_runtime.h>
#include <stdint.h>

typedef unsigned int u32;
typedef unsigned long long u64;
typedef unsigned short u16;
typedef u32 u32x4 __attribute__((ext_vector_type(4)));
typedef u32 u32x2 __attribute__((ext_vector_type(2)));
typedef float f32x4 __attribute__((ext_vector_type(4)));
typedef float f32x2 __attribute__((ext_vector_type(2)));
typedef short s16x4 __attribute__((ext_vector_type(4)));
typedef __bf16 bf16x8 __attribute__((ext_vector_type(8)));
typedef __bf16 bf16x2 __attribute__((ext_vector_type(2)));
#define GAS __attribute__((address_space(1)))
#define LAS __attribute__((address_space(3)))
#define RLX_AGENT __ATOMIC_RELAXED, __HIP_MEMORY_SCOPE_AGENT

constexpr int SK = 4096, WIN = 1024, FF = 1376, NP3 = 43;

constexpr int WS_FLAGS = 0x1000;
constexpr int WS_TS = 0x1800;
constexpr int WS_G1 = 0x2000;
constexpr int WS_B1 = 0x8000;
constexpr int WS_MODR = 0xA000;
constexpr int WS_Q = 0x12000;
constexpr int WS_B0 = 0x32000;
constexpr int WS_NV = 0x33000;
constexpr int WS_NP = 0x34000;
constexpr int LN_O = 0;
constexpr int LN_PART = 128;
constexpr int LN_X2 = 256;
constexpr int LN_YP = 384;
constexpr int WS_T = 0x38000, T_SPAN = 17 * 16384;
constexpr int WS_END = WS_T + 4 * T_SPAN;
constexpr int WS_STEP = 2 << 20;
static_assert(WS_END <= WS_STEP && LN_YP + 16 * NP3 <= 17 * 64, "");

constexpr int L_W = 0;
constexpr int L_TP = 65536;
constexpr int L_C1 = 98304;
constexpr int L_SINK = 130048;
constexpr int L_RED = 131072;
constexpr int L_H2 = L_RED + 16384;
constexpr int C1S = 1040, H2S = 1056, OSTR = 544, OBS = 288;
constexpr int L_Q = L_H2 + 8 * H2S;
constexpr int L_ML = L_Q + 512;
constexpr int L_HH = L_ML + 128;
constexpr int L_CTL = L_HH + 784;
constexpr int L_END = L_CTL + 64;
constexpr int C_T = 0, C_TB = 1, C_TX = 2, C_Q = 3, C_ML = 4, C_R = 6, C_T0 = 7, C_X = 8;
constexpr int RW = 12, WG_ = 10;

constexpr u32 SPIN_CAP = 1u << 17, LSPIN_CAP = 1u << 20;
constexpr int DUE0 = 800;
constexpr int TAU = 220;
constexpr int E1 = 16;
constexpr int E2 = 10;
constexpr int SKW = 200;
constexpr int MSH = 14;
constexpr int KA = 48;
constexpr int KW = 5;
constexpr int OFO = 420;
constexpr int OFB = 450;
constexpr int PB = 200;
constexpr int RB = 260;
constexpr int DO = 15;
constexpr int DL_ = 220;
constexpr int DP = 328;
constexpr int DX = 506;
constexpr int DYS = 984;
constexpr int DB1 = 830;
constexpr int DY = 250;
constexpr int WDT = 40;
constexpr int extra(int i) { return i < 8 ? E1 : (i >= 14 && i < 22 ? E2 : 0); }
constexpr int TEND = DUE0 + 32 * TAU + 8 * E1 + 8 * E2;

__device__ __forceinline__ float rbf(float f) { return (float)(__bf16)f; }
__device__ __forceinline__ u32 pack2(float a, float b) { bf16x2 v = {(__bf16)a, (__bf16)b}; return __builtin_bit_cast(u32, v); }
__device__ __forceinline__ float blo(u32 v) { return __builtin_bit_cast(float, v << 16); }
__device__ __forceinline__ float bhi(u32 v) { return __builtin_bit_cast(float, v & 0xffff0000u); }
__device__ __forceinline__ u32 canon(u32 v) { return v < 0xfffffffeu ? v : 0xfffffffeu; }
#define SEL4() u32x4 sel = {0x07060500u, 0x07060501u, 0x07060502u, 0x07060503u}; asm volatile("" : "+s"(sel))
__device__ __forceinline__ u32x4 tag8(f32x4 v, u32 tagv, u32x4 sel) {
  const u32x4 r = __builtin_bit_cast(u32x4, v);
  return u32x4{__builtin_amdgcn_perm(r[0], tagv, sel[0]), __builtin_amdgcn_perm(r[1], tagv, sel[1]), __builtin_amdgcn_perm(r[2], tagv, sel[2]), __builtin_amdgcn_perm(r[3], tagv, sel[3])};
}
__device__ __forceinline__ u32x4 bytes4(u32 tag) { return u32x4{tag & 255u, (tag >> 8) & 255u, (tag >> 16) & 255u, tag >> 24}; }
__device__ __forceinline__ u32 or4(u32x4 v) { return (v[0] | v[1]) | (v[2] | v[3]); }
__device__ __forceinline__ float rcp(float x) { return __builtin_amdgcn_rcpf(x); }
template <int CTRL> __device__ __forceinline__ float dppf(float x) {
  return __builtin_bit_cast(float, __builtin_amdgcn_mov_dpp(__builtin_bit_cast(int, x), CTRL, 0xf, 0xf, true));
}
template <int CTRL> __device__ __forceinline__ u32 dppu(u32 x) { return (u32)__builtin_amdgcn_mov_dpp((int)x, CTRL, 0xf, 0xf, true); }
constexpr int ROR8 = 0x128, ROR4 = 0x124, ROR2 = 0x122, ROR1 = 0x121, XOR1 = 0xB1, XOR2 = 0x4E, XOR7 = 0x141;
__device__ __forceinline__ float row_sum(float x) { x += dppf<ROR8>(x); x += dppf<ROR4>(x); x += dppf<ROR2>(x); x += dppf<ROR1>(x); return x; }
__device__ __forceinline__ float row_max(float x) {
  x = fmaxf(x, dppf<ROR8>(x)); x = fmaxf(x, dppf<ROR4>(x)); x = fmaxf(x, dppf<ROR2>(x)); x = fmaxf(x, dppf<ROR1>(x)); return x;
}
__device__ __forceinline__ float x16_sum(float x) {
  auto s = __builtin_amdgcn_permlane16_swap(__float_as_uint(x), __float_as_uint(x), false, false);
  return __uint_as_float(s[0]) + __uint_as_float(s[1]);
}
__device__ __forceinline__ float x32_sum(float x) {
  auto s = __builtin_amdgcn_permlane32_swap(__float_as_uint(x), __float_as_uint(x), false, false);
  return __uint_as_float(s[0]) + __uint_as_float(s[1]);
}
__device__ __forceinline__ float x16_max(float x) {
  auto s = __builtin_amdgcn_permlane16_swap(__float_as_uint(x), __float_as_uint(x), false, false);
  return fmaxf(__uint_as_float(s[0]), __uint_as_float(s[1]));
}
__device__ __forceinline__ float x32_max(float x) {
  auto s = __builtin_amdgcn_permlane32_swap(__float_as_uint(x), __float_as_uint(x), false, false);
  return fmaxf(__uint_as_float(s[0]), __uint_as_float(s[1]));
}
__device__ __forceinline__ float t16(float a, float b) {
  auto s = __builtin_amdgcn_permlane16_swap(__float_as_uint(a), __float_as_uint(b), false, false);
  return __uint_as_float(s[0]) + __uint_as_float(s[1]);
}
__device__ __forceinline__ float t32(float a, float b) {
  auto s = __builtin_amdgcn_permlane32_swap(__float_as_uint(a), __float_as_uint(b), false, false);
  return __uint_as_float(s[0]) + __uint_as_float(s[1]);
}
__device__ __forceinline__ f32x2 up2(u32 v) { return f32x2{blo(v), bhi(v)}; }
__device__ __forceinline__ u32 pk2(f32x2 v) { return pack2(v[0], v[1]); }
#define PIN(x) asm volatile("" : "+v"(x))
__device__ __forceinline__ f32x2 rb2(f32x2 v) { u32 p = pk2(v); asm("" : "+v"(p)); return up2(p); }
__device__ __forceinline__ float wave_sum(float x) { return x32_sum(x16_sum(row_sum(x))); }
__device__ __forceinline__ float wave_max(float x) { return x32_max(x16_max(row_max(x))); }
__device__ __forceinline__ float dot2(u32 a, u32 b, float c) {
  return __builtin_amdgcn_fdot2_f32_bf16(__builtin_bit_cast(bf16x2, a), __builtin_bit_cast(bf16x2, b), c, false);
}
__device__ __forceinline__ f32x4 mfma(u32x4 a, u32x4 b, f32x4 c) {
  return __builtin_amdgcn_mfma_f32_16x16x32_bf16(__builtin_bit_cast(bf16x8, a), __builtin_bit_cast(bf16x8, b), c, 0, 0, 0);
}
__device__ __forceinline__ u32x4 ldnt(const void* p) { return __builtin_nontemporal_load((const u32x4*)p); }
__device__ __forceinline__ void glds(const void* g, u32 lds) {
  u32 keep;
  lds = __builtin_amdgcn_readfirstlane(lds);
  asm volatile("s_mov_b32 %0, m0\n\ts_mov_b32 m0, %2\n\ts_nop 0\n\tglobal_load_lds_dwordx4 %1, off nt\n\ts_mov_b32 m0, %0"
               : "=&s"(keep) : "v"(g), "s"(lds) : "memory");
}
__device__ __forceinline__ void glds(const void* base, u32 off, u32 lds) {
  u32 keep;
  lds = __builtin_amdgcn_readfirstlane(lds);
  asm volatile("s_mov_b32 %0, m0\n\ts_mov_b32 m0, %3\n\ts_nop 0\n\tglobal_load_lds_dwordx4 %1, %2 nt\n\ts_mov_b32 m0, %0"
               : "=&s"(keep) : "v"(off), "s"(base), "s"(lds) : "memory");
}
__device__ __forceinline__ void touch(const void* base, u32 off, u32 lds) {
  u32 keep;
  lds = __builtin_amdgcn_readfirstlane(lds);
  asm volatile("s_mov_b32 %0, m0\n\ts_mov_b32 m0, %3\n\ts_nop 0\n\tglobal_load_lds_dwordx4 %1, %2\n\ts_mov_b32 m0, %0"
               : "=&s"(keep) : "v"(off), "s"(base), "s"(lds) : "memory");
}
__device__ __forceinline__ u32x4 trfrag(LAS unsigned char* p0, LAS unsigned char* p1) {
  s16x4 a = __builtin_amdgcn_ds_read_tr16_b64_v4i16((LAS s16x4*)p0);
  s16x4 b = __builtin_amdgcn_ds_read_tr16_b64_v4i16((LAS s16x4*)p1);
  u32x2 x = __builtin_bit_cast(u32x2, a), y = __builtin_bit_cast(u32x2, b);
  return u32x4{x[0], x[1], y[0], y[1]};
}
#define NEWPAGE() asm volatile("s_branch 9f\n\t.p2align 12\n9:")
#define SB() __builtin_amdgcn_sched_barrier(0)
#define MEMB() asm volatile("" ::: "memory")
#define VM0() asm volatile("s_waitcnt vmcnt(0)" ::: "memory")
#define LGKM0() asm volatile("s_waitcnt lgkmcnt(0)" ::: "memory")
#define BAR() asm volatile("s_waitcnt lgkmcnt(0)\n\ts_barrier" ::: "memory")
#define TAG() do { u32 ep_; asm volatile("s_load_dword %0, %1, 0x0\n\ts_waitcnt lgkmcnt(0)" : "=s"(ep_) : "s"(ws) : "memory"); \
    tag = ((ep_ & 0x7f7f7f7fu) + 0x01010101u) ^ (ep_ & 0x80808080u); } while (0)
constexpr int SC1 = 16;
__device__ __forceinline__ u32x4 ld_sc1(__amdgpu_buffer_rsrc_t rs, int off) { return __builtin_amdgcn_raw_buffer_load_b128(rs, off, 0, SC1); }
__device__ __forceinline__ u32x4 ld_one(__amdgpu_buffer_rsrc_t rs, int off) { return __builtin_amdgcn_raw_buffer_load_b128(rs, off, 0, SC1 | 2); }
__device__ __forceinline__ u32x2 ld_sc1_64(__amdgpu_buffer_rsrc_t rs, int off) { return __builtin_amdgcn_raw_buffer_load_b64(rs, off, 0, SC1); }
__device__ __forceinline__ void st_sc1(__amdgpu_buffer_rsrc_t rs, int off, u32x4 v) { __builtin_amdgcn_raw_buffer_store_b128(v, rs, off, 0, SC1); }
__device__ __forceinline__ void st_sc1_64(__amdgpu_buffer_rsrc_t rs, int off, u32x2 v) { __builtin_amdgcn_raw_buffer_store_b64(v, rs, off, 0, SC1); }

__device__ __forceinline__ void ln_stats(u32x4 xv, float& m, float& inv) {
  const float x[8] = {blo(xv[0]), bhi(xv[0]), blo(xv[1]), bhi(xv[1]), blo(xv[2]), bhi(xv[2]), blo(xv[3]), bhi(xv[3])};
  const float x0 = __builtin_bit_cast(float, __builtin_amdgcn_readfirstlane(xv[0] << 16));
  float s = 0.f, v = 0.f;
#pragma unroll
  for (int j = 0; j < 8; ++j) { const float d = x[j] - x0; s += d; v = fmaf(d, d, v); }
  const float md = wave_sum(s) * (1.f / 512.f), sq = wave_sum(v) * (1.f / 512.f);
  const float var = rbf(fmaxf(fmaf(-md, md, sq), 0.f));
  m = rbf(x0 + md);
  inv = rcp(rbf(__builtin_amdgcn_sqrtf(rbf(var + 1e-5f))));
}
#define PACE_(W, INC) do { asm volatile(W "\n1:\n\ts_memrealtime vcc\n\ts_waitcnt lgkmcnt(0)\n\ts_lshl_b32 vcc_lo, vcc_lo, 2\n\ts_sub_i32 vcc_hi, vcc_lo, %0\n\ts_cmp_lt_i32 vcc_hi, 0\n\t" \
    "s_cbranch_scc0 2f\n\ts_sleep 1\n\ts_branch 1b\n2:\n\ts_add_i32 %0, %0, %1" : "+s"(due) : "is"(INC) : "memory", "scc", "vcc"); SB(); } while (0)
#define PACE(INC) PACE_("s_waitcnt vmcnt(2)", INC)
#define PACEN(INC) PACE_("", INC)
__device__ __forceinline__ void until_s(u32 due) { PACE_("", 0); }
__device__ __forceinline__ void until(u32 t) {
  asm volatile("1:\n\ts_memrealtime vcc\n\ts_waitcnt lgkmcnt(0)\n\ts_lshl_b32 vcc_lo, vcc_lo, 2\n\ts_sub_i32 vcc_hi, vcc_lo, %0\n\ts_cmp_lt_i32 vcc_hi, 0\n\t"
               "s_cbranch_scc1 1b" :: "s"(t) : "memory", "scc", "vcc");
  SB();
}

__device__ __forceinline__ void lds_wait(volatile LAS u32* p, u32 want) {
  _Pragma("unroll 1") for (u32 s = 0; s < LSPIN_CAP; ++s) {
    if (__builtin_amdgcn_readfirstlane(*p) >= want) break;
    __builtin_amdgcn_s_sleep(2);
  }
  MEMB();
}
__device__ __forceinline__ void lds_add(LAS u32* p, int lane) {
  LGKM0();
  if (lane == 0) __hip_atomic_fetch_add(p, 1u, __ATOMIC_RELAXED, __HIP_MEMORY_SCOPE_WORKGROUP);
  MEMB();
}
__device__ __forceinline__ void tile_part(LAS unsigned char* T, LAS unsigned char* P, u32x4 hb, int n, int g) {
  LAS unsigned char* tb = T + (8 * g + (n >> 2)) * 128 + (n & 3) * 8;
  const int sx = (n >> 3) | ((g & 1) << 1);
  f32x4 a[4];
#pragma unroll
  for (int t = 0; t < 4; ++t) a[t] = mfma(trfrag(tb + (t ^ sx) * 32, tb + (t ^ sx) * 32 + 512), hb, f32x4{0.f, 0.f, 0.f, 0.f});
  *(LAS f32x4*)(P) = n < 8 ? a[0] : a[1];
  *(LAS f32x4*)(P + 1024) = n < 8 ? a[2] : a[3];
}
__device__ __forceinline__ f32x4 tile_sum(LAS unsigned char* P) {
  f32x4 s[8], r[2];
#pragma unroll
  for (int k = 0; k < 2; ++k) {
#pragma unroll
    for (int i = 0; i < 8; ++i) s[i] = *(LAS f32x4*)(P + (8 * k + i) * 2048);
    SB();
    r[k] = ((s[0] + s[1]) + (s[2] + s[3])) + ((s[4] + s[5]) + (s[6] + s[7]));
    SB();
  }
  return r[0] + r[1];
}
#define GU_MUL(P) _Pragma("unroll") for (int i = 0; i < 8; ++i) gb_[i] = *(LAS u32x4*)((P) + i * 64); SB(); \
    aL = f32x4{0.f, 0.f, 0.f, 0.f}; aH = aL; _Pragma("unroll") for (int i = 0; i < 8; ++i) { aL = mfma(gf[i], gb_[i], aL); aH = mfma(gf[8 + i], gb_[i], aH); }
__device__ __forceinline__ void gu_pair(f32x4 aL, f32x4 aH, f32x2& gt, f32x2& up) {
  const auto s0 = __builtin_amdgcn_permlane32_swap(__float_as_uint(aL[0] + dppf<ROR8>(aH[0])), __float_as_uint(aL[2] + dppf<ROR8>(aH[2])), false, false);
  const auto s1 = __builtin_amdgcn_permlane32_swap(__float_as_uint(aL[1] + dppf<ROR8>(aH[1])), __float_as_uint(aL[3] + dppf<ROR8>(aH[3])), false, false);
  gt = f32x2{__uint_as_float(s0[0]), __uint_as_float(s1[0])};
  up = f32x2{__uint_as_float(s0[1]), __uint_as_float(s1[1])};
}
__device__ __forceinline__ int tline(int c, int ln, u32 near) { return WS_T + (c & 3) * T_SPAN + (ln >> 6) * 16384 + ((near >> c) & 1) * 8192 + (ln & 63) * 128; }
__device__ __forceinline__ int mpos(int c) { return (c & 252) * 4 + (c >> 8) * 8 + (c & 3) * 2; }
__device__ __forceinline__ u32 smax(int m) { int r = (int)fminf(wave_max((float)m), 4e5f); PIN(r); return (u32)__builtin_amdgcn_readfirstlane(r); }
__device__ __forceinline__ u32 now4() { return (u32)__builtin_amdgcn_s_memrealtime() * 4u; }
#define B0_LOAD() { bp = ld_sc1(rs, bo); bq = ld_sc1(rs, bo + 1024); }
__device__ __forceinline__ bool on_time(u32 tag, u32 org, u32x4 bp, u32x4 bq) {
  const u32 lim = org + OFO - DO;
  return __all((((bp[1] ^ tag) | (bp[3] ^ tag) | (bq[1] ^ tag) | (bq[3] ^ tag)) | (((lim - bp[0]) | (lim - bp[2]) | (lim - bq[0]) | (lim - bq[2])) >> 31)) == 0u);
}
__device__ __forceinline__ u32 anchor(__amdgpu_buffer_rsrc_t rs, int bo, u32 tag, u32 org, u32x4 bp, u32x4 bq) {
  int lk = OFB;
#pragma unroll 1
  for (u32 s = 0; s < SPIN_CAP; ++s) {
    if (__all(((bp[1] ^ tag) | (bp[3] ^ tag) | (bq[1] ^ tag) | (bq[3] ^ tag)) == 0u)) break;
    MEMB();
    do lk += PB; while ((int)(now4() - org) > lk);
    until_s(org + lk);
    B0_LOAD()
  }
  const int m = (int)smax(max(max((int)(bp[0] - org), (int)(bp[2] - org)), max((int)(bq[0] - org), (int)(bq[2] - org))));
  return org + (u32)max(m + DO <= OFO ? OFO : m + DL_, lk + RB - DP);
}
__device__ __forceinline__ float asf(u32 v) { return __builtin_bit_cast(float, v); }

struct Args {
  const unsigned char* half[4][2];
  const u16 *x, *kc, *vc, *wd, *cc;
  u16* out;
  unsigned char* ws;
};

__global__ void __launch_bounds__(1024, 4) fused(const Args a) {
  extern __shared__ __attribute__((aligned(16))) unsigned char smem[];
  LAS unsigned char* L = (LAS unsigned char*)smem;
  const u32 lbase = (u32)(size_t)L;
  const int tid = threadIdx.x, w = __builtin_amdgcn_readfirstlane(tid >> 6), bid = blockIdx.x;
  int lane = tid & 63;
  const int xcd = bid & 7, idx = bid >> 3, hj = idx & 3, h = 4 * xcd + hj, b = idx >> 2, bh = b * 32 + h;
  const int sw8 = hj == 3 ? (w & 1) * 8 : 0, rw = (hj == 3 ? 24 : 32) * w;
  u64 hA, hB, t64, t1;
  u32 warm;
  asm volatile("s_load_dwordx2 %0, %4, %5\n\ts_load_dwordx2 %1, %4, %6\n\ts_load_dword %3, %4, 0x40\n\ts_memrealtime %2" : "=&s"(hA), "=&s"(hB), "=&s"(t64), "=&s"(warm)
               : "s"(__builtin_amdgcn_kernarg_segment_ptr()), "s"(16 * hj + sw8), "s"(16 * hj + (sw8 ^ 8)));
  SB();
  const int bb = (b + xcd) & 7;
  int p3r = 5 * xcd + min(xcd, 3) + bb;
  int mc = (b & 6) + ((xcd & 1) ^ 1), mg = ((xcd & 6) + b) & 7;
  const bool tq = hj == 0, to = hj == 2;
  int lim = hj == 1 ? 6 - b : (xcd < 3 ? 6 : 5) - bb;
  asm volatile("" : "+s"(p3r), "+s"(mc), "+s"(mg), "+s"(lim));
  const bool idle = (hj & 1) && lim <= 0;
  u64 tp0, tp1;
  {
    const u32 pitch = (u32)(0x0AC0040018002000ull >> (16 * hj)) & 0xffffu;
    u32 ce = (xcd << (to ? 13 : 4)) + 2 * b, c1 = 16 * mc + 2 * mg;
    asm volatile("" : "+s"(ce), "+s"(c1));
    const u32 col = 64 * ((hj & 1) ? (hj == 1 ? c1 : p3r) : ce);
    const u32 step = pitch << (to ? 1 : 3);
    u32 off = (rw + (to ? (lane & 24) + (lane >> 5) : lane >> 3)) * pitch + col + ((lane & 3) ^ ((lane >> 3) & 2)) * 16;
    tp0 = off;
    tp1 = off + step;
    u64 s2 = 2 * step;
    u32 ld = lbase + L_W + rw * 128;
    asm volatile("" : "+s"(s2), "+s"(ld), "+v"(tp0), "+v"(tp1));
    asm volatile("s_mov_b32 exec_lo, 0xf0f0f0f0\n\ts_mov_b32 exec_hi, 0xf0f0f0f0\n\ts_waitcnt lgkmcnt(0)\n\ts_memrealtime %6\n\tv_lshl_add_u64 %0, %0, 0, %3\n\tv_lshl_add_u64 %1, %1, 0, %2\n\t"
                 "s_not_b64 exec, exec\n\tv_lshl_add_u64 %0, %0, 0, %2\n\tv_lshl_add_u64 %1, %1, 0, %3\n\ts_mov_b64 exec, -1"
                 : "+v"(tp0), "+v"(tp1), "+s"(hA), "+s"(hB), "+s"(t64), "+s"(warm), "=&s"(t1) :: "scc");
    if (!idle) {
      u32 keep;
      asm volatile("s_mov_b32 %0, m0\n\ts_mov_b32 m0, %3\n\ts_nop 0\n\tglobal_load_lds_dwordx4 %1, off nt\n\ts_add_u32 m0, m0, 0x400\n\t"
                   "v_lshl_add_u64 %1, %1, 0, %4\n\tglobal_load_lds_dwordx4 %2, off nt\n\ts_mov_b32 m0, %0\n\tv_lshl_add_u64 %2, %2, 0, %4"
                   : "=&s"(keep), "+v"(tp0), "+v"(tp1) : "s"(ld), "s"(s2) : "memory", "scc");
      glds((const void*)tp0, ld + 2048);
      if (hj != 3) glds((const void*)tp1, ld + 3072);
    }
  }
  SB();
  u32 t0 = (u32)t64;
  const u16 *x = a.x, *kc = a.kc, *vc = a.vc, *wd = a.wd, *cc = a.cc;
  u16* out = a.out;
  unsigned char* ws = a.ws;
  const int p3 = (hj == 3 && !idle) ? p3r : -1;
  const bool tm = hj == 1 && !idle;
  u32 tag;
  TAG();
  asm volatile("" : "+s"(t1));
  t0 += (u32)(KW * max((int)((u32)t1 - t0) - KA, 0)) / 4u;
  const __amdgpu_buffer_rsrc_t rs = __builtin_amdgcn_make_buffer_rsrc((void*)(((size_t)ws + (size_t)((tag & 63u) + 1u) * WS_STEP) & ~(size_t)(WS_STEP - 1)), 0, WS_END, 0x00020000);
  if (idle && hj == 1) {
    if (w == 0) {
      st_sc1(rs, WS_G1 + ((b & 1) * 8 + xcd) * 1024 + lane * 16, u32x4{~0u, ~0u, ~0u, ~0u});
      VM0();
      if (lane == 0) st_sc1_64(rs, WS_TS + ((b & 1) * 8 + xcd) * 8, u32x2{t0, tag});
    } else {
      u64 pc;
      u32 len;
      asm volatile("s_getpc_b64 %0\n1:\n\ts_mov_b32 %1, fused_code_end - 1b + 1000\n\ts_max_u32 %1, %1, fused_cold_end - 1b + 1000" : "=s"(pc), "=&s"(len) :: "scc");
      const u32 o = ((b & 1) * 15 + w - 1) * 1024;
      if (o < len) touch((const void*)(pc & ~15ull), min(o + lane * 16, len & ~15u), lbase + L_SINK);
    }
  }
  if (w == 1) glds((const unsigned char*)((lane & 1) ? vc : kc) + ((size_t)bh * SK + (SK - WIN)) * 256, lbase + L_SINK);
  int ta = TAU;
  int n = lane & 15, g = lane >> 4;
  u32x4 cv, hb;
  const bool row = tm && (w >= 8 || mc == 1);
  if (row) cv = ldnt((w < 8 ? x : cc) + (w & 7) * 512 + lane * 8);
  if (tid < 16) ((volatile LAS u32*)(L + L_CTL))[tid] = tid == C_T0 ? t0 : 0u;
  BAR();
  t0 = __builtin_amdgcn_readfirstlane(((volatile LAS u32*)(L + L_CTL))[C_T0]);
  u32 due = t0 * 4u + (u32)((4 * bb + hj) * TAU) / 32u + DUE0;
  if (hj == 3 && bb >= 6 && w == 0) {
    const int k = (bb & 1) * 8 + xcd, o = WS_NP + k * 512 + (bb & 1) * 8192;
    __builtin_amdgcn_raw_buffer_store_b32(tag, rs, lane < 2 ? o ^ (lane * 8192) : WS_END, 0, SC1);
    u64 c0, c1, c2;
    u32 v;
    asm volatile("s_waitcnt vmcnt(0)\n\ts_memtime %0\n\ts_waitcnt lgkmcnt(0)\n\tbuffer_load_dword %3, %4, %6, 0 offen sc1\n\ts_waitcnt vmcnt(0)\n\ts_memtime %1\n\ts_waitcnt lgkmcnt(0)\n\t"
                 "buffer_load_dword %3, %5, %6, 0 offen sc1\n\ts_waitcnt vmcnt(0)\n\ts_memtime %2\n\ts_waitcnt lgkmcnt(0)"
                 : "=&s"(c0), "=&s"(c1), "=&s"(c2), "=&v"(v) : "v"(o), "v"(o ^ 8192), "s"(rs) : "memory");
    const int d = min(max(2 * (int)c1 - (int)c0 - (int)c2, -600), 600);
    if (lane == 0) st_sc1_64(rs, WS_NV + k * 8, u32x2{(u32)(2 * ((bb & 1) ? -d : d)) | ((__builtin_amdgcn_s_getreg(20 | (3 << 11)) >> 2) & 1u), tag});
  }
  int rot = idx;
#define RO(I) ((((I) + rot) & 15) * 8192)
  const u16* kp = kc + ((size_t)bh * SK + (SK - WIN) + 4 * w) * 128;
  u32 l16 = lane * 16;
  PIN(l16);
#define LDR(D, P, I, W) { const GAS unsigned char* p_ = (const GAS unsigned char*)((P) + RO(I)) + l16; PIN(p_); W; D = __builtin_nontemporal_load((const GAS u32x4*)p_); SB(); }
  u32x4 kr[16], vr[16];
  volatile LAS u32* ctl = (volatile LAS u32*)(L + L_CTL);
#define RELANE() do { asm volatile("" : "+v"(lane)); n = lane & 15; g = lane >> 4; } while (0)
  const int e = b * 16 + 2 * xcd + hj;
  const bool red = hj < 2;

#define KL(I) { const int t_ = ta + extra(I); LDR(kr[I], kp, I, PACE(t_)) }
  KL(0)
  if (row) {
    if (w >= 8) {
#pragma unroll
      for (int j = 0; j < 4; ++j) { const float a0 = blo(cv[j]), a1 = bhi(cv[j]); hb[j] = pack2(a0 * rcp(1.f + __expf(-a0)), a1 * rcp(1.f + __expf(-a1))); }
      *(LAS u32x4*)(L + L_H2 + (w & 7) * H2S + lane * 16) = hb;
      lds_add((LAS u32*)(L + L_CTL) + C_R, lane);
    } else {
      float m, inv;
      ln_stats(cv, m, inv);
      if ((lane >> 3) == mg) {
        LAS f32x4* d = (LAS f32x4*)(L + L_RED + w * 256 + (lane & 7) * 32);
        d[0] = f32x4{rbf(rbf(blo(cv[0]) - m) * inv), rbf(rbf(bhi(cv[0]) - m) * inv), rbf(rbf(blo(cv[1]) - m) * inv), rbf(rbf(bhi(cv[1]) - m) * inv)};
        d[1] = f32x4{rbf(rbf(blo(cv[2]) - m) * inv), rbf(rbf(bhi(cv[2]) - m) * inv), rbf(rbf(blo(cv[3]) - m) * inv), rbf(rbf(bhi(cv[3]) - m) * inv)};
      }
    }
  }
  KL(1)
  u32x2 tsv;
#define TS_WAIT() if (!__all(tsv[1] == tag)) { _Pragma("unroll 1") for (u32 s = 0; s < SPIN_CAP; ++s) { __builtin_amdgcn_s_sleep(16); MEMB(); \
      tsv = ld_sc1_64(rs, WS_TS + (lane & 15) * 8); if (__all(tsv[1] == tag)) break; } } MEMB();
  if (tm) {
    asm volatile("s_waitcnt vmcnt(2)" ::: "memory");
    lds_wait(ctl + C_R, 8u);
    hb = *(LAS u32x4*)(L + L_H2 + (n & 7) * H2S + (32 * w + 8 * g) * 2);
    tile_part(L + L_W + w * 4096, L + L_TP + w * 2048 + lane * 16, hb, n, g);
    lds_add((LAS u32*)(L + L_CTL) + C_T, lane);
  }
  KL(2)
  if (w == 0 || (tm && w >= 14)) tsv = ld_sc1_64(rs, WS_TS + (lane & 15) * 8);
  SB();
  if (tm && w >= 14) {
    lds_wait(ctl + C_T, 16u);
    const int col = 32 * (w & 1) + 16 * (n >> 3) + 4 * g;
    const f32x4 s = tile_sum(L + L_TP + (w & 1) * 1024 + lane * 16);
    float v[4] = {rbf(s[0]), rbf(s[1]), rbf(s[2]), rbf(s[3])};
    if (mc == 1) {
      const f32x4 ln = *(LAS f32x4*)(L + L_RED + (n & 7) * 256 + col * 4);
#pragma unroll
      for (int j = 0; j < 4; ++j) v[j] = ln[j] * rbf(1.f + v[j]);
    }
    TS_WAIT()
    st_sc1_64(rs, mc < 2 ? WS_G1 + (2 * mg + (w & 1)) * 1024 + ((2 * (n >> 3) + (g >> 1)) * 16 + mc * 8 + (n & 7)) * 16 + (g & 1) * 8
                         : WS_MODR + ((mc - 2) * 8 + (n & 7)) * 1024 + mpos(64 * mg + col),
              u32x2{canon(pack2(v[0], v[1])), canon(pack2(v[2], v[3]))});
  }
  RELANE();
  KL(3) KL(4)
  if (w == 0) {
    TS_WAIT()
    const float d = __builtin_amdgcn_fmed3f((float)(int)(tsv[0] - t0), -1e5f, 1e5f);
    ctl[C_TB] = (t0 * 4u + (u32)(int)fmaxf(row_sum(d) * 0.25f, row_max(d) * 4.f - SKW)) | 1u;
  }
  KL(5) KL(6)
  u32 org = __builtin_amdgcn_readfirstlane(ctl[C_TB]);
  if (org) { const int adj = ((int)(org - t0 * 4u - (p3 >= 0 ? bb * MSH : 0)) * 85 + 1024) >> 11; ta += adj; }
  KL(7)
  if (tm && mc >= 2 && w >= 14 && lane == 0) __builtin_amdgcn_raw_buffer_store_b32(tag, rs, WS_FLAGS + (((mc - 2) * 8 + mg) * 2 + (w & 1)) * 4, 0, SC1);
  KL(8)
  u32x4 g0;
  const int o0 = WS_G1 + w * 1024 + lane * 16;
  if (tq) lds_wait(ctl + C_TB, 1u);
#define KG(K) { KL(K) if (tq && (w & 1) == ((K) & 1)) { until_s(due - TAU / 2); \
    asm volatile("buffer_load_dwordx4 %0, %1, %2, 0 offen sc1" : "=&v"(g0) : "v"(o0), "s"(rs) : "memory"); } }
  KG(9) KG(10)
  KL(11) KL(12)
  if (tq) {
    asm volatile("s_waitcnt vmcnt(2)" : "+v"(g0));
#define G1_OK() __all(g0[0] != ~0u && g0[1] != ~0u && g0[2] != ~0u && g0[3] != ~0u)
    if (!G1_OK()) {
#pragma unroll 1
      for (u32 s = 0; s < SPIN_CAP; ++s) {
        __builtin_amdgcn_s_sleep(16);
        MEMB();
        g0 = ld_sc1(rs, o0);
        if (G1_OK()) break;
      }
    }
#pragma unroll
    for (int j = 0; j < 4; ++j) { const u32 o = dppu<ROR8>(g0[j]); hb[j] = pack2(blo(g0[j]) + blo(o), bhi(g0[j]) + bhi(o)); }
    PIN(hb);
  }
  KL(13)
  if (tq) {
    tile_part(L + L_W + w * 4096, L + L_TP + w * 2048 + lane * 16, hb, n, g);
    lds_add((LAS u32*)(L + L_CTL) + C_T, lane);
  }
  KL(14)
  if (tq && w >= 14) {
    lds_wait(ctl + C_T, 16u);
    const f32x4 s = tile_sum(L + L_TP + (w & 1) * 1024 + lane * 16);
    st_sc1(rs, WS_Q + ((n & 7) * 32 + 4 * xcd + (b >> 1)) * 512 + ((b & 1) * 64 + 32 * (w & 1) + 16 * (n >> 3) + 4 * g) * 4, u32x4{pack2(s[0], s[1]), tag, pack2(s[2], s[3]), tag});
  }
  RELANE();
  KL(15)
  asm volatile("" : "+s"(rot));
  const u16* vp = vc + ((size_t)bh * SK + (SK - WIN) + 4 * w) * 128;
#define VL(I) { const int t_ = ta + extra(16 + (I)); LDR(vr[I], vp, I, PACE(t_)) }
#define VLN(I) { const int t_ = ta + extra(16 + (I)); LDR(vr[I], vp, I, PACEN(t_)) }
  VL(0) VL(1)
  VL(2)
  u32x2 gq;
#define FL_WAIT() if (!__all(fl == tag)) {   _Pragma("unroll 1") for (u32 s = 0; s < SPIN_CAP; ++s) { __builtin_amdgcn_s_sleep(4); MEMB(); \
      fl = __builtin_amdgcn_raw_buffer_load_b32(rs, WS_FLAGS + lane * 4, 0, SC1); if (__all(fl == tag)) break; } } MEMB();
  u32 fl;
  u32x2 nv;
  if (w == WG_) {
    nv = ld_sc1_64(rs, WS_NV + (lane & 15) * 8);
    gq = ld_sc1_64(rs, WS_Q + bh * 512 + lane * 8);
    fl = __builtin_amdgcn_raw_buffer_load_b32(rs, !to ? WS_FLAGS + lane * 4 : WS_END, 0, SC1);
  }
  SB();
  VLN(3) VLN(4)
  if (w == WG_) {
    if (!__all(gq[1] == tag)) {
#pragma unroll 1
      for (u32 s = 0; s < SPIN_CAP; ++s) {
        __builtin_amdgcn_s_sleep(4);
        MEMB();
        gq = ld_sc1_64(rs, WS_Q + bh * 512 + lane * 8);
        if (__all(gq[1] == tag)) break;
      }
    }
    *(LAS u32*)(L + L_Q + lane * 4) = gq[0];
    PIN(fl);
    if (!to) { FL_WAIT() }
    PIN(nv);
    if (!__all(nv[1] == tag)) {
#pragma unroll 1
      for (u32 s = 0; s < SPIN_CAP; ++s) {
        __builtin_amdgcn_s_sleep(4);
        MEMB();
        nv = ld_sc1_64(rs, WS_NV + (lane & 15) * 8);
        if (__all(nv[1] == tag)) break;
      }
    }
    const float dv = (float)((int)nv[0] >> 1);
    const u32 m4 = (u32)__ballot((row_sum((nv[0] & 1u) ? -dv : dv) > 0.f) != ((nv[0] & 1u) != 0u)) & 15u;
    LGKM0();
    ctl[C_Q] = 256u | m4 | ((m4 ^ 15u) << 4);
  }
  RELANE();
  VL(5)
  lds_wait(ctl + C_Q, 1u);
  const bool prer = p3 >= 0 && w < 8;
  u32x4 pre0, pre1;
  if (prer) {
    pre0 = ld_sc1(rs, WS_MODR + (8 + w) * 1024 + lane * 16);
    pre1 = ld_sc1(rs, WS_MODR + (16 + w) * 1024 + lane * 16);
  }
  SB();
  const u32x4 qf = *(LAS u32x4*)(L + L_Q + n * 16);

#define DOT(I) dot2(kr[I][3], qf[3], dot2(kr[I][2], qf[2], dot2(kr[I][1], qf[1], dot2(kr[I][0], qf[0], 0.f))))
  float xs[8];
#define QK(C) { _Pragma("unroll") for (int i = 2 * (C); i < 2 * (C) + 2; ++i) { const float lo_ = DOT(i), hi_ = DOT(i + 8); \
      const float t1_ = lo_ + dppf<ROR8>(lo_), t2_ = hi_ + dppf<ROR8>(hi_); xs[i] = (n & 8) ? t2_ : t1_; PIN(xs[i]); } SB(); }
  QK(0) VLN(6) QK(1) VLN(7) QK(2) VL(8) QK(3) VL(9)
  if (prer) {
    const u32 a_h2w = w * C1S + lane * 8;
    *(LAS u32x2*)(L + L_C1 + a_h2w) = u32x2{pk2(up2(pre1[0]) + 1.f), pk2(up2(pre1[1]) + 1.f)};
    *(LAS u32x2*)(L + L_C1 + a_h2w + 512) = u32x2{pk2(up2(pre1[2]) + 1.f), pk2(up2(pre1[3]) + 1.f)};
    *(LAS u32x2*)(L + L_C1 + 8 * C1S + a_h2w) = u32x2{pre0[0], pre0[1]};
    *(LAS u32x2*)(L + L_C1 + 8 * C1S + a_h2w + 512) = u32x2{pre0[2], pre0[3]};
  }
#define TR_STEP(HALF, CTRL, BIT) _Pragma("unroll") for (int hh = 0; hh < (HALF); ++hh) { \
    const float lo_ = xs[hh], hi_ = xs[hh + (HALF)]; const float t1_ = lo_ + dppf<CTRL>(lo_), t2_ = hi_ + dppf<CTRL>(hi_); xs[hh] = (n & (BIT)) ? t2_ : t1_; }
  TR_STEP(4, XOR7, 4) TR_STEP(2, XOR2, 2) TR_STEP(1, XOR1, 1)
#undef TR_STEP
  float sc = rbf(rbf(xs[0]) * 0.08838834764831845f);
  PIN(sc);
  SB();
  VL(10)
  {
    const float mw_ = wave_max(sc);
    const float lw = wave_sum(__expf(sc - mw_));
    if (lane == 0) *(LAS f32x2*)(L + L_ML + w * 8) = f32x2{mw_, lw};
    lds_add((LAS u32*)(L + L_CTL) + C_ML, lane);
  }
  SB();
  VL(11)
  NEWPAGE();
  RELANE();
  u32 mk[4];
#pragma unroll
  for (int j = 0; j < 4; ++j) mk[j] = (n >> 1) == j ? ((n & 1) ? 0xffff0000u : 0x0000ffffu) : 0u;
  f32x4 oa = {0.f, 0.f, 0.f, 0.f};
#define PV(I) { const u32 pb_ = dppu<0x150 + (I)>(pd); oa = mfma(vr[I], u32x4{pb_ & mk[0], pb_ & mk[1], pb_ & mk[2], pb_ & mk[3]}, oa); asm volatile("" : "+v"(oa)); SB(); }
  PIN(mk[0]); PIN(mk[1]); PIN(mk[2]); PIN(mk[3]);
  LAS unsigned char* a_oa = L + (n < 8 ? L_RED + w * OSTR + (g * 8 + n) * 16 : L_SINK + lane * 16);
  LAS unsigned char* ra = L + L_RED + (lane >> 5) * 8 * OSTR + (lane & 31) * 16;
  const u32 near = __builtin_amdgcn_readfirstlane(ctl[C_Q]);
  u32 rb_ = tline(xcd, LN_O + (hj * 8 + b) * 4, near) + (lane & 31) * 16 + (lane >> 5) * 8;
  if (w == 14) rb_ = lane == 0 ? WS_B0 + bid * 8 : WS_END;
  int oo = tline(xcd, LN_O + 8 * w, near) + lane * 16;
  PIN(oo);
  u32 tagv = tag;
  PIN(a_oa); PIN(ra); PIN(rb_); PIN(tagv);
  org = __builtin_amdgcn_readfirstlane(ctl[C_TB]);
  org = org ? org + TEND : t0 * 4u;
  asm volatile("" : "+s"(org));
  VL(12)
  lds_wait(ctl + C_ML, 16u);
  u32 pd;
  {
    const f32x2 ml = *(LAS f32x2*)(L + L_ML + n * 8);
    const float M = row_max(ml[0]);
    const float pe = __expf(sc - M) * rcp(row_sum(__expf(ml[0] - M) * ml[1]));
    pd = pack2(pe, pe);
    PIN(pd);
  }
  SB();
  VL(13)
  PV(0) PV(1) PV(2) PV(3) PV(4)
  VL(14)
  PV(5) PV(6) PV(7) PV(8) PV(9)
  VL(15)
  PV(10) PV(11) PV(12) PV(13) PV(14)
  if (w == 13) __builtin_amdgcn_s_setprio(3);
  PV(15)
#undef PV
  *(LAS f32x4*)a_oa = oa;
  BAR();
  if (w == 14) st_sc1_64(rs, rb_, u32x2{now4(), tagv});
  if (w == 13) {
    f32x4 s[8];
#pragma unroll
    for (int ww = 0; ww < 8; ++ww) s[ww] = *(LAS f32x4*)(ra + ww * OSTR);
    SB();
    const f32x4 t = ((s[0] + s[1]) + (s[2] + s[3])) + ((s[4] + s[5]) + (s[6] + s[7]));
    st_sc1_64(rs, rb_, u32x2{pack2(t32(t[0], t[2]), t32(t[1], t[3])), tagv});
  }
  if (to) {
    until(org + OFO);
    u32x4 og = ld_sc1(rs, oo);
    SB();
    u32x4 wf[16];
    if (w < 4) {
      __builtin_amdgcn_s_setprio(3);
      const u32 af = L_W + (8 * g + (n >> 2)) * 128 + (w ^ ((n >> 3) | ((g & 1) << 1))) * 32 + (n & 3) * 8;
#pragma unroll
      for (int ks = 0; ks < 16; ++ks) wf[ks] = trfrag(L + af + ks * 4096, L + af + ks * 4096 + 512);
    }
    LAS unsigned char* p_ow = L + L_TP + (2 * w + (lane >> 5)) * OBS + (lane & 31) * 8;
    LAS unsigned char* p_or = L + L_TP + ((n >> 3) * 16 + (n & 7)) * OBS + g * 16;
    int rc = n < 8 ? tline(b, LN_PART + (2 * n + ((w >> 1) & 1)) * 8 + xcd, near) + (w & 1) * 64 + g * 16 : WS_END;
    PIN(p_ow); PIN(p_or); PIN(rc);
    SEL4();
    SB();
    *(LAS u32x2*)p_ow = u32x2{og[0], og[2]};
    if (__builtin_expect(!__all(((og[1] ^ tag) | (og[3] ^ tag)) == 0u), 0)) {
#pragma unroll 1
      for (u32 s = 0; s < SPIN_CAP; ++s) {
        MEMB();
        og = ld_sc1(rs, oo);
        if (__all(((og[1] ^ tag) | (og[3] ^ tag)) == 0u)) break;
        __builtin_amdgcn_s_sleep(2);
      }
      *(LAS u32x2*)p_ow = u32x2{og[0], og[2]};
    }
    BAR();
    if (w < 4) {
      u32x4 ob[8];
#pragma unroll
      for (int i = 0; i < 8; ++i) ob[i] = *(LAS u32x4*)(p_or + (i >> 2) * 8 * OBS + (i & 3) * 64);
      SB();
      f32x4 aL = {0.f, 0.f, 0.f, 0.f}, aH = {0.f, 0.f, 0.f, 0.f};
#pragma unroll
      for (int i = 0; i < 8; ++i) { aL = mfma(wf[i], ob[i], aL); aH = mfma(wf[8 + i], ob[i], aH); }
      const f32x4 v = {aL[0] + dppf<ROR8>(aH[0]), aL[1] + dppf<ROR8>(aH[1]), aL[2] + dppf<ROR8>(aH[2]), aL[3] + dppf<ROR8>(aH[3])};
      st_sc1(rs, rc, tag8(v, tagv, sel));
    }
  }

  const int bo = WS_B0 + lane * 16;
  if (p3 >= 0) {
    u32 a_h2w = w * C1S + lane * 8;
    if (w == 8 && lane < 36) *(LAS u32*)(L + (lane < 32 ? L_ML : L_HH + 640) + lane * 4) = 0u;
    BAR();
    glds((const void*)(tp0 + (u32)((368 - 16 * w) * FF * 2)), lbase + L_W + 49152 + w * 1024);
    u32x4 bp, bq;
    if (w == 12) {
      until_s(org + OFB + 20);
      asm volatile("buffer_load_dwordx4 %0, %2, %3, 0 offen sc1\n\tbuffer_load_dwordx4 %1, %2, %3, 0 offen offset:1024 sc1" : "=&v"(bp), "=&v"(bq) : "v"(bo), "s"(rs) : "memory");
    }
#define DL(R) glds(wd + (size_t)(32 * p3 + (R)) * 512, (lane ^ ((((R) & 3) | (((R) >> 1) & 4)) << 1)) * 16, lbase + L_W + 65536 + (R) * 1024)
    if (w < 8) {
      until_s(org + OFB + WDT);
#pragma unroll
      for (int i = 0; i < 4; ++i) DL(4 * w + i);
    }
    SB();
    u32 a_h2r = L_H2 + (n & 7) * H2S + (n >> 3) * 512 + g * 16;
    u32 a_hw = L_HH + (n < 8 ? n * 64 + (8 * (w & 3) + 4 * (g & 1) + 2 * (g >> 1)) * 2 : 512 + lane * 4);
    const u32x4 t16v = {tag & 0xffffu, tag >> 16, tag & 0xffffu, tag >> 16};
#define X_TIME() until_s(org + OFO + DX - 40); u32 tx = 2u; \
      _Pragma("unroll 1") for (u32 s = 0; s < LSPIN_CAP && tx == 2u; ++s) { tx = __builtin_amdgcn_readfirstlane(ctl[C_TX]); if (tx == 2u) __builtin_amdgcn_s_sleep(2); } \
      if (!(tx & 1u)) tx = org + OFO + DX;
#define ROWS_WAIT(A) { u32 v_, c_ = LSPIN_CAP; asm volatile("1:\n\tds_read_b32 %0, %2\n\ts_sub_u32 %1, %1, 1\n\ts_cbranch_scc1 2f\n\ts_waitcnt lgkmcnt(0)\n\t" \
      "v_cmp_eq_u32 vcc, 0, %0\n\ts_cbranch_vccnz 1b\n2:\n\ts_waitcnt lgkmcnt(0)" : "=&v"(v_), "+s"(c_) : "v"(A) : "memory", "scc", "vcc"); }
    u32 a_fl = lbase + L_CTL + (C_X + (lane & 7)) * 4;
    PIN(a_fl);
#define X_BAD(A, B) ((or4(A) | or4(B)) & 0xffffu)
    if (w < 8) {
      const u32x2 ca = *(LAS u32x2*)(L + L_C1 + a_h2w), cb = *(LAS u32x2*)(L + L_C1 + a_h2w + 512);
      f32x2 c1[4] = {up2(ca[0]), up2(ca[1]), up2(cb[0]), up2(cb[1])};
      LAS unsigned char* p_h2w = L + L_H2 + w * H2S + lane * 8;
      int xo = tline(xcd, LN_X2 + 16 * w, near) + lane * 16;
      LAS unsigned char *p_hr = L + L_HH + (n < 8 ? n * 64 + g * 16 : 768), *p_hs = L + L_HH + (n < 8 ? 768 : (n & 7) * 64 + g * 16);
      PIN(p_hs);
      SEL4();
      u32 a_d0 = L_W + 65536 + (8 * g + (n >> 2)) * 1024 + ((8 * w + ((n >> 1) & 1)) ^ (((n >> 2) | ((g & 1) << 2)) << 1)) * 16 + (n & 1) * 8;
      int yo = tline(w, LN_YP + (4 * (n >> 3) + g) * NP3 + p3, near) + (n & 7) * 16, yo2 = tline(w, LN_YP + (8 + 4 * (n >> 3) + g) * NP3 + p3, near) + (n & 7) * 16;
      PIN(xo); PIN(c1[0]); PIN(c1[1]); PIN(c1[2]); PIN(c1[3]); PIN(p_h2w); PIN(p_hr); PIN(yo); PIN(yo2); PIN(a_d0);
      u32 a_cx = lbase + L_CTL + (C_X + w) * 4, a_st = lbase + L_ML + w * 16;
      PIN(a_cx); PIN(a_st);
      asm volatile("s_waitcnt vmcnt(4)" ::: "memory");
      BAR();
      __builtin_amdgcn_s_setprio(3);
      X_TIME()
      until(tx);
      u32x4 xa, xb;
      f32x2 d0, d1, d2, d3;
      float x0;
#define U_ROW() { xa = ld_sc1(rs, xo) ^ t16v; xb = ld_sc1(rs, xo + 1024) ^ t16v; \
        x0 = __builtin_bit_cast(float, __builtin_amdgcn_readfirstlane(xa[0])); \
        d0 = f32x2{asf(xa[0]), asf(xa[1])} - x0; d1 = f32x2{asf(xa[2]), asf(xa[3])} - x0; d2 = f32x2{asf(xb[0]), asf(xb[1])} - x0; d3 = f32x2{asf(xb[2]), asf(xb[3])} - x0; \
        *(LAS u32x2*)p_h2w = u32x2{pk2(d0 * c1[0]), pk2(d1 * c1[1])}; \
        *(LAS u32x2*)(p_h2w + 512) = u32x2{pk2(d2 * c1[2]), pk2(d3 * c1[3])}; MEMB(); }
      U_ROW()
      if (__builtin_expect(!__all(X_BAD(xa, xb) == 0u), 0)) {
#pragma unroll 1
        for (u32 s = 0; s < SPIN_CAP; ++s) {
          U_ROW()
          if (__all(X_BAD(xa, xb) == 0u)) break;
        }
      }
      asm volatile("s_mov_b64 exec, 1\n\tds_write_b32 %0, %0\n\ts_mov_b64 exec, -1" :: "v"(a_cx) : "memory");
      {
        __builtin_amdgcn_s_setprio(2);
        PIN(d0); PIN(d1); PIN(d2); PIN(d3);
        const f32x2 sv = (d0 + d1) + (d2 + d3), qv = __builtin_elementwise_fma(d0, d0, d1 * d1) + __builtin_elementwise_fma(d2, d2, d3 * d3);
        const float r = x32_sum(row_sum(t16(sv[0] + sv[1], qv[0] + qv[1]))) * (1.f / 512.f);
        const float sq = __builtin_bit_cast(float, __builtin_amdgcn_readlane(__builtin_bit_cast(int, r), 16));
        const float inv = rcp(rbf(__builtin_amdgcn_sqrtf(rbf(rbf(fmaxf(fmaf(-r, r, sq), 0.f)) + 1e-5f))));
        const f32x4 st = {rbf(x0 + r) - x0, inv, inv * -1.4426950408889634f, 1.f};
        asm volatile("s_mov_b64 exec, 1\n\tds_write_b128 %0, %1\n\ts_mov_b64 exec, -1" :: "v"(a_st), "v"(st) : "memory");
        __builtin_amdgcn_s_setprio(0);
      }
      ROWS_WAIT(a_fl)
      u32x4 df[4];
#pragma unroll
      for (int i = 0; i < 4; ++i) df[i] = trfrag(L + (a_d0 ^ (32 * i)), L + (a_d0 ^ (32 * i)) + 4096);
      if (((near >> w) ^ (near >> xcd)) & 1) __builtin_amdgcn_s_setprio(3);
      SB();
      BAR();
      const u32x4 h0 = *(LAS u32x4*)p_hr, h1 = *(LAS u32x4*)p_hs;
      const f32x4 z = {0.f, 0.f, 0.f, 0.f};
      const f32x4 a0 = mfma(df[1], h1, mfma(df[0], h0, z)), a1 = mfma(df[3], h1, mfma(df[2], h0, z));
      st_sc1(rs, yo, tag8(a0, tagv, sel));
      st_sc1(rs, yo2, tag8(a1, tagv, sel));
    } else if (w < 12) {
      NEWPAGE();
      LAS unsigned char *p_st = L + L_ML + (n & 7) * 16, *p_h2r = L + a_h2r, *p_hw = L + a_hw;
      PIN(p_st); PIN(p_h2r); PIN(p_hw);
      __builtin_amdgcn_s_setprio(3);
      u32x4 gf[16], gb_[8];
      const u32 af = L_W + (8 * g + (n >> 2)) * 128 + (((w & 3) + (n & 2) * 2) ^ (((n >> 3) | ((g & 1) << 1)) << 1)) * 16 + (n & 1) * 8;
      f32x4 aL = {0.f, 0.f, 0.f, 0.f}, aH;
      f32x2 ag, au, sg, su, gt, up;
      {
#pragma unroll
        for (int q4 = 0; q4 < 4; ++q4) {
#pragma unroll
          for (int i = 0; i < 4; ++i) gb_[i] = *(LAS u32x4*)(L + L_C1 + n * C1S + g * 16 + q4 * 256 + i * 64);
          if (q4 == 3) {
            VM0();
            BAR();
          }
#pragma unroll
          for (int i = 0; i < 4; ++i) gf[4 * q4 + i] = trfrag(L + af + (4 * q4 + i) * 4096, L + af + (4 * q4 + i) * 4096 + 512);
          SB();
#pragma unroll
          for (int i = 0; i < 4; ++i) aL = mfma(gf[4 * q4 + i], gb_[i], aL);
          PIN(aL);
        }
        const auto s0 = __builtin_amdgcn_permlane32_swap(__float_as_uint(aL[0]), __float_as_uint(aL[2]), false, false);
        const auto s1 = __builtin_amdgcn_permlane32_swap(__float_as_uint(aL[1]), __float_as_uint(aL[3]), false, false);
        ag = -f32x2{__uint_as_float(s0[0]), __uint_as_float(s1[0])};
        au = -f32x2{__uint_as_float(s0[1]), __uint_as_float(s1[1])};
        sg = f32x2{dppf<ROR8>(ag[0]), dppf<ROR8>(ag[1])} * 1.4426950408889634f;
        su = -f32x2{dppf<ROR8>(au[0]), dppf<ROR8>(au[1])};
        PIN(ag); PIN(au); PIN(sg); PIN(su);
      }
      __builtin_amdgcn_s_setprio(2);
      ROWS_WAIT(a_fl)
      __builtin_amdgcn_s_setprio(1);
      GU_MUL(p_h2r)
      SB();
      f32x4 st = *(volatile LAS f32x4*)p_st;
      gu_pair(aL, aH, gt, up);
      if (!__all(st[3] != 0.f)) {
        _Pragma("unroll 1") for (u32 s = 0; s < LSPIN_CAP; ++s) { st = *(volatile LAS f32x4*)p_st; if (__all(st[3] != 0.f)) break; }
      }
      gt = __builtin_elementwise_fma(__builtin_elementwise_fma(ag, f32x2{st[0], st[0]}, gt), f32x2{st[2], st[2]}, sg);
      up = __builtin_elementwise_fma(__builtin_elementwise_fma(au, f32x2{st[0], st[0]}, up), f32x2{st[1], st[1]}, su);
      *(LAS u32*)p_hw = pk2(f32x2{rcp(1.f + __builtin_amdgcn_exp2f(gt[0])), rcp(1.f + __builtin_amdgcn_exp2f(gt[1]))} * up);
      BAR();
    } else {
      if (w == 12) asm volatile("s_waitcnt vmcnt(2)" ::: "memory"); else VM0();
      BAR();
      asm volatile("s_waitcnt vmcnt(0)" : "+v"(bp), "+v"(bq) :: "memory");
      if (w == 12 && __builtin_expect(!on_time(tag, org, bp, bq), 0)) {
        ctl[C_TX] = 2u;
        ctl[C_TX] = (anchor(rs, bo, tag, org, bp, bq) + DX) | 1u;
      }
      if (w == 15) {
        until_s(org + OFO + DX + 100);
        ROWS_WAIT(a_fl)
        if (lane < 8) st_sc1_64(rs, WS_B1 + lane * 512 + p3 * 8, u32x2{now4(), tagv});
      }
      BAR();
    }
    asm volatile("fused_code_end:");
  } else if (red && (w == RW || w == RW - 1)) {
    NEWPAGE();
    const int f = 16 * xcd + 2 * b + hj, ro_ = 4 * (w & 1) + (lane & 3);
    const int rb = e >> 4, rj = e & 15, c2 = 32 * rj + 4 * (lane & 7) + g;
    u32 xr_ = x[rb * 512 + c2];
    u32 gar = (__builtin_amdgcn_raw_buffer_load_b16(rs, WS_MODR + rb * 1024 + mpos(c2), 0, SC1));
    u32 gbr = (__builtin_amdgcn_raw_buffer_load_b16(rs, WS_MODR + (24 + ro_) * 1024 + mpos(4 * f + g), 0, SC1));
    int po = tline(xcd, LN_PART + (2 * b + hj) * 8, near) + lane * 16;
    int xo = tline((lane >> 3) ^ (~xcd & 4), LN_X2 + e, near) + (lane & 7) * 16;
    int xi = tline(xcd, LN_X2 + 16 * ro_, near) + (4 * f + g) * 4;
    u32 xt = (g & 1) ? tag >> 16 : tag & 0xffffu;
    const u32x4 tb4 = bytes4(tag);
    u32x4 tb5 = lane < 44 ? tb4 : u32x4{0, 0, 0, 0};
    const int yl = LN_YP + (f & 15) * NP3 + (lane >> 2);
    int yo = tline(xcd, yl, near) + ro_ * 16, y1 = tline(xcd, yl + 16, near) + ro_ * 16;
    int y2 = lane < 44 ? tline(xcd, yl + 32, near) + ro_ * 16 : WS_END;
    PIN(y1);
    u32 oof = (lane & 12) == 0 ? (ro_ * 512 + 4 * f + g) * 2 : 8192;
    __amdgpu_buffer_rsrc_t ro = __builtin_amdgcn_make_buffer_rsrc((void*)out, 0, 8192, 0x00020000);
    asm volatile("" : "+s"(ro));
    PIN(xt); PIN(tb5); PIN(po); PIN(xo); PIN(xi); PIN(yo); PIN(y2); PIN(oof);
    if (w == RW) {
      u32x4 bp, bq;
      until_s(org + OFB);
      B0_LOAD()
      SB();
      PIN(gar); PIN(xr_);
      float ga = blo(gar), xx = blo(xr_);
      PIN(ga); PIN(xx);
      u32 anc = org + OFO;
      if (__builtin_expect(!on_time(tag, org, bp, bq), 0)) anc = anchor(rs, bo, tag, org, bp, bq);
      until(anc + DP);
      u32x4 xq;
#pragma unroll 1
      for (u32 sp = 0; sp < SPIN_CAP; ++sp) {
        const u32x4 pv = ld_one(rs, po) ^ tb4;
        float t = t32(t16(asf(pv[0]), asf(pv[1])), t16(asf(pv[2]), asf(pv[3])));
        t += dppf<ROR8>(t);
        const u32 xw = __float_as_uint(rbf(xx + rbf(ga * rbf(t)))) | xt;
        const auto p1 = __builtin_amdgcn_permlane16_swap(xw, xw, false, false);
        const auto p2 = __builtin_amdgcn_permlane32_swap(p1[0], p1[0], false, false), p3_ = __builtin_amdgcn_permlane32_swap(p1[1], p1[1], false, false);
        xq = u32x4{p2[0], p3_[0], p2[1], p3_[1]};
        PIN(xq);
        if (__builtin_expect(__all((or4(pv) & 255u) == 0u), 1)) break;
        MEMB();
      }
      st_sc1(rs, xo, xq);
      if (bid == 1 && lane == 0) __hip_atomic_store((GAS u32*)ws, tag, RLX_AGENT);
      u32 ty = anc + DYS;
      const int lt = (int)(now4() - anc) - (DX + 20);
      if (__builtin_expect(lt > 0, 0)) {
        int lk = DB1;
        while (lk < DB1 + lt + 240) lk += PB;
        u32x2 bv;
#pragma unroll 1
        for (u32 sp = 0; sp < SPIN_CAP; ++sp) {
          until_s(anc + lk);
          bv = ld_sc1_64(rs, WS_B1 + xcd * 512 + (lane < NP3 ? lane : 0) * 8);
          if (__all(bv[1] == tag)) break;
          MEMB();
          do lk += PB; while ((int)(now4() - anc) > lk);
        }
        ty = org + smax((int)(bv[0] - org)) + DY;
        asm volatile("fused_cold_end:");
      }
      ctl[C_TX] = ty | 1u;
    }
    PIN(gbr);
    float gb = blo(gbr);
    PIN(gb);
    lds_wait(ctl + C_TX, 1u);
    until(__builtin_amdgcn_readfirstlane(ctl[C_TX]));
    u32 res;
#pragma unroll 1
    for (u32 sp = 0; sp < SPIN_CAP; ++sp) {
      const u32 x2 = __builtin_amdgcn_raw_buffer_load_b32(rs, xi, 0, SC1) ^ xt;
      const u32x4 y0 = ld_one(rs, yo) ^ tb4, y1_ = ld_one(rs, y1) ^ tb4, y2_ = ld_one(rs, y2) ^ tb5;
      const f32x4 s = (__builtin_bit_cast(f32x4, y0) + __builtin_bit_cast(f32x4, y1_)) + __builtin_bit_cast(f32x4, y2_);
      float t = t32(t16(s[0], s[1]), t16(s[2], s[3]));
      t += dppf<ROR8>(t);
      t += dppf<ROR4>(t);
      res = pack2(fmaf(gb, t, asf(x2)), 0.f);
      PIN(res);
      if (__builtin_expect(__all((((or4(y0) | or4(y1_) | or4(y2_)) & 255u) | (x2 & 0xffffu)) == 0u), 1)) break;
      MEMB();
    }
    __builtin_amdgcn_raw_buffer_store_b16((u16)res, ro, oof, 0, 0);
  }
}

extern "C" void kernel_launch(void* const* d_in, const int* in_sizes, int n_in, void* d_out, int out_size, void* d_ws, size_t ws_size, hipStream_t stream) {
  (void)hipFuncSetAttribute((const void*)fused, hipFuncAttributeMaxDynamicSharedMemorySize, L_END);
  Args a;
  const int src[4][2] = {{3, 3}, {10, 10}, {4, 4}, {6, 7}};
  for (int r = 0; r < 4; ++r)
    for (int k = 0; k < 2; ++k) a.half[r][k] = (const unsigned char*)d_in[src[r][k]] + (r < 3 && k ? 64 : 0);
  a.x = (const u16*)d_in[0]; a.kc = (const u16*)d_in[1]; a.vc = (const u16*)d_in[2]; a.wd = (const u16*)d_in[8]; a.cc = (const u16*)d_in[9];
  a.out = (u16*)d_out; a.ws = (unsigned char*)d_ws;
  fused<<<256, 1024, L_END, stream>>>(a);
}
```
